```python
import torch

def _softmax(x, axis=-1):
    return torch.softmax(x, dim=axis)
def _gelu(x):
    return torch.nn.functional.gelu(x, approximate="tanh")
def _silu(x):
    return torch.nn.functional.silu(x)
def _sigmoid(x):
    return torch.sigmoid(x)
def _layernorm(x, axis=-1, eps=1e-5):
    m = x.mean(dim=axis, keepdim=True)
    v = x.var(dim=axis, keepdim=True, unbiased=False)
    return (x - m) / torch.sqrt(v + eps)
def _scatter_add(tgt, idx, src, axis=0):
    return tgt.clone().index_add_(axis, idx, src)
def _segment_sum(src, seg, n):
    out = torch.zeros((n,) + src.shape[1:], dtype=src.dtype)
    return out.index_add_(0, seg, src)
def _pad(x, pw):
    flat = tuple(v for pair in reversed(pw) for v in pair)
    return torch.nn.functional.pad(x, flat)

kernel_name = 'parallel_sum__ssm_conv_s5__glu_gated_swiglu__prompt'

def setup_inputs():

    _w = (lambda a: (lambda t: t.to(torch.bfloat16) if t.is_floating_point() else t)(torch.as_tensor(a)))
    def _A_setup():
        B, S, D = 1, 2048, 640
        P = D
        Lambda_re = -0.5 * (lambda _x: torch.exp(torch.as_tensor(_x)))((lambda *s: torch.randn(*s, dtype=torch.bfloat16) * 0.2)(P))
        Lambda_im = (lambda *s: torch.randn(*s, dtype=torch.bfloat16) * 0.8)(P) * 1.0
        B_re = (lambda *s: torch.randn(*s, dtype=torch.bfloat16) * 0.2)(P, D) * (D ** -0.5)
        B_im = (lambda *s: torch.randn(*s, dtype=torch.bfloat16) * 0.2)(P, D) * (D ** -0.5)
        C_re = (lambda *s: torch.randn(*s, dtype=torch.bfloat16) * 0.2)(D, P) * (P ** -0.5)
        C_im = (lambda *s: torch.randn(*s, dtype=torch.bfloat16) * 0.2)(D, P) * (P ** -0.5)
        log_dt = (lambda *s: torch.randn(*s, dtype=torch.bfloat16) * 0.2)(P) * 0.3 - 2.0
        Dskip = (lambda *s: torch.randn(*s, dtype=torch.bfloat16) * 0.05)(D)
        return {
            "x": _w((lambda *s: torch.randn(*s, dtype=torch.bfloat16) * 0.05)(B, S, D)),
            "Lambda_re": _w(Lambda_re),
            "Lambda_im": _w(Lambda_im),
            "B_re": _w(B_re),
            "B_im": _w(B_im),
            "C_re": _w(C_re),
            "C_im": _w(C_im),
            "log_dt": _w(log_dt),
            "Dskip": _w(Dskip),
        }
    _A_in = _A_setup()

    def _B_setup():
        B, S, D, F = 1, 2048, 640, 1728
        return {
            "x": _w((lambda *s: torch.randn(*s, dtype=torch.bfloat16) * 0.05)(B, S, D)),
            "w1": _w((lambda *s: torch.randn(*s, dtype=torch.bfloat16) * 0.4)(D, F) * (2.0 / D) ** 0.5),
            "w3": _w((lambda *s: torch.randn(*s, dtype=torch.bfloat16) * 0.4)(D, F) * (2.0 / D) ** 0.5),
            "w2": _w((lambda *s: torch.randn(*s, dtype=torch.bfloat16) * 0.4)(F, D) * (2.0 / F) ** 0.5),
        }
    _B_in = _B_setup()

    return {
        "A_x": _A_in["x"],
        "A_Lambda_re": _A_in["Lambda_re"],
        "A_Lambda_im": _A_in["Lambda_im"],
        "A_B_re": _A_in["B_re"],
        "A_B_im": _A_in["B_im"],
        "A_C_re": _A_in["C_re"],
        "A_C_im": _A_in["C_im"],
        "A_log_dt": _A_in["log_dt"],
        "A_Dskip": _A_in["Dskip"],
        "B_x": _B_in["x"],
        "B_w1": _B_in["w1"],
        "B_w3": _B_in["w3"],
        "B_w2": _B_in["w2"],
    }

def reference(A_x, A_Lambda_re, A_Lambda_im, A_B_re, A_B_im, A_C_re, A_C_im, A_log_dt, A_Dskip, B_x, B_w1, B_w3, B_w2):
    def _A_ref(x, Lambda_re, Lambda_im, B_re, B_im, C_re, C_im, log_dt, Dskip):
        Bsz, L, H = x.shape
        Lam = Lambda_re + 1j * Lambda_im
        Bmat = B_re + 1j * B_im
        Cmat = C_re + 1j * C_im
        dt = torch.exp(log_dt)
        Lam_bar = torch.exp(dt * Lam)
        B_bar = ((Lam_bar - 1.0) / Lam)[:, None] * Bmat
        Bu = torch.einsum("bld,pd->blp", x + 0j, B_bar)
        h = torch.zeros_like(Bu[:, 0, :])
        hs = []
        for t in range(L):
            h = Lam_bar[None, :] * h + Bu[:, t, :]
            hs.append(h)
        hseq = torch.stack(hs, 1)
        y = 2.0 * torch.einsum("blp,dp->bld", hseq, Cmat).real + x * Dskip[None, None, :]
        return y

    def _B_ref(x, w1, w3, w2):
        gate = torch.einsum("bsd,df->bsf", x, w1)
        up = torch.einsum("bsd,df->bsf", x, w3)
        h = _silu(gate) * up
        out = torch.einsum("bsf,fd->bsd", h, w2)
        return out

    x = A_x
    xn = _layernorm(x, axis=-1)
    return x + _A_ref(xn, A_Lambda_re, A_Lambda_im, A_B_re, A_B_im, A_C_re, A_C_im, A_log_dt, A_Dskip) + _B_ref(xn, B_w1, B_w3, B_w2)
```

```cpp
#include <hip/hip_runtime.h>
#include <hip/hip_bf16.h>
#include <stdint.h>

typedef unsigned short u16;
constexpr int L = 2048, D = 640, P = 640, F = 1728;

__device__ __forceinline__ float b2f(u16 v) { return __uint_as_float(((unsigned)v) << 16); }
__device__ __forceinline__ u16 f2b(float f) { unsigned u = __float_as_uint(f); u += 0x7FFF + ((u >> 16) & 1); return (u16)(u >> 16); }
__device__ __forceinline__ float rb(float f) { return b2f(f2b(f)); }

__global__ void k_ln(const u16* x, u16* xn) {
  int row = blockIdx.x; int t = threadIdx.x;
  float v[10]; float s = 0;
  for (int i = 0; i < 10; ++i) { v[i] = b2f(x[row * D + t + 64 * i]); s += v[i]; }
  for (int o = 32; o; o >>= 1) s += __shfl_xor(s, o);
  float m = s / D; float q = 0;
  for (int i = 0; i < 10; ++i) { float d = v[i] - m; q += d * d; }
  for (int o = 32; o; o >>= 1) q += __shfl_xor(q, o);
  float var = rb(q / D); float mb = rb(m);
  float sd = rb(sqrtf(rb(var + 1e-5f)));
  for (int i = 0; i < 10; ++i) xn[row * D + t + 64 * i] = f2b(rb(v[i] - mb) / sd);
}
__global__ void k_bu(const u16* xn, const u16* Bre, const u16* Bim, float* ure, float* uim) {
  int p = blockIdx.x * 64 + threadIdx.x; int t = blockIdx.y;
  float a = 0, b = 0;
  for (int k = 0; k < D; ++k) { float xv = b2f(xn[t * D + k]); a += xv * b2f(Bre[p * D + k]); b += xv * b2f(Bim[p * D + k]); }
  ure[t * P + p] = a; uim[t * P + p] = b;
}
__global__ void k_scan(const u16* Lre, const u16* Lim, const u16* ldt, float* ure, float* uim) {
  int p = blockIdx.x * 64 + threadIdx.x;
  float dt = rb(expf(b2f(ldt[p]))); float lr = b2f(Lre[p]), li = b2f(Lim[p]);
  float e = expf(dt * lr); float br = e * cosf(dt * li), bi = e * sinf(dt * li);
  float nr = br - 1.f, ni = bi; float den = lr * lr + li * li;
  float cr = (nr * lr + ni * li) / den, ci = (ni * lr - nr * li) / den;
  float hr = 0, hi = 0;
  for (int t = 0; t < L; ++t) {
    float a = ure[t * P + p], b = uim[t * P + p];
    float vr = cr * a - ci * b, vi = cr * b + ci * a;
    float n_r = br * hr - bi * hi + vr, n_i = br * hi + bi * hr + vi;
    hr = n_r; hi = n_i; ure[t * P + p] = hr; uim[t * P + p] = hi;
  }
}
__global__ void k_ffn1(const u16* xn, const u16* w1, const u16* w3, u16* hm) {
  int f = blockIdx.x * 64 + threadIdx.x; int t = blockIdx.y;
  float a = 0, b = 0;
  for (int k = 0; k < D; ++k) { float xv = b2f(xn[t * D + k]); a += xv * b2f(w1[k * F + f]); b += xv * b2f(w3[k * F + f]); }
  a = rb(a); b = rb(b);
  float s = rb(a / (1.f + expf(-a)));
  hm[t * F + f] = f2b(s * b);
}
__global__ void k_out(const u16* x, const u16* xn, const float* hre, const float* him, const u16* Cre, const u16* Cim,
                      const u16* Dsk, const u16* hm, const u16* w2, float* out) {
  int d = blockIdx.x * 64 + threadIdx.x; int t = blockIdx.y;
  float a = 0, b = 0;
  for (int p = 0; p < P; ++p) a += hre[t * P + p] * b2f(Cre[d * P + p]) - him[t * P + p] * b2f(Cim[d * P + p]);
  for (int f = 0; f < F; ++f) b += b2f(hm[t * F + f]) * b2f(w2[f * D + d]);
  float y = 2.f * a + rb(b2f(xn[t * D + d]) * b2f(Dsk[d]));
  out[t * D + d] = b2f(x[t * D + d]) + y + rb(b);
}

extern "C" void kernel_launch(void* const* d_in, const int* in_sizes, int n_in,
                              void* d_out, int out_size, void* d_ws, size_t ws_size,
                              hipStream_t stream) {
  const u16* x = (const u16*)d_in[0]; const u16* Lre = (const u16*)d_in[1]; const u16* Lim = (const u16*)d_in[2];
  const u16* Bre = (const u16*)d_in[3]; const u16* Bim = (const u16*)d_in[4]; const u16* Cre = (const u16*)d_in[5];
  const u16* Cim = (const u16*)d_in[6]; const u16* ldt = (const u16*)d_in[7]; const u16* Dsk = (const u16*)d_in[8];
  const u16* w1 = (const u16*)d_in[10]; const u16* w3 = (const u16*)d_in[11]; const u16* w2 = (const u16*)d_in[12];
  char* ws = (char*)d_ws;
  u16* xn = (u16*)ws; float* ure = (float*)(ws + (4 << 20)); float* uim = (float*)(ws + (10 << 20)); u16* hm = (u16*)(ws + (16 << 20));
  k_ln<<<L, 64, 0, stream>>>(x, xn);
  k_bu<<<dim3(P / 64, L), 64, 0, stream>>>(xn, Bre, Bim, ure, uim);
  k_scan<<<P / 64, 64, 0, stream>>>(Lre, Lim, ldt, ure, uim);
  k_ffn1<<<dim3(F / 64, L), 64, 0, stream>>>(xn, w1, w3, hm);
  k_out<<<dim3(D / 64, L), 64, 0, stream>>>(x, xn, ure, uim, Cre, Cim, Dsk, hm, w2, (float*)d_out);
}
```

```cpp
#include <hip/hip_runtime.h>
#include <hip/hip_bf16.h>
#include <stdint.h>
#define CLOB "v1","v2","v3","v4","v5","v6","v7","v8","v9","v10","v11","v12","v13","v14","v15","v16","v17","v18","v19","v20","v21","v22","v23","v24","v25","v26","v27","v28","v29","v30","v31","v32","v33","v34","v35","v36","v37","v38","v39","v40","v41","v42","v43","v44","v45","v46","v47","v48","v49","v50","v51","v52","v53","v54","v55","v56","v57","v58","v59","v60","v61","v62","v63","v64","v65","v66","v67","v68","v69","v70","v71","v72","v73","v74","v75","v76","v77","v78","v79","v80","v81","v82","v83","v84","v85","v86","v87","v88","v89","v90","v91","v92","v93","v94","v95","v96","v97","v98","v99","v100","v101","v102","v103","v104","v105","v106","v107","v108","v109","v110","v111","v112","v113","v114","v115","v116","v117","v118","v119","v120","v121","v122","v123","v124","v125","v126","v127","v128","v129","v130","v131","v132","v133","v134","v135","v136","v137","v138","v139","v140","v141","v142","v143","v144","v145","v146","v147","v148","v149","v150","v151","v152","v153","v154","v155","v156","v157","v158","v159","v160","v161","v162","v163","v164","v165","v166","v167","v168","v169","v170","v171","v172","v173","v174","v175","v176","v177","v178","v179","v180","v181","v182","v183","v184","v185","v186","v187","v188","v189","v190","v191","v192","v193","v194","v195","v196","v197","v198","v199","v200","v201","v202","v203","v204","v205","v206","v207","v208","v209","v210","v211","v212","v213","v214","v215","v216","v217","v218","v219","v220","v221","v222","v223","v224","v225","v226","v227","v228","v229","v230","v231","v232","v233","v234","v235","v236","v237","v238","v239","v240","v241","v242","v243","v244","v245","v246","v247","v248","v249","v250","v251","v252","v253","v254","v255","a0","a1","a2","a3","a4","a5","a6","a7","a8","a9","a10","a11","a12","a13","a14","a15","a16","a17","a18","a19","a20","a21","a22","a23","a24","a25","a26","a27","a28","a29","a30","a31","a32","a33","a34","a35","a36","a37","a38","a39","a40","a41","a42","a43","a44","a45","a46","a47","a48","a49","a50","a51","a52","a53","a54","a55","a56","a57","a58","a59","a60","a61","a62","a63","a64","a65","a66","a67","a68","a69","a70","a71","a72","a73","a74","a75","a76","a77","a78","a79","a80","a81","a82","a83","a84","a85","a86","a87","a88","a89","a90","a91","a92","a93","a94","a95","a96","a97","a98","a99","a100","a101","a102","a103","a104","a105","a106","a107","a108","a109","a110","a111","a112","a113","a114","a115","a116","a117","a118","a119","a120","a121","a122","a123","a124","a125","a126","a127","a128","a129","a130","a131","a132","a133","a134","a135","a136","a137","a138","a139","a140","a141","a142","a143","a144","a145","a146","a147","a148","a149","a150","a151","a152","a153","a154","a155","a156","a157","a158","a159","a160","a161","a162","a163","a164","a165","a166","a167","a168","a169","a170","a171","a172","a173","a174","a175","a176","a177","a178","a179","a180","a181","a182","a183","a184","a185","a186","a187","a188","a189","a190","a191","a192","a193","a194","a195","a196","a197","a198","a199","a200","a201","a202","a203","a204","a205","a206","a207","a208","a209","a210","a211","a212","a213","a214","a215","a216","a217","a218","a219","a220","a221","a222","a223","a224","a225","a226","a227","a228","a229","a230","a231","a232","a233","a234","a235","a236","a237","a238","a239","a240","a241","a242","a243","a244","a245","a246","a247","a248","a249","a250","a251","a252","a253","a254","a255","s4","s5","s6","s7","s8","s9","s10","s11","s12","s13","s14","s15","s16","s17","s18","s19","s20","s21","s22","s23","s24","s25","s26","s27","s28","s29","s30","s31","s33","s34","s35","s36","s37","s38","s39","s40","s41","s42","s43","s44","s45","s46","s47","s48","s49","s50","s51","s52","s53","s54","s55","s56","s57","s58","s59","s60","s61","s62","s63","s64","s65","s66","s67","s68","s69","s70","s71","s72","s73","s74","s75","s76","s77","s78","s79","s80","s81","s82","s83","s84","s85","s86","s87","s88","s89","s90","s91","s92","s93","s94","s95","s96","s97","s98","s99","s100","s101","vcc","scc","memory"
struct Tab { uint32_t w[56]; };

__global__ __launch_bounds__(256, 1) void s5_block(const void* x, const void* lre, const void* lim, const void* bre, const void* bim, const void* cre,
                                                    const void* cim, const void* ldt, const void* w1, const void* w3, const void* w2, void* ws,
                                                    const void* dsk, void* out, Tab tab) {
  __shared__ __attribute__((aligned(16))) char lds[163840];
  asm volatile(
"s_mov_b64 s[4:5], %0\n"
"s_mov_b32 s6, %1\n"
"v_mov_b32 v1, %2\n"
"s_load_dwordx16 s[16:31], s[4:5], 0x0\n"
"s_load_dwordx8 s[32:39], s[4:5], 0x40\n"
"s_getpc_b64 s[60:61]\n"
"K1PC:\n"
"v_lshrrev_b32 v68, 6, v1\n"
"s_lshr_b32 s69, s6, 3\n"
"s_lshl_b32 s69, s69, 12\n"
"v_and_b32 v69, 7, v1\n"
"v_lshlrev_b32 v69, 7, v69\n"
"v_lshl_add_u32 v69, v68, 10, v69\n"
"v_add_u32 v69, s69, v69\n"
"v_min_u32 v69, K1END-K1PC-4, v69\n"
"global_load_dword v67, v69, s[60:61]\n"
"v_lshrrev_b32 v68, 6, v1\n"
"v_and_b32 v27, 63, v1\n"
"v_readfirstlane_b32 s7, v68\n"
"s_and_b32 s68, s6, 7\n"
"s_lshr_b32 s69, s6, 3\n"
"s_lshr_b32 s8, s68, 1\n"
"s_lshl_b32 s8, s8, 2\n"
"s_lshr_b32 s9, s69, 3\n"
"s_add_u32 s8, s8, s9\n"
"s_and_b32 s9, s68, 1\n"
"s_lshl_b32 s9, s9, 3\n"
"s_and_b32 s69, s69, 7\n"
"s_add_u32 s9, s9, s69\n"
"s_mul_i32 s10, s7, 40960\n"
"s_lshl_b32 s68, s8, 2\n"
"s_add_u32 s68, s68, s7\n"
"s_mul_i32 s68, s68, 40960\n"
"s_lshl_b32 s69, s9, 3\n"
"s_add_u32 s69, s69, 0x70\n"
"s_load_dwordx2 s[100:101], s[4:5], s69\n"
"s_mov_b32 s12, 0\n"
"v_lshrrev_b32 v68, 3, v27\n"
"v_and_b32 v69, 7, v27\n"
"v_lshrrev_b32 v70, 4, v27\n"
"v_xor_b32 v71, v69, v70\n"
"v_mul_u32_u24 v72, 1280, v68\n"
"v_lshl_add_u32 v22, v71, 4, v72\n"
"v_xor_b32 v23, 64, v22\n"
"v_add_u32 v23, 9216, v23\n"
"v_add_u32 v24, 18432, v22\n"
"v_add_u32 v25, 18432, v23\n"
"s_waitcnt lgkmcnt(0)\n"
"s_add_u32 s44, s16, s68\n"
"s_addc_u32 s45, s17, 0\n"
"s_and_b32 s45, s45, 0xffff\n"
"s_mov_b32 s46, 40960\n"
"s_mov_b32 s47, 0x20000\n"
"s_add_u32 m0, s10, 32768\n"
"s_nop 0\n"
"buffer_load_dwordx4 v22, s[44:47], s12 offen offset:0 lds\n"
"buffer_load_dwordx4 v23, s[44:47], s12 offen offset:1024 lds\n"
"buffer_load_dwordx4 v24, s[44:47], s12 offen offset:2048 lds\n"
"buffer_load_dwordx4 v25, s[44:47], s12 offen offset:3072 lds\n"
"s_cmp_ge_u32 s9, 14\n"
"s_cbranch_scc1 K1S\n"
"s_cmp_eq_u32 s9, 13\n"
"s_cbranch_scc1 K1X\n"
"K1F:\n"
"s_mov_b32 s15, s9\n"
"s_bitcmp1_b32 s7, 0\n"
"s_cselect_b32 s56, s24, s22\n"
"s_cselect_b32 s57, s25, s23\n"
"s_cselect_b32 s48, s34, s32\n"
"s_cselect_b32 s49, s35, s33\n"
"s_cselect_b32 s69, 4, 0\n"
"s_and_b32 s49, s49, 0xffff\n"
"s_mov_b32 s50, 2211840\n"
"s_mov_b32 s51, 0x20000\n"
"s_lshr_b32 s13, s7, 1\n"
"s_mul_i32 s13, s13, 864\n"
"s_lshl_b32 s68, s9, 1\n"
"s_add_u32 s13, s13, s68\n"
"s_lshl_b32 s13, s13, 7\n"
"v_and_b32 v71, 1, v70\n"
"v_lshl_or_b32 v71, v71, 1, s69\n"
"v_xor_b32 v71, v69, v71\n"
"v_mul_u32_u24 v73, 3456, v68\n"
"v_lshl_add_u32 v18, v71, 4, v73\n"
"v_add_u32 v19, 26624, v18\n"
"v_add_u32 v20, 53248, v18\n"
"v_add_u32 v21, 79872, v18\n"
"s_add_u32 s52, s13, 128\n"
"s_and_b32 s57, s57, 0xffff\n"
"s_mov_b32 s58, 819200\n"
"s_mov_b32 s59, 0x20000\n"
"s_lshl_b32 s14, s15, 1\n"
"s_lshr_b32 s68, s7, 1\n"
"s_add_u32 s14, s14, s68\n"
"s_mul_i32 s14, s14, 20480\n"
"s_add_u32 m0, s10, 36864\n"
"s_nop 0\n"
"buffer_load_dwordx4 v18, s[48:51], s13 offen offset:0 lds\n"
"buffer_load_dwordx4 v19, s[48:51], s13 offen offset:1024 lds\n"
"buffer_load_dwordx4 v20, s[48:51], s13 offen offset:2048 lds\n"
"buffer_load_dwordx4 v21, s[48:51], s13 offen offset:3072 lds\n"
"s_add_u32 m0, s10, 2048\n"
"s_nop 0\n"
"buffer_load_dwordx4 v22, s[56:59], s14 offen offset:0 lds\n"
"buffer_load_dwordx4 v23, s[56:59], s14 offen offset:1024 lds\n"
"s_mov_b64 s[64:65], s[38:39]\n"
"s_and_b32 s65, s65, 0xffff\n"
"s_mov_b32 s66, 35651584\n"
"s_mov_b32 s67, 0x20000\n"
"v_mov_b32 v71, 23068672\n"
"buffer_load_dword v80, v71, s[64:67], 0 offen offset:0 sc1\n"
"buffer_load_dword v81, v71, s[64:67], 0 offen offset:4 sc1\n"
"s_lshl_b32 s68, s8, 4\n"
"s_add_u32 s68, s68, s9\n"
"s_lshl_b32 s68, s68, 2\n"
"s_add_u32 s68, s68, s7\n"
"s_mul_i32 s69, s68, 2560\n"
"v_lshrrev_b32 v71, 5, v27\n"
"v_and_b32 v73, 31, v27\n"
"v_mul_u32_u24 v74, 1280, v71\n"
"v_add_u32 v74, s69, v74\n"
"v_lshl_add_u32 v75, v73, 4, v74\n"
"v_lshl_add_u32 v74, v73, 3, v74\n"
"global_load_dwordx4 v[44:47], v75, s[16:17]\n"
"global_load_dwordx4 v[48:51], v75, s[16:17] offset:512\n"
"global_load_dwordx2 v[52:53], v74, s[16:17] offset:1024\n"
"s_lshl_b32 s68, s68, 1\n"
"v_add_u32 v82, s68, v71\n"
"s_bitcmp1_b32 s8, 2\n"
"s_cselect_b32 s60, s24, s22\n"
"s_cselect_b32 s61, s25, s23\n"
"s_and_b32 s68, s8, 3\n"
"s_lshl_b32 s68, s68, 2\n"
"s_add_u32 s68, s68, s7\n"
"s_lshl_b32 s69, s15, 4\n"
"s_add_u32 s68, s68, s69\n"
"s_mul_i32 s68, s68, 2560\n"
"v_mul_u32_u24 v74, 1280, v71\n"
"v_add_u32 v74, s68, v74\n"
"v_lshl_add_u32 v75, v73, 4, v74\n"
"v_lshl_add_u32 v74, v73, 3, v74\n"
"global_load_dwordx4 v[54:57], v75, s[60:61]\n"
"global_load_dwordx4 v[58:61], v75, s[60:61] offset:512\n"
"global_load_dwordx2 v[62:63], v74, s[60:61] offset:1024\n"
"s_lshl_b32 s69, s15, 6\n"
"v_lshl_add_u32 v74, v73, 1, s69\n"
"global_load_ushort v64, v74, s[18:19] offset:0\n"
"global_load_ushort v65, v74, s[20:21] offset:0\n"
"global_load_ushort v66, v74, s[30:31] offset:0\n"
"v_lshrrev_b32 v68, 5, v27\n"
"v_and_b32 v69, 3, v27\n"
"v_bfe_u32 v70, v27, 3, 2\n"
"v_lshl_add_u32 v69, v70, 2, v69\n"
"v_bfe_u32 v70, v27, 2, 1\n"
"v_lshl_add_u32 v1, v70, 4, v69\n"
"v_lshrrev_b32 v69, 1, v69\n"
"v_xor_b32 v69, v69, v68\n"
"s_add_u32 s13, s13, 221184\n"
"s_add_u32 m0, s10, 28672\n"
"s_nop 0\n"
"buffer_load_dwordx4 v18, s[48:51], s13 offen offset:0 lds\n"
"v_lshlrev_b32 v69, 4, v69\n"
"v_lshl_add_u32 v69, v1, 7, v69\n"
"v_add_u32 v14, s10, v69\n"
"v_xor_b32 v15, 32, v14\n"
"v_xor_b32 v16, 64, v14\n"
"v_xor_b32 v17, 96, v14\n"
"v_bfe_u32 v69, v27, 1, 3\n"
"v_xor_b32 v69, v69, v68\n"
"buffer_load_dwordx4 v19, s[48:51], s13 offen offset:1024 lds\n"
"v_and_b32 v70, 15, v27\n"
"v_lshlrev_b32 v70, 7, v70\n"
"v_lshl_add_u32 v70, v69, 4, v70\n"
"v_bfe_u32 v69, v27, 4, 1\n"
"v_mul_u32_u24 v69, 81920, v69\n"
"v_add_u32 v10, v69, v70\n"
"v_xor_b32 v11, 32, v10\n"
"v_xor_b32 v12, 64, v10\n"
"buffer_load_dwordx4 v20, s[48:51], s13 offen offset:2048 lds\n"
"v_xor_b32 v13, 96, v10\n"
"v_bfe_u32 v69, v27, 2, 2\n"
"v_lshl_add_u32 v69, v68, 3, v69\n"
"v_lshlrev_b32 v69, 7, v69\n"
"v_bfe_u32 v70, v27, 1, 1\n"
"v_mul_u32_u24 v70, 41024, v70\n"
"v_bfe_u32 v71, v27, 3, 1\n"
"v_lshl_add_u32 v69, v71, 5, v69\n"
"buffer_load_dwordx4 v21, s[48:51], s13 offen offset:3072 lds\n"
"v_and_b32 v71, 1, v27\n"
"v_lshl_add_u32 v69, v71, 4, v69\n"
"v_bfe_u32 v71, v27, 4, 1\n"
"v_lshl_add_u32 v69, v71, 3, v69\n"
"v_add_u32 v2, v69, v70\n"
"v_xor_b32 v3, 32, v2\n"
"v_xor_b32 v4, 64, v2\n"
"v_xor_b32 v5, 96, v2\n"
"s_add_u32 s14, s14, 128\n"
"s_add_u32 m0, s10, 4096\n"
"s_nop 0\n"
"buffer_load_dwordx4 v22, s[56:59], s14 offen offset:0 lds\n"
"v_add_u32 v6, 81920, v2\n"
"v_add_u32 v7, 81920, v3\n"
"v_add_u32 v8, 81920, v4\n"
"v_add_u32 v9, 81920, v5\n"
"v_lshlrev_b32 v28, 4, v27\n"
"s_mov_b32 s74, 0xffff0000\n"
"s_lshl_b32 s69, s6, 2\n"
"s_add_u32 s69, s69, s7\n"
"buffer_load_dwordx4 v23, s[56:59], s14 offen offset:1024 lds\n"
"s_cmp_lt_u32 s69, 740\n"
"s_cselect_b32 s26, s26, s28\n"
"s_cselect_b32 s27, s27, s29\n"
"s_cselect_b32 s70, 0, 200\n"
"s_cselect_b32 s29, 0, 8\n"
"s_add_u32 s70, s70, 540\n"
"s_cmp_lt_u32 s69, 540\n"
"s_cselect_b32 s26, s36, s26\n"
"s_cselect_b32 s27, s37, s27\n"
"s_cselect_b32 s70, 0, s70\n"
"s_cselect_b32 s75, 1, 0\n"
"s_add_u32 s12, s12, 128\n"
"s_add_u32 m0, s10, 24576\n"
"s_nop 0\n"
"buffer_load_dwordx4 v22, s[44:47], s12 offen offset:0 lds\n"
"s_sub_u32 s70, s69, s70\n"
"s_mul_i32 s68, s70, 6554\n"
"s_lshr_b32 s68, s68, 16\n"
"s_mul_i32 s43, s68, 10\n"
"s_sub_u32 s70, s70, s43\n"
"s_cmp_lt_u32 s69, 940\n"
"s_cselect_b32 s28, 39808, 0\n"
"s_lshl_b32 s69, s70, 2\n"
"buffer_load_dwordx4 v23, s[44:47], s12 offen offset:1024 lds\n"
"s_add_u32 s43, s68, 40\n"
"s_cmp_eq_u32 s75, 1\n"
"s_cselect_b32 s43, s43, s69\n"
"s_and_b32 s69, s43, 0x7c\n"
"s_add_u32 s69, s69, 0xf0\n"
"s_load_dword s11, s[4:5], s69\n"
"s_mov_b32 s40, s68\n"
"s_mov_b32 s36, s70\n"
"buffer_load_dwordx4 v24, s[44:47], s12 offen offset:2048 lds\n"
"buffer_load_dwordx4 v25, s[44:47], s12 offen offset:3072 lds\n"
"s_add_u32 s13, s13, 221184\n"
"s_add_u32 m0, s10, 20480\n"
"s_nop 0\n"
"buffer_load_dwordx4 v18, s[48:51], s13 offen offset:0 lds\n"
"buffer_load_dwordx4 v19, s[48:51], s13 offen offset:1024 lds\n"
"buffer_load_dwordx4 v20, s[48:51], s13 offen offset:2048 lds\n"
"buffer_load_dwordx4 v21, s[48:51], s13 offen offset:3072 lds\n"
"s_waitcnt lgkmcnt(0)\n"
"s_mul_i32 s68, s40, 320\n"
"s_add_u32 s68, s68, s36\n"
"s_lshl_b32 s68, s68, 7\n"
"s_add_u32 s60, s26, s68\n"
"s_addc_u32 s61, s27, 0\n"
"s_mov_b32 s62, s28\n"
"s_and_b32 s61, s61, 0xffff\n"
"s_mov_b32 s63, 0x20000\n"
"s_and_b32 s68, s43, 3\n"
"s_lshl_b32 s68, s68, 3\n"
"s_add_u32 s68, s68, s29\n"
"s_lshr_b32 s11, s11, s68\n"
"s_and_b32 s28, s11, 0xff\n"
"s_bfe_u32 s29, s11, 0x80010\n"
"s_mul_i32 s28, s28, 40\n"
"s_mul_i32 s29, s29, 40\n"
"s_lshl_b32 s68, s36, 2\n"
"s_lshl_b32 s69, s40, 1\n"
"s_add_u32 s70, s28, 2\n"
"s_cmp_eq_u32 s75, 1\n"
"s_cselect_b32 s68, s68, s69\n"
"s_cselect_b32 s29, s70, s29\n"
"s_add_u32 s28, s28, s68\n"
"s_add_u32 s29, s29, s68\n"
"s_cmp_eq_u32 s62, 0\n"
"s_cselect_b32 s68, 2130706432, 0\n"
"s_add_u32 s68, s68, 16777216\n"
"s_lshl_b32 s28, s28, 10\n"
"s_lshl_b32 s29, s29, 10\n"
"s_add_u32 s28, s28, s68\n"
"s_add_u32 s29, s29, s68\n"
"s_lshl_b32 s68, s15, 3\n"
"s_and_b32 s69, s8, 7\n"
"s_add_u32 s68, s68, s69\n"
"s_lshl_b32 s68, s68, 2\n"
"s_add_u32 s68, s68, s7\n"
"s_lshl_b32 s68, s68, 5\n"
"s_add_u32 s68, s68, 21092352\n"
"s_cmp_lt_u32 s8, 8\n"
"s_cselect_b32 s88, s68, 2130706432\n"
"s_lshl_b32 s43, s8, 1\n"
"s_lshr_b32 s68, s7, 1\n"
"s_add_u32 s43, s43, s68\n"
"s_mul_i32 s43, s43, 393216\n"
"s_and_b32 s68, s7, 1\n"
"s_lshl_b32 s68, s68, 11\n"
"s_add_u32 s43, s43, s68\n"
"s_lshl_b32 s68, s15, 4\n"
"s_add_u32 s68, s68, s8\n"
"s_lshl_b32 s68, s68, 9\n"
"s_add_u32 s68, s68, 22020096\n"
"s_lshl_b32 s69, s7, 11\n"
"s_sub_u32 s90, s68, s69\n"
"s_sub_u32 s90, s90, 512\n"
"s_cmp_eq_u32 s7, 3\n"
"s_cselect_b32 s89, s68, 2130706432\n"
"s_lshl_b32 s68, s8, 2\n"
"s_add_u32 s68, s68, s7\n"
"s_lshl_b32 s68, s68, 9\n"
"s_add_u32 s91, s68, 20971520\n"
"s_lshl_b32 s92, s9, 12\n"
"s_lshl_b32 s93, s15, 10\n"
"s_add_u32 s93, s93, 55296\n"
"s_lshl_b32 s68, s6, 7\n"
"s_add_u32 s68, s68, 23072768\n"
"s_cmp_eq_u32 s7, 0\n"
"s_cselect_b32 s94, s68, 2130706432\n"
"s_and_b32 s68, s6, 6\n"
"s_lshl_b32 s30, s68, 1\n"
"s_lshr_b32 s68, s6, 6\n"
"s_add_u32 s30, s30, s68\n"
"s_bfe_u32 s31, s6, 0x30003\n"
"s_and_b32 s68, s6, 1\n"
"s_lshl_b32 s68, s68, 2\n"
"s_add_u32 s68, s68, s7\n"
"s_cmp_ge_u32 s68, 6\n"
"s_cselect_b32 s37, 11, 12\n"
"s_mul_i32 s32, s68, 49152\n"
"s_mul_i32 s68, s68, 96\n"
"s_add_u32 s68, s68, s31\n"
"s_mul_i32 s68, s68, 5120\n"
"s_add_u32 s68, s68, 16777216\n"
"s_add_u32 s76, s38, s68\n"
"s_addc_u32 s77, s39, 0\n"
"s_and_b32 s77, s77, 0xffff\n"
"s_mul_i32 s78, s37, 40960\n"
"s_sub_u32 s78, s78, 35840\n"
"s_mov_b32 s79, 0x20000\n"
"s_lshl_b32 s68, s6, 2\n"
"s_add_u32 s68, s68, s7\n"
"s_mul_i32 s34, s68, 5120\n"
"s_xor_b32 s68, s68, 4\n"
"s_mul_i32 s35, s68, 5120\n"
"s_add_u32 s34, s34, 24117248\n"
"s_add_u32 s35, s35, 24117248\n"
"s_lshl_b32 s36, s8, 4\n"
"s_add_u32 s36, s36, s9\n"
"s_lshl_b32 s36, s36, 4\n"
"s_add_u32 s36, s36, s7\n"
"s_lshl_b32 s36, s36, 3\n"
"s_add_u32 s36, s36, 23134208\n"
"s_and_b32 s68, s6, 1\n"
"s_xor_b32 s68, s68, 0\n"
"s_lshl_b32 s69, s30, 1\n"
"s_add_u32 s68, s68, s69\n"
"s_mul_i32 s68, s68, 393216\n"
"s_add_u32 s68, s68, s32\n"
"s_add_u32 s80, s38, s68\n"
"s_addc_u32 s81, s39, 0\n"
"s_and_b32 s81, s81, 0xffff\n"
"s_lshl_b32 s82, s37, 12\n"
"s_mov_b32 s83, 0x20000\n"
"s_and_b32 s68, s6, 1\n"
"s_xor_b32 s68, s68, 1\n"
"s_lshl_b32 s69, s30, 1\n"
"s_add_u32 s68, s68, s69\n"
"s_mul_i32 s68, s68, 393216\n"
"s_add_u32 s68, s68, s32\n"
"s_add_u32 s84, s38, s68\n"
"s_addc_u32 s85, s39, 0\n"
"s_and_b32 s85, s85, 0xffff\n"
"s_lshl_b32 s86, s37, 12\n"
"s_mov_b32 s87, 0x20000\n"
"s_mov_b32 s16, 0\n"
"s_mov_b32 s17, -1\n"
"v_lshrrev_b32 v68, 5, v27\n"
"s_lshl_b32 s68, s7, 2\n"
"v_add_u32 v69, s68, v68\n"
"v_cmp_gt_u32 s[18:19], s8, v69\n"
"v_add_u32 v69, 2, v69\n"
"v_cmp_gt_u32 s[20:21], s8, v69\n"
"v_and_b32 v70, 31, v27\n"
"s_lshl_b32 s68, s7, 9\n"
"v_lshl_add_u32 v29, v27, 3, s68\n"
"v_lshlrev_b32 v30, 3, v70\n"
"v_add_u32 v30, 256, v30\n"
"v_lshlrev_b32 v32, 6, v70\n"
"v_add_u32 v32, 122880, v32\n"
"s_lshl_b32 s68, s7, 4\n"
"v_lshl_add_u32 v31, v68, 3, v32\n"
"v_add_u32 v31, s68, v31\n"
"s_add_u32 s68, s10, 2048\n"
"v_mul_u32_u24 v33, 72, v70\n"
"v_lshl_add_u32 v33, v68, 3, v33\n"
"v_add_u32 v33, s68, v33\n"
"v_lshl_add_u32 v71, v70, 4, s89\n"
"v_mov_b32 v72, 2130706432\n"
"v_cndmask_b32 v35, v72, v71, s[16:17]\n"
"v_bfe_u32 v69, v27, 4, 1\n"
"v_lshlrev_b32 v69, 10, v69\n"
"v_lshl_add_u32 v69, v68, 8, v69\n"
"v_and_b32 v70, 15, v27\n"
"v_lshl_add_u32 v26, v70, 4, v69\n"
"v_and_b32 v68, 31, v27\n"
"v_lshrrev_b32 v69, 5, v27\n"
"v_lshl_add_u32 v68, v68, 4, s90\n"
"v_lshlrev_b32 v69, 9, v69\n"
"v_sub_u32 v68, v68, v69\n"
"v_mov_b32 v69, 2130706432\n"
"v_cndmask_b32 v38, v69, v68, s[18:19]\n"
"v_subrev_u32 v68, 1024, v68\n"
"v_cndmask_b32 v39, v69, v68, s[20:21]\n"
"s_waitcnt vmcnt(31)\n"
"ds_read_b128 a[0:3], v14 offset:32768\n"
"ds_read_b128 a[4:7], v15 offset:32768\n"
"ds_read_b128 a[8:11], v16 offset:32768\n"
"ds_read_b128 a[12:15], v17 offset:32768\n"
"s_waitcnt vmcnt(25)\n"
"s_barrier\n"
"ds_read_b64_tr_b16 a[160:161], v2 offset:36864\n"
"ds_read_b64_tr_b16 a[162:163], v2 offset:37376\n"
"ds_read_b64_tr_b16 a[164:165], v3 offset:36864\n"
"ds_read_b64_tr_b16 a[166:167], v3 offset:37376\n"
"ds_read_b64_tr_b16 a[168:169], v4 offset:36864\n"
"ds_read_b64_tr_b16 a[170:171], v4 offset:37376\n"
"ds_read_b64_tr_b16 a[172:173], v5 offset:36864\n"
"ds_read_b64_tr_b16 a[174:175], v5 offset:37376\n"
"ds_read_b128 a[176:179], v10 offset:2048\n"
"ds_read_b128 a[180:183], v10 offset:43008\n"
"ds_read_b64_tr_b16 a[184:185], v2 offset:38912\n"
"ds_read_b64_tr_b16 a[186:187], v2 offset:39424\n"
"ds_read_b64_tr_b16 a[188:189], v3 offset:38912\n"
"ds_read_b64_tr_b16 a[190:191], v3 offset:39424\n"
"s_waitcnt lgkmcnt(6)\n"
"v_mfma_f32_32x32x16_bf16 v[192:207], a[160:163], a[0:3], 0\n"
"ds_read_b64_tr_b16 a[160:161], v4 offset:38912\n"
"ds_read_b64_tr_b16 a[162:163], v4 offset:39424\n"
"s_add_u32 s14, s14, 128\n"
"s_add_u32 m0, s10, 6144\n"
"s_nop 0\n"
"buffer_load_dwordx4 v22, s[56:59], s14 offen offset:0 lds\n"
"v_mfma_f32_32x32x16_bf16 v[208:223], a[164:167], a[0:3], 0\n"
"ds_read_b64_tr_b16 a[164:165], v5 offset:38912\n"
"ds_read_b64_tr_b16 a[166:167], v5 offset:39424\n"
"s_waitcnt vmcnt(24)\n"
"v_readfirstlane_b32 s72, v80\n"
"v_readfirstlane_b32 s73, v81\n"
"s_or_b32 s72, s72, 0x40004000\n"
"s_or_b32 s73, s73, 0x40004000\n"
"v_mfma_f32_32x32x16_bf16 v[224:239], a[168:171], a[0:3], 0\n"
"ds_read_b128 a[168:171], v11 offset:2048\n"
"buffer_load_dwordx4 v23, s[56:59], s14 offen offset:1024 lds\n"
"s_add_u32 s72, s72, 1\n"
"v_mfma_f32_32x32x16_bf16 v[240:255], a[172:175], a[0:3], 0\n"
"ds_read_b128 a[172:175], v11 offset:43008\n"
"s_add_u32 s12, s12, 128\n"
"s_add_u32 m0, s10, 32768\n"
"s_add_u32 s73, s73, 0x1e3739b9\n"
"s_and_b32 s72, s72, 0xbfffbfff\n"
"s_waitcnt lgkmcnt(6)\n"
"v_mfma_f32_32x32x16_bf16 v[96:111], a[0:3], a[176:179], 0\n"
"ds_read_b64_tr_b16 a[176:177], v6 offset:36864\n"
"ds_read_b64_tr_b16 a[178:179], v6 offset:37376\n"
"buffer_load_dwordx4 v22, s[44:47], s12 offen offset:0 lds\n"
"s_and_b32 s73, s73, 0xbfffbfff\n"
"v_mfma_f32_32x32x16_bf16 v[112:127], a[0:3], a[180:183], 0\n"
"ds_read_b64_tr_b16 a[180:181], v7 offset:36864\n"
"ds_read_b64_tr_b16 a[182:183], v7 offset:37376\n"
"s_waitcnt vmcnt(25)\n"
"v_lshlrev_b32 v76, 16, v44\n"
"v_and_b32 v77, s74, v44\n"
"v_add_f32 v68, v76, v77\n"
"v_mul_f32 v69, v76, v76\n"
"v_fmac_f32 v69, v77, v77\n"
"v_mfma_f32_32x32x16_bf16 v[192:207], a[184:187], a[4:7], v[192:207]\n"
"ds_read_b64_tr_b16 a[184:185], v8 offset:36864\n"
"ds_read_b64_tr_b16 a[186:187], v8 offset:37376\n"
"buffer_load_dwordx4 v23, s[44:47], s12 offen offset:1024 lds\n"
"v_lshlrev_b32 v76, 16, v45\n"
"v_mfma_f32_32x32x16_bf16 v[208:223], a[188:191], a[4:7], v[208:223]\n"
"s_waitcnt lgkmcnt(6)\n"
"ds_read_b64_tr_b16 a[188:189], v9 offset:36864\n"
"ds_read_b64_tr_b16 a[190:191], v9 offset:37376\n"
"v_and_b32 v77, s74, v45\n"
"v_add_f32 v68, v68, v76\n"
"v_fmac_f32 v69, v76, v76\n"
"v_add_f32 v68, v68, v77\n"
"v_mfma_f32_32x32x16_bf16 v[224:239], a[160:163], a[4:7], v[224:239]\n"
"ds_read_b128 a[160:163], v12 offset:2048\n"
"buffer_load_dwordx4 v24, s[44:47], s12 offen offset:2048 lds\n"
"v_fmac_f32 v69, v77, v77\n"
"v_lshlrev_b32 v76, 16, v46\n"
"v_mfma_f32_32x32x16_bf16 v[240:255], a[164:167], a[4:7], v[240:255]\n"
"ds_read_b128 a[164:167], v12 offset:43008\n"
"v_and_b32 v77, s74, v46\n"
"v_add_f32 v68, v68, v76\n"
"v_fmac_f32 v69, v76, v76\n"
"v_add_f32 v68, v68, v77\n"
"v_fmac_f32 v69, v77, v77\n"
"v_lshlrev_b32 v76, 16, v47\n"
"v_mfma_f32_32x32x16_bf16 v[96:111], a[4:7], a[168:171], v[96:111]\n"
"ds_read_b64_tr_b16 a[168:169], v6 offset:38912\n"
"ds_read_b64_tr_b16 a[170:171], v6 offset:39424\n"
"buffer_load_dwordx4 v25, s[44:47], s12 offen offset:3072 lds\n"
"v_and_b32 v77, s74, v47\n"
"v_mfma_f32_32x32x16_bf16 v[112:127], a[4:7], a[172:175], v[112:127]\n"
"s_waitcnt lgkmcnt(4)\n"
"ds_read_b64_tr_b16 a[172:173], v7 offset:38912\n"
"ds_read_b64_tr_b16 a[174:175], v7 offset:39424\n"
"s_add_u32 s13, s13, 221184\n"
"s_add_u32 m0, s10, 16384\n"
"v_add_f32 v68, v68, v76\n"
"v_mfma_f32_32x32x16_bf16 v[192:207], a[176:179], a[8:11], v[192:207]\n"
"ds_read_b64_tr_b16 a[176:177], v8 offset:38912\n"
"ds_read_b64_tr_b16 a[178:179], v8 offset:39424\n"
"buffer_load_dwordx4 v18, s[48:51], s13 offen offset:0 lds\n"
"v_fmac_f32 v69, v76, v76\n"
"v_mfma_f32_32x32x16_bf16 v[208:223], a[180:183], a[8:11], v[208:223]\n"
"ds_read_b64_tr_b16 a[180:181], v9 offset:38912\n"
"ds_read_b64_tr_b16 a[182:183], v9 offset:39424\n"
"v_add_f32 v68, v68, v77\n"
"v_fmac_f32 v69, v77, v77\n"
"s_waitcnt vmcnt(28)\n"
"v_lshlrev_b32 v76, 16, v48\n"
"v_and_b32 v77, s74, v48\n"
"v_add_f32 v68, v68, v76\n"
"v_mfma_f32_32x32x16_bf16 v[224:239], a[184:187], a[8:11], v[224:239]\n"
"ds_read_b128 a[184:187], v13 offset:2048\n"
"buffer_load_dwordx4 v19, s[48:51], s13 offen offset:1024 lds\n"
"v_fmac_f32 v69, v76, v76\n"
"v_add_f32 v68, v68, v77\n"
"v_mfma_f32_32x32x16_bf16 v[240:255], a[188:191], a[8:11], v[240:255]\n"
"s_waitcnt lgkmcnt(5)\n"
"ds_read_b128 a[188:191], v13 offset:43008\n"
"v_fmac_f32 v69, v77, v77\n"
"v_lshlrev_b32 v76, 16, v49\n"
"v_and_b32 v77, s74, v49\n"
"v_add_f32 v68, v68, v76\n"
"v_fmac_f32 v69, v76, v76\n"
"v_mfma_f32_32x32x16_bf16 v[96:111], a[8:11], a[160:163], v[96:111]\n"
"s_waitcnt vmcnt(16)\n"
"s_barrier\n"
"ds_read_b64_tr_b16 a[160:161], v2 offset:28672\n"
"ds_read_b64_tr_b16 a[162:163], v2 offset:29184\n"
"buffer_load_dwordx4 v20, s[48:51], s13 offen offset:2048 lds\n"
"v_mfma_f32_32x32x16_bf16 v[112:127], a[8:11], a[164:167], v[112:127]\n"
"ds_read_b64_tr_b16 a[164:165], v3 offset:28672\n"
"ds_read_b64_tr_b16 a[166:167], v3 offset:29184\n"
"v_add_f32 v68, v68, v77\n"
"v_fmac_f32 v69, v77, v77\n"
"v_lshlrev_b32 v76, 16, v50\n"
"v_and_b32 v77, s74, v50\n"
"v_add_f32 v68, v68, v76\n"
"v_mfma_f32_32x32x16_bf16 v[192:207], a[168:171], a[12:15], v[192:207]\n"
"s_waitcnt vmcnt(13)\n"
"ds_read_b64_tr_b16 a[168:169], v4 offset:28672\n"
"ds_read_b64_tr_b16 a[170:171], v4 offset:29184\n"
"ds_read_b128 a[16:19], v14 offset:24576\n"
"buffer_load_dwordx4 v21, s[48:51], s13 offen offset:3072 lds\n"
"v_mfma_f32_32x32x16_bf16 v[208:223], a[172:175], a[12:15], v[208:223]\n"
"s_waitcnt lgkmcnt(7)\n"
"ds_read_b64_tr_b16 a[172:173], v5 offset:28672\n"
"ds_read_b64_tr_b16 a[174:175], v5 offset:29184\n"
"ds_read_b128 a[20:23], v15 offset:24576\n"
"s_add_u32 s14, s14, 128\n"
"s_add_u32 m0, s10, 8192\n"
"v_mfma_f32_32x32x16_bf16 v[224:239], a[176:179], a[12:15], v[224:239]\n"
"ds_read_b128 a[176:179], v10 offset:4096\n"
"ds_read_b128 a[24:27], v16 offset:24576\n"
"buffer_load_dwordx4 v22, s[56:59], s14 offen offset:0 lds\n"
"v_fmac_f32 v69, v76, v76\n"
"v_mfma_f32_32x32x16_bf16 v[240:255], a[180:183], a[12:15], v[240:255]\n"
"ds_read_b128 a[180:183], v10 offset:45056\n"
"ds_read_b128 a[28:31], v17 offset:24576\n"
"v_add_f32 v68, v68, v77\n"
"v_fmac_f32 v69, v77, v77\n"
"v_lshlrev_b32 v76, 16, v51\n"
"v_and_b32 v77, s74, v51\n"
"v_add_f32 v68, v68, v76\n"
"v_mfma_f32_32x32x16_bf16 v[96:111], a[12:15], a[184:187], v[96:111]\n"
"ds_read_b64_tr_b16 a[184:185], v2 offset:30720\n"
"ds_read_b64_tr_b16 a[186:187], v2 offset:31232\n"
"buffer_load_dwordx4 v23, s[56:59], s14 offen offset:1024 lds\n"
"v_fmac_f32 v69, v76, v76\n"
"v_mfma_f32_32x32x16_bf16 v[112:127], a[12:15], a[188:191], v[112:127]\n"
"s_waitcnt lgkmcnt(9)\n"
"ds_read_b64_tr_b16 a[188:189], v3 offset:30720\n"
"ds_read_b64_tr_b16 a[190:191], v3 offset:31232\n"
"s_add_u32 s12, s12, 128\n"
"s_add_u32 m0, s10, 12288\n"
"v_add_f32 v68, v68, v77\n"
"v_mfma_f32_32x32x16_bf16 v[192:207], a[160:163], a[16:19], v[192:207]\n"
"ds_read_b64_tr_b16 a[160:161], v4 offset:30720\n"
"ds_read_b64_tr_b16 a[162:163], v4 offset:31232\n"
"buffer_load_dwordx4 v22, s[44:47], s12 offen offset:0 lds\n"
"v_fmac_f32 v69, v77, v77\n"
"v_mfma_f32_32x32x16_bf16 v[208:223], a[164:167], a[16:19], v[208:223]\n"
"ds_read_b64_tr_b16 a[164:165], v5 offset:30720\n"
"ds_read_b64_tr_b16 a[166:167], v5 offset:31232\n"
"v_lshlrev_b32 v76, 16, v52\n"
"v_and_b32 v77, s74, v52\n"
"v_add_f32 v68, v68, v76\n"
"v_fmac_f32 v69, v76, v76\n"
"v_add_f32 v68, v68, v77\n"
"v_mfma_f32_32x32x16_bf16 v[224:239], a[168:171], a[16:19], v[224:239]\n"
"ds_read_b128 a[168:171], v11 offset:4096\n"
"buffer_load_dwordx4 v23, s[44:47], s12 offen offset:1024 lds\n"
"v_fmac_f32 v69, v77, v77\n"
"v_lshlrev_b32 v76, 16, v53\n"
"s_waitcnt lgkmcnt(14)\n"
"v_mfma_f32_32x32x16_bf16 v[240:255], a[172:175], a[16:19], v[240:255]\n"
"s_waitcnt lgkmcnt(5)\n"
"ds_read_b128 a[172:175], v11 offset:45056\n"
"v_and_b32 v77, s74, v53\n"
"v_add_f32 v68, v68, v76\n"
"v_fmac_f32 v69, v76, v76\n"
"v_add_f32 v68, v68, v77\n"
"v_fmac_f32 v69, v77, v77\n"
"v_mfma_f32_32x32x16_bf16 v[96:111], a[16:19], a[176:179], v[96:111]\n"
"ds_read_b64_tr_b16 a[176:177], v6 offset:28672\n"
"ds_read_b64_tr_b16 a[178:179], v6 offset:29184\n"
"buffer_load_dwordx4 v24, s[44:47], s12 offen offset:2048 lds\n"
"v_lshlrev_b32 v76, 16, v54\n"
"v_mfma_f32_32x32x16_bf16 v[112:127], a[16:19], a[180:183], v[112:127]\n"
"ds_read_b64_tr_b16 a[180:181], v7 offset:28672\n"
"ds_read_b64_tr_b16 a[182:183], v7 offset:29184\n"
"v_and_b32 v77, s74, v54\n"
"v_add_f32 v70, v76, v77\n"
"v_lshlrev_b32 v76, 16, v55\n"
"v_and_b32 v77, s74, v55\n"
"v_add_f32 v70, v70, v76\n"
"v_mfma_f32_32x32x16_bf16 v[192:207], a[184:187], a[20:23], v[192:207]\n"
"ds_read_b64_tr_b16 a[184:185], v8 offset:28672\n"
"ds_read_b64_tr_b16 a[186:187], v8 offset:29184\n"
"buffer_load_dwordx4 v25, s[44:47], s12 offen offset:3072 lds\n"
"v_add_f32 v70, v70, v77\n"
"v_mfma_f32_32x32x16_bf16 v[208:223], a[188:191], a[20:23], v[208:223]\n"
"s_waitcnt lgkmcnt(6)\n"
"ds_read_b64_tr_b16 a[188:189], v9 offset:28672\n"
"ds_read_b64_tr_b16 a[190:191], v9 offset:29184\n"
"s_add_u32 s13, s13, 221184\n"
"s_add_u32 m0, s10, 24576\n"
"v_lshlrev_b32 v76, 16, v56\n"
"v_mfma_f32_32x32x16_bf16 v[224:239], a[160:163], a[20:23], v[224:239]\n"
"ds_read_b128 a[160:163], v12 offset:4096\n"
"buffer_load_dwordx4 v18, s[48:51], s13 offen offset:0 lds\n"
"v_and_b32 v77, s74, v56\n"
"v_add_f32 v70, v70, v76\n"
"v_mfma_f32_32x32x16_bf16 v[240:255], a[164:167], a[20:23], v[240:255]\n"
"ds_read_b128 a[164:167], v12 offset:45056\n"
"v_add_f32 v70, v70, v77\n"
"v_lshlrev_b32 v76, 16, v57\n"
"v_and_b32 v77, s74, v57\n"
"v_add_f32 v70, v70, v76\n"
"v_add_f32 v70, v70, v77\n"
"v_lshlrev_b32 v76, 16, v58\n"
"v_mfma_f32_32x32x16_bf16 v[96:111], a[20:23], a[168:171], v[96:111]\n"
"ds_read_b64_tr_b16 a[168:169], v6 offset:30720\n"
"ds_read_b64_tr_b16 a[170:171], v6 offset:31232\n"
"buffer_load_dwordx4 v19, s[48:51], s13 offen offset:1024 lds\n"
"v_and_b32 v77, s74, v58\n"
"v_mfma_f32_32x32x16_bf16 v[112:127], a[20:23], a[172:175], v[112:127]\n"
"s_waitcnt lgkmcnt(4)\n"
"ds_read_b64_tr_b16 a[172:173], v7 offset:30720\n"
"ds_read_b64_tr_b16 a[174:175], v7 offset:31232\n"
"v_add_f32 v70, v70, v76\n"
"v_add_f32 v70, v70, v77\n"
"v_lshlrev_b32 v76, 16, v59\n"
"v_and_b32 v77, s74, v59\n"
"v_mfma_f32_32x32x16_bf16 v[192:207], a[176:179], a[24:27], v[192:207]\n"
"ds_read_b64_tr_b16 a[176:177], v8 offset:30720\n"
"ds_read_b64_tr_b16 a[178:179], v8 offset:31232\n"
"buffer_load_dwordx4 v20, s[48:51], s13 offen offset:2048 lds\n"
"v_add_f32 v70, v70, v76\n"
"v_mfma_f32_32x32x16_bf16 v[208:223], a[180:183], a[24:27], v[208:223]\n"
"ds_read_b64_tr_b16 a[180:181], v9 offset:30720\n"
"ds_read_b64_tr_b16 a[182:183], v9 offset:31232\n"
"v_add_f32 v70, v70, v77\n"
"v_lshlrev_b32 v76, 16, v60\n"
"v_and_b32 v77, s74, v60\n"
"v_add_f32 v70, v70, v76\n"
"v_add_f32 v70, v70, v77\n"
"v_mfma_f32_32x32x16_bf16 v[224:239], a[184:187], a[24:27], v[224:239]\n"
"ds_read_b128 a[184:187], v13 offset:4096\n"
"buffer_load_dwordx4 v21, s[48:51], s13 offen offset:3072 lds\n"
"v_lshlrev_b32 v76, 16, v61\n"
"v_and_b32 v77, s74, v61\n"
"v_mfma_f32_32x32x16_bf16 v[240:255], a[188:191], a[24:27], v[240:255]\n"
"s_waitcnt lgkmcnt(5)\n"
"ds_read_b128 a[188:191], v13 offset:45056\n"
"v_add_f32 v70, v70, v76\n"
"v_add_f32 v70, v70, v77\n"
"v_lshlrev_b32 v76, 16, v62\n"
"v_and_b32 v77, s74, v62\n"
"v_add_f32 v70, v70, v76\n"
"v_mfma_f32_32x32x16_bf16 v[96:111], a[24:27], a[160:163], v[96:111]\n"
"s_waitcnt vmcnt(18)\n"
"s_barrier\n"
"ds_read_b64_tr_b16 a[160:161], v2 offset:20480\n"
"ds_read_b64_tr_b16 a[162:163], v2 offset:20992\n"
"s_add_u32 s14, s14, 128\n"
"s_add_u32 m0, s10, 2048\n"
"s_nop 0\n"
"buffer_load_dwordx4 v22, s[56:59], s14 offen offset:0 lds\n"
"v_mfma_f32_32x32x16_bf16 v[112:127], a[24:27], a[164:167], v[112:127]\n"
"ds_read_b64_tr_b16 a[164:165], v3 offset:20480\n"
"ds_read_b64_tr_b16 a[166:167], v3 offset:20992\n"
"v_add_f32 v70, v70, v77\n"
"v_lshlrev_b32 v76, 16, v63\n"
"v_and_b32 v77, s74, v63\n"
"v_add_f32 v70, v70, v76\n"
"v_add_f32 v70, v70, v77\n"
"v_mfma_f32_32x32x16_bf16 v[192:207], a[168:171], a[28:31], v[192:207]\n"
"s_waitcnt vmcnt(15)\n"
"ds_read_b64_tr_b16 a[168:169], v4 offset:20480\n"
"ds_read_b64_tr_b16 a[170:171], v4 offset:20992\n"
"ds_read_b128 a[32:35], v14 offset:32768\n"
"buffer_load_dwordx4 v23, s[56:59], s14 offen offset:1024 lds\n"
"v_mfma_f32_32x32x16_bf16 v[208:223], a[172:175], a[28:31], v[208:223]\n"
"s_waitcnt lgkmcnt(7)\n"
"ds_read_b64_tr_b16 a[172:173], v5 offset:20480\n"
"ds_read_b64_tr_b16 a[174:175], v5 offset:20992\n"
"ds_read_b128 a[36:39], v15 offset:32768\n"
"s_add_u32 s12, s12, 128\n"
"s_add_u32 m0, s10, 36864\n"
"v_mfma_f32_32x32x16_bf16 v[224:239], a[176:179], a[28:31], v[224:239]\n"
"ds_read_b128 a[176:179], v10 offset:6144\n"
"ds_read_b128 a[40:43], v16 offset:32768\n"
"buffer_load_dwordx4 v22, s[44:47], s12 offen offset:0 lds\n"
"v_add_f32_dpp v68, v68, v68 row_ror:8 row_mask:0xf bank_mask:0xf\n"
"v_mfma_f32_32x32x16_bf16 v[240:255], a[180:183], a[28:31], v[240:255]\n"
"ds_read_b128 a[180:183], v10 offset:47104\n"
"ds_read_b128 a[44:47], v17 offset:32768\n"
"v_add_f32_dpp v69, v69, v69 row_ror:8 row_mask:0xf bank_mask:0xf\n"
"v_add_f32_dpp v70, v70, v70 row_ror:8 row_mask:0xf bank_mask:0xf\n"
"v_add_f32_dpp v68, v68, v68 row_ror:4 row_mask:0xf bank_mask:0xf\n"
"v_add_f32_dpp v69, v69, v69 row_ror:4 row_mask:0xf bank_mask:0xf\n"
"v_add_f32_dpp v70, v70, v70 row_ror:4 row_mask:0xf bank_mask:0xf\n"
"v_mfma_f32_32x32x16_bf16 v[96:111], a[28:31], a[184:187], v[96:111]\n"
"ds_read_b64_tr_b16 a[184:185], v2 offset:22528\n"
"ds_read_b64_tr_b16 a[186:187], v2 offset:23040\n"
"buffer_load_dwordx4 v23, s[44:47], s12 offen offset:1024 lds\n"
"v_add_f32_dpp v68, v68, v68 row_ror:2 row_mask:0xf bank_mask:0xf\n"
"v_mfma_f32_32x32x16_bf16 v[112:127], a[28:31], a[188:191], v[112:127]\n"
"s_waitcnt lgkmcnt(9)\n"
"ds_read_b64_tr_b16 a[188:189], v3 offset:22528\n"
"ds_read_b64_tr_b16 a[190:191], v3 offset:23040\n"
"v_add_f32_dpp v69, v69, v69 row_ror:2 row_mask:0xf bank_mask:0xf\n"
"v_add_f32_dpp v70, v70, v70 row_ror:2 row_mask:0xf bank_mask:0xf\n"
"v_add_f32_dpp v68, v68, v68 row_ror:1 row_mask:0xf bank_mask:0xf\n"
"v_add_f32_dpp v69, v69, v69 row_ror:1 row_mask:0xf bank_mask:0xf\n"
"v_mfma_f32_32x32x16_bf16 v[192:207], a[160:163], a[32:35], v[192:207]\n"
"ds_read_b64_tr_b16 a[160:161], v4 offset:22528\n"
"ds_read_b64_tr_b16 a[162:163], v4 offset:23040\n"
"buffer_load_dwordx4 v24, s[44:47], s12 offen offset:2048 lds\n"
"v_add_f32_dpp v70, v70, v70 row_ror:1 row_mask:0xf bank_mask:0xf\n"
"v_mfma_f32_32x32x16_bf16 v[208:223], a[164:167], a[32:35], v[208:223]\n"
"ds_read_b64_tr_b16 a[164:165], v5 offset:22528\n"
"ds_read_b64_tr_b16 a[166:167], v5 offset:23040\n"
"v_mov_b32 v74, v68\n"
"v_mov_b32 v75, v69\n"
"v_mov_b32 v76, v70\n"
"v_permlane16_swap_b32 v68, v74\n"
"v_permlane16_swap_b32 v69, v75\n"
"v_mfma_f32_32x32x16_bf16 v[224:239], a[168:171], a[32:35], v[224:239]\n"
"ds_read_b128 a[168:171], v11 offset:6144\n"
"buffer_load_dwordx4 v25, s[44:47], s12 offen offset:3072 lds\n"
"v_permlane16_swap_b32 v70, v76\n"
"v_add_f32 v68, v68, v74\n"
"s_waitcnt lgkmcnt(14)\n"
"v_mfma_f32_32x32x16_bf16 v[240:255], a[172:175], a[32:35], v[240:255]\n"
"s_waitcnt lgkmcnt(5)\n"
"ds_read_b128 a[172:175], v11 offset:47104\n"
"s_add_u32 s13, s13, 221184\n"
"s_add_u32 m0, s10, 32768\n"
"v_add_f32 v69, v69, v75\n"
"v_add_f32 v70, v70, v76\n"
"v_mfma_f32_32x32x16_bf16 v[96:111], a[32:35], a[176:179], v[96:111]\n"
"ds_read_b64_tr_b16 a[176:177], v6 offset:20480\n"
"ds_read_b64_tr_b16 a[178:179], v6 offset:20992\n"
"buffer_load_dwordx4 v18, s[48:51], s13 offen offset:0 lds\n"
"v_mul_f32 v68, 0x3acccccd, v68\n"
"v_mfma_f32_32x32x16_bf16 v[112:127], a[32:35], a[180:183], v[112:127]\n"
"ds_read_b64_tr_b16 a[180:181], v7 offset:20480\n"
"ds_read_b64_tr_b16 a[182:183], v7 offset:20992\n"
"v_mul_f32 v69, 0x3acccccd, v69\n"
"v_mov_b32 v45, s72\n"
"v_fma_f32 v69, -v68, v68, v69\n"
"v_mov_b32 v47, s73\n"
"v_max_f32 v69, 0, v69\n"
"v_mfma_f32_32x32x16_bf16 v[192:207], a[184:187], a[36:39], v[192:207]\n"
"ds_read_b64_tr_b16 a[184:185], v8 offset:20480\n"
"ds_read_b64_tr_b16 a[186:187], v8 offset:20992\n"
"buffer_load_dwordx4 v19, s[48:51], s13 offen offset:1024 lds\n"
"v_cvt_pk_bf16_f32 v68, v68, v69\n"
"v_mfma_f32_32x32x16_bf16 v[208:223], a[188:191], a[36:39], v[208:223]\n"
"s_waitcnt lgkmcnt(6)\n"
"ds_read_b64_tr_b16 a[188:189], v9 offset:20480\n"
"ds_read_b64_tr_b16 a[190:191], v9 offset:20992\n"
"v_and_b32 v69, s74, v68\n"
"v_lshlrev_b32 v46, 16, v68\n"
"v_add_f32 v69, 0x3727c5ac, v69\n"
"v_cvt_pk_bf16_f32 v69, v69, v69\n"
"v_mfma_f32_32x32x16_bf16 v[224:239], a[160:163], a[36:39], v[224:239]\n"
"ds_read_b128 a[160:163], v12 offset:6144\n"
"buffer_load_dwordx4 v20, s[48:51], s13 offen offset:2048 lds\n"
"v_and_b32 v69, s74, v69\n"
"v_mfma_f32_32x32x16_bf16 v[240:255], a[164:167], a[36:39], v[240:255]\n"
"ds_read_b128 a[164:167], v12 offset:47104\n"
"v_sqrt_f32 v69, v69\n"
"v_and_b32 v76, 31, v27\n"
"v_cvt_pk_bf16_f32 v69, v69, v69\n"
"v_cmp_eq_u32 vcc, 0, v76\n"
"v_and_b32 v69, s74, v69\n"
"v_mfma_f32_32x32x16_bf16 v[96:111], a[36:39], a[168:171], v[96:111]\n"
"ds_read_b64_tr_b16 a[168:169], v6 offset:22528\n"
"ds_read_b64_tr_b16 a[170:171], v6 offset:23040\n"
"buffer_load_dwordx4 v21, s[48:51], s13 offen offset:3072 lds\n"
"v_lshlrev_b32 v76, 4, v82\n"
"v_mfma_f32_32x32x16_bf16 v[112:127], a[36:39], a[172:175], v[112:127]\n"
"s_waitcnt lgkmcnt(4)\n"
"ds_read_b64_tr_b16 a[172:173], v7 offset:22528\n"
"ds_read_b64_tr_b16 a[174:175], v7 offset:23040\n"
"v_rcp_f32 v44, v69\n"
"v_add_u32 v76, 20971520, v76\n"
"v_mov_b32 v77, 2130706432\n"
"v_mfma_f32_32x32x16_bf16 v[192:207], a[176:179], a[40:43], v[192:207]\n"
"ds_read_b64_tr_b16 a[176:177], v8 offset:22528\n"
"ds_read_b64_tr_b16 a[178:179], v8 offset:23040\n"
"v_cndmask_b32 v76, v77, v76, vcc\n"
"v_mfma_f32_32x32x16_bf16 v[208:223], a[180:183], a[40:43], v[208:223]\n"
"ds_read_b64_tr_b16 a[180:181], v9 offset:22528\n"
"ds_read_b64_tr_b16 a[182:183], v9 offset:23040\n"
"buffer_store_dwordx4 v[44:47], v76, s[64:67], 0 offen sc1\n"
"v_lshrrev_b32 v76, 1, v27\n"
"v_cndmask_b32 v76, v77, v76, vcc\n"
"v_mfma_f32_32x32x16_bf16 v[224:239], a[184:187], a[40:43], v[224:239]\n"
"ds_read_b128 a[184:187], v13 offset:6144\n"
"v_cvt_pk_bf16_f32 v77, v70, v70\n"
"v_and_b32 v77, s74, v77\n"
"v_sub_f32 v70, v70, v77\n"
"v_cvt_pk_bf16_f32 v48, v77, v70\n"
"v_and_b32 v77, s74, v48\n"
"v_sub_f32 v70, v70, v77\n"
"v_mfma_f32_32x32x16_bf16 v[240:255], a[188:191], a[40:43], v[240:255]\n"
"s_waitcnt lgkmcnt(5)\n"
"ds_read_b128 a[188:191], v13 offset:47104\n"
"v_cvt_pk_bf16_f32 v50, v70, 0\n"
"v_mov_b32 v49, s72\n"
"v_mov_b32 v51, s73\n"
"v_mfma_f32_32x32x16_bf16 v[96:111], a[40:43], a[160:163], v[96:111]\n"
"s_waitcnt vmcnt(19)\n"
"s_barrier\n"
"ds_read_b64_tr_b16 a[160:161], v2 offset:16384\n"
"ds_read_b64_tr_b16 a[162:163], v2 offset:16896\n"
"s_add_u32 s14, s14, 128\n"
"s_add_u32 m0, s10, 4096\n"
"s_nop 0\n"
"buffer_load_dwordx4 v22, s[56:59], s14 offen offset:0 lds\n"
"v_mfma_f32_32x32x16_bf16 v[112:127], a[40:43], a[164:167], v[112:127]\n"
"ds_read_b64_tr_b16 a[164:165], v3 offset:16384\n"
"ds_read_b64_tr_b16 a[166:167], v3 offset:16896\n"
"buffer_store_dwordx4 v[48:51], v76, s[64:67], s88 offen offset:0 sc1\n"
"v_lshlrev_b32 v70, 16, v66\n"
"v_lshlrev_b32 v68, 16, v64\n"
"v_mfma_f32_32x32x16_bf16 v[192:207], a[168:171], a[44:47], v[192:207]\n"
"s_waitcnt vmcnt(17)\n"
"ds_read_b64_tr_b16 a[168:169], v4 offset:16384\n"
"ds_read_b64_tr_b16 a[170:171], v4 offset:16896\n"
"ds_read_b128 a[48:51], v14 offset:12288\n"
"buffer_load_dwordx4 v23, s[56:59], s14 offen offset:1024 lds\n"
"v_mfma_f32_32x32x16_bf16 v[208:223], a[172:175], a[44:47], v[208:223]\n"
"s_waitcnt lgkmcnt(7)\n"
"ds_read_b64_tr_b16 a[172:173], v5 offset:16384\n"
"ds_read_b64_tr_b16 a[174:175], v5 offset:16896\n"
"ds_read_b128 a[52:55], v15 offset:12288\n"
"s_add_u32 s12, s12, 128\n"
"s_add_u32 m0, s10, 28672\n"
"v_mfma_f32_32x32x16_bf16 v[224:239], a[176:179], a[44:47], v[224:239]\n"
"ds_read_b128 a[176:179], v10 offset:8192\n"
"ds_read_b128 a[56:59], v16 offset:12288\n"
"buffer_load_dwordx4 v22, s[44:47], s12 offen offset:0 lds\n"
"v_mul_f32 v70, 0x3fb8aa3b, v70\n"
"v_mfma_f32_32x32x16_bf16 v[240:255], a[180:183], a[44:47], v[240:255]\n"
"ds_read_b128 a[180:183], v10 offset:49152\n"
"ds_read_b128 a[60:63], v17 offset:12288\n"
"v_lshlrev_b32 v69, 16, v65\n"
"v_exp_f32 v70, v70\n"
"v_lshrrev_b32 v73, 5, v27\n"
"v_cvt_pk_bf16_f32 v70, v70, v70\n"
"v_mfma_f32_32x32x16_bf16 v[96:111], a[44:47], a[184:187], v[96:111]\n"
"ds_read_b64_tr_b16 a[184:185], v2 offset:18432\n"
"ds_read_b64_tr_b16 a[186:187], v2 offset:18944\n"
"buffer_load_dwordx4 v23, s[44:47], s12 offen offset:1024 lds\n"
"v_cvt_f32_u32 v73, v73\n"
"v_mfma_f32_32x32x16_bf16 v[112:127], a[44:47], a[188:191], v[112:127]\n"
"s_waitcnt lgkmcnt(9)\n"
"ds_read_b64_tr_b16 a[188:189], v3 offset:18432\n"
"ds_read_b64_tr_b16 a[190:191], v3 offset:18944\n"
"v_lshlrev_b32 v70, 16, v70\n"
"v_mul_f32 v71, v70, v69\n"
"v_mul_f32 v70, v70, v68\n"
"v_mul_f32 v71, 0x3e22f983, v71\n"
"v_mfma_f32_32x32x16_bf16 v[192:207], a[160:163], a[48:51], v[192:207]\n"
"ds_read_b64_tr_b16 a[160:161], v4 offset:18432\n"
"ds_read_b64_tr_b16 a[162:163], v4 offset:18944\n"
"buffer_load_dwordx4 v24, s[44:47], s12 offen offset:2048 lds\n"
"v_mul_f32 v70, 0x3fb8aa3b, v70\n"
"v_mfma_f32_32x32x16_bf16 v[208:223], a[164:167], a[48:51], v[208:223]\n"
"ds_read_b64_tr_b16 a[164:165], v5 offset:18432\n"
"ds_read_b64_tr_b16 a[166:167], v5 offset:18944\n"
"v_mul_f32 v74, 0x3f800000, v70\n"
"v_mul_f32 v75, 0x3f800000, v71\n"
"v_exp_f32 v74, v74\n"
"v_fract_f32 v75, v75\n"
"v_mfma_f32_32x32x16_bf16 v[224:239], a[168:171], a[48:51], v[224:239]\n"
"ds_read_b128 a[168:171], v11 offset:8192\n"
"buffer_load_dwordx4 v25, s[44:47], s12 offen offset:3072 lds\n"
"v_cos_f32 v44, v75\n"
"s_waitcnt lgkmcnt(14)\n"
"v_mfma_f32_32x32x16_bf16 v[240:255], a[172:175], a[48:51], v[240:255]\n"
"s_waitcnt lgkmcnt(5)\n"
"ds_read_b128 a[172:175], v11 offset:49152\n"
"s_add_u32 s13, s13, 221184\n"
"s_add_u32 m0, s10, 12288\n"
"v_sin_f32 v45, v75\n"
"v_mfma_f32_32x32x16_bf16 v[96:111], a[48:51], a[176:179], v[96:111]\n"
"ds_read_b64_tr_b16 a[176:177], v6 offset:16384\n"
"ds_read_b64_tr_b16 a[178:179], v6 offset:16896\n"
"buffer_load_dwordx4 v18, s[48:51], s13 offen offset:0 lds\n"
"v_mul_f32 v44, v44, v74\n"
"v_mfma_f32_32x32x16_bf16 v[112:127], a[48:51], a[180:183], v[112:127]\n"
"ds_read_b64_tr_b16 a[180:181], v7 offset:16384\n"
"ds_read_b64_tr_b16 a[182:183], v7 offset:16896\n"
"v_mul_f32 v45, v45, v74\n"
"v_mul_f32 v72, 0x41800000, v73\n"
"v_mul_f32 v74, v72, v70\n"
"v_mul_f32 v75, v72, v71\n"
"v_mfma_f32_32x32x16_bf16 v[192:207], a[184:187], a[52:55], v[192:207]\n"
"ds_read_b64_tr_b16 a[184:185], v8 offset:16384\n"
"ds_read_b64_tr_b16 a[186:187], v8 offset:16896\n"
"buffer_load_dwordx4 v19, s[48:51], s13 offen offset:1024 lds\n"
"v_mfma_f32_32x32x16_bf16 v[208:223], a[188:191], a[52:55], v[208:223]\n"
"s_waitcnt lgkmcnt(6)\n"
"ds_read_b64_tr_b16 a[188:189], v9 offset:16384\n"
"ds_read_b64_tr_b16 a[190:191], v9 offset:16896\n"
"v_exp_f32 v74, v74\n"
"v_fract_f32 v75, v75\n"
"v_mfma_f32_32x32x16_bf16 v[224:239], a[160:163], a[52:55], v[224:239]\n"
"ds_read_b128 a[160:163], v12 offset:8192\n"
"buffer_load_dwordx4 v20, s[48:51], s13 offen offset:2048 lds\n"
"v_cos_f32 v46, v75\n"
"v_mfma_f32_32x32x16_bf16 v[240:255], a[164:167], a[52:55], v[240:255]\n"
"ds_read_b128 a[164:167], v12 offset:49152\n"
"v_sin_f32 v47, v75\n"
"v_mul_f32 v46, v46, v74\n"
"v_mul_f32 v47, v47, v74\n"
"v_mul_f32 v74, 0x42000000, v70\n"
"v_mul_f32 v75, 0x42000000, v71\n"
"v_mfma_f32_32x32x16_bf16 v[96:111], a[52:55], a[168:171], v[96:111]\n"
"ds_read_b64_tr_b16 a[168:169], v6 offset:18432\n"
"ds_read_b64_tr_b16 a[170:171], v6 offset:18944\n"
"buffer_load_dwordx4 v21, s[48:51], s13 offen offset:3072 lds\n"
"v_mfma_f32_32x32x16_bf16 v[112:127], a[52:55], a[172:175], v[112:127]\n"
"s_waitcnt lgkmcnt(4)\n"
"ds_read_b64_tr_b16 a[172:173], v7 offset:18432\n"
"ds_read_b64_tr_b16 a[174:175], v7 offset:18944\n"
"v_exp_f32 v74, v74\n"
"v_fract_f32 v75, v75\n"
"v_mfma_f32_32x32x16_bf16 v[192:207], a[176:179], a[56:59], v[192:207]\n"
"ds_read_b64_tr_b16 a[176:177], v8 offset:18432\n"
"ds_read_b64_tr_b16 a[178:179], v8 offset:18944\n"
"v_cos_f32 v54, v75\n"
"v_sin_f32 v55, v75\n"
"v_mul_f32 v54, v54, v74\n"
"v_mfma_f32_32x32x16_bf16 v[208:223], a[180:183], a[56:59], v[208:223]\n"
"ds_read_b64_tr_b16 a[180:181], v9 offset:18432\n"
"ds_read_b64_tr_b16 a[182:183], v9 offset:18944\n"
"v_mul_f32 v55, v55, v74\n"
"v_mul_f32 v74, v55, v55\n"
"v_mul_f32 v75, v54, v55\n"
"v_fma_f32 v74, v54, v54, -v74\n"
"v_add_f32 v75, v75, v75\n"
"v_mfma_f32_32x32x16_bf16 v[224:239], a[184:187], a[56:59], v[224:239]\n"
"ds_read_b128 a[184:187], v13 offset:8192\n"
"s_cmp_eq_u32 s7, 3\n"
"s_cselect_b32 s68, 1.0, 0\n"
"s_cmp_eq_u32 s7, 2\n"
"s_cselect_b32 s69, 1.0, 0\n"
"v_mfma_f32_32x32x16_bf16 v[240:255], a[188:191], a[56:59], v[240:255]\n"
"s_waitcnt lgkmcnt(5)\n"
"ds_read_b128 a[188:191], v13 offset:49152\n"
"s_cmp_eq_u32 s7, 1\n"
"s_cselect_b32 s70, 1.0, 0\n"
"v_mov_b32 v52, s68\n"
"v_mov_b32 v53, 0\n"
"v_mfma_f32_32x32x16_bf16 v[96:111], a[56:59], a[160:163], v[96:111]\n"
"s_waitcnt vmcnt(20)\n"
"s_barrier\n"
"ds_read_b64_tr_b16 a[160:161], v2 offset:24576\n"
"ds_read_b64_tr_b16 a[162:163], v2 offset:25088\n"
"s_add_u32 s14, s14, 128\n"
"s_add_u32 m0, s10, 6144\n"
"s_nop 0\n"
"buffer_load_dwordx4 v22, s[56:59], s14 offen offset:0 lds\n"
"v_mfma_f32_32x32x16_bf16 v[112:127], a[56:59], a[164:167], v[112:127]\n"
"ds_read_b64_tr_b16 a[164:165], v3 offset:24576\n"
"ds_read_b64_tr_b16 a[166:167], v3 offset:25088\n"
"v_mov_b32 v50, s69\n"
"v_mul_f32 v51, s68, v55\n"
"v_fmac_f32 v50, s68, v54\n"
"v_mov_b32 v48, s70\n"
"v_mul_f32 v49, s69, v55\n"
"v_mfma_f32_32x32x16_bf16 v[192:207], a[168:171], a[60:63], v[192:207]\n"
"s_waitcnt vmcnt(17)\n"
"ds_read_b64_tr_b16 a[168:169], v4 offset:24576\n"
"ds_read_b64_tr_b16 a[170:171], v4 offset:25088\n"
"ds_read_b128 a[64:67], v14 offset:36864\n"
"buffer_load_dwordx4 v23, s[56:59], s14 offen offset:1024 lds\n"
"v_mfma_f32_32x32x16_bf16 v[208:223], a[172:175], a[60:63], v[208:223]\n"
"s_waitcnt lgkmcnt(7)\n"
"ds_read_b64_tr_b16 a[172:173], v5 offset:24576\n"
"ds_read_b64_tr_b16 a[174:175], v5 offset:25088\n"
"ds_read_b128 a[68:71], v15 offset:36864\n"
"s_add_u32 s12, s12, 128\n"
"s_add_u32 m0, s10, 20480\n"
"v_mfma_f32_32x32x16_bf16 v[224:239], a[176:179], a[60:63], v[224:239]\n"
"ds_read_b128 a[176:179], v10 offset:2048\n"
"ds_read_b128 a[72:75], v16 offset:36864\n"
"buffer_load_dwordx4 v22, s[44:47], s12 offen offset:0 lds\n"
"v_fmac_f32 v48, s69, v54\n"
"v_mfma_f32_32x32x16_bf16 v[240:255], a[180:183], a[60:63], v[240:255]\n"
"ds_read_b128 a[180:183], v10 offset:43008\n"
"ds_read_b128 a[76:79], v17 offset:36864\n"
"v_fmac_f32 v49, s68, v75\n"
"v_fmac_f32 v48, s68, v74\n"
"s_lshl_b32 s68, s7, 5\n"
"v_cvt_f32_u32 v72, s68\n"
"v_mfma_f32_32x32x16_bf16 v[96:111], a[60:63], a[184:187], v[96:111]\n"
"ds_read_b64_tr_b16 a[184:185], v2 offset:26624\n"
"ds_read_b64_tr_b16 a[186:187], v2 offset:27136\n"
"buffer_load_dwordx4 v23, s[44:47], s12 offen offset:1024 lds\n"
"v_fmac_f32 v72, 0x41800000, v73\n"
"v_mfma_f32_32x32x16_bf16 v[112:127], a[60:63], a[188:191], v[112:127]\n"
"s_waitcnt lgkmcnt(9)\n"
"ds_read_b64_tr_b16 a[188:189], v3 offset:26624\n"
"ds_read_b64_tr_b16 a[190:191], v3 offset:27136\n"
"v_mul_f32 v74, v72, v70\n"
"v_mul_f32 v75, v72, v71\n"
"v_exp_f32 v74, v74\n"
"v_mfma_f32_32x32x16_bf16 v[192:207], a[160:163], a[64:67], v[192:207]\n"
"ds_read_b64_tr_b16 a[160:161], v4 offset:26624\n"
"ds_read_b64_tr_b16 a[162:163], v4 offset:27136\n"
"buffer_load_dwordx4 v24, s[44:47], s12 offen offset:2048 lds\n"
"v_fract_f32 v75, v75\n"
"v_mfma_f32_32x32x16_bf16 v[208:223], a[164:167], a[64:67], v[208:223]\n"
"ds_read_b64_tr_b16 a[164:165], v5 offset:26624\n"
"ds_read_b64_tr_b16 a[166:167], v5 offset:27136\n"
"v_cos_f32 v56, v75\n"
"v_sin_f32 v57, v75\n"
"v_mul_f32 v56, v56, v74\n"
"v_mfma_f32_32x32x16_bf16 v[224:239], a[168:171], a[64:67], v[224:239]\n"
"ds_read_b128 a[168:171], v11 offset:2048\n"
"buffer_load_dwordx4 v25, s[44:47], s12 offen offset:3072 lds\n"
"v_mul_f32 v57, v57, v74\n"
"s_lshl_b32 s68, s7, 9\n"
"s_waitcnt lgkmcnt(14)\n"
"v_mfma_f32_32x32x16_bf16 v[240:255], a[172:175], a[64:67], v[240:255]\n"
"s_waitcnt lgkmcnt(5)\n"
"ds_read_b128 a[172:175], v11 offset:43008\n"
"s_add_u32 s13, s13, 221184\n"
"s_add_u32 m0, s10, 36864\n"
"s_add_u32 s68, s68, 0\n"
"v_cvt_f32_u32 v72, s68\n"
"v_mfma_f32_32x32x16_bf16 v[96:111], a[64:67], a[176:179], v[96:111]\n"
"ds_read_b64_tr_b16 a[176:177], v6 offset:24576\n"
"ds_read_b64_tr_b16 a[178:179], v6 offset:25088\n"
"buffer_load_dwordx4 v18, s[48:51], s13 offen offset:0 lds\n"
"v_fmac_f32 v72, 0x43000000, v73\n"
"v_mfma_f32_32x32x16_bf16 v[112:127], a[64:67], a[180:183], v[112:127]\n"
"ds_read_b64_tr_b16 a[180:181], v7 offset:24576\n"
"ds_read_b64_tr_b16 a[182:183], v7 offset:25088\n"
"v_mul_f32 v74, v72, v70\n"
"v_mul_f32 v75, v72, v71\n"
"v_exp_f32 v74, v74\n"
"v_fract_f32 v75, v75\n"
"v_mfma_f32_32x32x16_bf16 v[192:207], a[184:187], a[68:71], v[192:207]\n"
"ds_read_b64_tr_b16 a[184:185], v8 offset:24576\n"
"ds_read_b64_tr_b16 a[186:187], v8 offset:25088\n"
"buffer_load_dwordx4 v19, s[48:51], s13 offen offset:1024 lds\n"
"v_mfma_f32_32x32x16_bf16 v[208:223], a[188:191], a[68:71], v[208:223]\n"
"s_waitcnt lgkmcnt(6)\n"
"ds_read_b64_tr_b16 a[188:189], v9 offset:24576\n"
"ds_read_b64_tr_b16 a[190:191], v9 offset:25088\n"
"v_cos_f32 v58, v75\n"
"v_sin_f32 v59, v75\n"
"v_mfma_f32_32x32x16_bf16 v[224:239], a[160:163], a[68:71], v[224:239]\n"
"ds_read_b128 a[160:163], v12 offset:2048\n"
"buffer_load_dwordx4 v20, s[48:51], s13 offen offset:2048 lds\n"
"v_cndmask_b32 v74, 0, v74, s[18:19]\n"
"v_mul_f32 v58, v58, v74\n"
"v_mfma_f32_32x32x16_bf16 v[240:255], a[164:167], a[68:71], v[240:255]\n"
"ds_read_b128 a[164:167], v12 offset:43008\n"
"v_mul_f32 v59, v59, v74\n"
"s_lshl_b32 s68, s7, 9\n"
"s_add_u32 s68, s68, 256\n"
"v_cvt_f32_u32 v72, s68\n"
"v_fmac_f32 v72, 0x43000000, v73\n"
"v_mfma_f32_32x32x16_bf16 v[96:111], a[68:71], a[168:171], v[96:111]\n"
"ds_read_b64_tr_b16 a[168:169], v6 offset:26624\n"
"ds_read_b64_tr_b16 a[170:171], v6 offset:27136\n"
"buffer_load_dwordx4 v21, s[48:51], s13 offen offset:3072 lds\n"
"v_mul_f32 v74, v72, v70\n"
"v_mfma_f32_32x32x16_bf16 v[112:127], a[68:71], a[172:175], v[112:127]\n"
"s_waitcnt lgkmcnt(4)\n"
"ds_read_b64_tr_b16 a[172:173], v7 offset:26624\n"
"ds_read_b64_tr_b16 a[174:175], v7 offset:27136\n"
"v_mul_f32 v75, v72, v71\n"
"v_exp_f32 v74, v74\n"
"v_fract_f32 v75, v75\n"
"v_mfma_f32_32x32x16_bf16 v[192:207], a[176:179], a[72:75], v[192:207]\n"
"ds_read_b64_tr_b16 a[176:177], v8 offset:26624\n"
"ds_read_b64_tr_b16 a[178:179], v8 offset:27136\n"
"v_cos_f32 v60, v75\n"
"v_sin_f32 v61, v75\n"
"v_cndmask_b32 v74, 0, v74, s[20:21]\n"
"v_mfma_f32_32x32x16_bf16 v[208:223], a[180:183], a[72:75], v[208:223]\n"
"ds_read_b64_tr_b16 a[180:181], v9 offset:26624\n"
"ds_read_b64_tr_b16 a[182:183], v9 offset:27136\n"
"v_mul_f32 v60, v60, v74\n"
"v_mul_f32 v61, v61, v74\n"
"v_mul_f32 v71, v68, v68\n"
"v_add_f32 v70, -1.0, v44\n"
"v_fmac_f32 v71, v69, v69\n"
"v_mfma_f32_32x32x16_bf16 v[224:239], a[184:187], a[72:75], v[224:239]\n"
"ds_read_b128 a[184:187], v13 offset:2048\n"
"v_mul_f32 v62, v70, v68\n"
"v_rcp_f32 v71, v71\n"
"v_mul_f32 v63, v45, v68\n"
"v_fmac_f32 v62, v45, v69\n"
"v_fma_f32 v63, -v70, v69, v63\n"
"v_mfma_f32_32x32x16_bf16 v[240:255], a[188:191], a[72:75], v[240:255]\n"
"s_waitcnt lgkmcnt(5)\n"
"ds_read_b128 a[188:191], v13 offset:43008\n"
"v_add_f32 v71, v71, v71\n"
"v_mul_f32 v62, v62, v71\n"
"v_mul_f32 v63, v63, v71\n"
"v_mfma_f32_32x32x16_bf16 v[96:111], a[72:75], a[160:163], v[96:111]\n"
"s_waitcnt vmcnt(18)\n"
"s_barrier\n"
"ds_read_b64_tr_b16 a[160:161], v2 offset:32768\n"
"ds_read_b64_tr_b16 a[162:163], v2 offset:33280\n"
"s_add_u32 s14, s14, 128\n"
"s_add_u32 m0, s10, 8192\n"
"s_nop 0\n"
"buffer_load_dwordx4 v22, s[56:59], s14 offen offset:0 lds\n"
"v_mfma_f32_32x32x16_bf16 v[112:127], a[72:75], a[164:167], v[112:127]\n"
"ds_read_b64_tr_b16 a[164:165], v3 offset:32768\n"
"ds_read_b64_tr_b16 a[166:167], v3 offset:33280\n"
"v_mfma_f32_32x32x16_bf16 v[192:207], a[168:171], a[76:79], v[192:207]\n"
"s_waitcnt vmcnt(15)\n"
"ds_read_b64_tr_b16 a[168:169], v4 offset:32768\n"
"ds_read_b64_tr_b16 a[170:171], v4 offset:33280\n"
"ds_read_b128 a[80:83], v14 offset:28672\n"
"buffer_load_dwordx4 v23, s[56:59], s14 offen offset:1024 lds\n"
"v_mfma_f32_32x32x16_bf16 v[208:223], a[172:175], a[76:79], v[208:223]\n"
"s_waitcnt lgkmcnt(7)\n"
"ds_read_b64_tr_b16 a[172:173], v5 offset:32768\n"
"ds_read_b64_tr_b16 a[174:175], v5 offset:33280\n"
"ds_read_b128 a[84:87], v15 offset:28672\n"
"s_add_u32 s12, s12, 128\n"
"s_add_u32 m0, s10, 16384\n"
"v_mfma_f32_32x32x16_bf16 v[224:239], a[176:179], a[76:79], v[224:239]\n"
"ds_read_b128 a[176:179], v10 offset:4096\n"
"ds_read_b128 a[88:91], v16 offset:28672\n"
"buffer_load_dwordx4 v22, s[44:47], s12 offen offset:0 lds\n"
"v_mfma_f32_32x32x16_bf16 v[240:255], a[180:183], a[76:79], v[240:255]\n"
"ds_read_b128 a[180:183], v10 offset:45056\n"
"ds_read_b128 a[92:95], v17 offset:28672\n"
"v_mfma_f32_32x32x16_bf16 v[96:111], a[76:79], a[184:187], v[96:111]\n"
"ds_read_b64_tr_b16 a[184:185], v2 offset:34816\n"
"ds_read_b64_tr_b16 a[186:187], v2 offset:35328\n"
"buffer_load_dwordx4 v23, s[44:47], s12 offen offset:1024 lds\n"
"v_mfma_f32_32x32x16_bf16 v[112:127], a[76:79], a[188:191], v[112:127]\n"
"s_waitcnt lgkmcnt(9)\n"
"ds_read_b64_tr_b16 a[188:189], v3 offset:34816\n"
"ds_read_b64_tr_b16 a[190:191], v3 offset:35328\n"
"v_mfma_f32_32x32x16_bf16 v[192:207], a[160:163], a[80:83], v[192:207]\n"
"ds_read_b64_tr_b16 a[160:161], v4 offset:34816\n"
"ds_read_b64_tr_b16 a[162:163], v4 offset:35328\n"
"buffer_load_dwordx4 v24, s[44:47], s12 offen offset:2048 lds\n"
"v_mfma_f32_32x32x16_bf16 v[208:223], a[164:167], a[80:83], v[208:223]\n"
"ds_read_b64_tr_b16 a[164:165], v5 offset:34816\n"
"ds_read_b64_tr_b16 a[166:167], v5 offset:35328\n"
"v_mfma_f32_32x32x16_bf16 v[224:239], a[168:171], a[80:83], v[224:239]\n"
"ds_read_b128 a[168:171], v11 offset:4096\n"
"buffer_load_dwordx4 v25, s[44:47], s12 offen offset:3072 lds\n"
"s_waitcnt lgkmcnt(14)\n"
"v_mfma_f32_32x32x16_bf16 v[240:255], a[172:175], a[80:83], v[240:255]\n"
"s_waitcnt lgkmcnt(5)\n"
"ds_read_b128 a[172:175], v11 offset:45056\n"
"s_add_u32 s13, s13, 221184\n"
"s_add_u32 m0, s10, 28672\n"
"v_mfma_f32_32x32x16_bf16 v[96:111], a[80:83], a[176:179], v[96:111]\n"
"ds_read_b64_tr_b16 a[176:177], v6 offset:32768\n"
"ds_read_b64_tr_b16 a[178:179], v6 offset:33280\n"
"buffer_load_dwordx4 v18, s[48:51], s13 offen offset:0 lds\n"
"v_mfma_f32_32x32x16_bf16 v[112:127], a[80:83], a[180:183], v[112:127]\n"
"ds_read_b64_tr_b16 a[180:181], v7 offset:32768\n"
"ds_read_b64_tr_b16 a[182:183], v7 offset:33280\n"
"v_mfma_f32_32x32x16_bf16 v[192:207], a[184:187], a[84:87], v[192:207]\n"
"ds_read_b64_tr_b16 a[184:185], v8 offset:32768\n"
"ds_read_b64_tr_b16 a[186:187], v8 offset:33280\n"
"buffer_load_dwordx4 v19, s[48:51], s13 offen offset:1024 lds\n"
"v_mfma_f32_32x32x16_bf16 v[208:223], a[188:191], a[84:87], v[208:223]\n"
"s_waitcnt lgkmcnt(6)\n"
"ds_read_b64_tr_b16 a[188:189], v9 offset:32768\n"
"ds_read_b64_tr_b16 a[190:191], v9 offset:33280\n"
"v_mfma_f32_32x32x16_bf16 v[224:239], a[160:163], a[84:87], v[224:239]\n"
"ds_read_b128 a[160:163], v12 offset:4096\n"
"buffer_load_dwordx4 v20, s[48:51], s13 offen offset:2048 lds\n"
"v_mfma_f32_32x32x16_bf16 v[240:255], a[164:167], a[84:87], v[240:255]\n"
"ds_read_b128 a[164:167], v12 offset:45056\n"
"v_mfma_f32_32x32x16_bf16 v[96:111], a[84:87], a[168:171], v[96:111]\n"
"ds_read_b64_tr_b16 a[168:169], v6 offset:34816\n"
"ds_read_b64_tr_b16 a[170:171], v6 offset:35328\n"
"buffer_load_dwordx4 v21, s[48:51], s13 offen offset:3072 lds\n"
"v_mfma_f32_32x32x16_bf16 v[112:127], a[84:87], a[172:175], v[112:127]\n"
"s_waitcnt lgkmcnt(4)\n"
"ds_read_b64_tr_b16 a[172:173], v7 offset:34816\n"
"ds_read_b64_tr_b16 a[174:175], v7 offset:35328\n"
"v_mfma_f32_32x32x16_bf16 v[192:207], a[176:179], a[88:91], v[192:207]\n"
"ds_read_b64_tr_b16 a[176:177], v8 offset:34816\n"
"ds_read_b64_tr_b16 a[178:179], v8 offset:35328\n"
"v_mfma_f32_32x32x16_bf16 v[208:223], a[180:183], a[88:91], v[208:223]\n"
"ds_read_b64_tr_b16 a[180:181], v9 offset:34816\n"
"ds_read_b64_tr_b16 a[182:183], v9 offset:35328\n"
"v_mfma_f32_32x32x16_bf16 v[224:239], a[184:187], a[88:91], v[224:239]\n"
"ds_read_b128 a[184:187], v13 offset:4096\n"
"v_mfma_f32_32x32x16_bf16 v[240:255], a[188:191], a[88:91], v[240:255]\n"
"s_waitcnt lgkmcnt(5)\n"
"ds_read_b128 a[188:191], v13 offset:45056\n"
"v_mfma_f32_32x32x16_bf16 v[96:111], a[88:91], a[160:163], v[96:111]\n"
"s_waitcnt vmcnt(18)\n"
"s_barrier\n"
"ds_read_b64_tr_b16 a[160:161], v2 offset:12288\n"
"ds_read_b64_tr_b16 a[162:163], v2 offset:12800\n"
"s_add_u32 s14, s14, 128\n"
"s_add_u32 m0, s10, 2048\n"
"s_nop 0\n"
"buffer_load_dwordx4 v22, s[56:59], s14 offen offset:0 lds\n"
"v_mfma_f32_32x32x16_bf16 v[112:127], a[88:91], a[164:167], v[112:127]\n"
"ds_read_b64_tr_b16 a[164:165], v3 offset:12288\n"
"ds_read_b64_tr_b16 a[166:167], v3 offset:12800\n"
"v_mfma_f32_32x32x16_bf16 v[192:207], a[168:171], a[92:95], v[192:207]\n"
"s_waitcnt vmcnt(15)\n"
"ds_read_b64_tr_b16 a[168:169], v4 offset:12288\n"
"ds_read_b64_tr_b16 a[170:171], v4 offset:12800\n"
"ds_read_b128 a[96:99], v14 offset:20480\n"
"buffer_load_dwordx4 v23, s[56:59], s14 offen offset:1024 lds\n"
"v_mfma_f32_32x32x16_bf16 v[208:223], a[172:175], a[92:95], v[208:223]\n"
"s_waitcnt lgkmcnt(7)\n"
"ds_read_b64_tr_b16 a[172:173], v5 offset:12288\n"
"ds_read_b64_tr_b16 a[174:175], v5 offset:12800\n"
"ds_read_b128 a[100:103], v15 offset:20480\n"
"s_add_u32 s12, s12, 128\n"
"s_add_u32 m0, s10, 24576\n"
"v_mfma_f32_32x32x16_bf16 v[224:239], a[176:179], a[92:95], v[224:239]\n"
"ds_read_b128 a[176:179], v10 offset:6144\n"
"ds_read_b128 a[104:107], v16 offset:20480\n"
"buffer_load_dwordx4 v22, s[44:47], s12 offen offset:0 lds\n"
"v_mfma_f32_32x32x16_bf16 v[240:255], a[180:183], a[92:95], v[240:255]\n"
"ds_read_b128 a[180:183], v10 offset:47104\n"
"ds_read_b128 a[108:111], v17 offset:20480\n"
"v_mfma_f32_32x32x16_bf16 v[96:111], a[92:95], a[184:187], v[96:111]\n"
"ds_read_b64_tr_b16 a[184:185], v2 offset:14336\n"
"ds_read_b64_tr_b16 a[186:187], v2 offset:14848\n"
"buffer_load_dwordx4 v23, s[44:47], s12 offen offset:1024 lds\n"
"v_mfma_f32_32x32x16_bf16 v[112:127], a[92:95], a[188:191], v[112:127]\n"
"s_waitcnt lgkmcnt(9)\n"
"ds_read_b64_tr_b16 a[188:189], v3 offset:14336\n"
"ds_read_b64_tr_b16 a[190:191], v3 offset:14848\n"
"v_mfma_f32_32x32x16_bf16 v[192:207], a[160:163], a[96:99], v[192:207]\n"
"ds_read_b64_tr_b16 a[160:161], v4 offset:14336\n"
"ds_read_b64_tr_b16 a[162:163], v4 offset:14848\n"
"buffer_load_dwordx4 v24, s[44:47], s12 offen offset:2048 lds\n"
"v_mfma_f32_32x32x16_bf16 v[208:223], a[164:167], a[96:99], v[208:223]\n"
"ds_read_b64_tr_b16 a[164:165], v5 offset:14336\n"
"ds_read_b64_tr_b16 a[166:167], v5 offset:14848\n"
"v_mfma_f32_32x32x16_bf16 v[224:239], a[168:171], a[96:99], v[224:239]\n"
"ds_read_b128 a[168:171], v11 offset:6144\n"
"buffer_load_dwordx4 v25, s[44:47], s12 offen offset:3072 lds\n"
"s_waitcnt lgkmcnt(14)\n"
"v_mfma_f32_32x32x16_bf16 v[240:255], a[172:175], a[96:99], v[240:255]\n"
"s_waitcnt lgkmcnt(5)\n"
"ds_read_b128 a[172:175], v11 offset:47104\n"
"s_add_u32 s13, s13, 221184\n"
"s_add_u32 m0, s10, 20480\n"
"v_mfma_f32_32x32x16_bf16 v[96:111], a[96:99], a[176:179], v[96:111]\n"
"ds_read_b64_tr_b16 a[176:177], v6 offset:12288\n"
"ds_read_b64_tr_b16 a[178:179], v6 offset:12800\n"
"buffer_load_dwordx4 v18, s[48:51], s13 offen offset:0 lds\n"
"v_mfma_f32_32x32x16_bf16 v[112:127], a[96:99], a[180:183], v[112:127]\n"
"ds_read_b64_tr_b16 a[180:181], v7 offset:12288\n"
"ds_read_b64_tr_b16 a[182:183], v7 offset:12800\n"
"v_mfma_f32_32x32x16_bf16 v[192:207], a[184:187], a[100:103], v[192:207]\n"
"ds_read_b64_tr_b16 a[184:185], v8 offset:12288\n"
"ds_read_b64_tr_b16 a[186:187], v8 offset:12800\n"
"buffer_load_dwordx4 v19, s[48:51], s13 offen offset:1024 lds\n"
"v_mfma_f32_32x32x16_bf16 v[208:223], a[188:191], a[100:103], v[208:223]\n"
"s_waitcnt lgkmcnt(6)\n"
"ds_read_b64_tr_b16 a[188:189], v9 offset:12288\n"
"ds_read_b64_tr_b16 a[190:191], v9 offset:12800\n"
"v_mfma_f32_32x32x16_bf16 v[224:239], a[160:163], a[100:103], v[224:239]\n"
"ds_read_b128 a[160:163], v12 offset:6144\n"
"buffer_load_dwordx4 v20, s[48:51], s13 offen offset:2048 lds\n"
"v_mfma_f32_32x32x16_bf16 v[240:255], a[164:167], a[100:103], v[240:255]\n"
"ds_read_b128 a[164:167], v12 offset:47104\n"
"v_mfma_f32_32x32x16_bf16 v[96:111], a[100:103], a[168:171], v[96:111]\n"
"ds_read_b64_tr_b16 a[168:169], v6 offset:14336\n"
"ds_read_b64_tr_b16 a[170:171], v6 offset:14848\n"
"buffer_load_dwordx4 v21, s[48:51], s13 offen offset:3072 lds\n"
"v_mfma_f32_32x32x16_bf16 v[112:127], a[100:103], a[172:175], v[112:127]\n"
"s_waitcnt lgkmcnt(4)\n"
"ds_read_b64_tr_b16 a[172:173], v7 offset:14336\n"
"ds_read_b64_tr_b16 a[174:175], v7 offset:14848\n"
"v_mfma_f32_32x32x16_bf16 v[192:207], a[176:179], a[104:107], v[192:207]\n"
"ds_read_b64_tr_b16 a[176:177], v8 offset:14336\n"
"ds_read_b64_tr_b16 a[178:179], v8 offset:14848\n"
"v_mfma_f32_32x32x16_bf16 v[208:223], a[180:183], a[104:107], v[208:223]\n"
"ds_read_b64_tr_b16 a[180:181], v9 offset:14336\n"
"ds_read_b64_tr_b16 a[182:183], v9 offset:14848\n"
"v_mfma_f32_32x32x16_bf16 v[224:239], a[184:187], a[104:107], v[224:239]\n"
"ds_read_b128 a[184:187], v13 offset:6144\n"
"v_mfma_f32_32x32x16_bf16 v[240:255], a[188:191], a[104:107], v[240:255]\n"
"s_waitcnt lgkmcnt(5)\n"
"ds_read_b128 a[188:191], v13 offset:47104\n"
"v_mfma_f32_32x32x16_bf16 v[96:111], a[104:107], a[160:163], v[96:111]\n"
"s_waitcnt vmcnt(18)\n"
"s_barrier\n"
"ds_read_b64_tr_b16 a[160:161], v2 offset:36864\n"
"ds_read_b64_tr_b16 a[162:163], v2 offset:37376\n"
"s_add_u32 s14, s14, 128\n"
"s_add_u32 m0, s10, 4096\n"
"s_nop 0\n"
"buffer_load_dwordx4 v22, s[56:59], s14 offen offset:0 lds\n"
"v_mfma_f32_32x32x16_bf16 v[112:127], a[104:107], a[164:167], v[112:127]\n"
"ds_read_b64_tr_b16 a[164:165], v3 offset:36864\n"
"ds_read_b64_tr_b16 a[166:167], v3 offset:37376\n"
"v_lshl_add_u32 v78, v1, 4, s91\n"
"v_add_u32 v79, 21037056, v28\n"
"v_mfma_f32_32x32x16_bf16 v[192:207], a[168:171], a[108:111], v[192:207]\n"
"s_waitcnt vmcnt(15)\n"
"ds_read_b64_tr_b16 a[168:169], v4 offset:36864\n"
"ds_read_b64_tr_b16 a[170:171], v4 offset:37376\n"
"ds_read_b128 a[112:115], v14 offset:16384\n"
"buffer_load_dwordx4 v23, s[56:59], s14 offen offset:1024 lds\n"
"v_mfma_f32_32x32x16_bf16 v[208:223], a[172:175], a[108:111], v[208:223]\n"
"s_waitcnt lgkmcnt(7)\n"
"ds_read_b64_tr_b16 a[172:173], v5 offset:36864\n"
"ds_read_b64_tr_b16 a[174:175], v5 offset:37376\n"
"ds_read_b128 a[116:119], v15 offset:16384\n"
"s_add_u32 s12, s12, 128\n"
"s_add_u32 m0, s10, 32768\n"
"v_mfma_f32_32x32x16_bf16 v[224:239], a[176:179], a[108:111], v[224:239]\n"
"ds_read_b128 a[176:179], v10 offset:8192\n"
"ds_read_b128 a[120:123], v16 offset:16384\n"
"buffer_load_dwordx4 v22, s[44:47], s12 offen offset:0 lds\n"
"v_mfma_f32_32x32x16_bf16 v[240:255], a[180:183], a[108:111], v[240:255]\n"
"ds_read_b128 a[180:183], v10 offset:49152\n"
"ds_read_b128 a[124:127], v17 offset:16384\n"
"buffer_load_dwordx4 v[64:67], v78, s[64:67], 0 offen offset:0 sc1\n"
"v_mfma_f32_32x32x16_bf16 v[96:111], a[108:111], a[184:187], v[96:111]\n"
"ds_read_b64_tr_b16 a[184:185], v2 offset:38912\n"
"ds_read_b64_tr_b16 a[186:187], v2 offset:39424\n"
"buffer_load_dwordx4 v23, s[44:47], s12 offen offset:1024 lds\n"
"v_mfma_f32_32x32x16_bf16 v[112:127], a[108:111], a[188:191], v[112:127]\n"
"s_waitcnt lgkmcnt(9)\n"
"ds_read_b64_tr_b16 a[188:189], v3 offset:38912\n"
"ds_read_b64_tr_b16 a[190:191], v3 offset:39424\n"
"buffer_load_dwordx4 a[192:195], v79, s[64:67], s93 offen offset:0 sc1\n"
"v_mfma_f32_32x32x16_bf16 v[192:207], a[160:163], a[112:115], v[192:207]\n"
"ds_read_b64_tr_b16 a[160:161], v4 offset:38912\n"
"ds_read_b64_tr_b16 a[162:163], v4 offset:39424\n"
"buffer_load_dwordx4 v24, s[44:47], s12 offen offset:2048 lds\n"
"v_mfma_f32_32x32x16_bf16 v[208:223], a[164:167], a[112:115], v[208:223]\n"
"ds_read_b64_tr_b16 a[164:165], v5 offset:38912\n"
"ds_read_b64_tr_b16 a[166:167], v5 offset:39424\n"
"v_mfma_f32_32x32x16_bf16 v[224:239], a[168:171], a[112:115], v[224:239]\n"
"ds_read_b128 a[168:171], v11 offset:8192\n"
"buffer_load_dwordx4 v25, s[44:47], s12 offen offset:3072 lds\n"
"s_waitcnt lgkmcnt(14)\n"
"v_mfma_f32_32x32x16_bf16 v[240:255], a[172:175], a[112:115], v[240:255]\n"
"s_waitcnt lgkmcnt(5)\n"
"ds_read_b128 a[172:175], v11 offset:49152\n"
"s_add_u32 m0, s10, 16384\n"
"v_mfma_f32_32x32x16_bf16 v[96:111], a[112:115], a[176:179], v[96:111]\n"
"ds_read_b64_tr_b16 a[176:177], v6 offset:36864\n"
"ds_read_b64_tr_b16 a[178:179], v6 offset:37376\n"
"buffer_load_dwordx4 v18, s[48:51], s52 offen offset:0 lds\n"
"v_mfma_f32_32x32x16_bf16 v[112:127], a[112:115], a[180:183], v[112:127]\n"
"ds_read_b64_tr_b16 a[180:181], v7 offset:36864\n"
"ds_read_b64_tr_b16 a[182:183], v7 offset:37376\n"
"v_mfma_f32_32x32x16_bf16 v[192:207], a[184:187], a[116:119], v[192:207]\n"
"ds_read_b64_tr_b16 a[184:185], v8 offset:36864\n"
"ds_read_b64_tr_b16 a[186:187], v8 offset:37376\n"
"buffer_load_dwordx4 v19, s[48:51], s52 offen offset:1024 lds\n"
"v_mfma_f32_32x32x16_bf16 v[208:223], a[188:191], a[116:119], v[208:223]\n"
"s_waitcnt lgkmcnt(6)\n"
"ds_read_b64_tr_b16 a[188:189], v9 offset:36864\n"
"ds_read_b64_tr_b16 a[190:191], v9 offset:37376\n"
"v_mfma_f32_32x32x16_bf16 v[224:239], a[160:163], a[116:119], v[224:239]\n"
"ds_read_b128 a[160:163], v12 offset:8192\n"
"buffer_load_dwordx4 v20, s[48:51], s52 offen offset:2048 lds\n"
"v_mfma_f32_32x32x16_bf16 v[240:255], a[164:167], a[116:119], v[240:255]\n"
"ds_read_b128 a[164:167], v12 offset:49152\n"
"v_mfma_f32_32x32x16_bf16 v[96:111], a[116:119], a[168:171], v[96:111]\n"
"ds_read_b64_tr_b16 a[168:169], v6 offset:38912\n"
"ds_read_b64_tr_b16 a[170:171], v6 offset:39424\n"
"buffer_load_dwordx4 v21, s[48:51], s52 offen offset:3072 lds\n"
"v_mfma_f32_32x32x16_bf16 v[112:127], a[116:119], a[172:175], v[112:127]\n"
"s_waitcnt lgkmcnt(4)\n"
"ds_read_b64_tr_b16 a[172:173], v7 offset:38912\n"
"ds_read_b64_tr_b16 a[174:175], v7 offset:39424\n"
"v_mfma_f32_32x32x16_bf16 v[192:207], a[176:179], a[120:123], v[192:207]\n"
"ds_read_b64_tr_b16 a[176:177], v8 offset:38912\n"
"ds_read_b64_tr_b16 a[178:179], v8 offset:39424\n"
"v_mfma_f32_32x32x16_bf16 v[208:223], a[180:183], a[120:123], v[208:223]\n"
"ds_read_b64_tr_b16 a[180:181], v9 offset:38912\n"
"ds_read_b64_tr_b16 a[182:183], v9 offset:39424\n"
"v_mfma_f32_32x32x16_bf16 v[224:239], a[184:187], a[120:123], v[224:239]\n"
"ds_read_b128 a[184:187], v13 offset:8192\n"
"v_mfma_f32_32x32x16_bf16 v[240:255], a[188:191], a[120:123], v[240:255]\n"
"s_waitcnt lgkmcnt(5)\n"
"ds_read_b128 a[188:191], v13 offset:49152\n"
"v_mfma_f32_32x32x16_bf16 v[96:111], a[120:123], a[160:163], v[96:111]\n"
"s_waitcnt vmcnt(20)\n"
"s_barrier\n"
"ds_read_b64_tr_b16 a[160:161], v2 offset:28672\n"
"ds_read_b64_tr_b16 a[162:163], v2 offset:29184\n"
"s_add_u32 s52, s52, 221184\n"
"s_add_u32 m0, s10, 12288\n"
"s_nop 0\n"
"buffer_load_dwordx4 v18, s[48:51], s52 offen offset:0 lds\n"
"v_mfma_f32_32x32x16_bf16 v[112:127], a[120:123], a[164:167], v[112:127]\n"
"ds_read_b64_tr_b16 a[164:165], v3 offset:28672\n"
"ds_read_b64_tr_b16 a[166:167], v3 offset:29184\n"
"v_mfma_f32_32x32x16_bf16 v[192:207], a[168:171], a[124:127], v[192:207]\n"
"s_waitcnt vmcnt(17)\n"
"ds_read_b64_tr_b16 a[168:169], v4 offset:28672\n"
"ds_read_b64_tr_b16 a[170:171], v4 offset:29184\n"
"ds_read_b128 a[128:131], v14 offset:24576\n"
"buffer_load_dwordx4 v19, s[48:51], s52 offen offset:1024 lds\n"
"v_mfma_f32_32x32x16_bf16 v[208:223], a[172:175], a[124:127], v[208:223]\n"
"s_waitcnt lgkmcnt(7)\n"
"ds_read_b64_tr_b16 a[172:173], v5 offset:28672\n"
"ds_read_b64_tr_b16 a[174:175], v5 offset:29184\n"
"ds_read_b128 a[132:135], v15 offset:24576\n"
"v_mfma_f32_32x32x16_bf16 v[224:239], a[176:179], a[124:127], v[224:239]\n"
"ds_read_b128 a[176:179], v10 offset:2048\n"
"ds_read_b128 a[136:139], v16 offset:24576\n"
"buffer_load_dwordx4 v20, s[48:51], s52 offen offset:2048 lds\n"
"v_mfma_f32_32x32x16_bf16 v[240:255], a[180:183], a[124:127], v[240:255]\n"
"ds_read_b128 a[180:183], v10 offset:43008\n"
"ds_read_b128 a[140:143], v17 offset:24576\n"
"v_mfma_f32_32x32x16_bf16 v[96:111], a[124:127], a[184:187], v[96:111]\n"
"ds_read_b64_tr_b16 a[184:185], v2 offset:30720\n"
"ds_read_b64_tr_b16 a[186:187], v2 offset:31232\n"
"buffer_load_dwordx4 v21, s[48:51], s52 offen offset:3072 lds\n"
"v_mfma_f32_32x32x16_bf16 v[112:127], a[124:127], a[188:191], v[112:127]\n"
"s_waitcnt lgkmcnt(9)\n"
"ds_read_b64_tr_b16 a[188:189], v3 offset:30720\n"
"ds_read_b64_tr_b16 a[190:191], v3 offset:31232\n"
"v_mfma_f32_32x32x16_bf16 v[192:207], a[160:163], a[128:131], v[192:207]\n"
"ds_read_b64_tr_b16 a[160:161], v4 offset:30720\n"
"ds_read_b64_tr_b16 a[162:163], v4 offset:31232\n"
"s_waitcnt lgkmcnt(6)\n"
"s_add_u32 s52, s52, 221184\n"
"s_add_u32 m0, s10, 24576\n"
"s_nop 0\n"
"buffer_load_dwordx4 v18, s[48:51], s52 offen offset:0 lds\n"
"v_mfma_f32_32x32x16_bf16 v[208:223], a[164:167], a[128:131], v[208:223]\n"
"ds_read_b64_tr_b16 a[164:165], v5 offset:30720\n"
"ds_read_b64_tr_b16 a[166:167], v5 offset:31232\n"
"v_mfma_f32_32x32x16_bf16 v[224:239], a[168:171], a[128:131], v[224:239]\n"
"ds_read_b128 a[168:171], v11 offset:2048\n"
"buffer_load_dwordx4 v19, s[48:51], s52 offen offset:1024 lds\n"
"v_mfma_f32_32x32x16_bf16 v[240:255], a[172:175], a[128:131], v[240:255]\n"
"ds_read_b128 a[172:175], v11 offset:43008\n"
"v_mfma_f32_32x32x16_bf16 v[96:111], a[128:131], a[176:179], v[96:111]\n"
"ds_read_b64_tr_b16 a[176:177], v6 offset:28672\n"
"ds_read_b64_tr_b16 a[178:179], v6 offset:29184\n"
"buffer_load_dwordx4 v20, s[48:51], s52 offen offset:2048 lds\n"
"v_mfma_f32_32x32x16_bf16 v[112:127], a[128:131], a[180:183], v[112:127]\n"
"ds_read_b64_tr_b16 a[180:181], v7 offset:28672\n"
"ds_read_b64_tr_b16 a[182:183], v7 offset:29184\n"
"s_waitcnt lgkmcnt(10)\n"
"v_mfma_f32_32x32x16_bf16 v[192:207], a[184:187], a[132:135], v[192:207]\n"
"ds_read_b64_tr_b16 a[184:185], v8 offset:28672\n"
"ds_read_b64_tr_b16 a[186:187], v8 offset:29184\n"
"buffer_load_dwordx4 v21, s[48:51], s52 offen offset:3072 lds\n"
"v_mfma_f32_32x32x16_bf16 v[208:223], a[188:191], a[132:135], v[208:223]\n"
"s_waitcnt lgkmcnt(6)\n"
"ds_read_b64_tr_b16 a[188:189], v9 offset:28672\n"
"ds_read_b64_tr_b16 a[190:191], v9 offset:29184\n"
"v_mfma_f32_32x32x16_bf16 v[224:239], a[160:163], a[132:135], v[224:239]\n"
"ds_read_b128 a[160:163], v12 offset:2048\n"
"v_mfma_f32_32x32x16_bf16 v[240:255], a[164:167], a[132:135], v[240:255]\n"
"ds_read_b128 a[164:167], v12 offset:43008\n"
"v_mfma_f32_32x32x16_bf16 v[96:111], a[132:135], a[168:171], v[96:111]\n"
"ds_read_b64_tr_b16 a[168:169], v6 offset:30720\n"
"ds_read_b64_tr_b16 a[170:171], v6 offset:31232\n"
"v_mfma_f32_32x32x16_bf16 v[112:127], a[132:135], a[172:175], v[112:127]\n"
"s_waitcnt lgkmcnt(4)\n"
"ds_read_b64_tr_b16 a[172:173], v7 offset:30720\n"
"ds_read_b64_tr_b16 a[174:175], v7 offset:31232\n"
"v_mfma_f32_32x32x16_bf16 v[192:207], a[176:179], a[136:139], v[192:207]\n"
"ds_read_b64_tr_b16 a[176:177], v8 offset:30720\n"
"ds_read_b64_tr_b16 a[178:179], v8 offset:31232\n"
"v_mfma_f32_32x32x16_bf16 v[208:223], a[180:183], a[136:139], v[208:223]\n"
"ds_read_b64_tr_b16 a[180:181], v9 offset:30720\n"
"ds_read_b64_tr_b16 a[182:183], v9 offset:31232\n"
"v_mfma_f32_32x32x16_bf16 v[224:239], a[184:187], a[136:139], v[224:239]\n"
"ds_read_b128 a[184:187], v13 offset:2048\n"
"v_mfma_f32_32x32x16_bf16 v[240:255], a[188:191], a[136:139], v[240:255]\n"
"s_waitcnt lgkmcnt(5)\n"
"ds_read_b128 a[188:191], v13 offset:43008\n"
"v_mfma_f32_32x32x16_bf16 v[96:111], a[136:139], a[160:163], v[96:111]\n"
"s_waitcnt vmcnt(18)\n"
"s_barrier\n"
"ds_read_b64_tr_b16 a[160:161], v2 offset:20480\n"
"ds_read_b64_tr_b16 a[162:163], v2 offset:20992\n"
"s_add_u32 s52, s52, 221184\n"
"s_add_u32 m0, s10, 36864\n"
"s_nop 0\n"
"buffer_load_dwordx4 v18, s[48:51], s52 offen offset:0 lds\n"
"v_mfma_f32_32x32x16_bf16 v[112:127], a[136:139], a[164:167], v[112:127]\n"
"ds_read_b64_tr_b16 a[164:165], v3 offset:20480\n"
"ds_read_b64_tr_b16 a[166:167], v3 offset:20992\n"
"v_mfma_f32_32x32x16_bf16 v[192:207], a[168:171], a[140:143], v[192:207]\n"
"s_waitcnt vmcnt(13)\n"
"ds_read_b64_tr_b16 a[168:169], v4 offset:20480\n"
"ds_read_b64_tr_b16 a[170:171], v4 offset:20992\n"
"ds_read_b128 a[144:147], v14 offset:32768\n"
"buffer_load_dwordx4 v19, s[48:51], s52 offen offset:1024 lds\n"
"v_mfma_f32_32x32x16_bf16 v[208:223], a[172:175], a[140:143], v[208:223]\n"
"s_waitcnt lgkmcnt(7)\n"
"ds_read_b64_tr_b16 a[172:173], v5 offset:20480\n"
"ds_read_b64_tr_b16 a[174:175], v5 offset:20992\n"
"ds_read_b128 a[148:151], v15 offset:32768\n"
"v_mfma_f32_32x32x16_bf16 v[224:239], a[176:179], a[140:143], v[224:239]\n"
"ds_read_b128 a[176:179], v10 offset:4096\n"
"ds_read_b128 a[152:155], v16 offset:32768\n"
"buffer_load_dwordx4 v20, s[48:51], s52 offen offset:2048 lds\n"
"v_mfma_f32_32x32x16_bf16 v[240:255], a[180:183], a[140:143], v[240:255]\n"
"ds_read_b128 a[180:183], v10 offset:45056\n"
"ds_read_b128 a[156:159], v17 offset:32768\n"
"v_mfma_f32_32x32x16_bf16 v[96:111], a[140:143], a[184:187], v[96:111]\n"
"ds_read_b64_tr_b16 a[184:185], v2 offset:22528\n"
"ds_read_b64_tr_b16 a[186:187], v2 offset:23040\n"
"buffer_load_dwordx4 v21, s[48:51], s52 offen offset:3072 lds\n"
"v_mfma_f32_32x32x16_bf16 v[112:127], a[140:143], a[188:191], v[112:127]\n"
"s_waitcnt lgkmcnt(9)\n"
"ds_read_b64_tr_b16 a[188:189], v3 offset:22528\n"
"ds_read_b64_tr_b16 a[190:191], v3 offset:23040\n"
"s_add_u32 s52, s52, 221184\n"
"s_add_u32 m0, s10, 8192\n"
"v_mfma_f32_32x32x16_bf16 v[192:207], a[160:163], a[144:147], v[192:207]\n"
"ds_read_b64_tr_b16 a[160:161], v4 offset:22528\n"
"ds_read_b64_tr_b16 a[162:163], v4 offset:23040\n"
"buffer_load_dwordx4 v18, s[48:51], s52 offen offset:0 lds\n"
"v_xor_b32 v69, s72, v65\n"
"v_mfma_f32_32x32x16_bf16 v[208:223], a[164:167], a[144:147], v[208:223]\n"
"ds_read_b64_tr_b16 a[164:165], v5 offset:22528\n"
"ds_read_b64_tr_b16 a[166:167], v5 offset:23040\n"
"v_xor_b32 v70, s73, v67\n"
"v_or3_b32 v68, v69, v70, 0\n"
"v_accvgpr_read_b32 v69, a193\n"
"v_accvgpr_read_b32 v70, a195\n"
"v_xor_b32 v69, s72, v69\n"
"v_mfma_f32_32x32x16_bf16 v[224:239], a[168:171], a[144:147], v[224:239]\n"
"ds_read_b128 a[168:171], v11 offset:4096\n"
"buffer_load_dwordx4 v19, s[48:51], s52 offen offset:1024 lds\n"
"v_xor_b32 v70, s73, v70\n"
"v_or3_b32 v68, v69, v70, v68\n"
"s_waitcnt lgkmcnt(14)\n"
"v_mfma_f32_32x32x16_bf16 v[240:255], a[172:175], a[144:147], v[240:255]\n"
"s_waitcnt lgkmcnt(5)\n"
"ds_read_b128 a[172:175], v11 offset:45056\n"
"v_cmp_ne_u32 vcc, 0, v68\n"
"s_nop 0\n"
"s_cmp_lg_u64 vcc, 0\n"
"s_cbranch_scc1 K1F_l_slow\n"
"K1F_l_back:\n"
"s_lshl_b32 s68, s7, 8\n"
"v_mfma_f32_32x32x16_bf16 v[96:111], a[144:147], a[176:179], v[96:111]\n"
"ds_read_b64_tr_b16 a[176:177], v6 offset:20480\n"
"ds_read_b64_tr_b16 a[178:179], v6 offset:20992\n"
"buffer_load_dwordx4 v20, s[48:51], s52 offen offset:2048 lds\n"
"s_add_u32 s68, s68, 81920\n"
"v_mfma_f32_32x32x16_bf16 v[112:127], a[144:147], a[180:183], v[112:127]\n"
"ds_read_b64_tr_b16 a[180:181], v7 offset:20480\n"
"ds_read_b64_tr_b16 a[182:183], v7 offset:20992\n"
"v_and_b32 v69, 0x1ff, v28\n"
"v_cvt_pk_bf16_f32 v68, -v66, -v66\n"
"v_lshrrev_b32 v70, 5, v27\n"
"v_lshl_add_u32 v14, v70, 6, s68\n"
"v_cndmask_b32 v40, v68, 0, s[16:17]\n"
"v_mfma_f32_32x32x16_bf16 v[192:207], a[184:187], a[148:151], v[192:207]\n"
"ds_read_b64_tr_b16 a[184:185], v8 offset:20480\n"
"ds_read_b64_tr_b16 a[186:187], v8 offset:20992\n"
"buffer_load_dwordx4 v21, s[48:51], s52 offen offset:3072 lds\n"
"v_mov_b32 v41, 0\n"
"v_mfma_f32_32x32x16_bf16 v[208:223], a[188:191], a[148:151], v[208:223]\n"
"s_waitcnt lgkmcnt(6)\n"
"ds_read_b64_tr_b16 a[188:189], v9 offset:20480\n"
"ds_read_b64_tr_b16 a[190:191], v9 offset:20992\n"
"s_add_u32 s52, s52, 221184\n"
"s_add_u32 m0, s10, 32768\n"
"v_and_b32 v42, 0xffff, v40\n"
"v_mfma_f32_32x32x16_bf16 v[224:239], a[160:163], a[148:151], v[224:239]\n"
"ds_read_b128 a[160:163], v12 offset:4096\n"
"buffer_load_dwordx4 v18, s[48:51], s52 offen offset:0 lds\n"
"v_mov_b32 v43, 0\n"
"v_cndmask_b32 v68, 0, v68, s[16:17]\n"
"v_mfma_f32_32x32x16_bf16 v[240:255], a[164:167], a[148:151], v[240:255]\n"
"ds_read_b128 a[164:167], v12 offset:45056\n"
"v_accvgpr_write_b32 a197, 0\n"
"v_accvgpr_write_b32 a196, v68\n"
"v_and_b32 v68, 0xffff, v68\n"
"v_accvgpr_write_b32 a199, 0\n"
"v_accvgpr_write_b32 a198, v68\n"
"v_mfma_f32_32x32x16_bf16 v[96:111], a[148:151], a[168:171], v[96:111]\n"
"ds_read_b64_tr_b16 a[168:169], v6 offset:22528\n"
"ds_read_b64_tr_b16 a[170:171], v6 offset:23040\n"
"buffer_load_dwordx4 v19, s[48:51], s52 offen offset:1024 lds\n"
"s_mov_b32 s69, m0\n"
"s_mov_b32 m0, s68\n"
"s_nop 0\n"
"buffer_load_dword v69, s[64:67], s91 offen sc1 lds\n"
"s_mov_b32 m0, s69\n"
"v_mfma_f32_32x32x16_bf16 v[112:127], a[148:151], a[172:175], v[112:127]\n"
"s_waitcnt lgkmcnt(4)\n"
"ds_read_b64_tr_b16 a[172:173], v7 offset:22528\n"
"ds_read_b64_tr_b16 a[174:175], v7 offset:23040\n"
"v_mul_f32 v37, v64, v64\n"
"v_mul_f32 v36, 0xbfb8aa3b, v64\n"
"v_rcp_f32 v37, v37\n"
"v_mfma_f32_32x32x16_bf16 v[192:207], a[176:179], a[152:155], v[192:207]\n"
"ds_read_b64_tr_b16 a[176:177], v8 offset:22528\n"
"ds_read_b64_tr_b16 a[178:179], v8 offset:23040\n"
"buffer_load_dwordx4 v20, s[48:51], s52 offen offset:2048 lds\n"
"v_mfma_f32_32x32x16_bf16 v[208:223], a[180:183], a[152:155], v[208:223]\n"
"ds_read_b64_tr_b16 a[180:181], v9 offset:22528\n"
"ds_read_b64_tr_b16 a[182:183], v9 offset:23040\n"
"v_mfma_f32_32x32x16_bf16 v[224:239], a[184:187], a[152:155], v[224:239]\n"
"ds_read_b128 a[184:187], v13 offset:4096\n"
"buffer_load_dwordx4 v21, s[48:51], s52 offen offset:3072 lds\n"
"v_mfma_f32_32x32x16_bf16 v[240:255], a[188:191], a[152:155], v[240:255]\n"
"s_waitcnt lgkmcnt(5)\n"
"ds_read_b128 a[188:191], v13 offset:45056\n"
"v_mfma_f32_32x32x16_bf16 v[96:111], a[152:155], a[160:163], v[96:111]\n"
"s_waitcnt vmcnt(21)\n"
"s_barrier\n"
"ds_read_b64_tr_b16 a[160:161], v2 offset:16384\n"
"ds_read_b64_tr_b16 a[162:163], v2 offset:16896\n"
"s_add_u32 s52, s52, 221184\n"
"s_add_u32 m0, s10, 28672\n"
"s_nop 0\n"
"buffer_load_dwordx4 v18, s[48:51], s52 offen offset:0 lds\n"
"v_mfma_f32_32x32x16_bf16 v[112:127], a[152:155], a[164:167], v[112:127]\n"
"ds_read_b64_tr_b16 a[164:165], v3 offset:16384\n"
"ds_read_b64_tr_b16 a[166:167], v3 offset:16896\n"
"v_mfma_f32_32x32x16_bf16 v[192:207], a[168:171], a[156:159], v[192:207]\n"
"ds_read_b64_tr_b16 a[168:169], v4 offset:16384\n"
"ds_read_b64_tr_b16 a[170:171], v4 offset:16896\n"
"buffer_load_dwordx4 v19, s[48:51], s52 offen offset:1024 lds\n"
"v_mfma_f32_32x32x16_bf16 v[208:223], a[172:175], a[156:159], v[208:223]\n"
"s_waitcnt lgkmcnt(6)\n"
"ds_read_b64_tr_b16 a[172:173], v5 offset:16384\n"
"ds_read_b64_tr_b16 a[174:175], v5 offset:16896\n"
"v_mfma_f32_32x32x16_bf16 v[224:239], a[176:179], a[156:159], v[224:239]\n"
"ds_read_b64_tr_b16 a[176:177], v2 offset:18432\n"
"ds_read_b64_tr_b16 a[178:179], v2 offset:18944\n"
"buffer_load_dwordx4 v20, s[48:51], s52 offen offset:2048 lds\n"
"v_mfma_f32_32x32x16_bf16 v[240:255], a[180:183], a[156:159], v[240:255]\n"
"ds_read_b64_tr_b16 a[180:181], v3 offset:18432\n"
"ds_read_b64_tr_b16 a[182:183], v3 offset:18944\n"
"v_mfma_f32_32x32x16_bf16 v[96:111], a[156:159], a[184:187], v[96:111]\n"
"ds_read_b64_tr_b16 a[184:185], v4 offset:18432\n"
"ds_read_b64_tr_b16 a[186:187], v4 offset:18944\n"
"buffer_load_dwordx4 v21, s[48:51], s52 offen offset:3072 lds\n"
"v_mfma_f32_32x32x16_bf16 v[112:127], a[156:159], a[188:191], v[112:127]\n"
"ds_read_b64_tr_b16 a[188:189], v5 offset:18432\n"
"ds_read_b64_tr_b16 a[190:191], v5 offset:18944\n"
"v_mfma_f32_32x32x16_bf16 v[96:111], v[40:43], a[192:195], v[96:111]\n"
"v_mfma_f32_32x32x16_bf16 v[112:127], a[196:199], a[192:195], v[112:127]\n"
"s_waitcnt lgkmcnt(8)\n"
"v_mfma_f32_32x32x16_bf16 v[128:143], a[160:163], a[0:3], 0\n"
"ds_read_b64_tr_b16 a[160:161], v6 offset:16384\n"
"ds_read_b64_tr_b16 a[162:163], v6 offset:16896\n"
"v_mfma_f32_32x32x16_bf16 v[144:159], a[164:167], a[0:3], 0\n"
"ds_read_b64_tr_b16 a[164:165], v7 offset:16384\n"
"ds_read_b64_tr_b16 a[166:167], v7 offset:16896\n"
"v_mfma_f32_32x32x16_bf16 v[160:175], a[168:171], a[0:3], 0\n"
"ds_read_b64_tr_b16 a[168:169], v8 offset:16384\n"
"ds_read_b64_tr_b16 a[170:171], v8 offset:16896\n"
"s_waitcnt vmcnt(6)\n"
"ds_read_b128 v[68:71], v14 offset:0\n"
"ds_read_b128 v[72:75], v14 offset:16\n"
"v_mfma_f32_32x32x16_bf16 v[176:191], a[172:175], a[0:3], 0\n"
"s_waitcnt lgkmcnt(8)\n"
"ds_read_b64_tr_b16 a[172:173], v9 offset:16384\n"
"ds_read_b64_tr_b16 a[174:175], v9 offset:16896\n"
"v_mfma_f32_32x32x16_bf16 v[128:143], a[176:179], a[4:7], v[128:143]\n"
"ds_read_b64_tr_b16 a[176:177], v6 offset:18432\n"
"ds_read_b64_tr_b16 a[178:179], v6 offset:18944\n"
"s_waitcnt lgkmcnt(4)\n"
"v_mul_f32 v96, v96, v68\n"
"v_mul_f32 v112, v112, v68\n"
"v_mul_f32 v97, v97, v69\n"
"v_mul_f32 v113, v113, v69\n"
"v_mul_f32 v98, v98, v70\n"
"v_mfma_f32_32x32x16_bf16 v[144:159], a[180:183], a[4:7], v[144:159]\n"
"ds_read_b64_tr_b16 a[180:181], v7 offset:18432\n"
"ds_read_b64_tr_b16 a[182:183], v7 offset:18944\n"
"v_mul_f32 v114, v114, v70\n"
"v_mul_f32 v99, v99, v71\n"
"v_mul_f32 v115, v115, v71\n"
"v_mul_f32 v100, v100, v72\n"
"v_mul_f32 v116, v116, v72\n"
"v_mfma_f32_32x32x16_bf16 v[160:175], a[184:187], a[4:7], v[160:175]\n"
"ds_read_b64_tr_b16 a[184:185], v8 offset:18432\n"
"ds_read_b64_tr_b16 a[186:187], v8 offset:18944\n"
"v_mul_f32 v101, v101, v73\n"
"v_mul_f32 v117, v117, v73\n"
"v_mul_f32 v102, v102, v74\n"
"v_mul_f32 v118, v118, v74\n"
"v_mul_f32 v103, v103, v75\n"
"v_mfma_f32_32x32x16_bf16 v[176:191], a[188:191], a[4:7], v[176:191]\n"
"ds_read_b64_tr_b16 a[188:189], v9 offset:18432\n"
"ds_read_b64_tr_b16 a[190:191], v9 offset:18944\n"
"v_mul_f32 v119, v119, v75\n"
"v_mfma_f32_32x32x16_bf16 v[128:143], a[160:163], a[8:11], v[128:143]\n"
"s_barrier\n"
"ds_read_b64_tr_b16 a[160:161], v2 offset:12288\n"
"ds_read_b64_tr_b16 a[162:163], v2 offset:12800\n"
"s_add_u32 m0, s10, 4096\n"
"s_nop 0\n"
"buffer_load_dwordx4 v22, s[60:63], 0 offen offset:0 lds\n"
"v_mfma_f32_32x32x16_bf16 v[144:159], a[164:167], a[8:11], v[144:159]\n"
"ds_read_b64_tr_b16 a[164:165], v3 offset:12288\n"
"ds_read_b64_tr_b16 a[166:167], v3 offset:12800\n"
"ds_read_b128 v[68:71], v14 offset:32\n"
"ds_read_b128 v[72:75], v14 offset:48\n"
"v_mfma_f32_32x32x16_bf16 v[160:175], a[168:171], a[8:11], v[160:175]\n"
"ds_read_b64_tr_b16 a[168:169], v4 offset:12288\n"
"ds_read_b64_tr_b16 a[170:171], v4 offset:12800\n"
"buffer_load_dwordx4 v23, s[60:63], 0 offen offset:1024 lds\n"
"s_waitcnt lgkmcnt(15)\n"
"v_mfma_f32_32x32x16_bf16 v[176:191], a[172:175], a[8:11], v[176:191]\n"
"s_waitcnt lgkmcnt(8)\n"
"ds_read_b64_tr_b16 a[172:173], v5 offset:12288\n"
"ds_read_b64_tr_b16 a[174:175], v5 offset:12800\n"
"s_waitcnt lgkmcnt(4)\n"
"v_mul_f32 v104, v104, v68\n"
"v_mul_f32 v120, v120, v68\n"
"v_mul_f32 v105, v105, v69\n"
"v_mul_f32 v121, v121, v69\n"
"v_mfma_f32_32x32x16_bf16 v[128:143], a[176:179], a[12:15], v[128:143]\n"
"ds_read_b64_tr_b16 a[176:177], v2 offset:14336\n"
"ds_read_b64_tr_b16 a[178:179], v2 offset:14848\n"
"buffer_load_dwordx4 v24, s[60:63], 0 offen offset:2048 lds\n"
"v_mul_f32 v106, v106, v70\n"
"v_mfma_f32_32x32x16_bf16 v[144:159], a[180:183], a[12:15], v[144:159]\n"
"ds_read_b64_tr_b16 a[180:181], v3 offset:14336\n"
"ds_read_b64_tr_b16 a[182:183], v3 offset:14848\n"
"v_mul_f32 v122, v122, v70\n"
"v_mul_f32 v107, v107, v71\n"
"v_mul_f32 v123, v123, v71\n"
"v_mul_f32 v108, v108, v72\n"
"v_mul_f32 v124, v124, v72\n"
"v_mfma_f32_32x32x16_bf16 v[160:175], a[184:187], a[12:15], v[160:175]\n"
"ds_read_b64_tr_b16 a[184:185], v4 offset:14336\n"
"ds_read_b64_tr_b16 a[186:187], v4 offset:14848\n"
"buffer_load_dwordx4 v25, s[60:63], 0 offen offset:3072 lds\n"
"v_mul_f32 v109, v109, v73\n"
"v_mfma_f32_32x32x16_bf16 v[176:191], a[188:191], a[12:15], v[176:191]\n"
"ds_read_b64_tr_b16 a[188:189], v5 offset:14336\n"
"ds_read_b64_tr_b16 a[190:191], v5 offset:14848\n"
"s_add_u32 s52, s52, 221184\n"
"s_add_u32 m0, s10, 20480\n"
"v_mul_f32 v125, v125, v73\n"
"v_mfma_f32_32x32x16_bf16 v[128:143], a[160:163], a[16:19], v[128:143]\n"
"ds_read_b64_tr_b16 a[160:161], v6 offset:12288\n"
"ds_read_b64_tr_b16 a[162:163], v6 offset:12800\n"
"buffer_load_dwordx4 v18, s[48:51], s52 offen offset:0 lds\n"
"v_mul_f32 v110, v110, v74\n"
"v_mfma_f32_32x32x16_bf16 v[144:159], a[164:167], a[16:19], v[144:159]\n"
"ds_read_b64_tr_b16 a[164:165], v7 offset:12288\n"
"ds_read_b64_tr_b16 a[166:167], v7 offset:12800\n"
"v_mul_f32 v126, v126, v74\n"
"v_mul_f32 v111, v111, v75\n"
"v_mul_f32 v127, v127, v75\n"
"s_waitcnt lgkmcnt(12)\n"
"v_mfma_f32_32x32x16_bf16 v[160:175], a[168:171], a[16:19], v[160:175]\n"
"ds_read_b64_tr_b16 a[168:169], v8 offset:12288\n"
"ds_read_b64_tr_b16 a[170:171], v8 offset:12800\n"
"buffer_load_dwordx4 v19, s[48:51], s52 offen offset:1024 lds\n"
"v_mfma_f32_32x32x16_bf16 v[176:191], a[172:175], a[16:19], v[176:191]\n"
"s_waitcnt lgkmcnt(6)\n"
"ds_read_b64_tr_b16 a[172:173], v9 offset:12288\n"
"ds_read_b64_tr_b16 a[174:175], v9 offset:12800\n"
"buffer_load_dwordx4 v[80:83], v79, s[64:67], s92 offen offset:0 sc1\n"
"v_mfma_f32_32x32x16_bf16 v[128:143], a[176:179], a[20:23], v[128:143]\n"
"ds_read_b64_tr_b16 a[176:177], v6 offset:14336\n"
"ds_read_b64_tr_b16 a[178:179], v6 offset:14848\n"
"buffer_load_dwordx4 v20, s[48:51], s52 offen offset:2048 lds\n"
"v_mfma_f32_32x32x16_bf16 v[144:159], a[180:183], a[20:23], v[144:159]\n"
"ds_read_b64_tr_b16 a[180:181], v7 offset:14336\n"
"ds_read_b64_tr_b16 a[182:183], v7 offset:14848\n"
"buffer_load_dwordx4 v[84:87], v79, s[64:67], s92 offen offset:1024 sc1\n"
"v_mfma_f32_32x32x16_bf16 v[160:175], a[184:187], a[20:23], v[160:175]\n"
"ds_read_b64_tr_b16 a[184:185], v8 offset:14336\n"
"ds_read_b64_tr_b16 a[186:187], v8 offset:14848\n"
"buffer_load_dwordx4 v21, s[48:51], s52 offen offset:3072 lds\n"
"v_mfma_f32_32x32x16_bf16 v[176:191], a[188:191], a[20:23], v[176:191]\n"
"s_waitcnt lgkmcnt(6)\n"
"ds_read_b64_tr_b16 a[188:189], v9 offset:14336\n"
"ds_read_b64_tr_b16 a[190:191], v9 offset:14848\n"
"buffer_load_dwordx4 v[88:91], v79, s[64:67], s92 offen offset:2048 sc1\n"
"v_mfma_f32_32x32x16_bf16 v[128:143], a[160:163], a[24:27], v[128:143]\n"
"ds_read_b64_tr_b16 a[160:161], v2 offset:24576\n"
"ds_read_b64_tr_b16 a[162:163], v2 offset:25088\n"
"buffer_load_dwordx4 v[92:95], v79, s[64:67], s92 offen offset:3072 sc1\n"
"v_fma_f32 v70, v44, v96, v97\n"
"v_fma_f32 v71, v44, v112, v113\n"
"v_mfma_f32_32x32x16_bf16 v[144:159], a[164:167], a[24:27], v[144:159]\n"
"ds_read_b64_tr_b16 a[164:165], v3 offset:24576\n"
"ds_read_b64_tr_b16 a[166:167], v3 offset:25088\n"
"v_fma_f32 v70, -v45, v112, v70\n"
"v_fma_f32 v71, v45, v96, v71\n"
"v_fma_f32 v68, v44, v70, v98\n"
"v_fma_f32 v69, v44, v71, v114\n"
"v_fma_f32 v68, -v45, v71, v68\n"
"v_mfma_f32_32x32x16_bf16 v[160:175], a[168:171], a[24:27], v[160:175]\n"
"ds_read_b64_tr_b16 a[168:169], v4 offset:24576\n"
"ds_read_b64_tr_b16 a[170:171], v4 offset:25088\n"
"v_fma_f32 v69, v45, v70, v69\n"
"v_fma_f32 v70, v44, v68, v99\n"
"v_fma_f32 v71, v44, v69, v115\n"
"v_fma_f32 v70, -v45, v69, v70\n"
"v_fma_f32 v71, v45, v68, v71\n"
"v_mfma_f32_32x32x16_bf16 v[176:191], a[172:175], a[24:27], v[176:191]\n"
"s_waitcnt lgkmcnt(6)\n"
"ds_read_b64_tr_b16 a[172:173], v5 offset:24576\n"
"ds_read_b64_tr_b16 a[174:175], v5 offset:25088\n"
"v_fma_f32 v68, v44, v70, v100\n"
"v_fma_f32 v69, v44, v71, v116\n"
"v_fma_f32 v68, -v45, v71, v68\n"
"v_fma_f32 v69, v45, v70, v69\n"
"v_mfma_f32_32x32x16_bf16 v[128:143], a[176:179], a[28:31], v[128:143]\n"
"ds_read_b64_tr_b16 a[176:177], v2 offset:26624\n"
"ds_read_b64_tr_b16 a[178:179], v2 offset:27136\n"
"v_fma_f32 v70, v44, v68, v101\n"
"v_fma_f32 v71, v44, v69, v117\n"
"v_fma_f32 v70, -v45, v69, v70\n"
"v_fma_f32 v71, v45, v68, v71\n"
"v_fma_f32 v68, v44, v70, v102\n"
"v_mfma_f32_32x32x16_bf16 v[144:159], a[180:183], a[28:31], v[144:159]\n"
"ds_read_b64_tr_b16 a[180:181], v3 offset:26624\n"
"ds_read_b64_tr_b16 a[182:183], v3 offset:27136\n"
"v_fma_f32 v69, v44, v71, v118\n"
"v_fma_f32 v68, -v45, v71, v68\n"
"v_fma_f32 v69, v45, v70, v69\n"
"v_fma_f32 v70, v44, v68, v103\n"
"v_fma_f32 v71, v44, v69, v119\n"
"v_mfma_f32_32x32x16_bf16 v[160:175], a[184:187], a[28:31], v[160:175]\n"
"ds_read_b64_tr_b16 a[184:185], v4 offset:26624\n"
"ds_read_b64_tr_b16 a[186:187], v4 offset:27136\n"
"v_fma_f32 v70, -v45, v69, v70\n"
"v_fma_f32 v71, v45, v68, v71\n"
"v_fma_f32 v68, v44, v70, v104\n"
"v_fma_f32 v69, v44, v71, v120\n"
"v_fma_f32 v68, -v45, v71, v68\n"
"v_mfma_f32_32x32x16_bf16 v[176:191], a[188:191], a[28:31], v[176:191]\n"
"s_waitcnt lgkmcnt(6)\n"
"ds_read_b64_tr_b16 a[188:189], v5 offset:26624\n"
"ds_read_b64_tr_b16 a[190:191], v5 offset:27136\n"
"v_fma_f32 v69, v45, v70, v69\n"
"v_fma_f32 v70, v44, v68, v105\n"
"v_fma_f32 v71, v44, v69, v121\n"
"v_fma_f32 v70, -v45, v69, v70\n"
"v_mfma_f32_32x32x16_bf16 v[128:143], a[160:163], a[32:35], v[128:143]\n"
"ds_read_b64_tr_b16 a[160:161], v6 offset:24576\n"
"ds_read_b64_tr_b16 a[162:163], v6 offset:25088\n"
"v_fma_f32 v71, v45, v68, v71\n"
"v_fma_f32 v68, v44, v70, v106\n"
"v_fma_f32 v69, v44, v71, v122\n"
"v_fma_f32 v68, -v45, v71, v68\n"
"v_fma_f32 v69, v45, v70, v69\n"
"v_mfma_f32_32x32x16_bf16 v[144:159], a[164:167], a[32:35], v[144:159]\n"
"ds_read_b64_tr_b16 a[164:165], v7 offset:24576\n"
"ds_read_b64_tr_b16 a[166:167], v7 offset:25088\n"
"v_fma_f32 v70, v44, v68, v107\n"
"v_fma_f32 v71, v44, v69, v123\n"
"v_fma_f32 v70, -v45, v69, v70\n"
"v_fma_f32 v71, v45, v68, v71\n"
"v_fma_f32 v68, v44, v70, v108\n"
"v_mfma_f32_32x32x16_bf16 v[160:175], a[168:171], a[32:35], v[160:175]\n"
"ds_read_b64_tr_b16 a[168:169], v8 offset:24576\n"
"ds_read_b64_tr_b16 a[170:171], v8 offset:25088\n"
"v_fma_f32 v69, v44, v71, v124\n"
"v_fma_f32 v68, -v45, v71, v68\n"
"v_fma_f32 v69, v45, v70, v69\n"
"v_fma_f32 v70, v44, v68, v109\n"
"v_fma_f32 v71, v44, v69, v125\n"
"v_mfma_f32_32x32x16_bf16 v[176:191], a[172:175], a[32:35], v[176:191]\n"
"s_waitcnt lgkmcnt(6)\n"
"ds_read_b64_tr_b16 a[172:173], v9 offset:24576\n"
"ds_read_b64_tr_b16 a[174:175], v9 offset:25088\n"
"v_fma_f32 v70, -v45, v69, v70\n"
"v_fma_f32 v71, v45, v68, v71\n"
"v_fma_f32 v68, v44, v70, v110\n"
"v_fma_f32 v69, v44, v71, v126\n"
"v_mfma_f32_32x32x16_bf16 v[128:143], a[176:179], a[36:39], v[128:143]\n"
"ds_read_b64_tr_b16 a[176:177], v6 offset:26624\n"
"ds_read_b64_tr_b16 a[178:179], v6 offset:27136\n"
"v_fma_f32 v68, -v45, v71, v68\n"
"v_fma_f32 v69, v45, v70, v69\n"
"v_fma_f32 v70, v44, v68, v111\n"
"v_fma_f32 v71, v44, v69, v127\n"
"v_fma_f32 v70, -v45, v69, v70\n"
"v_mfma_f32_32x32x16_bf16 v[144:159], a[180:183], a[36:39], v[144:159]\n"
"ds_read_b64_tr_b16 a[180:181], v7 offset:26624\n"
"ds_read_b64_tr_b16 a[182:183], v7 offset:27136\n"
"v_fma_f32 v71, v45, v68, v71\n"
"v_mov_b32 v68, v70\n"
"v_mov_b32 v69, v71\n"
"s_nop 0\n"
"v_mfma_f32_32x32x16_bf16 v[160:175], a[184:187], a[36:39], v[160:175]\n"
"ds_read_b64_tr_b16 a[184:185], v8 offset:26624\n"
"ds_read_b64_tr_b16 a[186:187], v8 offset:27136\n"
"v_permlane32_swap_b32 v70, v68\n"
"v_permlane32_swap_b32 v71, v69\n"
"v_fma_f32 v66, v46, v70, v68\n"
"v_fma_f32 v67, v46, v71, v69\n"
"v_fma_f32 v66, -v47, v71, v66\n"
"v_mfma_f32_32x32x16_bf16 v[176:191], a[188:191], a[36:39], v[176:191]\n"
"s_waitcnt lgkmcnt(6)\n"
"ds_read_b64_tr_b16 a[188:189], v9 offset:26624\n"
"ds_read_b64_tr_b16 a[190:191], v9 offset:27136\n"
"v_fma_f32 v67, v47, v70, v67\n"
"v_cndmask_b32 v64, 0, v70, s[16:17]\n"
"v_cndmask_b32 v65, 0, v71, s[16:17]\n"
"v_mfma_f32_32x32x16_bf16 v[128:143], a[160:163], a[40:43], v[128:143]\n"
"s_barrier\n"
"ds_read_b64_tr_b16 a[160:161], v2 offset:36864\n"
"ds_read_b64_tr_b16 a[162:163], v2 offset:37376\n"
"s_add_u32 s52, s52, 221184\n"
"s_add_u32 m0, s10, 16384\n"
"s_nop 0\n"
"buffer_load_dwordx4 v18, s[48:51], s52 offen offset:0 lds\n"
"v_mfma_f32_32x32x16_bf16 v[144:159], a[164:167], a[40:43], v[144:159]\n"
"ds_read_b64_tr_b16 a[164:165], v3 offset:36864\n"
"ds_read_b64_tr_b16 a[166:167], v3 offset:37376\n"
"ds_write_b64 v29, v[66:67] offset:0\n"
"v_mfma_f32_32x32x16_bf16 v[160:175], a[168:171], a[40:43], v[160:175]\n"
"s_waitcnt lgkmcnt(0)\n"
"s_barrier\n"
"ds_read_b64_tr_b16 a[168:169], v4 offset:36864\n"
"ds_read_b64_tr_b16 a[170:171], v4 offset:37376\n"
"buffer_load_dwordx4 v19, s[48:51], s52 offen offset:1024 lds\n"
"v_mfma_f32_32x32x16_bf16 v[176:191], a[172:175], a[40:43], v[176:191]\n"
"ds_read_b64_tr_b16 a[172:173], v5 offset:36864\n"
"ds_read_b64_tr_b16 a[174:175], v5 offset:37376\n"
"v_mov_b32 v15, v79\n"
"v_mfma_f32_32x32x16_bf16 v[128:143], a[176:179], a[44:47], v[128:143]\n"
"ds_read_b64_tr_b16 a[176:177], v2 offset:38912\n"
"ds_read_b64_tr_b16 a[178:179], v2 offset:39424\n"
"buffer_load_dwordx4 v20, s[48:51], s52 offen offset:2048 lds\n"
"ds_read_b64 v[68:69], v30 offset:0\n"
"v_mfma_f32_32x32x16_bf16 v[144:159], a[180:183], a[44:47], v[144:159]\n"
"ds_read_b64_tr_b16 a[180:181], v3 offset:38912\n"
"ds_read_b64_tr_b16 a[182:183], v3 offset:39424\n"
"ds_read_b64 v[70:71], v30 offset:512\n"
"ds_read_b64 v[72:73], v30 offset:1024\n"
"v_mov_b32 v77, s72\n"
"v_mov_b32 v79, s73\n"
"v_mfma_f32_32x32x16_bf16 v[160:175], a[184:187], a[44:47], v[160:175]\n"
"ds_read_b64_tr_b16 a[184:185], v4 offset:38912\n"
"ds_read_b64_tr_b16 a[186:187], v4 offset:39424\n"
"buffer_load_dwordx4 v21, s[48:51], s52 offen offset:3072 lds\n"
"s_mov_b32 s70, 0\n"
"v_mfma_f32_32x32x16_bf16 v[176:191], a[188:191], a[44:47], v[176:191]\n"
"ds_read_b64_tr_b16 a[188:189], v5 offset:38912\n"
"ds_read_b64_tr_b16 a[190:191], v5 offset:39424\n"
"s_add_u32 s52, s52, 221184\n"
"s_add_u32 m0, s10, 12288\n"
"v_mfma_f32_32x32x16_bf16 v[128:143], a[160:163], a[48:51], v[128:143]\n"
"ds_read_b64_tr_b16 a[160:161], v6 offset:36864\n"
"ds_read_b64_tr_b16 a[162:163], v6 offset:37376\n"
"buffer_load_dwordx4 v18, s[48:51], s52 offen offset:0 lds\n"
"s_waitcnt lgkmcnt(6)\n"
"v_mul_f32 v74, v48, v68\n"
"v_mfma_f32_32x32x16_bf16 v[144:159], a[164:167], a[48:51], v[144:159]\n"
"ds_read_b64_tr_b16 a[164:165], v7 offset:36864\n"
"ds_read_b64_tr_b16 a[166:167], v7 offset:37376\n"
"v_mul_f32 v75, v48, v69\n"
"v_fma_f32 v74, -v49, v69, v74\n"
"v_fma_f32 v75, v49, v68, v75\n"
"v_fma_f32 v68, v50, v70, v74\n"
"v_fma_f32 v69, v50, v71, v75\n"
"v_mfma_f32_32x32x16_bf16 v[160:175], a[168:171], a[48:51], v[160:175]\n"
"ds_read_b64_tr_b16 a[168:169], v8 offset:36864\n"
"ds_read_b64_tr_b16 a[170:171], v8 offset:37376\n"
"buffer_load_dwordx4 v19, s[48:51], s52 offen offset:1024 lds\n"
"v_fma_f32 v68, -v51, v71, v68\n"
"v_mfma_f32_32x32x16_bf16 v[176:191], a[172:175], a[48:51], v[176:191]\n"
"ds_read_b64_tr_b16 a[172:173], v9 offset:36864\n"
"ds_read_b64_tr_b16 a[174:175], v9 offset:37376\n"
"v_fma_f32 v69, v51, v70, v69\n"
"v_fma_f32 v74, v52, v72, v68\n"
"v_fma_f32 v75, v52, v73, v69\n"
"v_fma_f32 v74, -v53, v73, v74\n"
"v_mfma_f32_32x32x16_bf16 v[128:143], a[176:179], a[52:55], v[128:143]\n"
"ds_read_b64_tr_b16 a[176:177], v6 offset:38912\n"
"ds_read_b64_tr_b16 a[178:179], v6 offset:39424\n"
"buffer_load_dwordx4 v20, s[48:51], s52 offen offset:2048 lds\n"
"v_fma_f32 v75, v53, v72, v75\n"
"v_mfma_f32_32x32x16_bf16 v[144:159], a[180:183], a[52:55], v[144:159]\n"
"ds_read_b64_tr_b16 a[180:181], v7 offset:38912\n"
"ds_read_b64_tr_b16 a[182:183], v7 offset:39424\n"
"v_fma_f32 v76, v54, v74, v66\n"
"v_fma_f32 v78, v54, v75, v67\n"
"v_fma_f32 v76, -v55, v75, v76\n"
"v_fma_f32 v78, v55, v74, v78\n"
"s_waitcnt lgkmcnt(12)\n"
"v_mfma_f32_32x32x16_bf16 v[160:175], a[184:187], a[52:55], v[160:175]\n"
"ds_read_b64_tr_b16 a[184:185], v8 offset:38912\n"
"ds_read_b64_tr_b16 a[186:187], v8 offset:39424\n"
"buffer_load_dwordx4 v21, s[48:51], s52 offen offset:3072 lds\n"
"v_mfma_f32_32x32x16_bf16 v[176:191], a[188:191], a[52:55], v[176:191]\n"
"s_waitcnt lgkmcnt(6)\n"
"ds_read_b64_tr_b16 a[188:189], v9 offset:38912\n"
"ds_read_b64_tr_b16 a[190:191], v9 offset:39424\n"
"buffer_store_dwordx4 v[76:79], v35, s[64:67], s70 offen sc1\n"
"v_fma_f32 v66, v46, v74, v64\n"
"v_mfma_f32_32x32x16_bf16 v[128:143], a[160:163], a[56:59], v[128:143]\n"
"ds_read_b64_tr_b16 a[160:161], v2 offset:8192\n"
"ds_read_b64_tr_b16 a[162:163], v2 offset:8704\n"
"v_fma_f32 v67, v46, v75, v65\n"
"v_fma_f32 v66, -v47, v75, v66\n"
"v_fma_f32 v67, v47, v74, v67\n"
"v_lshrrev_b32 v68, 4, v27\n"
"v_bfe_u32 v69, v27, 2, 2\n"
"v_mfma_f32_32x32x16_bf16 v[144:159], a[164:167], a[56:59], v[144:159]\n"
"ds_read_b64_tr_b16 a[164:165], v3 offset:8192\n"
"ds_read_b64_tr_b16 a[166:167], v3 offset:8704\n"
"v_and_b32 v70, 3, v27\n"
"v_lshl_add_u32 v69, v68, 3, v69\n"
"v_mul_u32_u24 v34, 72, v69\n"
"v_lshl_add_u32 v34, v70, 3, v34\n"
"v_mfma_f32_32x32x16_bf16 v[160:175], a[168:171], a[56:59], v[160:175]\n"
"ds_read_b64_tr_b16 a[168:169], v4 offset:8192\n"
"ds_read_b64_tr_b16 a[170:171], v4 offset:8704\n"
"s_add_u32 s68, s10, 2048\n"
"v_add_u32 v34, s68, v34\n"
"v_bfe_u32 v71, v69, 1, 3\n"
"v_lshlrev_b32 v71, 4, v71\n"
"v_mfma_f32_32x32x16_bf16 v[176:191], a[172:175], a[56:59], v[176:191]\n"
"s_waitcnt lgkmcnt(6)\n"
"ds_read_b64_tr_b16 a[172:173], v5 offset:8192\n"
"ds_read_b64_tr_b16 a[174:175], v5 offset:8704\n"
"v_lshl_add_u32 v69, v69, 7, s10\n"
"v_lshlrev_b32 v70, 3, v70\n"
"v_xor_b32 v70, v70, v71\n"
"v_add_u32 v10, v69, v70\n"
"v_mfma_f32_32x32x16_bf16 v[128:143], a[176:179], a[60:63], v[128:143]\n"
"ds_read_b64_tr_b16 a[176:177], v2 offset:10240\n"
"ds_read_b64_tr_b16 a[178:179], v2 offset:10752\n"
"v_xor_b32 v11, 32, v10\n"
"v_xor_b32 v12, 64, v10\n"
"v_xor_b32 v13, 96, v10\n"
"v_and_b32 v70, 15, v27\n"
"v_lshrrev_b32 v71, 1, v70\n"
"v_mfma_f32_32x32x16_bf16 v[144:159], a[180:183], a[60:63], v[144:159]\n"
"ds_read_b64_tr_b16 a[180:181], v3 offset:10240\n"
"ds_read_b64_tr_b16 a[182:183], v3 offset:10752\n"
"v_xor_b32 v71, v71, v68\n"
"v_lshl_add_u32 v70, v70, 7, s10\n"
"v_lshl_add_u32 v16, v71, 4, v70\n"
"v_xor_b32 v17, 64, v16\n"
"s_waitcnt vmcnt(9)\n"
"v_xor_b32 v69, s72, v81\n"
"v_mfma_f32_32x32x16_bf16 v[160:175], a[184:187], a[60:63], v[160:175]\n"
"ds_read_b64_tr_b16 a[184:185], v4 offset:10240\n"
"ds_read_b64_tr_b16 a[186:187], v4 offset:10752\n"
"v_xor_b32 v70, s73, v83\n"
"v_or3_b32 v68, v69, v70, 0\n"
"v_xor_b32 v69, s72, v85\n"
"v_xor_b32 v70, s73, v87\n"
"v_or3_b32 v68, v69, v70, v68\n"
"v_mfma_f32_32x32x16_bf16 v[176:191], a[188:191], a[60:63], v[176:191]\n"
"s_waitcnt lgkmcnt(6)\n"
"ds_read_b64_tr_b16 a[188:189], v5 offset:10240\n"
"ds_read_b64_tr_b16 a[190:191], v5 offset:10752\n"
"v_xor_b32 v69, s72, v89\n"
"v_xor_b32 v70, s73, v91\n"
"v_or3_b32 v68, v69, v70, v68\n"
"v_xor_b32 v69, s72, v93\n"
"v_mfma_f32_32x32x16_bf16 v[128:143], a[160:163], a[64:67], v[128:143]\n"
"ds_read_b64_tr_b16 a[160:161], v6 offset:8192\n"
"ds_read_b64_tr_b16 a[162:163], v6 offset:8704\n"
"v_xor_b32 v70, s73, v95\n"
"v_or3_b32 v68, v69, v70, v68\n"
"v_cmp_ne_u32 vcc, 0, v68\n"
"s_nop 0\n"
"s_cmp_lg_u64 vcc, 0\n"
"s_cbranch_scc1 K1F_m_slow\n"
"K1F_m_back:\n"
"v_mfma_f32_32x32x16_bf16 v[144:159], a[164:167], a[64:67], v[144:159]\n"
"ds_read_b64_tr_b16 a[164:165], v7 offset:8192\n"
"ds_read_b64_tr_b16 a[166:167], v7 offset:8704\n"
"v_mfma_f32_32x32x16_bf16 v[192:207], v[80:83], v[40:43], v[192:207]\n"
"v_mfma_f32_32x32x16_bf16 v[160:175], a[168:171], a[64:67], v[160:175]\n"
"ds_read_b64_tr_b16 a[168:169], v8 offset:8192\n"
"ds_read_b64_tr_b16 a[170:171], v8 offset:8704\n"
"v_mfma_f32_32x32x16_bf16 v[208:223], v[80:83], a[196:199], v[208:223]\n"
"v_mfma_f32_32x32x16_bf16 v[176:191], a[172:175], a[64:67], v[176:191]\n"
"s_waitcnt lgkmcnt(6)\n"
"ds_read_b64_tr_b16 a[172:173], v9 offset:8192\n"
"ds_read_b64_tr_b16 a[174:175], v9 offset:8704\n"
"v_mfma_f32_32x32x16_bf16 v[224:239], v[84:87], v[40:43], v[224:239]\n"
"v_mfma_f32_32x32x16_bf16 v[128:143], a[176:179], a[68:71], v[128:143]\n"
"ds_read_b64_tr_b16 a[176:177], v6 offset:10240\n"
"ds_read_b64_tr_b16 a[178:179], v6 offset:10752\n"
"v_mfma_f32_32x32x16_bf16 v[240:255], v[84:87], a[196:199], v[240:255]\n"
"v_mfma_f32_32x32x16_bf16 v[144:159], a[180:183], a[68:71], v[144:159]\n"
"ds_read_b64_tr_b16 a[180:181], v7 offset:10240\n"
"ds_read_b64_tr_b16 a[182:183], v7 offset:10752\n"
"v_mfma_f32_32x32x16_bf16 v[160:175], a[184:187], a[68:71], v[160:175]\n"
"ds_read_b64_tr_b16 a[184:185], v8 offset:10240\n"
"ds_read_b64_tr_b16 a[186:187], v8 offset:10752\n"
"v_mfma_f32_32x32x16_bf16 v[176:191], a[188:191], a[68:71], v[176:191]\n"
"s_waitcnt lgkmcnt(6)\n"
"ds_read_b64_tr_b16 a[188:189], v9 offset:10240\n"
"ds_read_b64_tr_b16 a[190:191], v9 offset:10752\n"
"s_cmp_eq_u32 s75, 0\n"
"s_cbranch_scc1 K1F_rtc\n"
"ds_read_b64_tr_b16 a[200:201], v10 offset:4096\n"
"ds_read_b64_tr_b16 a[202:203], v11 offset:4608\n"
"ds_read_b64_tr_b16 a[204:205], v11 offset:4096\n"
"ds_read_b64_tr_b16 a[206:207], v10 offset:4608\n"
"ds_read_b64_tr_b16 a[208:209], v12 offset:4096\n"
"ds_read_b64_tr_b16 a[210:211], v13 offset:4608\n"
"ds_read_b64_tr_b16 a[212:213], v13 offset:4096\n"
"ds_read_b64_tr_b16 a[214:215], v12 offset:4608\n"
"s_branch K1F_rtd\n"
"K1F_rtc:\n"
"ds_read_b128 a[200:203], v16 offset:4096\n"
"ds_read_b128 a[204:207], v16 offset:6144\n"
"ds_read_b128 a[208:211], v17 offset:4096\n"
"ds_read_b128 a[212:215], v17 offset:6144\n"
"K1F_rtd:\n"
"v_mfma_f32_32x32x16_bf16 v[128:143], a[160:163], a[72:75], v[128:143]\n"
"s_barrier\n"
"ds_read_b64_tr_b16 a[160:161], v2 offset:32768\n"
"ds_read_b64_tr_b16 a[162:163], v2 offset:33280\n"
"v_mfma_f32_32x32x16_bf16 v[144:159], a[164:167], a[72:75], v[144:159]\n"
"ds_read_b64_tr_b16 a[164:165], v3 offset:32768\n"
"ds_read_b64_tr_b16 a[166:167], v3 offset:33280\n"
"v_mfma_f32_32x32x16_bf16 v[160:175], a[168:171], a[72:75], v[160:175]\n"
"ds_read_b64_tr_b16 a[168:169], v4 offset:32768\n"
"ds_read_b64_tr_b16 a[170:171], v4 offset:33280\n"
"v_mfma_f32_32x32x16_bf16 v[176:191], a[172:175], a[72:75], v[176:191]\n"
"s_waitcnt lgkmcnt(10)\n"
"ds_read_b64_tr_b16 a[172:173], v5 offset:32768\n"
"ds_read_b64_tr_b16 a[174:175], v5 offset:33280\n"
"s_bfe_u32 s26, s100, 0x80000\n"
"s_lshl_b32 s26, s26, 12\n"
"s_add_u32 s26, s26, s43\n"
"v_mfma_f32_32x32x16_bf16 v[128:143], a[176:179], a[76:79], v[128:143]\n"
"ds_read_b64_tr_b16 a[176:177], v2 offset:34816\n"
"ds_read_b64_tr_b16 a[178:179], v2 offset:35328\n"
"s_bfe_u32 s27, s100, 0x80008\n"
"s_lshl_b32 s27, s27, 12\n"
"s_add_u32 s27, s27, s43\n"
"v_mfma_f32_32x32x16_bf16 v[144:159], a[180:183], a[76:79], v[144:159]\n"
"ds_read_b64_tr_b16 a[180:181], v3 offset:34816\n"
"ds_read_b64_tr_b16 a[182:183], v3 offset:35328\n"
"s_waitcnt lgkmcnt(12)\n"
"buffer_store_dwordx4 a[200:203], v28, s[64:67], s28 offen offset:0 sc1\n"
"v_mul_f32 v76, v36, v192\n"
"v_mul_f32 v77, v36, v193\n"
"v_mfma_f32_32x32x16_bf16 v[160:175], a[184:187], a[76:79], v[160:175]\n"
"ds_read_b64_tr_b16 a[184:185], v4 offset:34816\n"
"ds_read_b64_tr_b16 a[186:187], v4 offset:35328\n"
"v_exp_f32 v76, v76\n"
"v_exp_f32 v77, v77\n"
"v_mul_f32 v192, v192, v196\n"
"v_mfma_f32_32x32x16_bf16 v[176:191], a[188:191], a[76:79], v[176:191]\n"
"s_waitcnt lgkmcnt(6)\n"
"ds_read_b64_tr_b16 a[188:189], v5 offset:34816\n"
"ds_read_b64_tr_b16 a[190:191], v5 offset:35328\n"
"v_mul_f32 v193, v193, v197\n"
"v_fma_f32 v76, v76, v37, v37\n"
"v_fma_f32 v77, v77, v37, v37\n"
"v_mfma_f32_32x32x16_bf16 v[128:143], a[160:163], a[80:83], v[128:143]\n"
"ds_read_b64_tr_b16 a[160:161], v6 offset:32768\n"
"ds_read_b64_tr_b16 a[162:163], v6 offset:33280\n"
"v_rcp_f32 v76, v76\n"
"v_rcp_f32 v77, v77\n"
"v_mul_f32 v192, v192, v76\n"
"v_mfma_f32_32x32x16_bf16 v[144:159], a[164:167], a[80:83], v[144:159]\n"
"ds_read_b64_tr_b16 a[164:165], v7 offset:32768\n"
"ds_read_b64_tr_b16 a[166:167], v7 offset:33280\n"
"v_mul_f32 v193, v193, v77\n"
"v_mul_f32 v76, v36, v194\n"
"v_mul_f32 v77, v36, v195\n"
"v_exp_f32 v76, v76\n"
"v_mfma_f32_32x32x16_bf16 v[160:175], a[168:171], a[80:83], v[160:175]\n"
"ds_read_b64_tr_b16 a[168:169], v8 offset:32768\n"
"ds_read_b64_tr_b16 a[170:171], v8 offset:33280\n"
"v_exp_f32 v77, v77\n"
"v_mul_f32 v194, v194, v198\n"
"v_mul_f32 v195, v195, v199\n"
"v_fma_f32 v76, v76, v37, v37\n"
"v_mfma_f32_32x32x16_bf16 v[176:191], a[172:175], a[80:83], v[176:191]\n"
"s_waitcnt lgkmcnt(6)\n"
"ds_read_b64_tr_b16 a[172:173], v9 offset:32768\n"
"ds_read_b64_tr_b16 a[174:175], v9 offset:33280\n"
"v_fma_f32 v77, v77, v37, v37\n"
"v_rcp_f32 v76, v76\n"
"v_mfma_f32_32x32x16_bf16 v[128:143], a[176:179], a[84:87], v[128:143]\n"
"ds_read_b64_tr_b16 a[176:177], v6 offset:34816\n"
"ds_read_b64_tr_b16 a[178:179], v6 offset:35328\n"
"v_rcp_f32 v77, v77\n"
"v_mfma_f32_32x32x16_bf16 v[144:159], a[180:183], a[84:87], v[144:159]\n"
"ds_read_b64_tr_b16 a[180:181], v7 offset:34816\n"
"ds_read_b64_tr_b16 a[182:183], v7 offset:35328\n"
"buffer_store_dwordx4 a[204:207], v28, s[64:67], s28 offen offset:1024 sc1\n"
"v_mul_f32 v194, v194, v76\n"
"v_mul_f32 v195, v195, v77\n"
"v_mfma_f32_32x32x16_bf16 v[160:175], a[184:187], a[84:87], v[160:175]\n"
"ds_read_b64_tr_b16 a[184:185], v8 offset:34816\n"
"ds_read_b64_tr_b16 a[186:187], v8 offset:35328\n"
"v_cvt_pk_bf16_f32 v196, v192, v193\n"
"v_cvt_pk_bf16_f32 v197, v194, v195\n"
"v_mul_f32 v76, v36, v200\n"
"v_mul_f32 v77, v36, v201\n"
"v_mfma_f32_32x32x16_bf16 v[176:191], a[188:191], a[84:87], v[176:191]\n"
"s_waitcnt lgkmcnt(6)\n"
"ds_read_b64_tr_b16 a[188:189], v9 offset:34816\n"
"ds_read_b64_tr_b16 a[190:191], v9 offset:35328\n"
"v_exp_f32 v76, v76\n"
"v_exp_f32 v77, v77\n"
"v_mfma_f32_32x32x16_bf16 v[128:143], a[160:163], a[88:91], v[128:143]\n"
"ds_read_b64_tr_b16 a[160:161], v2 offset:28672\n"
"ds_read_b64_tr_b16 a[162:163], v2 offset:29184\n"
"v_mul_f32 v200, v200, v204\n"
"v_mul_f32 v201, v201, v205\n"
"v_fma_f32 v76, v76, v37, v37\n"
"v_fma_f32 v77, v77, v37, v37\n"
"v_mfma_f32_32x32x16_bf16 v[144:159], a[164:167], a[88:91], v[144:159]\n"
"ds_read_b64_tr_b16 a[164:165], v3 offset:28672\n"
"ds_read_b64_tr_b16 a[166:167], v3 offset:29184\n"
"v_rcp_f32 v76, v76\n"
"v_rcp_f32 v77, v77\n"
"v_mul_f32 v200, v200, v76\n"
"v_mfma_f32_32x32x16_bf16 v[160:175], a[168:171], a[88:91], v[160:175]\n"
"ds_read_b64_tr_b16 a[168:169], v4 offset:28672\n"
"ds_read_b64_tr_b16 a[170:171], v4 offset:29184\n"
"v_mul_f32 v201, v201, v77\n"
"v_cvt_pk_bf16_f32 v198, v200, v201\n"
"v_mul_f32 v76, v36, v202\n"
"v_mul_f32 v77, v36, v203\n"
"v_mfma_f32_32x32x16_bf16 v[176:191], a[172:175], a[88:91], v[176:191]\n"
"s_waitcnt lgkmcnt(6)\n"
"ds_read_b64_tr_b16 a[172:173], v5 offset:28672\n"
"ds_read_b64_tr_b16 a[174:175], v5 offset:29184\n"
"v_exp_f32 v76, v76\n"
"v_exp_f32 v77, v77\n"
"v_mfma_f32_32x32x16_bf16 v[128:143], a[176:179], a[92:95], v[128:143]\n"
"ds_read_b64_tr_b16 a[176:177], v2 offset:30720\n"
"ds_read_b64_tr_b16 a[178:179], v2 offset:31232\n"
"v_mul_f32 v202, v202, v206\n"
"v_mfma_f32_32x32x16_bf16 v[144:159], a[180:183], a[92:95], v[144:159]\n"
"ds_read_b64_tr_b16 a[180:181], v3 offset:30720\n"
"ds_read_b64_tr_b16 a[182:183], v3 offset:31232\n"
"buffer_store_dwordx4 a[208:211], v28, s[64:67], s29 offen offset:0 sc1\n"
"v_mul_f32 v203, v203, v207\n"
"v_fma_f32 v76, v76, v37, v37\n"
"v_mfma_f32_32x32x16_bf16 v[160:175], a[184:187], a[92:95], v[160:175]\n"
"ds_read_b64_tr_b16 a[184:185], v4 offset:30720\n"
"ds_read_b64_tr_b16 a[186:187], v4 offset:31232\n"
"v_fma_f32 v77, v77, v37, v37\n"
"v_rcp_f32 v76, v76\n"
"v_rcp_f32 v77, v77\n"
"v_mfma_f32_32x32x16_bf16 v[176:191], a[188:191], a[92:95], v[176:191]\n"
"s_waitcnt lgkmcnt(6)\n"
"ds_read_b64_tr_b16 a[188:189], v5 offset:30720\n"
"ds_read_b64_tr_b16 a[190:191], v5 offset:31232\n"
"v_mul_f32 v202, v202, v76\n"
"v_mul_f32 v203, v203, v77\n"
"v_cvt_pk_bf16_f32 v199, v202, v203\n"
"v_mfma_f32_32x32x16_bf16 v[128:143], a[160:163], a[96:99], v[128:143]\n"
"ds_read_b64_tr_b16 a[160:161], v6 offset:28672\n"
"ds_read_b64_tr_b16 a[162:163], v6 offset:29184\n"
"buffer_store_dwordx4 v[196:199], v26, s[64:67], s26 offen offset:0 sc1\n"
"v_mul_f32 v76, v36, v208\n"
"v_mul_f32 v77, v36, v209\n"
"v_mfma_f32_32x32x16_bf16 v[144:159], a[164:167], a[96:99], v[144:159]\n"
"ds_read_b64_tr_b16 a[164:165], v7 offset:28672\n"
"ds_read_b64_tr_b16 a[166:167], v7 offset:29184\n"
"v_exp_f32 v76, v76\n"
"v_exp_f32 v77, v77\n"
"v_mul_f32 v208, v208, v212\n"
"v_mfma_f32_32x32x16_bf16 v[160:175], a[168:171], a[96:99], v[160:175]\n"
"ds_read_b64_tr_b16 a[168:169], v8 offset:28672\n"
"ds_read_b64_tr_b16 a[170:171], v8 offset:29184\n"
"v_mul_f32 v209, v209, v213\n"
"v_fma_f32 v76, v76, v37, v37\n"
"v_fma_f32 v77, v77, v37, v37\n"
"v_rcp_f32 v76, v76\n"
"v_mfma_f32_32x32x16_bf16 v[176:191], a[172:175], a[96:99], v[176:191]\n"
"s_waitcnt lgkmcnt(6)\n"
"ds_read_b64_tr_b16 a[172:173], v9 offset:28672\n"
"ds_read_b64_tr_b16 a[174:175], v9 offset:29184\n"
"v_rcp_f32 v77, v77\n"
"v_mul_f32 v208, v208, v76\n"
"v_mul_f32 v209, v209, v77\n"
"v_mfma_f32_32x32x16_bf16 v[128:143], a[176:179], a[100:103], v[128:143]\n"
"ds_read_b64_tr_b16 a[176:177], v6 offset:30720\n"
"ds_read_b64_tr_b16 a[178:179], v6 offset:31232\n"
"v_mul_f32 v76, v36, v210\n"
"v_mfma_f32_32x32x16_bf16 v[144:159], a[180:183], a[100:103], v[144:159]\n"
"ds_read_b64_tr_b16 a[180:181], v7 offset:30720\n"
"ds_read_b64_tr_b16 a[182:183], v7 offset:31232\n"
"buffer_store_dwordx4 a[212:215], v28, s[64:67], s29 offen offset:1024 sc1\n"
"v_mul_f32 v77, v36, v211\n"
"v_mfma_f32_32x32x16_bf16 v[160:175], a[184:187], a[100:103], v[160:175]\n"
"ds_read_b64_tr_b16 a[184:185], v8 offset:30720\n"
"ds_read_b64_tr_b16 a[186:187], v8 offset:31232\n"
"v_exp_f32 v76, v76\n"
"v_exp_f32 v77, v77\n"
"v_mul_f32 v210, v210, v214\n"
"v_mfma_f32_32x32x16_bf16 v[176:191], a[188:191], a[100:103], v[176:191]\n"
"s_waitcnt lgkmcnt(6)\n"
"ds_read_b64_tr_b16 a[188:189], v9 offset:30720\n"
"ds_read_b64_tr_b16 a[190:191], v9 offset:31232\n"
"v_mul_f32 v211, v211, v215\n"
"v_fma_f32 v76, v76, v37, v37\n"
"v_fma_f32 v77, v77, v37, v37\n"
"v_mfma_f32_32x32x16_bf16 v[128:143], a[160:163], a[104:107], v[128:143]\n"
"s_waitcnt vmcnt(10)\n"
"s_barrier\n"
"ds_read_b64_tr_b16 a[160:161], v2 offset:20480\n"
"ds_read_b64_tr_b16 a[162:163], v2 offset:20992\n"
"v_rcp_f32 v76, v76\n"
"v_mfma_f32_32x32x16_bf16 v[144:159], a[164:167], a[104:107], v[144:159]\n"
"ds_read_b64_tr_b16 a[164:165], v3 offset:20480\n"
"ds_read_b64_tr_b16 a[166:167], v3 offset:20992\n"
"v_rcp_f32 v77, v77\n"
"v_mul_f32 v210, v210, v76\n"
"v_mul_f32 v211, v211, v77\n"
"v_cvt_pk_bf16_f32 v212, v208, v209\n"
"v_mfma_f32_32x32x16_bf16 v[160:175], a[168:171], a[104:107], v[160:175]\n"
"ds_read_b64_tr_b16 a[168:169], v4 offset:20480\n"
"ds_read_b64_tr_b16 a[170:171], v4 offset:20992\n"
"v_cvt_pk_bf16_f32 v213, v210, v211\n"
"v_mul_f32 v76, v36, v216\n"
"v_mul_f32 v77, v36, v217\n"
"v_exp_f32 v76, v76\n"
"v_mfma_f32_32x32x16_bf16 v[176:191], a[172:175], a[104:107], v[176:191]\n"
"s_waitcnt lgkmcnt(6)\n"
"ds_read_b64_tr_b16 a[172:173], v5 offset:20480\n"
"ds_read_b64_tr_b16 a[174:175], v5 offset:20992\n"
"v_exp_f32 v77, v77\n"
"v_mul_f32 v216, v216, v220\n"
"v_mul_f32 v217, v217, v221\n"
"v_mfma_f32_32x32x16_bf16 v[128:143], a[176:179], a[108:111], v[128:143]\n"
"ds_read_b64_tr_b16 a[176:177], v2 offset:22528\n"
"ds_read_b64_tr_b16 a[178:179], v2 offset:23040\n"
"v_fma_f32 v76, v76, v37, v37\n"
"v_fma_f32 v77, v77, v37, v37\n"
"v_rcp_f32 v76, v76\n"
"v_mfma_f32_32x32x16_bf16 v[144:159], a[180:183], a[108:111], v[144:159]\n"
"ds_read_b64_tr_b16 a[180:181], v3 offset:22528\n"
"ds_read_b64_tr_b16 a[182:183], v3 offset:23040\n"
"v_rcp_f32 v77, v77\n"
"v_mul_f32 v216, v216, v76\n"
"v_mul_f32 v217, v217, v77\n"
"v_cvt_pk_bf16_f32 v214, v216, v217\n"
"v_mfma_f32_32x32x16_bf16 v[160:175], a[184:187], a[108:111], v[160:175]\n"
"ds_read_b64_tr_b16 a[184:185], v4 offset:22528\n"
"ds_read_b64_tr_b16 a[186:187], v4 offset:23040\n"
"v_mul_f32 v76, v36, v218\n"
"v_mul_f32 v77, v36, v219\n"
"v_exp_f32 v76, v76\n"
"v_mfma_f32_32x32x16_bf16 v[176:191], a[188:191], a[108:111], v[176:191]\n"
"s_waitcnt lgkmcnt(6)\n"
"ds_read_b64_tr_b16 a[188:189], v5 offset:22528\n"
"ds_read_b64_tr_b16 a[190:191], v5 offset:23040\n"
"v_exp_f32 v77, v77\n"
"v_mul_f32 v218, v218, v222\n"
"v_mul_f32 v219, v219, v223\n"
"v_mfma_f32_32x32x16_bf16 v[128:143], a[160:163], a[112:115], v[128:143]\n"
"ds_read_b64_tr_b16 a[160:161], v6 offset:20480\n"
"ds_read_b64_tr_b16 a[162:163], v6 offset:20992\n"
"v_fma_f32 v76, v76, v37, v37\n"
"v_fma_f32 v77, v77, v37, v37\n"
"v_rcp_f32 v76, v76\n"
"v_mfma_f32_32x32x16_bf16 v[144:159], a[164:167], a[112:115], v[144:159]\n"
"ds_read_b64_tr_b16 a[164:165], v7 offset:20480\n"
"ds_read_b64_tr_b16 a[166:167], v7 offset:20992\n"
"v_rcp_f32 v77, v77\n"
"v_mul_f32 v218, v218, v76\n"
"v_mul_f32 v219, v219, v77\n"
"v_cvt_pk_bf16_f32 v215, v218, v219\n"
"v_mfma_f32_32x32x16_bf16 v[160:175], a[168:171], a[112:115], v[160:175]\n"
"ds_read_b64_tr_b16 a[168:169], v8 offset:20480\n"
"ds_read_b64_tr_b16 a[170:171], v8 offset:20992\n"
"buffer_store_dwordx4 v[212:215], v26, s[64:67], s26 offen offset:512 sc1\n"
"v_mul_f32 v76, v36, v224\n"
"v_mul_f32 v77, v36, v225\n"
"v_mfma_f32_32x32x16_bf16 v[176:191], a[172:175], a[112:115], v[176:191]\n"
"s_waitcnt lgkmcnt(6)\n"
"ds_read_b64_tr_b16 a[172:173], v9 offset:20480\n"
"ds_read_b64_tr_b16 a[174:175], v9 offset:20992\n"
"v_exp_f32 v76, v76\n"
"v_exp_f32 v77, v77\n"
"v_mfma_f32_32x32x16_bf16 v[128:143], a[176:179], a[116:119], v[128:143]\n"
"ds_read_b64_tr_b16 a[176:177], v6 offset:22528\n"
"ds_read_b64_tr_b16 a[178:179], v6 offset:23040\n"
"v_mul_f32 v224, v224, v228\n"
"v_mul_f32 v225, v225, v229\n"
"v_fma_f32 v76, v76, v37, v37\n"
"v_fma_f32 v77, v77, v37, v37\n"
"v_mfma_f32_32x32x16_bf16 v[144:159], a[180:183], a[116:119], v[144:159]\n"
"ds_read_b64_tr_b16 a[180:181], v7 offset:22528\n"
"ds_read_b64_tr_b16 a[182:183], v7 offset:23040\n"
"v_rcp_f32 v76, v76\n"
"v_rcp_f32 v77, v77\n"
"v_mul_f32 v224, v224, v76\n"
"v_mfma_f32_32x32x16_bf16 v[160:175], a[184:187], a[116:119], v[160:175]\n"
"ds_read_b64_tr_b16 a[184:185], v8 offset:22528\n"
"ds_read_b64_tr_b16 a[186:187], v8 offset:23040\n"
"v_mul_f32 v225, v225, v77\n"
"v_mul_f32 v76, v36, v226\n"
"v_mul_f32 v77, v36, v227\n"
"v_exp_f32 v76, v76\n"
"v_mfma_f32_32x32x16_bf16 v[176:191], a[188:191], a[116:119], v[176:191]\n"
"s_waitcnt lgkmcnt(6)\n"
"ds_read_b64_tr_b16 a[188:189], v9 offset:22528\n"
"ds_read_b64_tr_b16 a[190:191], v9 offset:23040\n"
"v_exp_f32 v77, v77\n"
"v_mul_f32 v226, v226, v230\n"
"v_mul_f32 v227, v227, v231\n"
"v_mfma_f32_32x32x16_bf16 v[128:143], a[160:163], a[120:123], v[128:143]\n"
"ds_read_b64_tr_b16 a[160:161], v2 offset:16384\n"
"ds_read_b64_tr_b16 a[162:163], v2 offset:16896\n"
"v_fma_f32 v76, v76, v37, v37\n"
"v_fma_f32 v77, v77, v37, v37\n"
"v_rcp_f32 v76, v76\n"
"v_mfma_f32_32x32x16_bf16 v[144:159], a[164:167], a[120:123], v[144:159]\n"
"ds_read_b64_tr_b16 a[164:165], v3 offset:16384\n"
"ds_read_b64_tr_b16 a[166:167], v3 offset:16896\n"
"v_rcp_f32 v77, v77\n"
"v_mul_f32 v226, v226, v76\n"
"v_mul_f32 v227, v227, v77\n"
"v_cvt_pk_bf16_f32 v228, v224, v225\n"
"v_mfma_f32_32x32x16_bf16 v[160:175], a[168:171], a[120:123], v[160:175]\n"
"ds_read_b64_tr_b16 a[168:169], v4 offset:16384\n"
"ds_read_b64_tr_b16 a[170:171], v4 offset:16896\n"
"v_cvt_pk_bf16_f32 v229, v226, v227\n"
"v_mul_f32 v76, v36, v232\n"
"v_mul_f32 v77, v36, v233\n"
"v_exp_f32 v76, v76\n"
"v_mfma_f32_32x32x16_bf16 v[176:191], a[172:175], a[120:123], v[176:191]\n"
"s_waitcnt lgkmcnt(6)\n"
"ds_read_b64_tr_b16 a[172:173], v5 offset:16384\n"
"ds_read_b64_tr_b16 a[174:175], v5 offset:16896\n"
"v_exp_f32 v77, v77\n"
"v_mul_f32 v232, v232, v236\n"
"v_mul_f32 v233, v233, v237\n"
"v_mfma_f32_32x32x16_bf16 v[128:143], a[176:179], a[124:127], v[128:143]\n"
"ds_read_b64_tr_b16 a[176:177], v2 offset:18432\n"
"ds_read_b64_tr_b16 a[178:179], v2 offset:18944\n"
"v_fma_f32 v76, v76, v37, v37\n"
"v_fma_f32 v77, v77, v37, v37\n"
"v_rcp_f32 v76, v76\n"
"v_mfma_f32_32x32x16_bf16 v[144:159], a[180:183], a[124:127], v[144:159]\n"
"ds_read_b64_tr_b16 a[180:181], v3 offset:18432\n"
"ds_read_b64_tr_b16 a[182:183], v3 offset:18944\n"
"v_rcp_f32 v77, v77\n"
"v_mul_f32 v232, v232, v76\n"
"v_mfma_f32_32x32x16_bf16 v[160:175], a[184:187], a[124:127], v[160:175]\n"
"ds_read_b64_tr_b16 a[184:185], v4 offset:18432\n"
"ds_read_b64_tr_b16 a[186:187], v4 offset:18944\n"
"buffer_load_dwordx4 v[68:71], v38, s[64:67], 0 offen sc1\n"
"v_mfma_f32_32x32x16_bf16 v[176:191], a[188:191], a[124:127], v[176:191]\n"
"s_waitcnt lgkmcnt(6)\n"
"ds_read_b64_tr_b16 a[188:189], v5 offset:18432\n"
"ds_read_b64_tr_b16 a[190:191], v5 offset:18944\n"
"buffer_load_dwordx4 v[72:75], v39, s[64:67], 0 offen sc1\n"
"v_mul_f32 v233, v233, v77\n"
"v_mfma_f32_32x32x16_bf16 v[128:143], a[160:163], a[128:131], v[128:143]\n"
"ds_read_b64_tr_b16 a[160:161], v6 offset:16384\n"
"ds_read_b64_tr_b16 a[162:163], v6 offset:16896\n"
"v_cvt_pk_bf16_f32 v230, v232, v233\n"
"v_mul_f32 v76, v36, v234\n"
"v_mul_f32 v77, v36, v235\n"
"v_exp_f32 v76, v76\n"
"v_mfma_f32_32x32x16_bf16 v[144:159], a[164:167], a[128:131], v[144:159]\n"
"ds_read_b64_tr_b16 a[164:165], v7 offset:16384\n"
"ds_read_b64_tr_b16 a[166:167], v7 offset:16896\n"
"v_exp_f32 v77, v77\n"
"v_mul_f32 v234, v234, v238\n"
"v_mul_f32 v235, v235, v239\n"
"v_fma_f32 v76, v76, v37, v37\n"
"v_mfma_f32_32x32x16_bf16 v[160:175], a[168:171], a[128:131], v[160:175]\n"
"ds_read_b64_tr_b16 a[168:169], v8 offset:16384\n"
"ds_read_b64_tr_b16 a[170:171], v8 offset:16896\n"
"v_fma_f32 v77, v77, v37, v37\n"
"v_rcp_f32 v76, v76\n"
"v_rcp_f32 v77, v77\n"
"v_mfma_f32_32x32x16_bf16 v[176:191], a[172:175], a[128:131], v[176:191]\n"
"s_waitcnt lgkmcnt(6)\n"
"ds_read_b64_tr_b16 a[172:173], v9 offset:16384\n"
"ds_read_b64_tr_b16 a[174:175], v9 offset:16896\n"
"v_mul_f32 v234, v234, v76\n"
"v_mul_f32 v235, v235, v77\n"
"v_cvt_pk_bf16_f32 v231, v234, v235\n"
"v_mfma_f32_32x32x16_bf16 v[128:143], a[176:179], a[132:135], v[128:143]\n"
"ds_read_b64_tr_b16 a[176:177], v6 offset:18432\n"
"ds_read_b64_tr_b16 a[178:179], v6 offset:18944\n"
"buffer_store_dwordx4 v[228:231], v26, s[64:67], s27 offen offset:0 sc1\n"
"v_mul_f32 v76, v36, v240\n"
"v_mul_f32 v77, v36, v241\n"
"v_mfma_f32_32x32x16_bf16 v[144:159], a[180:183], a[132:135], v[144:159]\n"
"ds_read_b64_tr_b16 a[180:181], v7 offset:18432\n"
"ds_read_b64_tr_b16 a[182:183], v7 offset:18944\n"
"v_exp_f32 v76, v76\n"
"v_exp_f32 v77, v77\n"
"v_mul_f32 v240, v240, v244\n"
"v_mfma_f32_32x32x16_bf16 v[160:175], a[184:187], a[132:135], v[160:175]\n"
"ds_read_b64_tr_b16 a[184:185], v8 offset:18432\n"
"ds_read_b64_tr_b16 a[186:187], v8 offset:18944\n"
"v_mul_f32 v241, v241, v245\n"
"v_fma_f32 v76, v76, v37, v37\n"
"v_fma_f32 v77, v77, v37, v37\n"
"v_rcp_f32 v76, v76\n"
"v_mfma_f32_32x32x16_bf16 v[176:191], a[188:191], a[132:135], v[176:191]\n"
"s_waitcnt lgkmcnt(6)\n"
"ds_read_b64_tr_b16 a[188:189], v9 offset:18432\n"
"ds_read_b64_tr_b16 a[190:191], v9 offset:18944\n"
"v_rcp_f32 v77, v77\n"
"v_mul_f32 v240, v240, v76\n"
"v_mul_f32 v241, v241, v77\n"
"v_mfma_f32_32x32x16_bf16 v[128:143], a[160:163], a[136:139], v[128:143]\n"
"s_waitcnt vmcnt(4)\n"
"s_barrier\n"
"ds_read_b64_tr_b16 a[160:161], v2 offset:12288\n"
"ds_read_b64_tr_b16 a[162:163], v2 offset:12800\n"
"v_mov_b32 v22, s72\n"
"v_mov_b32 v23, s73\n"
"v_mfma_f32_32x32x16_bf16 v[144:159], a[164:167], a[136:139], v[144:159]\n"
"ds_read_b64_tr_b16 a[164:165], v3 offset:12288\n"
"ds_read_b64_tr_b16 a[166:167], v3 offset:12800\n"
"v_mov_b32 v24, 0\n"
"v_mfma_f32_32x32x16_bf16 v[160:175], a[168:171], a[136:139], v[160:175]\n"
"ds_read_b64_tr_b16 a[168:169], v4 offset:12288\n"
"ds_read_b64_tr_b16 a[170:171], v4 offset:12800\n"
"buffer_store_dwordx2 v[22:23], v24, s[64:67], s94 offen sc1\n"
"v_mov_b32 v22, s72\n"
"v_mov_b32 v23, s73\n"
"v_mfma_f32_32x32x16_bf16 v[176:191], a[172:175], a[136:139], v[176:191]\n"
"s_waitcnt lgkmcnt(6)\n"
"ds_read_b64_tr_b16 a[172:173], v5 offset:12288\n"
"ds_read_b64_tr_b16 a[174:175], v5 offset:12800\n"
"v_mov_b32 v24, 65536\n"
"v_mfma_f32_32x32x16_bf16 v[128:143], a[176:179], a[140:143], v[128:143]\n"
"ds_read_b64_tr_b16 a[176:177], v2 offset:14336\n"
"ds_read_b64_tr_b16 a[178:179], v2 offset:14848\n"
"buffer_store_dwordx2 v[22:23], v24, s[64:67], s36 offen sc1\n"
"v_mul_f32 v76, v36, v242\n"
"v_mul_f32 v77, v36, v243\n"
"v_mfma_f32_32x32x16_bf16 v[144:159], a[180:183], a[140:143], v[144:159]\n"
"ds_read_b64_tr_b16 a[180:181], v3 offset:14336\n"
"ds_read_b64_tr_b16 a[182:183], v3 offset:14848\n"
"v_exp_f32 v76, v76\n"
"v_exp_f32 v77, v77\n"
"v_mul_f32 v242, v242, v246\n"
"v_mfma_f32_32x32x16_bf16 v[160:175], a[184:187], a[140:143], v[160:175]\n"
"ds_read_b64_tr_b16 a[184:185], v4 offset:14336\n"
"ds_read_b64_tr_b16 a[186:187], v4 offset:14848\n"
"v_mul_f32 v243, v243, v247\n"
"v_fma_f32 v76, v76, v37, v37\n"
"v_fma_f32 v77, v77, v37, v37\n"
"v_rcp_f32 v76, v76\n"
"v_mfma_f32_32x32x16_bf16 v[176:191], a[188:191], a[140:143], v[176:191]\n"
"s_waitcnt lgkmcnt(6)\n"
"ds_read_b64_tr_b16 a[188:189], v5 offset:14336\n"
"ds_read_b64_tr_b16 a[190:191], v5 offset:14848\n"
"v_rcp_f32 v77, v77\n"
"v_mul_f32 v242, v242, v76\n"
"v_mul_f32 v243, v243, v77\n"
"v_mfma_f32_32x32x16_bf16 v[128:143], a[160:163], a[144:147], v[128:143]\n"
"ds_read_b64_tr_b16 a[160:161], v6 offset:12288\n"
"ds_read_b64_tr_b16 a[162:163], v6 offset:12800\n"
"v_cvt_pk_bf16_f32 v244, v240, v241\n"
"v_cvt_pk_bf16_f32 v245, v242, v243\n"
"v_mul_f32 v76, v36, v248\n"
"v_mul_f32 v77, v36, v249\n"
"v_mfma_f32_32x32x16_bf16 v[144:159], a[164:167], a[144:147], v[144:159]\n"
"ds_read_b64_tr_b16 a[164:165], v7 offset:12288\n"
"ds_read_b64_tr_b16 a[166:167], v7 offset:12800\n"
"v_exp_f32 v76, v76\n"
"v_exp_f32 v77, v77\n"
"v_mul_f32 v248, v248, v252\n"
"v_mfma_f32_32x32x16_bf16 v[160:175], a[168:171], a[144:147], v[160:175]\n"
"ds_read_b64_tr_b16 a[168:169], v8 offset:12288\n"
"ds_read_b64_tr_b16 a[170:171], v8 offset:12800\n"
"v_mul_f32 v249, v249, v253\n"
"v_fma_f32 v76, v76, v37, v37\n"
"v_fma_f32 v77, v77, v37, v37\n"
"v_rcp_f32 v76, v76\n"
"v_mfma_f32_32x32x16_bf16 v[176:191], a[172:175], a[144:147], v[176:191]\n"
"s_waitcnt lgkmcnt(6)\n"
"ds_read_b64_tr_b16 a[172:173], v9 offset:12288\n"
"ds_read_b64_tr_b16 a[174:175], v9 offset:12800\n"
"v_rcp_f32 v77, v77\n"
"v_mul_f32 v248, v248, v76\n"
"v_mul_f32 v249, v249, v77\n"
"v_mfma_f32_32x32x16_bf16 v[128:143], a[176:179], a[148:151], v[128:143]\n"
"ds_read_b64_tr_b16 a[176:177], v6 offset:14336\n"
"ds_read_b64_tr_b16 a[178:179], v6 offset:14848\n"
"v_cvt_pk_bf16_f32 v246, v248, v249\n"
"v_mul_f32 v76, v36, v250\n"
"v_mul_f32 v77, v36, v251\n"
"v_exp_f32 v76, v76\n"
"v_mfma_f32_32x32x16_bf16 v[144:159], a[180:183], a[148:151], v[144:159]\n"
"ds_read_b64_tr_b16 a[180:181], v7 offset:14336\n"
"ds_read_b64_tr_b16 a[182:183], v7 offset:14848\n"
"v_exp_f32 v77, v77\n"
"v_mul_f32 v250, v250, v254\n"
"v_mul_f32 v251, v251, v255\n"
"v_fma_f32 v76, v76, v37, v37\n"
"v_mfma_f32_32x32x16_bf16 v[160:175], a[184:187], a[148:151], v[160:175]\n"
"ds_read_b64_tr_b16 a[184:185], v8 offset:14336\n"
"ds_read_b64_tr_b16 a[186:187], v8 offset:14848\n"
"v_fma_f32 v77, v77, v37, v37\n"
"v_rcp_f32 v76, v76\n"
"v_rcp_f32 v77, v77\n"
"v_mfma_f32_32x32x16_bf16 v[176:191], a[188:191], a[148:151], v[176:191]\n"
"s_waitcnt lgkmcnt(6)\n"
"ds_read_b64_tr_b16 a[188:189], v9 offset:14336\n"
"ds_read_b64_tr_b16 a[190:191], v9 offset:14848\n"
"v_mul_f32 v250, v250, v76\n"
"v_mul_f32 v251, v251, v77\n"
"v_cvt_pk_bf16_f32 v247, v250, v251\n"
"v_mfma_f32_32x32x16_bf16 v[128:143], a[160:163], a[152:155], v[128:143]\n"
"buffer_store_dwordx4 v[244:247], v26, s[64:67], s27 offen offset:512 sc1\n"
"s_waitcnt vmcnt(4)\n"
"v_xor_b32 v76, s72, v69\n"
"v_xor_b32 v77, s73, v71\n"
"v_or_b32 v76, v76, v77\n"
"v_cmp_ne_u32 s[22:23], 0, v76\n"
"v_mfma_f32_32x32x16_bf16 v[144:159], a[164:167], a[152:155], v[144:159]\n"
"v_xor_b32 v76, s72, v73\n"
"v_xor_b32 v77, s73, v75\n"
"v_or_b32 v76, v76, v77\n"
"v_cmp_ne_u32 s[24:25], 0, v76\n"
"s_and_b64 s[22:23], s[22:23], s[18:19]\n"
"s_and_b64 s[24:25], s[24:25], s[20:21]\n"
"v_mfma_f32_32x32x16_bf16 v[160:175], a[168:171], a[152:155], v[160:175]\n"
"s_or_b64 s[22:23], s[22:23], s[24:25]\n"
"s_cbranch_scc1 K1F_g0_slow\n"
"K1F_g0_back:\n"
"v_mul_f32 v76, v58, v68\n"
"v_mul_f32 v77, v58, v70\n"
"v_fma_f32 v76, -v59, v70, v76\n"
"v_fmac_f32 v77, v59, v68\n"
"v_mfma_f32_32x32x16_bf16 v[176:191], a[172:175], a[152:155], v[176:191]\n"
"s_waitcnt lgkmcnt(0)\n"
"v_fmac_f32 v76, v60, v72\n"
"v_fmac_f32 v77, v60, v74\n"
"v_fma_f32 v76, -v61, v74, v76\n"
"v_fmac_f32 v77, v61, v72\n"
"v_mfma_f32_32x32x16_bf16 v[128:143], a[176:179], a[156:159], v[128:143]\n"
"ds_write_b64 v31, v[76:77] offset:0\n"
"v_mfma_f32_32x32x16_bf16 v[144:159], a[180:183], a[156:159], v[144:159]\n"
"s_waitcnt lgkmcnt(0)\n"
"s_barrier\n"
"ds_read_b128 v[68:71], v32 offset:0\n"
"ds_read_b128 v[72:75], v32 offset:16\n"
"v_mfma_f32_32x32x16_bf16 v[160:175], a[184:187], a[156:159], v[160:175]\n"
"v_mfma_f32_32x32x16_bf16 v[176:191], a[188:191], a[156:159], v[176:191]\n"
"s_waitcnt lgkmcnt(0)\n"
"v_add_f32 v68, v68, v70\n"
"v_add_f32 v72, v72, v74\n"
"v_add_f32 v69, v69, v71\n"
"v_add_f32 v73, v73, v75\n"
"v_add_f32 v76, v68, v72\n"
"v_add_f32 v77, v69, v73\n"
"v_mfma_f32_32x32x16_bf16 v[128:143], v[88:91], v[40:43], v[128:143]\n"
"ds_read_b128 v[68:71], v32 offset:32\n"
"ds_read_b128 v[72:75], v32 offset:48\n"
"v_mfma_f32_32x32x16_bf16 v[144:159], v[88:91], a[196:199], v[144:159]\n"
"v_mfma_f32_32x32x16_bf16 v[160:175], v[92:95], v[40:43], v[160:175]\n"
"s_waitcnt lgkmcnt(0)\n"
"v_add_f32 v68, v68, v70\n"
"v_add_f32 v72, v72, v74\n"
"v_add_f32 v69, v69, v71\n"
"v_add_f32 v73, v73, v75\n"
"v_add_f32 v68, v68, v72\n"
"v_add_f32 v76, v76, v68\n"
"v_add_f32 v69, v69, v73\n"
"v_mfma_f32_32x32x16_bf16 v[176:191], v[92:95], a[196:199], v[176:191]\n"
"v_add_f32 v77, v77, v69\n"
"v_fma_f32 v68, v56, v76, v66\n"
"v_fma_f32 v69, v56, v77, v67\n"
"v_fma_f32 v68, -v57, v77, v68\n"
"v_fma_f32 v69, v57, v76, v69\n"
"v_fma_f32 v70, v44, v68, v96\n"
"v_fma_f32 v71, v44, v69, v112\n"
"K1F_c3:\n"
"v_fma_f32 v70, -v45, v69, v70\n"
"v_fma_f32 v71, v45, v68, v71\n"
"v_mul_f32 v96, v62, v70\n"
"v_mul_f32 v112, v62, v71\n"
"v_fma_f32 v96, -v63, v71, v96\n"
"v_fma_f32 v112, -v63, v70, -v112\n"
"v_fma_f32 v68, v44, v70, v97\n"
"v_fma_f32 v69, v44, v71, v113\n"
"v_fma_f32 v68, -v45, v71, v68\n"
"v_fma_f32 v69, v45, v70, v69\n"
"s_waitcnt vmcnt(0)\n"
"v_mov_b32 v22, s72\n"
"v_mov_b32 v23, s73\n"
"v_mov_b32 v24, 0\n"
"buffer_store_dwordx2 v[22:23], v24, s[64:67], s36 offen sc1\n"
"v_mul_f32 v97, v62, v68\n"
"v_mul_f32 v113, v62, v69\n"
"v_fma_f32 v97, -v63, v69, v97\n"
"v_fma_f32 v113, -v63, v68, -v113\n"
"v_fma_f32 v70, v44, v68, v98\n"
"v_fma_f32 v71, v44, v69, v114\n"
"v_fma_f32 v70, -v45, v69, v70\n"
"v_fma_f32 v71, v45, v68, v71\n"
"v_mul_f32 v98, v62, v70\n"
"v_mul_f32 v114, v62, v71\n"
"v_fma_f32 v98, -v63, v71, v98\n"
"v_fma_f32 v114, -v63, v70, -v114\n"
"v_fma_f32 v68, v44, v70, v99\n"
"v_fma_f32 v69, v44, v71, v115\n"
"v_fma_f32 v68, -v45, v71, v68\n"
"v_fma_f32 v69, v45, v70, v69\n"
"v_mul_f32 v99, v62, v68\n"
"v_mul_f32 v115, v62, v69\n"
"v_fma_f32 v99, -v63, v69, v99\n"
"v_fma_f32 v115, -v63, v68, -v115\n"
"v_cvt_pk_bf16_f32 v96, v96, v97\n"
"v_cvt_pk_bf16_f32 v97, v98, v99\n"
"ds_write_b64 v33, v[96:97] offset:0\n"
"v_cvt_pk_bf16_f32 v112, v112, v113\n"
"v_cvt_pk_bf16_f32 v113, v114, v115\n"
"ds_write_b64 v33, v[112:113] offset:2304\n"
"v_fma_f32 v70, v44, v68, v100\n"
"v_fma_f32 v71, v44, v69, v116\n"
"v_fma_f32 v70, -v45, v69, v70\n"
"v_fma_f32 v71, v45, v68, v71\n"
"v_mul_f32 v100, v62, v70\n"
"v_mul_f32 v116, v62, v71\n"
"v_fma_f32 v100, -v63, v71, v100\n"
"v_fma_f32 v116, -v63, v70, -v116\n"
"v_fma_f32 v68, v44, v70, v101\n"
"v_fma_f32 v69, v44, v71, v117\n"
"v_fma_f32 v68, -v45, v71, v68\n"
"v_fma_f32 v69, v45, v70, v69\n"
"v_mul_f32 v101, v62, v68\n"
"v_mul_f32 v117, v62, v69\n"
"v_fma_f32 v101, -v63, v69, v101\n"
"s_lshl_b32 s68, s7, 13\n"
"s_add_u32 s68, s68, 23072768\n"
"v_lshl_add_u32 v24, v27, 7, s68\n"
"buffer_load_dwordx2 v[22:23], v24, s[64:67], 0 offen sc1\n"
"v_add_u32 v25, 0x400, v28\n"
"v_fma_f32 v117, -v63, v68, -v117\n"
"v_fma_f32 v70, v44, v68, v102\n"
"v_fma_f32 v71, v44, v69, v118\n"
"v_fma_f32 v70, -v45, v69, v70\n"
"v_fma_f32 v71, v45, v68, v71\n"
"v_mul_f32 v102, v62, v70\n"
"v_mul_f32 v118, v62, v71\n"
"v_fma_f32 v102, -v63, v71, v102\n"
"v_fma_f32 v118, -v63, v70, -v118\n"
"v_fma_f32 v68, v44, v70, v103\n"
"v_fma_f32 v69, v44, v71, v119\n"
"v_fma_f32 v68, -v45, v71, v68\n"
"v_fma_f32 v69, v45, v70, v69\n"
"v_mul_f32 v103, v62, v68\n"
"v_mul_f32 v119, v62, v69\n"
"v_fma_f32 v103, -v63, v69, v103\n"
"v_fma_f32 v119, -v63, v68, -v119\n"
"v_cvt_pk_bf16_f32 v100, v100, v101\n"
"v_cvt_pk_bf16_f32 v101, v102, v103\n"
"ds_write_b64 v33, v[100:101] offset:16\n"
"v_cvt_pk_bf16_f32 v116, v116, v117\n"
"v_cvt_pk_bf16_f32 v117, v118, v119\n"
"ds_write_b64 v33, v[116:117] offset:2320\n"
"v_fma_f32 v70, v44, v68, v104\n"
"v_fma_f32 v71, v44, v69, v120\n"
"v_fma_f32 v70, -v45, v69, v70\n"
"v_fma_f32 v71, v45, v68, v71\n"
"v_mul_f32 v104, v62, v70\n"
"v_mul_f32 v120, v62, v71\n"
"v_fma_f32 v104, -v63, v71, v104\n"
"v_fma_f32 v120, -v63, v70, -v120\n"
"v_fma_f32 v68, v44, v70, v105\n"
"v_fma_f32 v69, v44, v71, v121\n"
"v_fma_f32 v68, -v45, v71, v68\n"
"v_fma_f32 v69, v45, v70, v69\n"
"v_mul_f32 v105, v62, v68\n"
"v_mul_f32 v121, v62, v69\n"
"v_fma_f32 v105, -v63, v69, v105\n"
"v_fma_f32 v121, -v63, v68, -v121\n"
"v_fma_f32 v70, v44, v68, v106\n"
"v_fma_f32 v71, v44, v69, v122\n"
"v_fma_f32 v70, -v45, v69, v70\n"
"v_fma_f32 v71, v45, v68, v71\n"
"v_mul_f32 v106, v62, v70\n"
"v_mul_f32 v122, v62, v71\n"
"v_fma_f32 v106, -v63, v71, v106\n"
"v_fma_f32 v122, -v63, v70, -v122\n"
"v_fma_f32 v68, v44, v70, v107\n"
"v_fma_f32 v69, v44, v71, v123\n"
"v_fma_f32 v68, -v45, v71, v68\n"
"v_fma_f32 v69, v45, v70, v69\n"
"v_mul_f32 v107, v62, v68\n"
"v_mul_f32 v123, v62, v69\n"
"v_fma_f32 v107, -v63, v69, v107\n"
"v_fma_f32 v123, -v63, v68, -v123\n"
"v_cvt_pk_bf16_f32 v104, v104, v105\n"
"v_cvt_pk_bf16_f32 v105, v106, v107\n"
"ds_write_b64 v33, v[104:105] offset:32\n"
"v_cvt_pk_bf16_f32 v120, v120, v121\n"
"v_cvt_pk_bf16_f32 v121, v122, v123\n"
"ds_write_b64 v33, v[120:121] offset:2336\n"
"v_fma_f32 v70, v44, v68, v108\n"
"v_fma_f32 v71, v44, v69, v124\n"
"v_fma_f32 v70, -v45, v69, v70\n"
"v_fma_f32 v71, v45, v68, v71\n"
"v_mul_f32 v108, v62, v70\n"
"v_mul_f32 v124, v62, v71\n"
"v_fma_f32 v108, -v63, v71, v108\n"
"v_fma_f32 v124, -v63, v70, -v124\n"
"v_fma_f32 v68, v44, v70, v109\n"
"v_fma_f32 v69, v44, v71, v125\n"
"v_fma_f32 v68, -v45, v71, v68\n"
"v_fma_f32 v69, v45, v70, v69\n"
"v_mul_f32 v109, v62, v68\n"
"v_mul_f32 v125, v62, v69\n"
"v_fma_f32 v109, -v63, v69, v109\n"
"v_fma_f32 v125, -v63, v68, -v125\n"
"v_fma_f32 v70, v44, v68, v110\n"
"v_fma_f32 v71, v44, v69, v126\n"
"v_fma_f32 v70, -v45, v69, v70\n"
"v_fma_f32 v71, v45, v68, v71\n"
"v_mul_f32 v110, v62, v70\n"
"v_mul_f32 v126, v62, v71\n"
"v_fma_f32 v110, -v63, v71, v110\n"
"v_fma_f32 v126, -v63, v70, -v126\n"
"v_fma_f32 v68, v44, v70, v111\n"
"v_fma_f32 v69, v44, v71, v127\n"
"v_fma_f32 v68, -v45, v71, v68\n"
"v_fma_f32 v69, v45, v70, v69\n"
"v_mul_f32 v111, v62, v68\n"
"v_mul_f32 v127, v62, v69\n"
"v_fma_f32 v111, -v63, v69, v111\n"
"v_fma_f32 v127, -v63, v68, -v127\n"
"v_cvt_pk_bf16_f32 v108, v108, v109\n"
"v_cvt_pk_bf16_f32 v109, v110, v111\n"
"ds_write_b64 v33, v[108:109] offset:48\n"
"v_cvt_pk_bf16_f32 v124, v124, v125\n"
"v_cvt_pk_bf16_f32 v125, v126, v127\n"
"ds_write_b64 v33, v[124:125] offset:2352\n"
"ds_read_b64_tr_b16 v[96:97], v34 offset:0\n"
"ds_read_b64_tr_b16 v[98:99], v34 offset:288\n"
"ds_read_b64_tr_b16 v[100:101], v34 offset:32\n"
"ds_read_b64_tr_b16 v[102:103], v34 offset:320\n"
"ds_read_b64_tr_b16 v[104:105], v34 offset:2304\n"
"ds_read_b64_tr_b16 v[106:107], v34 offset:2592\n"
"ds_read_b64_tr_b16 v[108:109], v34 offset:2336\n"
"ds_read_b64_tr_b16 v[110:111], v34 offset:2624\n"
"s_bfe_u32 s26, s101, 0x80000\n"
"s_lshl_b32 s26, s26, 12\n"
"s_add_u32 s26, s26, s43\n"
"s_bfe_u32 s27, s101, 0x80008\n"
"s_lshl_b32 s27, s27, 12\n"
"s_add_u32 s27, s27, s43\n"
"s_waitcnt vmcnt(0)\n"
"v_xor_b32 v22, s72, v22\n"
"v_xor_b32 v23, s73, v23\n"
"v_or_b32 v22, v22, v23\n"
"v_cmp_ne_u32 vcc, 0, v22\n"
"s_nop 0\n"
"s_cmp_lg_u64 vcc, 0\n"
"s_cbranch_scc1 K1F_s_slow\n"
"K1F_s_back:\n"
"s_barrier\n"
"s_or_b32 s68, s6, s7\n"
"s_cselect_b32 s68, 2130706432, 0\n"
"v_mov_b32 v22, s72\n"
"v_mov_b32 v23, s73\n"
"v_mov_b32 v24, 23068672\n"
"buffer_store_dwordx2 v[22:23], v24, s[64:67], s68 offen sc1\n"
"s_lshl_b32 s68, s30, 11\n"
"s_add_u32 s68, s68, 23134208\n"
"v_and_b32 v3, 3, v27\n"
"v_lshl_add_u32 v3, v3, 3, s68\n"
"v_lshrrev_b32 v42, 2, v27\n"
"v_lshl_add_u32 v3, v42, 7, v3\n"
"s_mov_b32 s70, 0\n"
"buffer_load_dwordx2 v[42:43], v3, s[64:67], s70 offen sc1\n"
"s_mov_b32 s33, 0\n"
"buffer_load_dwordx4 a[200:203], v28, s[76:79], s33 offen offset:0 sc1\n"
"buffer_load_dwordx4 a[204:207], v28, s[76:79], s33 offen offset:1024 sc1\n"
"buffer_load_dwordx4 a[208:211], v28, s[76:79], s33 offen offset:2048 sc1\n"
"buffer_load_dwordx4 a[212:215], v28, s[76:79], s33 offen offset:3072 sc1\n"
"buffer_load_dwordx4 a[216:219], v25, s[76:79], s33 offen offset:3072 sc1\n"
"s_mov_b32 s33, 40960\n"
"buffer_load_dwordx4 a[220:223], v28, s[76:79], s33 offen offset:0 sc1\n"
"buffer_load_dwordx4 a[224:227], v28, s[76:79], s33 offen offset:1024 sc1\n"
"buffer_load_dwordx4 a[228:231], v28, s[76:79], s33 offen offset:2048 sc1\n"
"s_waitcnt vmcnt(8)\n"
"v_xor_b32 v42, s72, v42\n"
"v_xor_b32 v43, s73, v43\n"
"v_or_b32 v42, v42, v43\n"
"v_cmp_ne_u32 vcc, 0, v42\n"
"s_nop 0\n"
"s_cmp_lg_u64 vcc, 0\n"
"s_cbranch_scc1 K1F_r0_slow\n"
"K1F_r0_back:\n"
"s_mov_b32 s92, s26\n"
"s_mov_b32 s93, s27\n"
"s_waitcnt lgkmcnt(0)\n"
"buffer_store_dwordx4 v[96:99], v28, s[64:67], s92 offen offset:0 sc1\n"
"s_bfe_u32 s26, s100, 0x80010\n"
"s_lshl_b32 s26, s26, 12\n"
"s_add_u32 s26, s26, s43\n"
"s_bfe_u32 s27, s100, 0x80018\n"
"s_lshl_b32 s27, s27, 12\n"
"s_add_u32 s27, s27, s43\n"
"v_mul_f32 v76, v36, v128\n"
"s_mov_b32 s32, 0\n"
"buffer_load_dwordx4 v[192:195], v28, s[84:87], s32 offen offset:0 sc1\n"
"v_mul_f32 v77, v36, v129\n"
"v_exp_f32 v76, v76\n"
"v_exp_f32 v77, v77\n"
"v_mul_f32 v128, v128, v132\n"
"v_mul_f32 v129, v129, v133\n"
"v_fma_f32 v76, v76, v37, v37\n"
"v_fma_f32 v77, v77, v37, v37\n"
"buffer_load_dwordx4 v[196:199], v28, s[84:87], s32 offen offset:1024 sc1\n"
"v_rcp_f32 v76, v76\n"
"v_rcp_f32 v77, v77\n"
"v_mul_f32 v128, v128, v76\n"
"v_mul_f32 v129, v129, v77\n"
"v_mul_f32 v76, v36, v130\n"
"v_mul_f32 v77, v36, v131\n"
"v_exp_f32 v76, v76\n"
"buffer_load_dwordx4 v[200:203], v28, s[84:87], s32 offen offset:2048 sc1\n"
"v_exp_f32 v77, v77\n"
"v_mul_f32 v130, v130, v134\n"
"v_mul_f32 v131, v131, v135\n"
"v_fma_f32 v76, v76, v37, v37\n"
"v_fma_f32 v77, v77, v37, v37\n"
"v_rcp_f32 v76, v76\n"
"v_rcp_f32 v77, v77\n"
"buffer_load_dwordx4 v[204:207], v28, s[84:87], s32 offen offset:3072 sc1\n"
"v_mul_f32 v130, v130, v76\n"
"v_mul_f32 v131, v131, v77\n"
"v_cvt_pk_bf16_f32 v132, v128, v129\n"
"v_cvt_pk_bf16_f32 v133, v130, v131\n"
"v_mul_f32 v76, v36, v136\n"
"v_mul_f32 v77, v36, v137\n"
"v_exp_f32 v76, v76\n"
"v_exp_f32 v77, v77\n"
"buffer_store_dwordx4 v[100:103], v28, s[64:67], s92 offen offset:1024 sc1\n"
"v_mul_f32 v136, v136, v140\n"
"v_mul_f32 v137, v137, v141\n"
"v_fma_f32 v76, v76, v37, v37\n"
"v_fma_f32 v77, v77, v37, v37\n"
"v_rcp_f32 v76, v76\n"
"v_rcp_f32 v77, v77\n"
"v_mul_f32 v136, v136, v76\n"
"s_mov_b32 s32, 4096\n"
"buffer_load_dwordx4 v[208:211], v28, s[84:87], s32 offen offset:0 sc1\n"
"v_mul_f32 v137, v137, v77\n"
"v_cvt_pk_bf16_f32 v134, v136, v137\n"
"v_mul_f32 v76, v36, v138\n"
"v_mul_f32 v77, v36, v139\n"
"v_exp_f32 v76, v76\n"
"v_exp_f32 v77, v77\n"
"v_mul_f32 v138, v138, v142\n"
"buffer_load_dwordx4 v[212:215], v28, s[84:87], s32 offen offset:1024 sc1\n"
"v_mul_f32 v139, v139, v143\n"
"v_fma_f32 v76, v76, v37, v37\n"
"v_fma_f32 v77, v77, v37, v37\n"
"v_rcp_f32 v76, v76\n"
"v_rcp_f32 v77, v77\n"
"v_mul_f32 v138, v138, v76\n"
"v_mul_f32 v139, v139, v77\n"
"buffer_load_dwordx4 v[216:219], v28, s[84:87], s32 offen offset:2048 sc1\n"
"v_cvt_pk_bf16_f32 v135, v138, v139\n"
"v_mul_f32 v76, v36, v144\n"
"v_mul_f32 v77, v36, v145\n"
"v_exp_f32 v76, v76\n"
"v_exp_f32 v77, v77\n"
"v_mul_f32 v144, v144, v148\n"
"v_mul_f32 v145, v145, v149\n"
"buffer_load_dwordx4 v[220:223], v28, s[84:87], s32 offen offset:3072 sc1\n"
"v_fma_f32 v76, v76, v37, v37\n"
"v_fma_f32 v77, v77, v37, v37\n"
"v_rcp_f32 v76, v76\n"
"v_rcp_f32 v77, v77\n"
"v_mul_f32 v144, v144, v76\n"
"v_mul_f32 v145, v145, v77\n"
"v_mul_f32 v76, v36, v146\n"
"v_mul_f32 v77, v36, v147\n"
"buffer_store_dwordx4 v[132:135], v26, s[64:67], s26 offen offset:0 sc1\n"
"v_exp_f32 v76, v76\n"
"v_exp_f32 v77, v77\n"
"v_mul_f32 v146, v146, v150\n"
"v_mul_f32 v147, v147, v151\n"
"v_fma_f32 v76, v76, v37, v37\n"
"v_fma_f32 v77, v77, v37, v37\n"
"v_rcp_f32 v76, v76\n"
"buffer_load_dwordx4 a[232:235], v28, s[76:79], s33 offen offset:3072 sc1\n"
"v_rcp_f32 v77, v77\n"
"v_mul_f32 v146, v146, v76\n"
"v_mul_f32 v147, v147, v77\n"
"v_cvt_pk_bf16_f32 v148, v144, v145\n"
"v_cvt_pk_bf16_f32 v149, v146, v147\n"
"v_mul_f32 v76, v36, v152\n"
"v_mul_f32 v77, v36, v153\n"
"buffer_load_dwordx4 a[236:239], v25, s[76:79], s33 offen offset:3072 sc1\n"
"v_exp_f32 v76, v76\n"
"v_exp_f32 v77, v77\n"
"v_mul_f32 v152, v152, v156\n"
"v_mul_f32 v153, v153, v157\n"
"v_fma_f32 v76, v76, v37, v37\n"
"v_fma_f32 v77, v77, v37, v37\n"
"v_rcp_f32 v76, v76\n"
"s_mov_b32 s33, 81920\n"
"buffer_load_dwordx4 a[240:243], v28, s[76:79], s33 offen offset:0 sc1\n"
"v_rcp_f32 v77, v77\n"
"v_mul_f32 v152, v152, v76\n"
"v_mul_f32 v153, v153, v77\n"
"v_cvt_pk_bf16_f32 v150, v152, v153\n"
"v_mul_f32 v76, v36, v154\n"
"v_mul_f32 v77, v36, v155\n"
"v_exp_f32 v76, v76\n"
"buffer_load_dwordx4 a[244:247], v28, s[76:79], s33 offen offset:1024 sc1\n"
"v_exp_f32 v77, v77\n"
"v_mul_f32 v154, v154, v158\n"
"v_mul_f32 v155, v155, v159\n"
"v_fma_f32 v76, v76, v37, v37\n"
"v_fma_f32 v77, v77, v37, v37\n"
"v_rcp_f32 v76, v76\n"
"v_rcp_f32 v77, v77\n"
"v_mul_f32 v154, v154, v76\n"
"buffer_store_dwordx4 v[104:107], v28, s[64:67], s93 offen offset:0 sc1\n"
"v_mul_f32 v155, v155, v77\n"
"v_cvt_pk_bf16_f32 v151, v154, v155\n"
"v_mul_f32 v76, v36, v160\n"
"v_mul_f32 v77, v36, v161\n"
"v_exp_f32 v76, v76\n"
"v_exp_f32 v77, v77\n"
"v_mul_f32 v160, v160, v164\n"
"buffer_load_dwordx4 a[248:251], v28, s[76:79], s33 offen offset:2048 sc1\n"
"v_mul_f32 v161, v161, v165\n"
"v_fma_f32 v76, v76, v37, v37\n"
"v_fma_f32 v77, v77, v37, v37\n"
"v_rcp_f32 v76, v76\n"
"v_rcp_f32 v77, v77\n"
"v_mul_f32 v160, v160, v76\n"
"v_mul_f32 v161, v161, v77\n"
"buffer_load_dwordx4 a[252:255], v28, s[76:79], s33 offen offset:3072 sc1\n"
"v_mul_f32 v76, v36, v162\n"
"v_mul_f32 v77, v36, v163\n"
"v_exp_f32 v76, v76\n"
"v_exp_f32 v77, v77\n"
"v_mul_f32 v162, v162, v166\n"
"v_mul_f32 v163, v163, v167\n"
"v_fma_f32 v76, v76, v37, v37\n"
"buffer_load_dwordx4 a[192:195], v25, s[76:79], s33 offen offset:3072 sc1\n"
"v_fma_f32 v77, v77, v37, v37\n"
"v_rcp_f32 v76, v76\n"
"v_rcp_f32 v77, v77\n"
"v_mul_f32 v162, v162, v76\n"
"v_mul_f32 v163, v163, v77\n"
"v_cvt_pk_bf16_f32 v164, v160, v161\n"
"v_cvt_pk_bf16_f32 v165, v162, v163\n"
"s_mov_b32 s33, 122880\n"
"buffer_load_dwordx4 a[196:199], v28, s[76:79], s33 offen offset:0 sc1\n"
"v_mul_f32 v76, v36, v168\n"
"v_mul_f32 v77, v36, v169\n"
"v_exp_f32 v76, v76\n"
"v_exp_f32 v77, v77\n"
"v_mul_f32 v168, v168, v172\n"
"v_mul_f32 v169, v169, v173\n"
"v_fma_f32 v76, v76, v37, v37\n"
"v_fma_f32 v77, v77, v37, v37\n"
"buffer_store_dwordx4 v[148:151], v26, s[64:67], s26 offen offset:512 sc1\n"
"v_rcp_f32 v76, v76\n"
"v_rcp_f32 v77, v77\n"
"v_mul_f32 v168, v168, v76\n"
"v_mul_f32 v169, v169, v77\n"
"v_cvt_pk_bf16_f32 v166, v168, v169\n"
"v_mul_f32 v76, v36, v170\n"
"v_mul_f32 v77, v36, v171\n"
"buffer_load_dwordx4 v[80:83], v28, s[76:79], s33 offen offset:1024 sc1\n"
"v_exp_f32 v76, v76\n"
"v_exp_f32 v77, v77\n"
"v_mul_f32 v170, v170, v174\n"
"v_mul_f32 v171, v171, v175\n"
"v_fma_f32 v76, v76, v37, v37\n"
"v_fma_f32 v77, v77, v37, v37\n"
"v_rcp_f32 v76, v76\n"
"buffer_load_dwordx4 v[84:87], v28, s[76:79], s33 offen offset:2048 sc1\n"
"v_rcp_f32 v77, v77\n"
"v_mul_f32 v170, v170, v76\n"
"v_mul_f32 v171, v171, v77\n"
"v_cvt_pk_bf16_f32 v167, v170, v171\n"
"v_mul_f32 v76, v36, v176\n"
"v_mul_f32 v77, v36, v177\n"
"v_exp_f32 v76, v76\n"
"buffer_load_dwordx4 v[88:91], v28, s[76:79], s33 offen offset:3072 sc1\n"
"v_exp_f32 v77, v77\n"
"v_mul_f32 v176, v176, v180\n"
"v_mul_f32 v177, v177, v181\n"
"v_fma_f32 v76, v76, v37, v37\n"
"v_fma_f32 v77, v77, v37, v37\n"
"v_rcp_f32 v76, v76\n"
"v_rcp_f32 v77, v77\n"
"buffer_load_dwordx4 v[92:95], v25, s[76:79], s33 offen offset:3072 sc1\n"
"v_mul_f32 v176, v176, v76\n"
"v_mul_f32 v177, v177, v77\n"
"v_mul_f32 v76, v36, v178\n"
"v_mul_f32 v77, v36, v179\n"
"v_exp_f32 v76, v76\n"
"v_exp_f32 v77, v77\n"
"v_mul_f32 v178, v178, v182\n"
"v_mul_f32 v179, v179, v183\n"
"buffer_store_dwordx4 v[108:111], v28, s[64:67], s93 offen offset:1024 sc1\n"
"v_fma_f32 v76, v76, v37, v37\n"
"v_fma_f32 v77, v77, v37, v37\n"
"v_rcp_f32 v76, v76\n"
"v_rcp_f32 v77, v77\n"
"v_mul_f32 v178, v178, v76\n"
"v_mul_f32 v179, v179, v77\n"
"v_cvt_pk_bf16_f32 v180, v176, v177\n"
"s_mov_b32 s33, 163840\n"
"buffer_load_dwordx4 v[224:227], v28, s[76:79], s33 offen offset:0 sc1\n"
"v_cvt_pk_bf16_f32 v181, v178, v179\n"
"v_mul_f32 v76, v36, v184\n"
"v_mul_f32 v77, v36, v185\n"
"v_exp_f32 v76, v76\n"
"v_exp_f32 v77, v77\n"
"v_mul_f32 v184, v184, v188\n"
"v_mul_f32 v185, v185, v189\n"
"buffer_load_dwordx4 v[228:231], v28, s[76:79], s33 offen offset:1024 sc1\n"
"v_fma_f32 v76, v76, v37, v37\n"
"v_fma_f32 v77, v77, v37, v37\n"
"v_rcp_f32 v76, v76\n"
"v_rcp_f32 v77, v77\n"
"v_mul_f32 v184, v184, v76\n"
"v_mul_f32 v185, v185, v77\n"
"v_cvt_pk_bf16_f32 v182, v184, v185\n"
"buffer_load_dwordx4 v[232:235], v28, s[76:79], s33 offen offset:2048 sc1\n"
"v_mul_f32 v76, v36, v186\n"
"v_mul_f32 v77, v36, v187\n"
"v_exp_f32 v76, v76\n"
"v_exp_f32 v77, v77\n"
"v_mul_f32 v186, v186, v190\n"
"v_mul_f32 v187, v187, v191\n"
"v_fma_f32 v76, v76, v37, v37\n"
"buffer_load_dwordx4 v[236:239], v28, s[76:79], s33 offen offset:3072 sc1\n"
"v_fma_f32 v77, v77, v37, v37\n"
"v_rcp_f32 v76, v76\n"
"v_rcp_f32 v77, v77\n"
"v_mul_f32 v186, v186, v76\n"
"v_mul_f32 v187, v187, v77\n"
"v_cvt_pk_bf16_f32 v183, v186, v187\n"
"buffer_store_dwordx4 v[164:167], v26, s[64:67], s27 offen offset:0 sc1\n"
"buffer_load_dwordx4 v[240:243], v25, s[76:79], s33 offen offset:3072 sc1\n"
"s_mov_b32 s33, 204800\n"
"buffer_load_dwordx4 v[244:247], v28, s[76:79], s33 offen offset:0 sc1\n"
"buffer_load_dwordx4 v[248:251], v28, s[76:79], s33 offen offset:1024 sc1\n"
"buffer_load_dwordx4 v[252:255], v28, s[76:79], s33 offen offset:2048 sc1\n"
"buffer_store_dwordx4 v[180:183], v26, s[64:67], s27 offen offset:512 sc1\n"
"s_mov_b32 s32, 8192\n"
"buffer_load_dwordx4 a[160:163], v28, s[84:87], s32 offen offset:0 sc1\n"
"buffer_load_dwordx4 a[164:167], v28, s[84:87], s32 offen offset:1024 sc1\n"
"buffer_load_dwordx4 a[168:171], v28, s[84:87], s32 offen offset:2048 sc1\n"
"buffer_load_dwordx4 a[172:175], v28, s[84:87], s32 offen offset:3072 sc1\n"
"s_mov_b32 s32, 0\n"
"buffer_load_dwordx4 a[176:179], v28, s[80:83], s32 offen offset:0 sc1\n"
"buffer_load_dwordx4 a[180:183], v28, s[80:83], s32 offen offset:1024 sc1\n"
"buffer_load_dwordx4 a[184:187], v28, s[80:83], s32 offen offset:2048 sc1\n"
"buffer_load_dwordx4 a[188:191], v28, s[80:83], s32 offen offset:3072 sc1\n"
"buffer_load_dwordx4 v[44:47], v28, s[76:79], s33 offen offset:3072 sc1\n"
"buffer_load_dwordx4 v[48:51], v25, s[76:79], s33 offen offset:3072 sc1\n"
"s_waitcnt vmcnt(10)\n"
"v_mov_b32 v40, s72\n"
"v_mov_b32 v41, s73\n"
"v_mov_b32 v42, 32768\n"
"buffer_store_dwordx2 v[40:41], v42, s[64:67], s36 offen sc1\n"
"s_mov_b32 s33, 245760\n"
"buffer_load_dwordx4 v[52:55], v28, s[76:79], s33 offen offset:0 sc1\n"
"buffer_load_dwordx4 v[56:59], v28, s[76:79], s33 offen offset:1024 sc1\n"
"buffer_load_dwordx4 v[60:63], v28, s[76:79], s33 offen offset:2048 sc1\n"
"buffer_load_dwordx4 v[64:67], v28, s[76:79], s33 offen offset:3072 sc1\n"
"buffer_load_dwordx4 v[68:71], v25, s[76:79], s33 offen offset:3072 sc1\n"
"s_mov_b32 s33, 286720\n"
"buffer_load_dwordx4 v[72:75], v28, s[76:79], s33 offen offset:0 sc1\n"
"buffer_load_dwordx4 v[116:119], v28, s[76:79], s33 offen offset:1024 sc1\n"
"buffer_load_dwordx4 v[120:123], v28, s[76:79], s33 offen offset:2048 sc1\n"
"buffer_load_dwordx4 v[124:127], v28, s[76:79], s33 offen offset:3072 sc1\n"
"buffer_load_dwordx4 v[128:131], v25, s[76:79], s33 offen offset:3072 sc1\n"
"s_mov_b32 s33, 327680\n"
"buffer_load_dwordx4 v[132:135], v28, s[76:79], s33 offen offset:0 sc1\n"
"buffer_load_dwordx4 v[136:139], v28, s[76:79], s33 offen offset:1024 sc1\n"
"K1F_c4:\n"
"v_mfma_f32_16x16x32_bf16 a[80:83], a[200:203], v[192:195], 0\n"
"v_mfma_f32_16x16x32_bf16 a[84:87], a[200:203], v[196:199], 0\n"
"s_mov_b32 s32, 4096\n"
"buffer_load_dwordx4 v[140:143], v28, s[80:83], s32 offen offset:0 sc1\n"
"v_mfma_f32_16x16x32_bf16 a[88:91], a[200:203], v[200:203], 0\n"
"v_mfma_f32_16x16x32_bf16 a[92:95], a[200:203], v[204:207], 0\n"
"buffer_load_dwordx4 v[144:147], v28, s[80:83], s32 offen offset:1024 sc1\n"
"v_mfma_f32_16x16x32_bf16 a[96:99], a[204:207], v[192:195], 0\n"
"v_mfma_f32_16x16x32_bf16 a[100:103], a[204:207], v[196:199], 0\n"
"buffer_load_dwordx4 v[148:151], v28, s[80:83], s32 offen offset:2048 sc1\n"
"v_mfma_f32_16x16x32_bf16 a[104:107], a[204:207], v[200:203], 0\n"
"v_mfma_f32_16x16x32_bf16 a[108:111], a[204:207], v[204:207], 0\n"
"v_mfma_f32_16x16x32_bf16 a[112:115], a[208:211], v[192:195], 0\n"
"v_mfma_f32_16x16x32_bf16 a[116:119], a[208:211], v[196:199], 0\n"
"buffer_load_dwordx4 v[152:155], v28, s[80:83], s32 offen offset:3072 sc1\n"
"v_mfma_f32_16x16x32_bf16 a[120:123], a[208:211], v[200:203], 0\n"
"v_mfma_f32_16x16x32_bf16 a[124:127], a[208:211], v[204:207], 0\n"
"s_mov_b32 s32, 8192\n"
"buffer_load_dwordx4 v[156:159], v28, s[80:83], s32 offen offset:0 sc1\n"
"v_mfma_f32_16x16x32_bf16 a[128:131], a[212:215], v[192:195], 0\n"
"v_mfma_f32_16x16x32_bf16 a[132:135], a[212:215], v[196:199], 0\n"
"buffer_load_dwordx4 v[160:163], v28, s[80:83], s32 offen offset:1024 sc1\n"
"v_mfma_f32_16x16x32_bf16 a[136:139], a[212:215], v[200:203], 0\n"
"v_mfma_f32_16x16x32_bf16 a[140:143], a[212:215], v[204:207], 0\n"
"v_mfma_f32_16x16x32_bf16 a[144:147], a[216:219], v[192:195], 0\n"
"v_mfma_f32_16x16x32_bf16 a[148:151], a[216:219], v[196:199], 0\n"
"buffer_load_dwordx4 v[164:167], v28, s[80:83], s32 offen offset:2048 sc1\n"
"v_mfma_f32_16x16x32_bf16 a[152:155], a[216:219], v[200:203], 0\n"
"v_mfma_f32_16x16x32_bf16 a[156:159], a[216:219], v[204:207], 0\n"
"v_mfma_f32_16x16x32_bf16 a[80:83], a[220:223], v[208:211], a[80:83]\n"
"v_mfma_f32_16x16x32_bf16 a[84:87], a[220:223], v[212:215], a[84:87]\n"
"buffer_load_dwordx4 v[168:171], v28, s[80:83], s32 offen offset:3072 sc1\n"
"v_mfma_f32_16x16x32_bf16 a[88:91], a[220:223], v[216:219], a[88:91]\n"
"v_mfma_f32_16x16x32_bf16 a[92:95], a[220:223], v[220:223], a[92:95]\n"
"buffer_load_dwordx4 v[172:175], v28, s[76:79], s33 offen offset:2048 sc1\n"
"v_mfma_f32_16x16x32_bf16 a[96:99], a[224:227], v[208:211], a[96:99]\n"
"v_mfma_f32_16x16x32_bf16 a[100:103], a[224:227], v[212:215], a[100:103]\n"
"buffer_load_dwordx4 v[176:179], v28, s[76:79], s33 offen offset:3072 sc1\n"
"v_mfma_f32_16x16x32_bf16 a[104:107], a[224:227], v[216:219], a[104:107]\n"
"v_mfma_f32_16x16x32_bf16 a[108:111], a[224:227], v[220:223], a[108:111]\n"
"v_mfma_f32_16x16x32_bf16 a[112:115], a[228:231], v[208:211], a[112:115]\n"
"v_mfma_f32_16x16x32_bf16 a[116:119], a[228:231], v[212:215], a[116:119]\n"
"buffer_load_dwordx4 v[180:183], v25, s[76:79], s33 offen offset:3072 sc1\n"
"v_mfma_f32_16x16x32_bf16 a[120:123], a[228:231], v[216:219], a[120:123]\n"
"v_mfma_f32_16x16x32_bf16 a[124:127], a[228:231], v[220:223], a[124:127]\n"
"s_mov_b32 s33, 368640\n"
"buffer_load_dwordx4 v[184:187], v28, s[76:79], s33 offen offset:0 sc1\n"
"v_mfma_f32_16x16x32_bf16 a[128:131], a[232:235], v[208:211], a[128:131]\n"
"v_mfma_f32_16x16x32_bf16 a[132:135], a[232:235], v[212:215], a[132:135]\n"
"buffer_load_dwordx4 v[188:191], v28, s[76:79], s33 offen offset:1024 sc1\n"
"v_mfma_f32_16x16x32_bf16 a[136:139], a[232:235], v[216:219], a[136:139]\n"
"v_mfma_f32_16x16x32_bf16 a[140:143], a[232:235], v[220:223], a[140:143]\n"
"v_mfma_f32_16x16x32_bf16 a[144:147], a[236:239], v[208:211], a[144:147]\n"
"s_waitcnt vmcnt(32)\n"
"v_mfma_f32_16x16x32_bf16 a[148:151], a[236:239], v[212:215], a[148:151]\n"
"buffer_load_dwordx4 v[192:195], v28, s[76:79], s33 offen offset:2048 sc1\n"
"v_mfma_f32_16x16x32_bf16 a[152:155], a[236:239], v[216:219], a[152:155]\n"
"v_mfma_f32_16x16x32_bf16 a[156:159], a[236:239], v[220:223], a[156:159]\n"
"v_mfma_f32_16x16x32_bf16 a[80:83], a[240:243], a[160:163], a[80:83]\n"
"v_mfma_f32_16x16x32_bf16 a[84:87], a[240:243], a[164:167], a[84:87]\n"
"buffer_load_dwordx4 v[196:199], v28, s[76:79], s33 offen offset:3072 sc1\n"
"v_mfma_f32_16x16x32_bf16 a[88:91], a[240:243], a[168:171], a[88:91]\n"
"v_mfma_f32_16x16x32_bf16 a[92:95], a[240:243], a[172:175], a[92:95]\n"
"buffer_load_dwordx4 v[200:203], v25, s[76:79], s33 offen offset:3072 sc1\n"
"v_mfma_f32_16x16x32_bf16 a[96:99], a[244:247], a[160:163], a[96:99]\n"
"v_mfma_f32_16x16x32_bf16 a[100:103], a[244:247], a[164:167], a[100:103]\n"
"s_mov_b32 s33, 409600\n"
"buffer_load_dwordx4 v[204:207], v28, s[76:79], s33 offen offset:0 sc1\n"
"v_mfma_f32_16x16x32_bf16 a[104:107], a[244:247], a[168:171], a[104:107]\n"
"s_load_dwordx2 s[20:21], s[4:5], 0x0\n"
"s_load_dwordx4 s[88:91], s[4:5], 0x60\n"
"v_mfma_f32_16x16x32_bf16 a[108:111], a[244:247], a[172:175], a[108:111]\n"
"s_mov_b32 s70, 32768\n"
"buffer_load_dwordx2 v[42:43], v3, s[64:67], s70 offen sc1\n"
"v_mfma_f32_16x16x32_bf16 a[112:115], a[248:251], a[160:163], a[112:115]\n"
"s_and_b32 s68, s6, 1\n"
"s_lshl_b32 s69, s30, 1\n"
"v_mfma_f32_16x16x32_bf16 a[116:119], a[248:251], a[164:167], a[116:119]\n"
"buffer_load_dwordx4 v[208:211], v28, s[76:79], s33 offen offset:1024 sc1\n"
"v_mfma_f32_16x16x32_bf16 a[120:123], a[248:251], a[168:171], a[120:123]\n"
"s_add_u32 s68, s68, s69\n"
"s_lshl_b32 s68, s68, 2\n"
"v_mfma_f32_16x16x32_bf16 a[124:127], a[248:251], a[172:175], a[124:127]\n"
"buffer_load_dwordx4 v[212:215], v28, s[76:79], s33 offen offset:2048 sc1\n"
"v_mfma_f32_16x16x32_bf16 a[128:131], a[252:255], a[160:163], a[128:131]\n"
"s_add_u32 s68, s68, s7\n"
"s_lshl_b32 s100, s68, 8\n"
"v_mfma_f32_16x16x32_bf16 a[132:135], a[252:255], a[164:167], a[132:135]\n"
"buffer_load_dwordx4 v[216:219], v28, s[76:79], s33 offen offset:3072 sc1\n"
"v_mfma_f32_16x16x32_bf16 a[136:139], a[252:255], a[168:171], a[136:139]\n"
"s_mul_i32 s69, s68, 20480\n"
"s_mul_i32 s68, s31, 160\n"
"v_mfma_f32_16x16x32_bf16 a[140:143], a[252:255], a[172:175], a[140:143]\n"
"s_add_u32 s69, s69, s68\n"
"v_mfma_f32_16x16x32_bf16 a[144:147], a[192:195], a[160:163], a[144:147]\n"
"s_waitcnt vmcnt(36)\n"
"v_mfma_f32_16x16x32_bf16 a[148:151], a[192:195], a[164:167], a[148:151]\n"
"buffer_load_dwordx4 v[220:223], v25, s[76:79], s33 offen offset:3072 sc1\n"
"v_mfma_f32_16x16x32_bf16 a[152:155], a[192:195], a[168:171], a[152:155]\n"
"v_mfma_f32_16x16x32_bf16 a[156:159], a[192:195], a[172:175], a[156:159]\n"
"v_mfma_f32_16x16x32_bf16 a[0:3], a[200:203], a[176:179], 0\n"
"v_mfma_f32_16x16x32_bf16 a[4:7], a[200:203], a[180:183], 0\n"
"s_mov_b32 s33, 450560\n"
"buffer_load_dwordx4 a[160:163], v28, s[76:79], s33 offen offset:0 sc1\n"
"v_mfma_f32_16x16x32_bf16 a[8:11], a[200:203], a[184:187], 0\n"
"v_mfma_f32_16x16x32_bf16 a[12:15], a[200:203], a[188:191], 0\n"
"buffer_load_dwordx4 a[164:167], v28, s[76:79], s33 offen offset:1024 sc1\n"
"s_waitcnt lgkmcnt(0)\n"
"v_mfma_f32_16x16x32_bf16 a[16:19], a[204:207], a[176:179], 0\n"
"s_add_u32 s48, s20, s69\n"
"s_addc_u32 s49, s21, 0\n"
"v_mfma_f32_16x16x32_bf16 a[20:23], a[204:207], a[180:183], 0\n"
"buffer_load_dwordx4 a[168:171], v28, s[76:79], s33 offen offset:2048 sc1\n"
"v_mfma_f32_16x16x32_bf16 a[24:27], a[204:207], a[184:187], 0\n"
"s_and_b32 s49, s49, 0xffff\n"
"s_mov_b32 s50, 19360\n"
"v_mfma_f32_16x16x32_bf16 a[28:31], a[204:207], a[188:191], 0\n"
"s_mov_b32 s51, 0x20000\n"
"s_lshl_b32 s69, s69, 1\n"
"v_mfma_f32_16x16x32_bf16 a[32:35], a[208:211], a[176:179], 0\n"
"s_add_u32 s52, s90, s69\n"
"s_addc_u32 s53, s91, 0\n"
"v_mfma_f32_16x16x32_bf16 a[36:39], a[208:211], a[180:183], 0\n"
"buffer_load_dwordx4 a[172:175], v28, s[76:79], s33 offen offset:3072 sc1\n"
"v_mfma_f32_16x16x32_bf16 a[40:43], a[208:211], a[184:187], 0\n"
"s_and_b32 s53, s53, 0xffff\n"
"s_mov_b32 s54, 38720\n"
"v_mfma_f32_16x16x32_bf16 a[44:47], a[208:211], a[188:191], 0\n"
"buffer_load_dwordx4 a[200:203], v25, s[76:79], s33 offen offset:3072 sc1\n"
"v_mfma_f32_16x16x32_bf16 a[48:51], a[212:215], a[176:179], 0\n"
"s_mov_b32 s55, 0x20000\n"
"s_add_u32 s24, s88, s68\n"
"s_addc_u32 s25, s89, 0\n"
"v_mfma_f32_16x16x32_bf16 a[52:55], a[212:215], a[180:183], 0\n"
"s_and_b32 s25, s25, 0xffff\n"
"s_mov_b32 s26, 160\n"
"v_mfma_f32_16x16x32_bf16 a[56:59], a[212:215], a[184:187], 0\n"
"s_mov_b32 s27, 0x20000\n"
"s_add_u32 s69, s100, 20971520\n"
"v_mfma_f32_16x16x32_bf16 a[60:63], a[212:215], a[188:191], 0\n"
"s_add_u32 s16, s38, s69\n"
"s_addc_u32 s17, s39, 0\n"
"v_mfma_f32_16x16x32_bf16 a[64:67], a[216:219], a[176:179], 0\n"
"s_waitcnt vmcnt(23)\n"
"s_and_b32 s17, s17, 0xffff\n"
"v_mfma_f32_16x16x32_bf16 a[68:71], a[216:219], a[180:183], 0\n"
"s_mov_b32 s18, 256\n"
"s_mov_b32 s19, 0x20000\n"
"v_mfma_f32_16x16x32_bf16 a[72:75], a[216:219], a[184:187], 0\n"
"v_and_b32 v76, 3, v27\n"
"v_bfe_u32 v77, v27, 3, 1\n"
"v_lshl_add_u32 v76, v77, 2, v76\n"
"v_mfma_f32_16x16x32_bf16 a[76:79], a[216:219], a[188:191], 0\n"
"s_barrier\n"
"v_bfe_u32 v77, v27, 2, 1\n"
"v_mfma_f32_16x16x32_bf16 a[0:3], a[220:223], v[140:143], a[0:3]\n"
"s_waitcnt vmcnt(9)\n"
"v_xor_b32 v42, s72, v42\n"
"v_xor_b32 v43, s73, v43\n"
"v_or_b32 v42, v42, v43\n"
"v_cmp_ne_u32 vcc, 0, v42\n"
"s_nop 0\n"
"s_cmp_lg_u64 vcc, 0\n"
"s_cbranch_scc1 K1F_r1_slow\n"
"K1F_r1_back:\n"
"v_mfma_f32_16x16x32_bf16 a[4:7], a[220:223], v[144:147], a[4:7]\n"
"s_mov_b32 s32, 12288\n"
"buffer_load_dwordx4 a[204:207], v28, s[84:87], s32 offen offset:0 sc1\n"
"v_mfma_f32_16x16x32_bf16 a[8:11], a[220:223], v[148:151], a[8:11]\n"
"v_lshl_add_u32 v76, v77, 4, v76\n"
"v_mul_u32_u24 v76, 320, v76\n"
"v_bfe_u32 v78, v27, 1, 2\n"
"v_mfma_f32_16x16x32_bf16 a[12:15], a[220:223], v[152:155], a[12:15]\n"
"v_lshrrev_b32 v77, 4, v27\n"
"v_xor_b32 v78, v78, v77\n"
"v_lshl_add_u32 v76, v78, 4, v76\n"
"v_mfma_f32_16x16x32_bf16 a[16:19], a[224:227], v[140:143], a[16:19]\n"
"s_mul_i32 s68, s7, 20480\n"
"v_add_u32 v2, s68, v76\n"
"v_mfma_f32_16x16x32_bf16 a[20:23], a[224:227], v[144:147], a[20:23]\n"
"buffer_load_dwordx4 a[208:211], v28, s[84:87], s32 offen offset:1024 sc1\n"
"v_mfma_f32_16x16x32_bf16 a[24:27], a[224:227], v[148:151], a[24:27]\n"
"v_add_u32 v1, 81920, v2\n"
"s_mul_i32 s68, s7, 5120\n"
"v_mfma_f32_16x16x32_bf16 a[28:31], a[224:227], v[152:155], a[28:31]\n"
"s_and_b32 s69, s7, 1\n"
"s_lshl_b32 s69, s69, 1\n"
"v_mfma_f32_16x16x32_bf16 a[32:35], a[228:231], v[140:143], a[32:35]\n"
"v_add_u32 v76, 0, v27\n"
"v_mul_u32_u24 v77, 3277, v76\n"
"v_lshrrev_b32 v77, 16, v77\n"
"v_mfma_f32_16x16x32_bf16 a[36:39], a[228:231], v[144:147], a[36:39]\n"
"buffer_load_dwordx4 a[212:215], v28, s[84:87], s32 offen offset:2048 sc1\n"
"v_mfma_f32_16x16x32_bf16 a[40:43], a[228:231], v[148:151], a[40:43]\n"
"v_mul_u32_u24 v78, 20, v77\n"
"v_sub_u32 v78, v76, v78\n"
"v_mul_u32_u24 v76, 1280, v77\n"
"v_mfma_f32_16x16x32_bf16 a[44:47], a[228:231], v[152:155], a[44:47]\n"
"v_lshl_add_u32 v10, v78, 3, v76\n"
"v_bfe_u32 v76, v77, 1, 1\n"
"v_or_b32 v76, s69, v76\n"
"v_mfma_f32_16x16x32_bf16 a[48:51], a[232:235], v[140:143], a[48:51]\n"
"v_xor_b32 v78, v78, v76\n"
"v_mul_u32_u24 v76, 320, v77\n"
"v_lshl_add_u32 v76, v78, 4, v76\n"
"v_mfma_f32_16x16x32_bf16 a[52:55], a[232:235], v[144:147], a[52:55]\n"
"buffer_load_dwordx4 a[216:219], v28, s[84:87], s32 offen offset:3072 sc1\n"
"v_mfma_f32_16x16x32_bf16 a[56:59], a[232:235], v[148:151], a[56:59]\n"
"v_add_u32 v4, s68, v76\n"
"v_add_u32 v76, 64, v27\n"
"v_mul_u32_u24 v77, 3277, v76\n"
"v_mfma_f32_16x16x32_bf16 a[60:63], a[232:235], v[152:155], a[60:63]\n"
"v_lshrrev_b32 v77, 16, v77\n"
"v_mul_u32_u24 v78, 20, v77\n"
"v_sub_u32 v78, v76, v78\n"
"v_mfma_f32_16x16x32_bf16 a[64:67], a[236:239], v[140:143], a[64:67]\n"
"v_mul_u32_u24 v76, 1280, v77\n"
"v_lshl_add_u32 v11, v78, 3, v76\n"
"v_mfma_f32_16x16x32_bf16 a[68:71], a[236:239], v[144:147], a[68:71]\n"
"s_mov_b32 s32, 16384\n"
"buffer_load_dwordx4 a[176:179], v28, s[84:87], s32 offen offset:0 sc1\n"
"v_mfma_f32_16x16x32_bf16 a[72:75], a[236:239], v[148:151], a[72:75]\n"
"v_bfe_u32 v76, v77, 1, 1\n"
"v_or_b32 v76, s69, v76\n"
"v_xor_b32 v78, v78, v76\n"
"v_mfma_f32_16x16x32_bf16 a[76:79], a[236:239], v[152:155], a[76:79]\n"
"v_mul_u32_u24 v76, 320, v77\n"
"v_lshl_add_u32 v76, v78, 4, v76\n"
"v_add_u32 v5, s68, v76\n"
"v_mfma_f32_16x16x32_bf16 a[0:3], a[240:243], v[156:159], a[0:3]\n"
"v_add_u32 v76, 128, v27\n"
"v_mul_u32_u24 v77, 3277, v76\n"
"v_lshrrev_b32 v77, 16, v77\n"
"v_mfma_f32_16x16x32_bf16 a[4:7], a[240:243], v[160:163], a[4:7]\n"
"buffer_load_dwordx4 a[180:183], v28, s[84:87], s32 offen offset:1024 sc1\n"
"v_mfma_f32_16x16x32_bf16 a[8:11], a[240:243], v[164:167], a[8:11]\n"
"v_mul_u32_u24 v78, 20, v77\n"
"v_sub_u32 v78, v76, v78\n"
"v_mul_u32_u24 v76, 1280, v77\n"
"v_mfma_f32_16x16x32_bf16 a[12:15], a[240:243], v[168:171], a[12:15]\n"
"v_lshl_add_u32 v12, v78, 3, v76\n"
"v_bfe_u32 v76, v77, 1, 1\n"
"v_or_b32 v76, s69, v76\n"
"v_mfma_f32_16x16x32_bf16 a[16:19], a[244:247], v[156:159], a[16:19]\n"
"v_xor_b32 v78, v78, v76\n"
"v_mul_u32_u24 v76, 320, v77\n"
"v_lshl_add_u32 v76, v78, 4, v76\n"
"v_mfma_f32_16x16x32_bf16 a[20:23], a[244:247], v[160:163], a[20:23]\n"
"buffer_load_dwordx4 a[184:187], v28, s[84:87], s32 offen offset:2048 sc1\n"
"v_mfma_f32_16x16x32_bf16 a[24:27], a[244:247], v[164:167], a[24:27]\n"
"v_add_u32 v6, s68, v76\n"
"v_add_u32 v76, 192, v27\n"
"v_mul_u32_u24 v77, 3277, v76\n"
"v_mfma_f32_16x16x32_bf16 a[28:31], a[244:247], v[168:171], a[28:31]\n"
"v_lshrrev_b32 v77, 16, v77\n"
"v_mul_u32_u24 v78, 20, v77\n"
"v_sub_u32 v78, v76, v78\n"
"v_mfma_f32_16x16x32_bf16 a[32:35], a[248:251], v[156:159], a[32:35]\n"
"v_mul_u32_u24 v76, 1280, v77\n"
"v_lshl_add_u32 v13, v78, 3, v76\n"
"v_bfe_u32 v76, v77, 1, 1\n"
"v_mfma_f32_16x16x32_bf16 a[36:39], a[248:251], v[160:163], a[36:39]\n"
"buffer_load_dwordx4 a[188:191], v28, s[84:87], s32 offen offset:3072 sc1\n"
"v_mfma_f32_16x16x32_bf16 a[40:43], a[248:251], v[164:167], a[40:43]\n"
"v_or_b32 v76, s69, v76\n"
"v_xor_b32 v78, v78, v76\n"
"v_mul_u32_u24 v76, 320, v77\n"
"v_mfma_f32_16x16x32_bf16 a[44:47], a[248:251], v[168:171], a[44:47]\n"
"v_lshl_add_u32 v76, v78, 4, v76\n"
"v_add_u32 v7, s68, v76\n"
"v_add_u32 v76, 256, v27\n"
"v_mfma_f32_16x16x32_bf16 a[48:51], a[252:255], v[156:159], a[48:51]\n"
"v_mul_u32_u24 v77, 3277, v76\n"
"v_lshrrev_b32 v77, 16, v77\n"
"v_mul_u32_u24 v78, 20, v77\n"
"v_mfma_f32_16x16x32_bf16 a[52:55], a[252:255], v[160:163], a[52:55]\n"
"s_mov_b32 s32, 20480\n"
"buffer_load_dwordx4 a[220:223], v28, s[84:87], s32 offen offset:0 sc1\n"
"v_mfma_f32_16x16x32_bf16 a[56:59], a[252:255], v[164:167], a[56:59]\n"
"v_sub_u32 v78, v76, v78\n"
"v_mul_u32_u24 v76, 1280, v77\n"
"v_lshl_add_u32 v14, v78, 3, v76\n"
"v_mfma_f32_16x16x32_bf16 a[60:63], a[252:255], v[168:171], a[60:63]\n"
"v_bfe_u32 v76, v77, 1, 1\n"
"v_or_b32 v76, s69, v76\n"
"v_xor_b32 v78, v78, v76\n"
"v_mfma_f32_16x16x32_bf16 a[64:67], a[192:195], v[156:159], a[64:67]\n"
"s_waitcnt vmcnt(5)\n"
"v_mul_u32_u24 v76, 320, v77\n"
"v_lshl_add_u32 v76, v78, 4, v76\n"
"v_mfma_f32_16x16x32_bf16 a[68:71], a[192:195], v[160:163], a[68:71]\n"
"buffer_load_dwordx4 a[224:227], v28, s[84:87], s32 offen offset:1024 sc1\n"
"v_mfma_f32_16x16x32_bf16 a[72:75], a[192:195], v[164:167], a[72:75]\n"
"v_add_u32 v8, s68, v76\n"
"s_and_b32 s68, s72, 0xfff\n"
"v_mfma_f32_16x16x32_bf16 a[76:79], a[192:195], v[168:171], a[76:79]\n"
"v_mov_b32 v20, s68\n"
"v_mov_b32 v21, 0xfffff000\n"
"v_add_u32 v4, 81920, v4\n"
"v_mfma_f32_16x16x32_bf16 a[80:83], a[196:199], a[204:207], a[80:83]\n"
"v_add_u32 v5, 81920, v5\n"
"v_add_u32 v6, 81920, v6\n"
"v_add_u32 v7, 81920, v7\n"
"v_mfma_f32_16x16x32_bf16 a[84:87], a[196:199], a[208:211], a[84:87]\n"
"buffer_load_dwordx4 a[228:231], v28, s[84:87], s32 offen offset:2048 sc1\n"
"v_mfma_f32_16x16x32_bf16 a[88:91], a[196:199], a[212:215], a[88:91]\n"
"v_add_u32 v8, 81920, v8\n"
"v_mfma_f32_16x16x32_bf16 a[92:95], a[196:199], a[216:219], a[92:95]\n"
"v_mfma_f32_16x16x32_bf16 a[96:99], v[80:83], a[204:207], a[96:99]\n"
"v_mfma_f32_16x16x32_bf16 a[100:103], v[80:83], a[208:211], a[100:103]\n"
"buffer_load_dwordx4 a[232:235], v28, s[84:87], s32 offen offset:3072 sc1\n"
"v_mfma_f32_16x16x32_bf16 a[104:107], v[80:83], a[212:215], a[104:107]\n"
"v_mfma_f32_16x16x32_bf16 a[108:111], v[80:83], a[216:219], a[108:111]\n"
"v_mfma_f32_16x16x32_bf16 a[112:115], v[84:87], a[204:207], a[112:115]\n"
"v_mfma_f32_16x16x32_bf16 a[116:119], v[84:87], a[208:211], a[116:119]\n"
"s_mov_b32 s32, 24576\n"
"buffer_load_dwordx4 a[236:239], v28, s[84:87], s32 offen offset:0 sc1\n"
"v_mfma_f32_16x16x32_bf16 a[120:123], v[84:87], a[212:215], a[120:123]\n"
"v_mfma_f32_16x16x32_bf16 a[124:127], v[84:87], a[216:219], a[124:127]\n"
"v_mfma_f32_16x16x32_bf16 a[128:131], v[88:91], a[204:207], a[128:131]\n"
"v_mfma_f32_16x16x32_bf16 a[132:135], v[88:91], a[208:211], a[132:135]\n"
"buffer_load_dwordx4 v[140:143], v28, s[84:87], s32 offen offset:1024 sc1\n"
"v_mfma_f32_16x16x32_bf16 a[136:139], v[88:91], a[212:215], a[136:139]\n"
"v_mfma_f32_16x16x32_bf16 a[140:143], v[88:91], a[216:219], a[140:143]\n"
"v_mfma_f32_16x16x32_bf16 a[144:147], v[92:95], a[204:207], a[144:147]\n"
"s_waitcnt vmcnt(6)\n"
"v_mfma_f32_16x16x32_bf16 a[148:151], v[92:95], a[208:211], a[148:151]\n"
"buffer_load_dwordx4 v[144:147], v28, s[84:87], s32 offen offset:2048 sc1\n"
"v_mfma_f32_16x16x32_bf16 a[152:155], v[92:95], a[212:215], a[152:155]\n"
"v_mfma_f32_16x16x32_bf16 a[156:159], v[92:95], a[216:219], a[156:159]\n"
"v_mfma_f32_16x16x32_bf16 a[80:83], v[224:227], a[176:179], a[80:83]\n"
"v_mfma_f32_16x16x32_bf16 a[84:87], v[224:227], a[180:183], a[84:87]\n"
"buffer_load_dwordx4 v[148:151], v28, s[84:87], s32 offen offset:3072 sc1\n"
"v_mfma_f32_16x16x32_bf16 a[88:91], v[224:227], a[184:187], a[88:91]\n"
"v_mfma_f32_16x16x32_bf16 a[92:95], v[224:227], a[188:191], a[92:95]\n"
"v_mfma_f32_16x16x32_bf16 a[96:99], v[228:231], a[176:179], a[96:99]\n"
"v_mfma_f32_16x16x32_bf16 a[100:103], v[228:231], a[180:183], a[100:103]\n"
"s_mov_b32 s32, 28672\n"
"buffer_load_dwordx4 v[152:155], v28, s[84:87], s32 offen offset:0 sc1\n"
"v_mfma_f32_16x16x32_bf16 a[104:107], v[228:231], a[184:187], a[104:107]\n"
"v_mfma_f32_16x16x32_bf16 a[108:111], v[228:231], a[188:191], a[108:111]\n"
"s_mov_b32 s70, 65536\n"
"buffer_load_dwordx2 v[42:43], v3, s[64:67], s70 offen sc1\n"
"v_mfma_f32_16x16x32_bf16 a[112:115], v[232:235], a[176:179], a[112:115]\n"
"v_mfma_f32_16x16x32_bf16 a[116:119], v[232:235], a[180:183], a[116:119]\n"
"buffer_load_dwordx4 a[240:243], v28, s[84:87], s32 offen offset:1024 sc1\n"
"v_mfma_f32_16x16x32_bf16 a[120:123], v[232:235], a[184:187], a[120:123]\n"
"v_mfma_f32_16x16x32_bf16 a[124:127], v[232:235], a[188:191], a[124:127]\n"
"v_mfma_f32_16x16x32_bf16 a[128:131], v[236:239], a[176:179], a[128:131]\n"
"v_mfma_f32_16x16x32_bf16 a[132:135], v[236:239], a[180:183], a[132:135]\n"
"buffer_load_dwordx4 a[244:247], v28, s[84:87], s32 offen offset:2048 sc1\n"
"v_mfma_f32_16x16x32_bf16 a[136:139], v[236:239], a[184:187], a[136:139]\n"
"v_mfma_f32_16x16x32_bf16 a[140:143], v[236:239], a[188:191], a[140:143]\n"
"v_mfma_f32_16x16x32_bf16 a[144:147], v[240:243], a[176:179], a[144:147]\n"
"s_waitcnt vmcnt(8)\n"
"v_mfma_f32_16x16x32_bf16 a[148:151], v[240:243], a[180:183], a[148:151]\n"
"buffer_load_dwordx4 a[248:251], v28, s[84:87], s32 offen offset:3072 sc1\n"
"v_mfma_f32_16x16x32_bf16 a[152:155], v[240:243], a[184:187], a[152:155]\n"
"v_mfma_f32_16x16x32_bf16 a[156:159], v[240:243], a[188:191], a[156:159]\n"
"v_mfma_f32_16x16x32_bf16 a[80:83], v[244:247], a[220:223], a[80:83]\n"
"v_mfma_f32_16x16x32_bf16 a[84:87], v[244:247], a[224:227], a[84:87]\n"
"s_mov_b32 s32, 32768\n"
"buffer_load_dwordx4 a[252:255], v28, s[84:87], s32 offen offset:0 sc1\n"
"v_mfma_f32_16x16x32_bf16 a[88:91], v[244:247], a[228:231], a[88:91]\n"
"v_mfma_f32_16x16x32_bf16 a[92:95], v[244:247], a[232:235], a[92:95]\n"
"v_mfma_f32_16x16x32_bf16 a[96:99], v[248:251], a[220:223], a[96:99]\n"
"v_mfma_f32_16x16x32_bf16 a[100:103], v[248:251], a[224:227], a[100:103]\n"
"buffer_load_dwordx4 a[192:195], v28, s[84:87], s32 offen offset:1024 sc1\n"
"v_mfma_f32_16x16x32_bf16 a[104:107], v[248:251], a[228:231], a[104:107]\n"
"v_mfma_f32_16x16x32_bf16 a[108:111], v[248:251], a[232:235], a[108:111]\n"
"v_mfma_f32_16x16x32_bf16 a[112:115], v[252:255], a[220:223], a[112:115]\n"
"v_mfma_f32_16x16x32_bf16 a[116:119], v[252:255], a[224:227], a[116:119]\n"
"buffer_load_dwordx4 v[156:159], v28, s[84:87], s32 offen offset:2048 sc1\n"
"v_mfma_f32_16x16x32_bf16 a[120:123], v[252:255], a[228:231], a[120:123]\n"
"v_mfma_f32_16x16x32_bf16 a[124:127], v[252:255], a[232:235], a[124:127]\n"
"v_mfma_f32_16x16x32_bf16 a[128:131], v[44:47], a[220:223], a[128:131]\n"
"v_mfma_f32_16x16x32_bf16 a[132:135], v[44:47], a[224:227], a[132:135]\n"
"buffer_load_dwordx4 v[160:163], v28, s[84:87], s32 offen offset:3072 sc1\n"
"v_mfma_f32_16x16x32_bf16 a[136:139], v[44:47], a[228:231], a[136:139]\n"
"v_mfma_f32_16x16x32_bf16 a[140:143], v[44:47], a[232:235], a[140:143]\n"
"v_mfma_f32_16x16x32_bf16 a[144:147], v[48:51], a[220:223], a[144:147]\n"
"s_waitcnt vmcnt(9)\n"
"v_mfma_f32_16x16x32_bf16 a[148:151], v[48:51], a[224:227], a[148:151]\n"
"v_mfma_f32_16x16x32_bf16 a[152:155], v[48:51], a[228:231], a[152:155]\n"
"v_mfma_f32_16x16x32_bf16 a[156:159], v[48:51], a[232:235], a[156:159]\n"
"v_mfma_f32_16x16x32_bf16 a[80:83], v[52:55], a[236:239], a[80:83]\n"
"s_waitcnt vmcnt(7)\n"
"v_xor_b32 v42, s72, v42\n"
"v_xor_b32 v43, s73, v43\n"
"v_or_b32 v42, v42, v43\n"
"v_cmp_ne_u32 vcc, 0, v42\n"
"s_nop 0\n"
"s_cmp_lg_u64 vcc, 0\n"
"s_cbranch_scc1 K1F_r2_slow\n"
"K1F_r2_back:\n"
"v_mfma_f32_16x16x32_bf16 a[84:87], v[52:55], v[140:143], a[84:87]\n"
"s_mov_b32 s32, 36864\n"
"buffer_load_dwordx4 v[164:167], v28, s[84:87], s32 offen offset:0 sc1\n"
"v_mfma_f32_16x16x32_bf16 a[88:91], v[52:55], v[144:147], a[88:91]\n"
"v_mfma_f32_16x16x32_bf16 a[92:95], v[52:55], v[148:151], a[92:95]\n"
"v_mfma_f32_16x16x32_bf16 a[96:99], v[56:59], a[236:239], a[96:99]\n"
"v_mfma_f32_16x16x32_bf16 a[100:103], v[56:59], v[140:143], a[100:103]\n"
"buffer_load_dwordx4 v[168:171], v28, s[84:87], s32 offen offset:1024 sc1\n"
"v_mfma_f32_16x16x32_bf16 a[104:107], v[56:59], v[144:147], a[104:107]\n"
"v_mfma_f32_16x16x32_bf16 a[108:111], v[56:59], v[148:151], a[108:111]\n"
"v_mfma_f32_16x16x32_bf16 a[112:115], v[60:63], a[236:239], a[112:115]\n"
"v_mfma_f32_16x16x32_bf16 a[116:119], v[60:63], v[140:143], a[116:119]\n"
"buffer_load_dwordx4 a[204:207], v28, s[84:87], s32 offen offset:2048 sc1\n"
"v_mfma_f32_16x16x32_bf16 a[120:123], v[60:63], v[144:147], a[120:123]\n"
"v_mfma_f32_16x16x32_bf16 a[124:127], v[60:63], v[148:151], a[124:127]\n"
"v_mfma_f32_16x16x32_bf16 a[128:131], v[64:67], a[236:239], a[128:131]\n"
"v_mfma_f32_16x16x32_bf16 a[132:135], v[64:67], v[140:143], a[132:135]\n"
"buffer_load_dwordx4 a[208:211], v28, s[84:87], s32 offen offset:3072 sc1\n"
"v_mfma_f32_16x16x32_bf16 a[136:139], v[64:67], v[144:147], a[136:139]\n"
"v_mfma_f32_16x16x32_bf16 a[140:143], v[64:67], v[148:151], a[140:143]\n"
"v_mfma_f32_16x16x32_bf16 a[144:147], v[68:71], a[236:239], a[144:147]\n"
"s_waitcnt vmcnt(8)\n"
"v_mfma_f32_16x16x32_bf16 a[148:151], v[68:71], v[140:143], a[148:151]\n"
"s_mov_b32 s32, 40960\n"
"buffer_load_dwordx4 a[212:215], v28, s[84:87], s32 offen offset:0 sc1\n"
"v_mfma_f32_16x16x32_bf16 a[152:155], v[68:71], v[144:147], a[152:155]\n"
"v_mfma_f32_16x16x32_bf16 a[156:159], v[68:71], v[148:151], a[156:159]\n"
"v_mfma_f32_16x16x32_bf16 a[80:83], v[72:75], v[152:155], a[80:83]\n"
"v_mfma_f32_16x16x32_bf16 a[84:87], v[72:75], a[240:243], a[84:87]\n"
"buffer_load_dwordx4 a[216:219], v28, s[84:87], s32 offen offset:1024 sc1\n"
"v_mfma_f32_16x16x32_bf16 a[88:91], v[72:75], a[244:247], a[88:91]\n"
"v_mfma_f32_16x16x32_bf16 a[92:95], v[72:75], a[248:251], a[92:95]\n"
"v_mfma_f32_16x16x32_bf16 a[96:99], v[116:119], v[152:155], a[96:99]\n"
"v_mfma_f32_16x16x32_bf16 a[100:103], v[116:119], a[240:243], a[100:103]\n"
"buffer_load_dwordx4 a[176:179], v28, s[84:87], s32 offen offset:2048 sc1\n"
"v_mfma_f32_16x16x32_bf16 a[104:107], v[116:119], a[244:247], a[104:107]\n"
"v_mfma_f32_16x16x32_bf16 a[108:111], v[116:119], a[248:251], a[108:111]\n"
"v_mfma_f32_16x16x32_bf16 a[112:115], v[120:123], v[152:155], a[112:115]\n"
"v_mfma_f32_16x16x32_bf16 a[116:119], v[120:123], a[240:243], a[116:119]\n"
"buffer_load_dwordx4 a[180:183], v28, s[84:87], s32 offen offset:3072 sc1\n"
"v_mfma_f32_16x16x32_bf16 a[120:123], v[120:123], a[244:247], a[120:123]\n"
"v_mfma_f32_16x16x32_bf16 a[124:127], v[120:123], a[248:251], a[124:127]\n"
"v_mfma_f32_16x16x32_bf16 a[128:131], v[124:127], v[152:155], a[128:131]\n"
"v_mfma_f32_16x16x32_bf16 a[132:135], v[124:127], a[240:243], a[132:135]\n"
"s_mov_b32 s32, 45056\n"
"buffer_load_dwordx4 a[184:187], v28, s[84:87], s32 offen offset:0 sc1\n"
"v_mfma_f32_16x16x32_bf16 a[136:139], v[124:127], a[244:247], a[136:139]\n"
"v_mfma_f32_16x16x32_bf16 a[140:143], v[124:127], a[248:251], a[140:143]\n"
"v_mfma_f32_16x16x32_bf16 a[144:147], v[128:131], v[152:155], a[144:147]\n"
"s_waitcnt vmcnt(9)\n"
"v_mfma_f32_16x16x32_bf16 a[148:151], v[128:131], a[240:243], a[148:151]\n"
"buffer_load_dwordx4 a[188:191], v28, s[84:87], s32 offen offset:1024 sc1\n"
"v_mfma_f32_16x16x32_bf16 a[152:155], v[128:131], a[244:247], a[152:155]\n"
"v_mfma_f32_16x16x32_bf16 a[156:159], v[128:131], a[248:251], a[156:159]\n"
"v_mfma_f32_16x16x32_bf16 a[80:83], v[132:135], a[252:255], a[80:83]\n"
"v_mfma_f32_16x16x32_bf16 a[84:87], v[132:135], a[192:195], a[84:87]\n"
"buffer_load_dwordx4 a[220:223], v28, s[84:87], s32 offen offset:2048 sc1\n"
"v_mfma_f32_16x16x32_bf16 a[88:91], v[132:135], v[156:159], a[88:91]\n"
"v_mfma_f32_16x16x32_bf16 a[92:95], v[132:135], v[160:163], a[92:95]\n"
"v_mfma_f32_16x16x32_bf16 a[96:99], v[136:139], a[252:255], a[96:99]\n"
"v_mfma_f32_16x16x32_bf16 a[100:103], v[136:139], a[192:195], a[100:103]\n"
"buffer_load_dwordx4 a[224:227], v28, s[84:87], s32 offen offset:3072 sc1\n"
"v_mfma_f32_16x16x32_bf16 a[104:107], v[136:139], v[156:159], a[104:107]\n"
"v_mfma_f32_16x16x32_bf16 a[108:111], v[136:139], v[160:163], a[108:111]\n"
"v_mfma_f32_16x16x32_bf16 a[112:115], v[172:175], a[252:255], a[112:115]\n"
"v_mfma_f32_16x16x32_bf16 a[116:119], v[172:175], a[192:195], a[116:119]\n"
"s_mov_b32 s32, 12288\n"
"buffer_load_dwordx4 a[228:231], v28, s[80:83], s32 offen offset:0 sc1\n"
"v_mfma_f32_16x16x32_bf16 a[120:123], v[172:175], v[156:159], a[120:123]\n"
"v_mfma_f32_16x16x32_bf16 a[124:127], v[172:175], v[160:163], a[124:127]\n"
"v_mfma_f32_16x16x32_bf16 a[128:131], v[176:179], a[252:255], a[128:131]\n"
"v_mfma_f32_16x16x32_bf16 a[132:135], v[176:179], a[192:195], a[132:135]\n"
"buffer_load_dwordx4 a[232:235], v28, s[80:83], s32 offen offset:1024 sc1\n"
"v_mfma_f32_16x16x32_bf16 a[136:139], v[176:179], v[156:159], a[136:139]\n"
"v_mfma_f32_16x16x32_bf16 a[140:143], v[176:179], v[160:163], a[140:143]\n"
"v_mfma_f32_16x16x32_bf16 a[144:147], v[180:183], a[252:255], a[144:147]\n"
"s_waitcnt vmcnt(10)\n"
"v_mfma_f32_16x16x32_bf16 a[148:151], v[180:183], a[192:195], a[148:151]\n"
"buffer_load_dwordx4 a[236:239], v28, s[80:83], s32 offen offset:2048 sc1\n"
"v_mfma_f32_16x16x32_bf16 a[152:155], v[180:183], v[156:159], a[152:155]\n"
"s_waitcnt vmcnt(3)\n"
"v_mfma_f32_16x16x32_bf16 a[156:159], v[180:183], v[160:163], a[156:159]\n"
"v_mfma_f32_16x16x32_bf16 a[80:83], v[184:187], v[164:167], a[80:83]\n"
"v_mfma_f32_16x16x32_bf16 a[84:87], v[184:187], v[168:171], a[84:87]\n"
"buffer_load_dwordx4 v[140:143], v28, s[80:83], s32 offen offset:3072 sc1\n"
"v_mfma_f32_16x16x32_bf16 a[88:91], v[184:187], a[204:207], a[88:91]\n"
"v_mfma_f32_16x16x32_bf16 a[92:95], v[184:187], a[208:211], a[92:95]\n"
"v_mfma_f32_16x16x32_bf16 a[80:83], v[204:207], a[212:215], a[80:83]\n"
"v_mfma_f32_16x16x32_bf16 a[84:87], v[204:207], a[216:219], a[84:87]\n"
"s_mov_b32 s32, 16384\n"
"buffer_load_dwordx4 v[144:147], v28, s[80:83], s32 offen offset:0 sc1\n"
"v_mfma_f32_16x16x32_bf16 a[88:91], v[204:207], a[176:179], a[88:91]\n"
"v_mfma_f32_16x16x32_bf16 a[92:95], v[204:207], a[180:183], a[92:95]\n"
"v_mfma_f32_16x16x32_bf16 a[80:83], a[160:163], a[184:187], a[80:83]\n"
"v_mfma_f32_16x16x32_bf16 a[84:87], a[160:163], a[188:191], a[84:87]\n"
"buffer_load_dwordx4 v[148:151], v28, s[80:83], s32 offen offset:1024 sc1\n"
"v_mfma_f32_16x16x32_bf16 a[88:91], a[160:163], a[220:223], a[88:91]\n"
"v_mfma_f32_16x16x32_bf16 a[92:95], a[160:163], a[224:227], a[92:95]\n"
"v_mfma_f32_16x16x32_bf16 a[96:99], v[188:191], v[164:167], a[96:99]\n"
"v_mfma_f32_16x16x32_bf16 a[100:103], v[188:191], v[168:171], a[100:103]\n"
"buffer_load_dwordx4 v[152:155], v28, s[80:83], s32 offen offset:2048 sc1\n"
"v_mfma_f32_16x16x32_bf16 a[104:107], v[188:191], a[204:207], a[104:107]\n"
"ds_write_b128 v1, a[80:83] offset:0\n"
"v_mfma_f32_16x16x32_bf16 a[108:111], v[188:191], a[208:211], a[108:111]\n"
"v_mfma_f32_16x16x32_bf16 a[96:99], v[208:211], a[212:215], a[96:99]\n"
"v_mfma_f32_16x16x32_bf16 a[100:103], v[208:211], a[216:219], a[100:103]\n"
"buffer_load_dwordx4 a[240:243], v28, s[80:83], s32 offen offset:3072 sc1\n"
"ds_write_b128 v1, a[84:87] offset:2560\n"
"v_mfma_f32_16x16x32_bf16 a[104:107], v[208:211], a[176:179], a[104:107]\n"
"v_mfma_f32_16x16x32_bf16 a[108:111], v[208:211], a[180:183], a[108:111]\n"
"v_mfma_f32_16x16x32_bf16 a[96:99], a[164:167], a[184:187], a[96:99]\n"
"ds_write_b128 v1, a[88:91] offset:10240\n"
"v_mfma_f32_16x16x32_bf16 a[100:103], a[164:167], a[188:191], a[100:103]\n"
"s_mov_b32 s32, 20480\n"
"buffer_load_dwordx4 a[244:247], v28, s[80:83], s32 offen offset:0 sc1\n"
"v_mfma_f32_16x16x32_bf16 a[104:107], a[164:167], a[220:223], a[104:107]\n"
"v_mfma_f32_16x16x32_bf16 a[108:111], a[164:167], a[224:227], a[108:111]\n"
"ds_write_b128 v1, a[92:95] offset:12800\n"
"v_mfma_f32_16x16x32_bf16 a[112:115], v[192:195], v[164:167], a[112:115]\n"
"v_mfma_f32_16x16x32_bf16 a[116:119], v[192:195], v[168:171], a[116:119]\n"
"buffer_load_dwordx4 a[248:251], v28, s[80:83], s32 offen offset:1024 sc1\n"
"v_mfma_f32_16x16x32_bf16 a[120:123], v[192:195], a[204:207], a[120:123]\n"
"ds_write_b128 v1, a[96:99] offset:64\n"
"v_mfma_f32_16x16x32_bf16 a[124:127], v[192:195], a[208:211], a[124:127]\n"
"v_mfma_f32_16x16x32_bf16 a[112:115], v[212:215], a[212:215], a[112:115]\n"
"v_mfma_f32_16x16x32_bf16 a[116:119], v[212:215], a[216:219], a[116:119]\n"
"buffer_load_dwordx4 a[252:255], v28, s[80:83], s32 offen offset:2048 sc1\n"
"ds_write_b128 v1, a[100:103] offset:2624\n"
"v_mfma_f32_16x16x32_bf16 a[120:123], v[212:215], a[176:179], a[120:123]\n"
"v_mfma_f32_16x16x32_bf16 a[124:127], v[212:215], a[180:183], a[124:127]\n"
"v_mfma_f32_16x16x32_bf16 a[112:115], a[168:171], a[184:187], a[112:115]\n"
"ds_write_b128 v1, a[104:107] offset:10304\n"
"v_mfma_f32_16x16x32_bf16 a[116:119], a[168:171], a[188:191], a[116:119]\n"
"buffer_load_dwordx4 a[192:195], v28, s[80:83], s32 offen offset:3072 sc1\n"
"v_mfma_f32_16x16x32_bf16 a[120:123], a[168:171], a[220:223], a[120:123]\n"
"v_mfma_f32_16x16x32_bf16 a[124:127], a[168:171], a[224:227], a[124:127]\n"
"ds_write_b128 v1, a[108:111] offset:12864\n"
"v_mfma_f32_16x16x32_bf16 a[128:131], v[196:199], v[164:167], a[128:131]\n"
"v_mfma_f32_16x16x32_bf16 a[132:135], v[196:199], v[168:171], a[132:135]\n"
"s_mov_b32 s32, 24576\n"
"buffer_load_dwordx4 v[156:159], v28, s[80:83], s32 offen offset:0 sc1\n"
"v_mfma_f32_16x16x32_bf16 a[136:139], v[196:199], a[204:207], a[136:139]\n"
"ds_write_b128 v1, a[112:115] offset:128\n"
"v_mfma_f32_16x16x32_bf16 a[140:143], v[196:199], a[208:211], a[140:143]\n"
"v_mfma_f32_16x16x32_bf16 a[128:131], v[216:219], a[212:215], a[128:131]\n"
"v_mfma_f32_16x16x32_bf16 a[132:135], v[216:219], a[216:219], a[132:135]\n"
"buffer_load_dwordx4 v[160:163], v28, s[80:83], s32 offen offset:1024 sc1\n"
"ds_write_b128 v1, a[116:119] offset:2688\n"
"v_mfma_f32_16x16x32_bf16 a[136:139], v[216:219], a[176:179], a[136:139]\n"
"v_mfma_f32_16x16x32_bf16 a[140:143], v[216:219], a[180:183], a[140:143]\n"
"v_mfma_f32_16x16x32_bf16 a[128:131], a[172:175], a[184:187], a[128:131]\n"
"ds_write_b128 v1, a[120:123] offset:10368\n"
"v_mfma_f32_16x16x32_bf16 a[132:135], a[172:175], a[188:191], a[132:135]\n"
"v_mfma_f32_16x16x32_bf16 a[136:139], a[172:175], a[220:223], a[136:139]\n"
"v_mfma_f32_16x16x32_bf16 a[140:143], a[172:175], a[224:227], a[140:143]\n"
"ds_write_b128 v1, a[124:127] offset:12928\n"
"v_mfma_f32_16x16x32_bf16 a[144:147], v[200:203], v[164:167], a[144:147]\n"
"v_mfma_f32_16x16x32_bf16 a[148:151], v[200:203], v[168:171], a[148:151]\n"
"v_mfma_f32_16x16x32_bf16 a[152:155], v[200:203], a[204:207], a[152:155]\n"
"ds_write_b128 v1, a[128:131] offset:192\n"
"v_mfma_f32_16x16x32_bf16 a[156:159], v[200:203], a[208:211], a[156:159]\n"
"v_mfma_f32_16x16x32_bf16 a[144:147], v[220:223], a[212:215], a[144:147]\n"
"v_mfma_f32_16x16x32_bf16 a[148:151], v[220:223], a[216:219], a[148:151]\n"
"ds_write_b128 v1, a[132:135] offset:2752\n"
"v_mfma_f32_16x16x32_bf16 a[152:155], v[220:223], a[176:179], a[152:155]\n"
"v_mfma_f32_16x16x32_bf16 a[156:159], v[220:223], a[180:183], a[156:159]\n"
"v_mfma_f32_16x16x32_bf16 a[144:147], a[200:203], a[184:187], a[144:147]\n"
"s_waitcnt vmcnt(10)\n"
"ds_write_b128 v1, a[136:139] offset:10432\n"
"v_mfma_f32_16x16x32_bf16 a[148:151], a[200:203], a[188:191], a[148:151]\n"
"v_mfma_f32_16x16x32_bf16 a[152:155], a[200:203], a[220:223], a[152:155]\n"
"v_mfma_f32_16x16x32_bf16 a[156:159], a[200:203], a[224:227], a[156:159]\n"
"ds_write_b128 v1, a[140:143] offset:12992\n"
"v_mfma_f32_16x16x32_bf16 a[0:3], a[196:199], a[228:231], a[0:3]\n"
"v_mfma_f32_16x16x32_bf16 a[4:7], a[196:199], a[232:235], a[4:7]\n"
"buffer_load_dwordx4 v[164:167], v28, s[80:83], s32 offen offset:2048 sc1\n"
"v_mfma_f32_16x16x32_bf16 a[8:11], a[196:199], a[236:239], a[8:11]\n"
"ds_write_b128 v1, a[144:147] offset:256\n"
"v_mfma_f32_16x16x32_bf16 a[12:15], a[196:199], v[140:143], a[12:15]\n"
"v_mfma_f32_16x16x32_bf16 a[16:19], v[80:83], a[228:231], a[16:19]\n"
"v_mfma_f32_16x16x32_bf16 a[20:23], v[80:83], a[232:235], a[20:23]\n"
"buffer_load_dwordx4 v[168:171], v28, s[80:83], s32 offen offset:3072 sc1\n"
"ds_write_b128 v1, a[148:151] offset:2816\n"
"v_mfma_f32_16x16x32_bf16 a[24:27], v[80:83], a[236:239], a[24:27]\n"
"v_mfma_f32_16x16x32_bf16 a[28:31], v[80:83], v[140:143], a[28:31]\n"
"v_mfma_f32_16x16x32_bf16 a[32:35], v[84:87], a[228:231], a[32:35]\n"
"ds_write_b128 v1, a[152:155] offset:10496\n"
"v_mfma_f32_16x16x32_bf16 a[36:39], v[84:87], a[232:235], a[36:39]\n"
"s_mov_b32 s32, 28672\n"
"buffer_load_dwordx4 a[204:207], v28, s[80:83], s32 offen offset:0 sc1\n"
"v_mfma_f32_16x16x32_bf16 a[40:43], v[84:87], a[236:239], a[40:43]\n"
"v_mfma_f32_16x16x32_bf16 a[44:47], v[84:87], v[140:143], a[44:47]\n"
"ds_write_b128 v1, a[156:159] offset:13056\n"
"v_mfma_f32_16x16x32_bf16 a[48:51], v[88:91], a[228:231], a[48:51]\n"
"v_mfma_f32_16x16x32_bf16 a[52:55], v[88:91], a[232:235], a[52:55]\n"
"s_waitcnt lgkmcnt(0)\n"
"s_barrier\n"
"buffer_load_dwordx4 a[208:211], v28, s[80:83], s32 offen offset:1024 sc1\n"
"v_mfma_f32_16x16x32_bf16 a[56:59], v[88:91], a[236:239], a[56:59]\n"
"ds_read_b128 v[30:33], v4 offset:0\n"
"ds_read_b128 v[34:37], v4 offset:20480\n"
"ds_read_b128 v[38:41], v4 offset:40960\n"
"v_mfma_f32_16x16x32_bf16 a[60:63], v[88:91], v[140:143], a[60:63]\n"
"ds_read_b128 v[76:79], v4 offset:61440\n"
"ds_read_b128 v[96:99], v5 offset:0\n"
"ds_read_b128 v[100:103], v5 offset:20480\n"
"v_mfma_f32_16x16x32_bf16 a[64:67], v[92:95], a[228:231], a[64:67]\n"
"s_waitcnt vmcnt(10)\n"
"ds_read_b128 v[104:107], v5 offset:40960\n"
"ds_read_b128 v[108:111], v5 offset:61440\n"
"v_mfma_f32_16x16x32_bf16 a[68:71], v[92:95], a[232:235], a[68:71]\n"
"buffer_load_dwordx4 a[212:215], v28, s[80:83], s32 offen offset:2048 sc1\n"
"v_mfma_f32_16x16x32_bf16 a[72:75], v[92:95], a[236:239], a[72:75]\n"
"v_mfma_f32_16x16x32_bf16 a[76:79], v[92:95], v[140:143], a[76:79]\n"
"s_waitcnt lgkmcnt(4)\n"
"v_add_f32 v30, v30, v34\n"
"v_add_f32 v38, v38, v76\n"
"v_add_f32 v31, v31, v35\n"
"v_mfma_f32_16x16x32_bf16 a[0:3], v[224:227], v[144:147], a[0:3]\n"
"v_add_f32 v39, v39, v77\n"
"v_add_f32 v32, v32, v36\n"
"v_add_f32 v40, v40, v78\n"
"v_mfma_f32_16x16x32_bf16 a[4:7], v[224:227], v[148:151], a[4:7]\n"
"buffer_load_dwordx4 a[216:219], v28, s[80:83], s32 offen offset:3072 sc1\n"
"v_mfma_f32_16x16x32_bf16 a[8:11], v[224:227], v[152:155], a[8:11]\n"
"v_add_f32 v33, v33, v37\n"
"v_add_f32 v41, v41, v79\n"
"v_add_f32 v30, v30, v38\n"
"v_mfma_f32_16x16x32_bf16 a[12:15], v[224:227], a[240:243], a[12:15]\n"
"v_add_f32 v31, v31, v39\n"
"v_add_f32 v32, v32, v40\n"
"v_add_f32 v33, v33, v41\n"
"v_mfma_f32_16x16x32_bf16 a[16:19], v[228:231], v[144:147], a[16:19]\n"
"s_add_u32 s69, s34, 0\n"
"v_and_or_b32 v16, v30, v21, v20\n"
"v_mfma_f32_16x16x32_bf16 a[20:23], v[228:231], v[148:151], a[20:23]\n"
"s_mov_b32 s32, 32768\n"
"buffer_load_dwordx4 a[176:179], v28, s[80:83], s32 offen offset:0 sc1\n"
"v_mfma_f32_16x16x32_bf16 a[24:27], v[228:231], v[152:155], a[24:27]\n"
"v_and_or_b32 v17, v31, v21, v20\n"
"v_and_or_b32 v18, v32, v21, v20\n"
"v_and_or_b32 v19, v33, v21, v20\n"
"v_mfma_f32_16x16x32_bf16 a[28:31], v[228:231], a[240:243], a[28:31]\n"
"buffer_store_dwordx4 v[16:19], v28, s[64:67], s69 offen offset:0 sc1\n"
"v_mfma_f32_16x16x32_bf16 a[32:35], v[232:235], v[144:147], a[32:35]\n"
"ds_read_b128 v[30:33], v6 offset:0\n"
"ds_read_b128 v[34:37], v6 offset:20480\n"
"ds_read_b128 v[38:41], v6 offset:40960\n"
"v_mfma_f32_16x16x32_bf16 a[36:39], v[232:235], v[148:151], a[36:39]\n"
"buffer_load_dwordx4 a[180:183], v28, s[80:83], s32 offen offset:1024 sc1\n"
"v_mfma_f32_16x16x32_bf16 a[40:43], v[232:235], v[152:155], a[40:43]\n"
"ds_read_b128 v[76:79], v6 offset:61440\n"
"v_mfma_f32_16x16x32_bf16 a[44:47], v[232:235], a[240:243], a[44:47]\n"
"v_mfma_f32_16x16x32_bf16 a[48:51], v[236:239], v[144:147], a[48:51]\n"
"v_mfma_f32_16x16x32_bf16 a[52:55], v[236:239], v[148:151], a[52:55]\n"
"buffer_load_dwordx4 a[184:187], v28, s[80:83], s32 offen offset:2048 sc1\n"
"v_mfma_f32_16x16x32_bf16 a[56:59], v[236:239], v[152:155], a[56:59]\n"
"v_mfma_f32_16x16x32_bf16 a[60:63], v[236:239], a[240:243], a[60:63]\n"
"v_mfma_f32_16x16x32_bf16 a[64:67], v[240:243], v[144:147], a[64:67]\n"
"s_waitcnt vmcnt(12)\n"
"v_mfma_f32_16x16x32_bf16 a[68:71], v[240:243], v[148:151], a[68:71]\n"
"buffer_load_dwordx4 a[188:191], v28, s[80:83], s32 offen offset:3072 sc1\n"
"v_mfma_f32_16x16x32_bf16 a[72:75], v[240:243], v[152:155], a[72:75]\n"
"v_mfma_f32_16x16x32_bf16 a[76:79], v[240:243], a[240:243], a[76:79]\n"
"v_mfma_f32_16x16x32_bf16 a[0:3], v[244:247], a[244:247], a[0:3]\n"
"v_mfma_f32_16x16x32_bf16 a[4:7], v[244:247], a[248:251], a[4:7]\n"
"s_mov_b32 s32, 36864\n"
"buffer_load_dwordx4 a[220:223], v28, s[80:83], s32 offen offset:0 sc1\n"
"v_mfma_f32_16x16x32_bf16 a[8:11], v[244:247], a[252:255], a[8:11]\n"
"s_waitcnt lgkmcnt(4)\n"
"v_add_f32 v96, v96, v100\n"
"v_add_f32 v104, v104, v108\n"
"v_add_f32 v97, v97, v101\n"
"v_mfma_f32_16x16x32_bf16 a[12:15], v[244:247], a[192:195], a[12:15]\n"
"v_add_f32 v105, v105, v109\n"
"v_add_f32 v98, v98, v102\n"
"v_add_f32 v106, v106, v110\n"
"v_mfma_f32_16x16x32_bf16 a[16:19], v[248:251], a[244:247], a[16:19]\n"
"v_add_f32 v99, v99, v103\n"
"v_add_f32 v107, v107, v111\n"
"v_add_f32 v96, v96, v104\n"
"v_mfma_f32_16x16x32_bf16 a[20:23], v[248:251], a[248:251], a[20:23]\n"
"buffer_load_dwordx4 a[224:227], v28, s[80:83], s32 offen offset:1024 sc1\n"
"v_mfma_f32_16x16x32_bf16 a[24:27], v[248:251], a[252:255], a[24:27]\n"
"v_add_f32 v97, v97, v105\n"
"v_add_f32 v98, v98, v106\n"
"v_add_f32 v99, v99, v107\n"
"v_mfma_f32_16x16x32_bf16 a[28:31], v[248:251], a[192:195], a[28:31]\n"
"v_and_or_b32 v16, v96, v21, v20\n"
"v_and_or_b32 v17, v97, v21, v20\n"
"v_and_or_b32 v18, v98, v21, v20\n"
"v_mfma_f32_16x16x32_bf16 a[32:35], v[252:255], a[244:247], a[32:35]\n"
"v_and_or_b32 v19, v99, v21, v20\n"
"v_mfma_f32_16x16x32_bf16 a[36:39], v[252:255], a[248:251], a[36:39]\n"
"buffer_load_dwordx4 a[196:199], v28, s[80:83], s32 offen offset:2048 sc1\n"
"v_mfma_f32_16x16x32_bf16 a[40:43], v[252:255], a[252:255], a[40:43]\n"
"buffer_store_dwordx4 v[16:19], v28, s[64:67], s69 offen offset:1024 sc1\n"
"v_mfma_f32_16x16x32_bf16 a[44:47], v[252:255], a[192:195], a[44:47]\n"
"ds_read_b128 v[96:99], v7 offset:0\n"
"ds_read_b128 v[100:103], v7 offset:20480\n"
"ds_read_b128 v[104:107], v7 offset:40960\n"
"v_mfma_f32_16x16x32_bf16 a[48:51], v[44:47], a[244:247], a[48:51]\n"
"ds_read_b128 v[108:111], v7 offset:61440\n"
"v_mfma_f32_16x16x32_bf16 a[52:55], v[44:47], a[248:251], a[52:55]\n"
"buffer_load_dwordx4 v[80:83], v28, s[80:83], s32 offen offset:3072 sc1\n"
"v_mfma_f32_16x16x32_bf16 a[56:59], v[44:47], a[252:255], a[56:59]\n"
"v_mfma_f32_16x16x32_bf16 a[60:63], v[44:47], a[192:195], a[60:63]\n"
"v_mfma_f32_16x16x32_bf16 a[64:67], v[48:51], a[244:247], a[64:67]\n"
"s_waitcnt vmcnt(14)\n"
"v_mfma_f32_16x16x32_bf16 a[68:71], v[48:51], a[248:251], a[68:71]\n"
"s_mov_b32 s32, 40960\n"
"buffer_load_dwordx4 v[84:87], v28, s[80:83], s32 offen offset:0 sc1\n"
"v_mfma_f32_16x16x32_bf16 a[72:75], v[48:51], a[252:255], a[72:75]\n"
"v_mfma_f32_16x16x32_bf16 a[76:79], v[48:51], a[192:195], a[76:79]\n"
"v_mfma_f32_16x16x32_bf16 a[0:3], v[52:55], v[156:159], a[0:3]\n"
"v_mfma_f32_16x16x32_bf16 a[4:7], v[52:55], v[160:163], a[4:7]\n"
"buffer_load_dwordx4 v[88:91], v28, s[80:83], s32 offen offset:1024 sc1\n"
"v_mfma_f32_16x16x32_bf16 a[8:11], v[52:55], v[164:167], a[8:11]\n"
"v_mfma_f32_16x16x32_bf16 a[12:15], v[52:55], v[168:171], a[12:15]\n"
"s_waitcnt lgkmcnt(4)\n"
"v_add_f32 v30, v30, v34\n"
"v_add_f32 v38, v38, v76\n"
"v_add_f32 v31, v31, v35\n"
"v_mfma_f32_16x16x32_bf16 a[16:19], v[56:59], v[156:159], a[16:19]\n"
"v_add_f32 v39, v39, v77\n"
"v_add_f32 v32, v32, v36\n"
"v_add_f32 v40, v40, v78\n"
"v_mfma_f32_16x16x32_bf16 a[20:23], v[56:59], v[160:163], a[20:23]\n"
"buffer_load_dwordx4 v[92:95], v28, s[80:83], s32 offen offset:2048 sc1\n"
"v_mfma_f32_16x16x32_bf16 a[24:27], v[56:59], v[164:167], a[24:27]\n"
"v_add_f32 v33, v33, v37\n"
"v_add_f32 v41, v41, v79\n"
"v_add_f32 v30, v30, v38\n"
"v_mfma_f32_16x16x32_bf16 a[28:31], v[56:59], v[168:171], a[28:31]\n"
"v_add_f32 v31, v31, v39\n"
"v_add_f32 v32, v32, v40\n"
"v_add_f32 v33, v33, v41\n"
"v_mfma_f32_16x16x32_bf16 a[32:35], v[60:63], v[156:159], a[32:35]\n"
"v_and_or_b32 v16, v30, v21, v20\n"
"v_and_or_b32 v17, v31, v21, v20\n"
"v_and_or_b32 v18, v32, v21, v20\n"
"v_mfma_f32_16x16x32_bf16 a[36:39], v[60:63], v[160:163], a[36:39]\n"
"buffer_load_dwordx4 a[228:231], v28, s[80:83], s32 offen offset:3072 sc1\n"
"v_mfma_f32_16x16x32_bf16 a[40:43], v[60:63], v[164:167], a[40:43]\n"
"v_and_or_b32 v19, v33, v21, v20\n"
"v_mfma_f32_16x16x32_bf16 a[44:47], v[60:63], v[168:171], a[44:47]\n"
"buffer_store_dwordx4 v[16:19], v28, s[64:67], s69 offen offset:2048 sc1\n"
"v_mfma_f32_16x16x32_bf16 a[48:51], v[64:67], v[156:159], a[48:51]\n"
"ds_read_b128 v[30:33], v8 offset:0\n"
"ds_read_b128 v[34:37], v8 offset:20480\n"
"ds_read_b128 v[38:41], v8 offset:40960\n"
"v_mfma_f32_16x16x32_bf16 a[52:55], v[64:67], v[160:163], a[52:55]\n"
"s_mov_b32 s32, 45056\n"
"buffer_load_dwordx4 a[232:235], v28, s[80:83], s32 offen offset:0 sc1\n"
"v_mfma_f32_16x16x32_bf16 a[56:59], v[64:67], v[164:167], a[56:59]\n"
"ds_read_b128 v[76:79], v8 offset:61440\n"
"v_mfma_f32_16x16x32_bf16 a[60:63], v[64:67], v[168:171], a[60:63]\n"
"v_mfma_f32_16x16x32_bf16 a[64:67], v[68:71], v[156:159], a[64:67]\n"
"s_waitcnt vmcnt(16)\n"
"v_mfma_f32_16x16x32_bf16 a[68:71], v[68:71], v[160:163], a[68:71]\n"
"buffer_load_dwordx4 a[236:239], v28, s[80:83], s32 offen offset:1024 sc1\n"
"v_mfma_f32_16x16x32_bf16 a[72:75], v[68:71], v[164:167], a[72:75]\n"
"v_mfma_f32_16x16x32_bf16 a[76:79], v[68:71], v[168:171], a[76:79]\n"
"v_mfma_f32_16x16x32_bf16 a[0:3], v[72:75], a[204:207], a[0:3]\n"
"v_mfma_f32_16x16x32_bf16 a[4:7], v[72:75], a[208:211], a[4:7]\n"
"buffer_load_dwordx4 v[140:143], v28, s[80:83], s32 offen offset:2048 sc1\n"
"v_mfma_f32_16x16x32_bf16 a[8:11], v[72:75], a[212:215], a[8:11]\n"
"v_mfma_f32_16x16x32_bf16 a[12:15], v[72:75], a[216:219], a[12:15]\n"
"v_mfma_f32_16x16x32_bf16 a[16:19], v[116:119], a[204:207], a[16:19]\n"
"v_mfma_f32_16x16x32_bf16 a[20:23], v[116:119], a[208:211], a[20:23]\n"
"buffer_load_dwordx4 v[144:147], v28, s[80:83], s32 offen offset:3072 sc1\n"
"v_mfma_f32_16x16x32_bf16 a[24:27], v[116:119], a[212:215], a[24:27]\n"
"s_waitcnt lgkmcnt(4)\n"
"v_add_f32 v96, v96, v100\n"
"v_add_f32 v104, v104, v108\n"
"v_add_f32 v97, v97, v101\n"
"v_mfma_f32_16x16x32_bf16 a[28:31], v[116:119], a[216:219], a[28:31]\n"
"v_add_f32 v105, v105, v109\n"
"v_add_f32 v98, v98, v102\n"
"v_add_f32 v106, v106, v110\n"
"v_mfma_f32_16x16x32_bf16 a[32:35], v[120:123], a[204:207], a[32:35]\n"
"v_add_f32 v99, v99, v103\n"
"v_add_f32 v107, v107, v111\n"
"v_add_f32 v96, v96, v104\n"
"v_mfma_f32_16x16x32_bf16 a[36:39], v[120:123], a[208:211], a[36:39]\n"
"v_add_f32 v97, v97, v105\n"
"v_add_f32 v98, v98, v106\n"
"v_add_f32 v99, v99, v107\n"
"v_mfma_f32_16x16x32_bf16 a[40:43], v[120:123], a[212:215], a[40:43]\n"
"v_and_or_b32 v16, v96, v21, v20\n"
"v_and_or_b32 v17, v97, v21, v20\n"
"v_and_or_b32 v18, v98, v21, v20\n"
"v_mfma_f32_16x16x32_bf16 a[44:47], v[120:123], a[216:219], a[44:47]\n"
"v_and_or_b32 v19, v99, v21, v20\n"
"v_mfma_f32_16x16x32_bf16 a[48:51], v[124:127], a[204:207], a[48:51]\n"
"buffer_store_dwordx4 v[16:19], v28, s[64:67], s69 offen offset:3072 sc1\n"
"v_mfma_f32_16x16x32_bf16 a[52:55], v[124:127], a[208:211], a[52:55]\n"
"v_mfma_f32_16x16x32_bf16 a[56:59], v[124:127], a[212:215], a[56:59]\n"
"v_mfma_f32_16x16x32_bf16 a[60:63], v[124:127], a[216:219], a[60:63]\n"
"v_mfma_f32_16x16x32_bf16 a[64:67], v[128:131], a[204:207], a[64:67]\n"
"s_waitcnt vmcnt(15)\n"
"v_mfma_f32_16x16x32_bf16 a[68:71], v[128:131], a[208:211], a[68:71]\n"
"v_mfma_f32_16x16x32_bf16 a[72:75], v[128:131], a[212:215], a[72:75]\n"
"v_mfma_f32_16x16x32_bf16 a[76:79], v[128:131], a[216:219], a[76:79]\n"
"v_mfma_f32_16x16x32_bf16 a[0:3], v[132:135], a[176:179], a[0:3]\n"
"v_mfma_f32_16x16x32_bf16 a[4:7], v[132:135], a[180:183], a[4:7]\n"
"v_mfma_f32_16x16x32_bf16 a[8:11], v[132:135], a[184:187], a[8:11]\n"
"v_mfma_f32_16x16x32_bf16 a[12:15], v[132:135], a[188:191], a[12:15]\n"
"v_mfma_f32_16x16x32_bf16 a[16:19], v[136:139], a[176:179], a[16:19]\n"
"v_mfma_f32_16x16x32_bf16 a[20:23], v[136:139], a[180:183], a[20:23]\n"
"v_mfma_f32_16x16x32_bf16 a[24:27], v[136:139], a[184:187], a[24:27]\n"
"v_mfma_f32_16x16x32_bf16 a[28:31], v[136:139], a[188:191], a[28:31]\n"
"s_waitcnt lgkmcnt(0)\n"
"v_add_f32 v30, v30, v34\n"
"v_add_f32 v38, v38, v76\n"
"v_add_f32 v31, v31, v35\n"
"v_mfma_f32_16x16x32_bf16 a[32:35], v[172:175], a[176:179], a[32:35]\n"
"v_add_f32 v39, v39, v77\n"
"v_add_f32 v32, v32, v36\n"
"v_add_f32 v40, v40, v78\n"
"v_mfma_f32_16x16x32_bf16 a[36:39], v[172:175], a[180:183], a[36:39]\n"
"v_add_f32 v33, v33, v37\n"
"v_add_f32 v41, v41, v79\n"
"v_add_f32 v30, v30, v38\n"
"v_mfma_f32_16x16x32_bf16 a[40:43], v[172:175], a[184:187], a[40:43]\n"
"v_add_f32 v31, v31, v39\n"
"v_add_f32 v32, v32, v40\n"
"v_add_f32 v33, v33, v41\n"
"v_mfma_f32_16x16x32_bf16 a[44:47], v[172:175], a[188:191], a[44:47]\n"
"s_add_u32 s69, s34, 4096\n"
"v_and_or_b32 v16, v30, v21, v20\n"
"v_mfma_f32_16x16x32_bf16 a[48:51], v[176:179], a[176:179], a[48:51]\n"
"v_and_or_b32 v17, v31, v21, v20\n"
"v_and_or_b32 v18, v32, v21, v20\n"
"v_and_or_b32 v19, v33, v21, v20\n"
"v_mfma_f32_16x16x32_bf16 a[52:55], v[176:179], a[180:183], a[52:55]\n"
"buffer_store_dwordx4 v[16:19], v28, s[64:67], s69 offen offset:0 sc1\n"
"v_mfma_f32_16x16x32_bf16 a[56:59], v[176:179], a[184:187], a[56:59]\n"
"v_subrev_u32 v4, 81920, v4\n"
"v_subrev_u32 v5, 81920, v5\n"
"v_subrev_u32 v6, 81920, v6\n"
"v_mfma_f32_16x16x32_bf16 a[60:63], v[176:179], a[188:191], a[60:63]\n"
"v_subrev_u32 v7, 81920, v7\n"
"v_subrev_u32 v8, 81920, v8\n"
"v_mfma_f32_16x16x32_bf16 a[64:67], v[180:183], a[176:179], a[64:67]\n"
"s_waitcnt vmcnt(11)\n"
"buffer_load_dword v224, v10, s[48:51], 0 offen offset:0\n"
"v_mfma_f32_16x16x32_bf16 a[68:71], v[180:183], a[180:183], a[68:71]\n"
"buffer_load_dword v225, v10, s[48:51], 0 offen offset:4\n"
"v_mfma_f32_16x16x32_bf16 a[72:75], v[180:183], a[184:187], a[72:75]\n"
"s_waitcnt vmcnt(4)\n"
"v_mul_u32_u24 v9, 52, v10\n"
"v_mfma_f32_16x16x32_bf16 a[76:79], v[180:183], a[188:191], a[76:79]\n"
"v_lshrrev_b32 v9, 16, v9\n"
"v_mul_u32_u24 v15, 1280, v9\n"
"v_sub_u32 v15, v10, v15\n"
"v_mfma_f32_16x16x32_bf16 a[0:3], v[184:187], a[220:223], a[0:3]\n"
"v_lshlrev_b32 v9, 4, v9\n"
"v_mfma_f32_16x16x32_bf16 a[4:7], v[184:187], a[224:227], a[4:7]\n"
"buffer_load_dword v234, v15, s[24:27], 0 offen offset:0\n"
"v_mfma_f32_16x16x32_bf16 a[8:11], v[184:187], a[196:199], a[8:11]\n"
"buffer_load_dword v235, v15, s[24:27], 0 offen offset:4\n"
"v_mfma_f32_16x16x32_bf16 a[12:15], v[184:187], v[80:83], a[12:15]\n"
"buffer_load_dword v244, v9, s[16:19], 0 offen offset:0 sc1\n"
"v_mfma_f32_16x16x32_bf16 a[0:3], v[204:207], v[84:87], a[0:3]\n"
"buffer_load_dword v245, v9, s[16:19], 0 offen offset:8 sc1\n"
"v_mfma_f32_16x16x32_bf16 a[4:7], v[204:207], v[88:91], a[4:7]\n"
"buffer_load_dword v226, v11, s[48:51], 0 offen offset:0\n"
"v_mfma_f32_16x16x32_bf16 a[8:11], v[204:207], v[92:95], a[8:11]\n"
"buffer_load_dword v227, v11, s[48:51], 0 offen offset:4\n"
"v_mfma_f32_16x16x32_bf16 a[12:15], v[204:207], a[228:231], a[12:15]\n"
"v_mul_u32_u24 v9, 52, v11\n"
"v_lshrrev_b32 v9, 16, v9\n"
"v_mul_u32_u24 v15, 1280, v9\n"
"v_mfma_f32_16x16x32_bf16 a[0:3], a[160:163], a[232:235], a[0:3]\n"
"v_sub_u32 v15, v11, v15\n"
"v_lshlrev_b32 v9, 4, v9\n"
"v_mfma_f32_16x16x32_bf16 a[4:7], a[160:163], a[236:239], a[4:7]\n"
"buffer_load_dword v236, v15, s[24:27], 0 offen offset:0\n"
"v_mfma_f32_16x16x32_bf16 a[8:11], a[160:163], v[140:143], a[8:11]\n"
"buffer_load_dword v237, v15, s[24:27], 0 offen offset:4\n"
"v_mfma_f32_16x16x32_bf16 a[12:15], a[160:163], v[144:147], a[12:15]\n"
"buffer_load_dword v246, v9, s[16:19], 0 offen offset:0 sc1\n"
"v_mfma_f32_16x16x32_bf16 a[16:19], v[188:191], a[220:223], a[16:19]\n"
"buffer_load_dword v247, v9, s[16:19], 0 offen offset:8 sc1\n"
"v_mfma_f32_16x16x32_bf16 a[20:23], v[188:191], a[224:227], a[20:23]\n"
"buffer_load_dword v228, v12, s[48:51], 0 offen offset:0\n"
"v_mfma_f32_16x16x32_bf16 a[24:27], v[188:191], a[196:199], a[24:27]\n"
"ds_write_b128 v2, a[0:3] offset:0\n"
"buffer_load_dword v229, v12, s[48:51], 0 offen offset:4\n"
"v_mfma_f32_16x16x32_bf16 a[28:31], v[188:191], v[80:83], a[28:31]\n"
"v_mul_u32_u24 v9, 52, v12\n"
"v_lshrrev_b32 v9, 16, v9\n"
"v_mul_u32_u24 v15, 1280, v9\n"
"v_mfma_f32_16x16x32_bf16 a[16:19], v[208:211], v[84:87], a[16:19]\n"
"v_sub_u32 v15, v12, v15\n"
"v_lshlrev_b32 v9, 4, v9\n"
"v_mfma_f32_16x16x32_bf16 a[20:23], v[208:211], v[88:91], a[20:23]\n"
"ds_write_b128 v2, a[4:7] offset:2560\n"
"buffer_load_dword v238, v15, s[24:27], 0 offen offset:0\n"
"v_mfma_f32_16x16x32_bf16 a[24:27], v[208:211], v[92:95], a[24:27]\n"
"buffer_load_dword v239, v15, s[24:27], 0 offen offset:4\n"
"v_mfma_f32_16x16x32_bf16 a[28:31], v[208:211], a[228:231], a[28:31]\n"
"buffer_load_dword v248, v9, s[16:19], 0 offen offset:0 sc1\n"
"v_mfma_f32_16x16x32_bf16 a[16:19], a[164:167], a[232:235], a[16:19]\n"
"ds_write_b128 v2, a[8:11] offset:10240\n"
"buffer_load_dword v249, v9, s[16:19], 0 offen offset:8 sc1\n"
"v_mfma_f32_16x16x32_bf16 a[20:23], a[164:167], a[236:239], a[20:23]\n"
"buffer_load_dword v230, v13, s[48:51], 0 offen offset:0\n"
"v_mfma_f32_16x16x32_bf16 a[24:27], a[164:167], v[140:143], a[24:27]\n"
"buffer_load_dword v231, v13, s[48:51], 0 offen offset:4\n"
"v_mfma_f32_16x16x32_bf16 a[28:31], a[164:167], v[144:147], a[28:31]\n"
"ds_write_b128 v2, a[12:15] offset:12800\n"
"v_mul_u32_u24 v9, 52, v13\n"
"v_lshrrev_b32 v9, 16, v9\n"
"v_mfma_f32_16x16x32_bf16 a[32:35], v[192:195], a[220:223], a[32:35]\n"
"v_mul_u32_u24 v15, 1280, v9\n"
"v_sub_u32 v15, v13, v15\n"
"v_lshlrev_b32 v9, 4, v9\n"
"v_mfma_f32_16x16x32_bf16 a[36:39], v[192:195], a[224:227], a[36:39]\n"
"buffer_load_dword v240, v15, s[24:27], 0 offen offset:0\n"
"v_mfma_f32_16x16x32_bf16 a[40:43], v[192:195], a[196:199], a[40:43]\n"
"ds_write_b128 v2, a[16:19] offset:64\n"
"buffer_load_dword v241, v15, s[24:27], 0 offen offset:4\n"
"v_mfma_f32_16x16x32_bf16 a[44:47], v[192:195], v[80:83], a[44:47]\n"
"buffer_load_dword v250, v9, s[16:19], 0 offen offset:0 sc1\n"
"v_mfma_f32_16x16x32_bf16 a[32:35], v[212:215], v[84:87], a[32:35]\n"
"buffer_load_dword v251, v9, s[16:19], 0 offen offset:8 sc1\n"
"v_mfma_f32_16x16x32_bf16 a[36:39], v[212:215], v[88:91], a[36:39]\n"
"ds_write_b128 v2, a[20:23] offset:2624\n"
"buffer_load_dword v232, v14, s[48:51], 0 offen offset:0\n"
"v_mfma_f32_16x16x32_bf16 a[40:43], v[212:215], v[92:95], a[40:43]\n"
"buffer_load_dword v233, v14, s[48:51], 0 offen offset:4\n"
"v_mfma_f32_16x16x32_bf16 a[44:47], v[212:215], a[228:231], a[44:47]\n"
"v_mul_u32_u24 v9, 52, v14\n"
"v_lshrrev_b32 v9, 16, v9\n"
"v_mul_u32_u24 v15, 1280, v9\n"
"v_mfma_f32_16x16x32_bf16 a[32:35], a[168:171], a[232:235], a[32:35]\n"
"ds_write_b128 v2, a[24:27] offset:10304\n"
"v_sub_u32 v15, v14, v15\n"
"v_lshlrev_b32 v9, 4, v9\n"
"v_mfma_f32_16x16x32_bf16 a[36:39], a[168:171], a[236:239], a[36:39]\n"
"buffer_load_dword v242, v15, s[24:27], 0 offen offset:0\n"
"v_mfma_f32_16x16x32_bf16 a[40:43], a[168:171], v[140:143], a[40:43]\n"
"buffer_load_dword v243, v15, s[24:27], 0 offen offset:4\n"
"v_mfma_f32_16x16x32_bf16 a[44:47], a[168:171], v[144:147], a[44:47]\n"
"ds_write_b128 v2, a[28:31] offset:12864\n"
"buffer_load_dword v252, v9, s[16:19], 0 offen offset:0 sc1\n"
"v_mfma_f32_16x16x32_bf16 a[48:51], v[196:199], a[220:223], a[48:51]\n"
"buffer_load_dword v253, v9, s[16:19], 0 offen offset:8 sc1\n"
"v_mfma_f32_16x16x32_bf16 a[52:55], v[196:199], a[224:227], a[52:55]\n"
"v_mfma_f32_16x16x32_bf16 a[56:59], v[196:199], a[196:199], a[56:59]\n"
"ds_write_b128 v2, a[32:35] offset:128\n"
"v_mfma_f32_16x16x32_bf16 a[60:63], v[196:199], v[80:83], a[60:63]\n"
"v_mfma_f32_16x16x32_bf16 a[48:51], v[216:219], v[84:87], a[48:51]\n"
"v_mfma_f32_16x16x32_bf16 a[52:55], v[216:219], v[88:91], a[52:55]\n"
"ds_write_b128 v2, a[36:39] offset:2688\n"
"v_mfma_f32_16x16x32_bf16 a[56:59], v[216:219], v[92:95], a[56:59]\n"
"v_mfma_f32_16x16x32_bf16 a[60:63], v[216:219], a[228:231], a[60:63]\n"
"v_mfma_f32_16x16x32_bf16 a[48:51], a[172:175], a[232:235], a[48:51]\n"
"ds_write_b128 v2, a[40:43] offset:10368\n"
"v_mfma_f32_16x16x32_bf16 a[52:55], a[172:175], a[236:239], a[52:55]\n"
"v_mfma_f32_16x16x32_bf16 a[56:59], a[172:175], v[140:143], a[56:59]\n"
"v_mfma_f32_16x16x32_bf16 a[60:63], a[172:175], v[144:147], a[60:63]\n"
"ds_write_b128 v2, a[44:47] offset:12928\n"
"v_mfma_f32_16x16x32_bf16 a[64:67], v[200:203], a[220:223], a[64:67]\n"
"v_mfma_f32_16x16x32_bf16 a[68:71], v[200:203], a[224:227], a[68:71]\n"
"v_mfma_f32_16x16x32_bf16 a[72:75], v[200:203], a[196:199], a[72:75]\n"
"ds_write_b128 v2, a[48:51] offset:192\n"
"v_mfma_f32_16x16x32_bf16 a[76:79], v[200:203], v[80:83], a[76:79]\n"
"v_mfma_f32_16x16x32_bf16 a[64:67], v[220:223], v[84:87], a[64:67]\n"
"s_add_u32 s68, s35, 0\n"
"v_mfma_f32_16x16x32_bf16 a[68:71], v[220:223], v[88:91], a[68:71]\n"
"ds_write_b128 v2, a[52:55] offset:2752\n"
"buffer_load_dwordx4 v[96:99], v28, s[64:67], s68 offen offset:0 sc1\n"
"v_mfma_f32_16x16x32_bf16 a[72:75], v[220:223], v[92:95], a[72:75]\n"
"buffer_load_dwordx4 v[100:103], v28, s[64:67], s68 offen offset:1024 sc1\n"
"v_mfma_f32_16x16x32_bf16 a[76:79], v[220:223], a[228:231], a[76:79]\n"
"buffer_load_dwordx4 v[104:107], v28, s[64:67], s68 offen offset:2048 sc1\n"
"v_mfma_f32_16x16x32_bf16 a[64:67], a[200:203], a[232:235], a[64:67]\n"
"ds_write_b128 v2, a[56:59] offset:10432\n"
"buffer_load_dwordx4 v[108:111], v28, s[64:67], s68 offen offset:3072 sc1\n"
"v_mfma_f32_16x16x32_bf16 a[68:71], a[200:203], a[236:239], a[68:71]\n"
"s_add_u32 s68, s35, 4096\n"
"v_mfma_f32_16x16x32_bf16 a[72:75], a[200:203], v[140:143], a[72:75]\n"
"buffer_load_dwordx4 v[112:115], v28, s[64:67], s68 offen offset:0 sc1\n"
"v_mfma_f32_16x16x32_bf16 a[76:79], a[200:203], v[144:147], a[76:79]\n"
"ds_write_b128 v2, a[60:63] offset:12992\n"
"ds_write_b128 v2, a[64:67] offset:256\n"
"s_nop 1\n"
"ds_write_b128 v2, a[68:71] offset:2816\n"
"s_nop 0\n"
"ds_write_b128 v2, a[72:75] offset:10496\n"
"s_nop 0\n"
"ds_write_b128 v2, a[76:79] offset:13056\n"
"s_waitcnt vmcnt(5)\n"
"v_mul_f32 v245, -v245, v244\n"
"v_mul_f32 v247, -v247, v246\n"
"v_mul_f32 v249, -v249, v248\n"
"v_mul_f32 v251, -v251, v250\n"
"v_mul_f32 v253, -v253, v252\n"
"s_waitcnt lgkmcnt(0)\n"
"s_barrier\n"
"ds_read_b128 v[128:131], v4 offset:0\n"
"ds_read_b128 v[132:135], v4 offset:20480\n"
"ds_read_b128 v[136:139], v4 offset:40960\n"
"ds_read_b128 v[140:143], v4 offset:61440\n"
"ds_read_b128 v[144:147], v5 offset:0\n"
"ds_read_b128 v[148:151], v5 offset:20480\n"
"ds_read_b128 v[152:155], v5 offset:40960\n"
"ds_read_b128 v[156:159], v5 offset:61440\n"
"ds_read_b128 v[160:163], v6 offset:0\n"
"ds_read_b128 v[164:167], v6 offset:20480\n"
"ds_read_b128 v[168:171], v6 offset:40960\n"
"ds_read_b128 v[172:175], v6 offset:61440\n"
"ds_read_b128 v[176:179], v7 offset:0\n"
"ds_read_b128 v[180:183], v7 offset:20480\n"
"ds_read_b128 v[184:187], v7 offset:40960\n"
"ds_read_b128 v[188:191], v7 offset:61440\n"
"ds_read_b128 v[192:195], v8 offset:0\n"
"ds_read_b128 v[196:199], v8 offset:20480\n"
"ds_read_b128 v[200:203], v8 offset:40960\n"
"ds_read_b128 v[204:207], v8 offset:61440\n"
"s_waitcnt vmcnt(0)\n"
"v_or3_b32 v76, v96, v97, v96\n"
"v_bitop3_b32 v77, v96, v97, v96 bitop3:0x80\n"
"v_or3_b32 v76, v98, v99, v76\n"
"v_bitop3_b32 v77, v98, v99, v77 bitop3:0x80\n"
"v_or3_b32 v76, v100, v101, v76\n"
"v_bitop3_b32 v77, v100, v101, v77 bitop3:0x80\n"
"v_or3_b32 v76, v102, v103, v76\n"
"v_bitop3_b32 v77, v102, v103, v77 bitop3:0x80\n"
"v_or3_b32 v76, v104, v105, v76\n"
"v_bitop3_b32 v77, v104, v105, v77 bitop3:0x80\n"
"v_or3_b32 v76, v106, v107, v76\n"
"v_bitop3_b32 v77, v106, v107, v77 bitop3:0x80\n"
"v_or3_b32 v76, v108, v109, v76\n"
"v_bitop3_b32 v77, v108, v109, v77 bitop3:0x80\n"
"v_or3_b32 v76, v110, v111, v76\n"
"v_bitop3_b32 v77, v110, v111, v77 bitop3:0x80\n"
"v_or3_b32 v76, v112, v113, v76\n"
"v_bitop3_b32 v77, v112, v113, v77 bitop3:0x80\n"
"v_or3_b32 v76, v114, v115, v76\n"
"v_bitop3_b32 v77, v114, v115, v77 bitop3:0x80\n"
"v_xor_b32 v76, v76, v20\n"
"v_xor_b32 v77, v77, v20\n"
"v_or_b32 v76, v76, v77\n"
"v_and_b32 v76, 0xfff, v76\n"
"v_cmp_ne_u32 vcc, 0, v76\n"
"s_nop 0\n"
"s_cmp_lg_u64 vcc, 0\n"
"s_cbranch_scc1 K1F_x_slow\n"
"K1F_x_back:\n"
"s_waitcnt lgkmcnt(15)\n"
"v_pk_add_f32 v[128:129], v[128:129], v[132:133]\n"
"v_pk_add_f32 v[136:137], v[136:137], v[140:141]\n"
"v_pk_add_f32 v[130:131], v[130:131], v[134:135]\n"
"v_pk_add_f32 v[138:139], v[138:139], v[142:143]\n"
"v_lshlrev_b32 v132, 16, v224\n"
"v_and_b32 v133, s74, v224\n"
"v_lshlrev_b32 v140, 16, v234\n"
"v_and_b32 v141, s74, v234\n"
"v_lshlrev_b32 v134, 16, v225\n"
"v_and_b32 v135, s74, v225\n"
"v_lshlrev_b32 v142, 16, v235\n"
"v_and_b32 v143, s74, v235\n"
"v_pk_add_f32 v[128:129], v[128:129], v[136:137]\n"
"v_pk_add_f32 v[130:131], v[130:131], v[138:139]\n"
"v_and_b32 v96, v21, v96\n"
"v_and_b32 v97, v21, v97\n"
"v_and_b32 v98, v21, v98\n"
"v_and_b32 v99, v21, v99\n"
"v_pk_add_f32 v[136:137], v[132:133], v[96:97]\n"
"v_pk_add_f32 v[138:139], v[134:135], v[98:99]\n"
"v_pk_fma_f32 v[132:133], v[132:133], v[244:245], v[244:245] op_sel:[0,0,1] op_sel_hi:[1,0,1]\n"
"v_pk_add_f32 v[128:129], v[128:129], v[136:137]\n"
"v_pk_fma_f32 v[134:135], v[134:135], v[244:245], v[244:245] op_sel:[0,0,1] op_sel_hi:[1,0,1]\n"
"v_pk_add_f32 v[130:131], v[130:131], v[138:139]\n"
"v_lshlrev_b32 v76, 1, v10\n"
"v_pk_fma_f32 v[128:129], v[132:133], v[140:141], v[128:129]\n"
"v_pk_fma_f32 v[130:131], v[134:135], v[142:143], v[130:131]\n"
"buffer_store_dwordx4 v[128:131], v76, s[52:55], 0 offen sc1\n"
"s_waitcnt lgkmcnt(12)\n"
"v_pk_add_f32 v[144:145], v[144:145], v[148:149]\n"
"v_pk_add_f32 v[152:153], v[152:153], v[156:157]\n"
"v_pk_add_f32 v[146:147], v[146:147], v[150:151]\n"
"v_pk_add_f32 v[154:155], v[154:155], v[158:159]\n"
"v_lshlrev_b32 v148, 16, v226\n"
"v_and_b32 v149, s74, v226\n"
"v_lshlrev_b32 v156, 16, v236\n"
"v_and_b32 v157, s74, v236\n"
"v_lshlrev_b32 v150, 16, v227\n"
"v_and_b32 v151, s74, v227\n"
"v_lshlrev_b32 v158, 16, v237\n"
"v_and_b32 v159, s74, v237\n"
"v_pk_add_f32 v[144:145], v[144:145], v[152:153]\n"
"v_pk_add_f32 v[146:147], v[146:147], v[154:155]\n"
"v_and_b32 v100, v21, v100\n"
"v_and_b32 v101, v21, v101\n"
"v_and_b32 v102, v21, v102\n"
"v_and_b32 v103, v21, v103\n"
"v_pk_add_f32 v[152:153], v[148:149], v[100:101]\n"
"v_pk_add_f32 v[154:155], v[150:151], v[102:103]\n"
"v_pk_fma_f32 v[148:149], v[148:149], v[246:247], v[246:247] op_sel:[0,0,1] op_sel_hi:[1,0,1]\n"
"v_pk_add_f32 v[144:145], v[144:145], v[152:153]\n"
"v_pk_fma_f32 v[150:151], v[150:151], v[246:247], v[246:247] op_sel:[0,0,1] op_sel_hi:[1,0,1]\n"
"v_pk_add_f32 v[146:147], v[146:147], v[154:155]\n"
"v_lshlrev_b32 v76, 1, v11\n"
"v_pk_fma_f32 v[144:145], v[148:149], v[156:157], v[144:145]\n"
"v_pk_fma_f32 v[146:147], v[150:151], v[158:159], v[146:147]\n"
"buffer_store_dwordx4 v[144:147], v76, s[52:55], 0 offen sc1\n"
"s_waitcnt lgkmcnt(8)\n"
"v_pk_add_f32 v[160:161], v[160:161], v[164:165]\n"
"v_pk_add_f32 v[168:169], v[168:169], v[172:173]\n"
"v_pk_add_f32 v[162:163], v[162:163], v[166:167]\n"
"v_pk_add_f32 v[170:171], v[170:171], v[174:175]\n"
"v_lshlrev_b32 v164, 16, v228\n"
"v_and_b32 v165, s74, v228\n"
"v_lshlrev_b32 v172, 16, v238\n"
"v_and_b32 v173, s74, v238\n"
"v_lshlrev_b32 v166, 16, v229\n"
"v_and_b32 v167, s74, v229\n"
"v_lshlrev_b32 v174, 16, v239\n"
"v_and_b32 v175, s74, v239\n"
"v_pk_add_f32 v[160:161], v[160:161], v[168:169]\n"
"v_pk_add_f32 v[162:163], v[162:163], v[170:171]\n"
"v_and_b32 v104, v21, v104\n"
"v_and_b32 v105, v21, v105\n"
"v_and_b32 v106, v21, v106\n"
"v_and_b32 v107, v21, v107\n"
"v_pk_add_f32 v[168:169], v[164:165], v[104:105]\n"
"v_pk_add_f32 v[170:171], v[166:167], v[106:107]\n"
"v_pk_fma_f32 v[164:165], v[164:165], v[248:249], v[248:249] op_sel:[0,0,1] op_sel_hi:[1,0,1]\n"
"v_pk_add_f32 v[160:161], v[160:161], v[168:169]\n"
"v_pk_fma_f32 v[166:167], v[166:167], v[248:249], v[248:249] op_sel:[0,0,1] op_sel_hi:[1,0,1]\n"
"v_pk_add_f32 v[162:163], v[162:163], v[170:171]\n"
"v_lshlrev_b32 v76, 1, v12\n"
"v_pk_fma_f32 v[160:161], v[164:165], v[172:173], v[160:161]\n"
"v_pk_fma_f32 v[162:163], v[166:167], v[174:175], v[162:163]\n"
"buffer_store_dwordx4 v[160:163], v76, s[52:55], 0 offen sc1\n"
"s_waitcnt lgkmcnt(4)\n"
"v_pk_add_f32 v[176:177], v[176:177], v[180:181]\n"
"v_pk_add_f32 v[184:185], v[184:185], v[188:189]\n"
"v_pk_add_f32 v[178:179], v[178:179], v[182:183]\n"
"v_pk_add_f32 v[186:187], v[186:187], v[190:191]\n"
"v_lshlrev_b32 v180, 16, v230\n"
"v_and_b32 v181, s74, v230\n"
"v_lshlrev_b32 v188, 16, v240\n"
"v_and_b32 v189, s74, v240\n"
"v_lshlrev_b32 v182, 16, v231\n"
"v_and_b32 v183, s74, v231\n"
"v_lshlrev_b32 v190, 16, v241\n"
"v_and_b32 v191, s74, v241\n"
"v_pk_add_f32 v[176:177], v[176:177], v[184:185]\n"
"v_pk_add_f32 v[178:179], v[178:179], v[186:187]\n"
"v_and_b32 v108, v21, v108\n"
"v_and_b32 v109, v21, v109\n"
"v_and_b32 v110, v21, v110\n"
"v_and_b32 v111, v21, v111\n"
"v_pk_add_f32 v[184:185], v[180:181], v[108:109]\n"
"v_pk_add_f32 v[186:187], v[182:183], v[110:111]\n"
"v_pk_fma_f32 v[180:181], v[180:181], v[250:251], v[250:251] op_sel:[0,0,1] op_sel_hi:[1,0,1]\n"
"v_pk_add_f32 v[176:177], v[176:177], v[184:185]\n"
"v_pk_fma_f32 v[182:183], v[182:183], v[250:251], v[250:251] op_sel:[0,0,1] op_sel_hi:[1,0,1]\n"
"v_pk_add_f32 v[178:179], v[178:179], v[186:187]\n"
"v_lshlrev_b32 v76, 1, v13\n"
"v_pk_fma_f32 v[176:177], v[180:181], v[188:189], v[176:177]\n"
"v_pk_fma_f32 v[178:179], v[182:183], v[190:191], v[178:179]\n"
"buffer_store_dwordx4 v[176:179], v76, s[52:55], 0 offen sc1\n"
"s_waitcnt lgkmcnt(0)\n"
"v_pk_add_f32 v[192:193], v[192:193], v[196:197]\n"
"v_pk_add_f32 v[200:201], v[200:201], v[204:205]\n"
"v_pk_add_f32 v[194:195], v[194:195], v[198:199]\n"
"v_pk_add_f32 v[202:203], v[202:203], v[206:207]\n"
"v_lshlrev_b32 v196, 16, v232\n"
"v_and_b32 v197, s74, v232\n"
"v_lshlrev_b32 v204, 16, v242\n"
"v_and_b32 v205, s74, v242\n"
"v_lshlrev_b32 v198, 16, v233\n"
"v_and_b32 v199, s74, v233\n"
"v_lshlrev_b32 v206, 16, v243\n"
"v_and_b32 v207, s74, v243\n"
"v_pk_add_f32 v[192:193], v[192:193], v[200:201]\n"
"v_pk_add_f32 v[194:195], v[194:195], v[202:203]\n"
"v_and_b32 v112, v21, v112\n"
"v_and_b32 v113, v21, v113\n"
"v_and_b32 v114, v21, v114\n"
"v_and_b32 v115, v21, v115\n"
"v_pk_add_f32 v[200:201], v[196:197], v[112:113]\n"
"v_pk_add_f32 v[202:203], v[198:199], v[114:115]\n"
"v_pk_fma_f32 v[196:197], v[196:197], v[252:253], v[252:253] op_sel:[0,0,1] op_sel_hi:[1,0,1]\n"
"v_pk_add_f32 v[192:193], v[192:193], v[200:201]\n"
"v_pk_fma_f32 v[198:199], v[198:199], v[252:253], v[252:253] op_sel:[0,0,1] op_sel_hi:[1,0,1]\n"
"v_pk_add_f32 v[194:195], v[194:195], v[202:203]\n"
"v_lshlrev_b32 v76, 1, v14\n"
"v_pk_fma_f32 v[192:193], v[196:197], v[204:205], v[192:193]\n"
"v_pk_fma_f32 v[194:195], v[198:199], v[206:207], v[194:195]\n"
"buffer_store_dwordx4 v[192:195], v76, s[52:55], 0 offen sc1\n"
"s_endpgm\n"
"K1X:\n"
"s_nop 4\n"
"s_mov_b32 s15, s9\n"
"s_bitcmp1_b32 s7, 0\n"
"s_cselect_b32 s56, s24, s22\n"
"s_cselect_b32 s57, s25, s23\n"
"s_cselect_b32 s48, s34, s32\n"
"s_cselect_b32 s49, s35, s33\n"
"s_cselect_b32 s69, 4, 0\n"
"s_and_b32 s49, s49, 0xffff\n"
"s_mov_b32 s50, 2211840\n"
"s_mov_b32 s51, 0x20000\n"
"s_lshr_b32 s13, s7, 1\n"
"s_mul_i32 s13, s13, 864\n"
"s_lshl_b32 s68, s9, 1\n"
"s_add_u32 s13, s13, s68\n"
"s_lshl_b32 s13, s13, 7\n"
"v_and_b32 v71, 1, v70\n"
"v_lshl_or_b32 v71, v71, 1, s69\n"
"v_xor_b32 v71, v69, v71\n"
"v_mul_u32_u24 v73, 3456, v68\n"
"v_lshl_add_u32 v18, v71, 4, v73\n"
"v_add_u32 v19, 26624, v18\n"
"v_add_u32 v20, 53248, v18\n"
"v_add_u32 v21, 79872, v18\n"
"s_and_b32 s57, s57, 0xffff\n"
"s_mov_b32 s58, 819200\n"
"s_mov_b32 s59, 0x20000\n"
"s_lshl_b32 s14, s15, 1\n"
"s_lshr_b32 s68, s7, 1\n"
"s_add_u32 s14, s14, s68\n"
"s_mul_i32 s14, s14, 20480\n"
"s_mov_b64 s[52:53], s[34:35]\n"
"s_and_b32 s53, s53, 0xffff\n"
"s_mov_b32 s54, 2211840\n"
"s_mov_b32 s55, 0x20000\n"
"s_mov_b64 s[96:97], s[32:33]\n"
"s_and_b32 s97, s97, 0xffff\n"
"s_mov_b32 s98, 2211840\n"
"s_mov_b32 s99, 0x20000\n"
"s_mov_b32 s42, s8\n"
"s_mov_b64 s[64:65], s[38:39]\n"
"s_and_b32 s65, s65, 0xffff\n"
"s_mov_b32 s66, 35651584\n"
"s_mov_b32 s67, 0x20000\n"
"v_mov_b32 v71, 23068672\n"
"buffer_load_dword v152, v71, s[64:67], 0 offen offset:0 sc1\n"
"buffer_load_dword v153, v71, s[64:67], 0 offen offset:4 sc1\n"
"s_lshl_b32 s68, s8, 4\n"
"s_add_u32 s68, s68, s9\n"
"s_lshl_b32 s68, s68, 2\n"
"s_add_u32 s68, s68, s7\n"
"s_mul_i32 s69, s68, 2560\n"
"v_lshrrev_b32 v71, 5, v27\n"
"v_and_b32 v73, 31, v27\n"
"v_mul_u32_u24 v74, 1280, v71\n"
"v_add_u32 v74, s69, v74\n"
"v_lshl_add_u32 v75, v73, 4, v74\n"
"v_lshl_add_u32 v74, v73, 3, v74\n"
"global_load_dwordx4 v[128:131], v75, s[16:17]\n"
"global_load_dwordx4 v[132:135], v75, s[16:17] offset:512\n"
"global_load_dwordx2 v[136:137], v74, s[16:17] offset:1024\n"
"s_lshl_b32 s68, s68, 1\n"
"v_add_u32 v154, s68, v71\n"
"s_bitcmp1_b32 s8, 2\n"
"s_cselect_b32 s60, s24, s22\n"
"s_cselect_b32 s61, s25, s23\n"
"s_and_b32 s68, s8, 3\n"
"s_lshl_b32 s68, s68, 2\n"
"s_add_u32 s68, s68, s7\n"
"s_lshl_b32 s69, s15, 4\n"
"s_add_u32 s68, s68, s69\n"
"s_mul_i32 s68, s68, 2560\n"
"v_mul_u32_u24 v74, 1280, v71\n"
"v_add_u32 v74, s68, v74\n"
"v_lshl_add_u32 v75, v73, 4, v74\n"
"v_lshl_add_u32 v74, v73, 3, v74\n"
"global_load_dwordx4 v[138:141], v75, s[60:61]\n"
"global_load_dwordx4 v[142:145], v75, s[60:61] offset:512\n"
"global_load_dwordx2 v[146:147], v74, s[60:61] offset:1024\n"
"s_lshl_b32 s69, s15, 6\n"
"v_lshl_add_u32 v74, v73, 1, s69\n"
"global_load_ushort v148, v74, s[18:19] offset:0\n"
"global_load_ushort v149, v74, s[20:21] offset:0\n"
"global_load_ushort v150, v74, s[30:31] offset:0\n"
"v_lshrrev_b32 v68, 3, v27\n"
"v_and_b32 v69, 7, v27\n"
"v_mul_u32_u24 v156, 3456, v68\n"
"v_lshl_add_u32 v156, v69, 4, v156\n"
"s_mul_i32 s68, s7, 27648\n"
"s_lshl_b32 s71, s42, 7\n"
"s_add_u32 s71, s71, s68\n"
"v_lshrrev_b32 v68, 5, v27\n"
"buffer_load_dwordx4 a[200:203], v156, s[96:99], s71 offen\n"
"v_and_b32 v69, 3, v27\n"
"v_bfe_u32 v70, v27, 3, 2\n"
"v_lshl_add_u32 v69, v70, 2, v69\n"
"v_bfe_u32 v70, v27, 2, 1\n"
"v_lshl_add_u32 v1, v70, 4, v69\n"
"v_lshrrev_b32 v69, 1, v69\n"
"v_xor_b32 v69, v69, v68\n"
"v_lshlrev_b32 v69, 4, v69\n"
"v_lshl_add_u32 v69, v1, 7, v69\n"
"buffer_load_dwordx4 a[204:207], v156, s[52:55], s71 offen\n"
"v_add_u32 v14, s10, v69\n"
"v_xor_b32 v15, 32, v14\n"
"v_xor_b32 v16, 64, v14\n"
"v_xor_b32 v17, 96, v14\n"
"v_bfe_u32 v69, v27, 1, 3\n"
"v_xor_b32 v69, v69, v68\n"
"v_and_b32 v70, 15, v27\n"
"v_lshlrev_b32 v70, 7, v70\n"
"v_lshl_add_u32 v70, v69, 4, v70\n"
"s_add_u32 s71, s71, 110592\n"
"buffer_load_dwordx4 a[208:211], v156, s[96:99], s71 offen\n"
"v_bfe_u32 v69, v27, 4, 1\n"
"v_mul_u32_u24 v69, 81920, v69\n"
"v_add_u32 v10, v69, v70\n"
"v_xor_b32 v11, 32, v10\n"
"v_xor_b32 v12, 64, v10\n"
"v_xor_b32 v13, 96, v10\n"
"v_bfe_u32 v69, v27, 2, 2\n"
"v_lshl_add_u32 v69, v68, 3, v69\n"
"v_lshlrev_b32 v69, 7, v69\n"
"buffer_load_dwordx4 a[212:215], v156, s[52:55], s71 offen\n"
"v_bfe_u32 v70, v27, 1, 1\n"
"v_mul_u32_u24 v70, 41024, v70\n"
"v_bfe_u32 v71, v27, 3, 1\n"
"v_lshl_add_u32 v69, v71, 5, v69\n"
"v_and_b32 v71, 1, v27\n"
"v_lshl_add_u32 v69, v71, 4, v69\n"
"v_bfe_u32 v71, v27, 4, 1\n"
"v_lshl_add_u32 v69, v71, 3, v69\n"
"v_add_u32 v2, v69, v70\n"
"s_add_u32 s71, s71, 110592\n"
"buffer_load_dwordx4 a[216:219], v156, s[96:99], s71 offen\n"
"v_xor_b32 v3, 32, v2\n"
"v_xor_b32 v4, 64, v2\n"
"v_xor_b32 v5, 96, v2\n"
"v_add_u32 v6, 81920, v2\n"
"v_add_u32 v7, 81920, v3\n"
"v_add_u32 v8, 81920, v4\n"
"v_add_u32 v9, 81920, v5\n"
"v_lshlrev_b32 v28, 4, v27\n"
"s_mov_b32 s74, 0xffff0000\n"
"buffer_load_dwordx4 a[220:223], v156, s[52:55], s71 offen\n"
"s_lshl_b32 s69, s6, 2\n"
"s_add_u32 s69, s69, s7\n"
"s_cmp_lt_u32 s69, 740\n"
"s_cselect_b32 s26, s26, s28\n"
"s_cselect_b32 s27, s27, s29\n"
"s_cselect_b32 s70, 0, 200\n"
"s_cselect_b32 s29, 0, 8\n"
"s_add_u32 s70, s70, 540\n"
"s_cmp_lt_u32 s69, 540\n"
"s_cselect_b32 s26, s36, s26\n"
"s_cselect_b32 s27, s37, s27\n"
"s_cselect_b32 s70, 0, s70\n"
"s_cselect_b32 s75, 1, 0\n"
"s_add_u32 s71, s71, 110592\n"
"buffer_load_dwordx4 a[224:227], v156, s[96:99], s71 offen\n"
"s_sub_u32 s70, s69, s70\n"
"s_mul_i32 s68, s70, 6554\n"
"s_lshr_b32 s68, s68, 16\n"
"s_mul_i32 s43, s68, 10\n"
"s_sub_u32 s70, s70, s43\n"
"s_cmp_lt_u32 s69, 940\n"
"s_cselect_b32 s28, 39808, 0\n"
"s_lshl_b32 s69, s70, 2\n"
"s_add_u32 s43, s68, 40\n"
"buffer_load_dwordx4 a[228:231], v156, s[52:55], s71 offen\n"
"s_cmp_eq_u32 s75, 1\n"
"s_cselect_b32 s43, s43, s69\n"
"s_and_b32 s69, s43, 0x7c\n"
"s_add_u32 s69, s69, 0xf0\n"
"s_load_dword s11, s[4:5], s69\n"
"s_mov_b32 s40, s68\n"
"s_mov_b32 s36, s70\n"
"s_waitcnt lgkmcnt(0)\n"
"s_mul_i32 s68, s40, 320\n"
"s_add_u32 s68, s68, s36\n"
"s_add_u32 s71, s71, 110592\n"
"buffer_load_dwordx4 a[232:235], v156, s[96:99], s71 offen\n"
"s_lshl_b32 s68, s68, 7\n"
"s_add_u32 s60, s26, s68\n"
"s_addc_u32 s61, s27, 0\n"
"s_mov_b32 s62, s28\n"
"s_and_b32 s61, s61, 0xffff\n"
"s_mov_b32 s63, 0x20000\n"
"s_and_b32 s68, s43, 3\n"
"s_lshl_b32 s68, s68, 3\n"
"s_add_u32 s68, s68, s29\n"
"buffer_load_dwordx4 a[236:239], v156, s[52:55], s71 offen\n"
"s_lshr_b32 s11, s11, s68\n"
"s_and_b32 s28, s11, 0xff\n"
"s_bfe_u32 s29, s11, 0x80010\n"
"s_mul_i32 s28, s28, 40\n"
"s_mul_i32 s29, s29, 40\n"
"s_lshl_b32 s68, s36, 2\n"
"s_lshl_b32 s69, s40, 1\n"
"s_add_u32 s70, s28, 2\n"
"s_cmp_eq_u32 s75, 1\n"
"s_cselect_b32 s68, s68, s69\n"
"s_cselect_b32 s29, s70, s29\n"
"s_add_u32 s71, s71, 110592\n"
"buffer_load_dwordx4 a[240:243], v156, s[96:99], s71 offen\n"
"s_add_u32 s28, s28, s68\n"
"s_add_u32 s29, s29, s68\n"
"s_cmp_eq_u32 s62, 0\n"
"s_cselect_b32 s68, 2130706432, 0\n"
"s_add_u32 s68, s68, 16777216\n"
"s_lshl_b32 s28, s28, 10\n"
"s_lshl_b32 s29, s29, 10\n"
"s_add_u32 s28, s28, s68\n"
"s_add_u32 s29, s29, s68\n"
"buffer_load_dwordx4 a[244:247], v156, s[52:55], s71 offen\n"
"s_lshl_b32 s68, s15, 3\n"
"s_and_b32 s69, s8, 7\n"
"s_add_u32 s68, s68, s69\n"
"s_lshl_b32 s68, s68, 2\n"
"s_add_u32 s68, s68, s7\n"
"s_lshl_b32 s68, s68, 5\n"
"s_add_u32 s68, s68, 21092352\n"
"s_cmp_lt_u32 s8, 8\n"
"s_cselect_b32 s88, s68, 2130706432\n"
"s_add_u32 s71, s71, 110592\n"
"buffer_load_dwordx4 a[248:251], v156, s[96:99], s71 offen\n"
"s_lshl_b32 s43, s8, 1\n"
"s_lshr_b32 s68, s7, 1\n"
"s_add_u32 s43, s43, s68\n"
"s_mul_i32 s43, s43, 393216\n"
"s_and_b32 s68, s7, 1\n"
"s_lshl_b32 s68, s68, 11\n"
"s_add_u32 s43, s43, s68\n"
"s_lshl_b32 s68, s15, 4\n"
"s_add_u32 s68, s68, s8\n"
"buffer_load_dwordx4 a[252:255], v156, s[52:55], s71 offen\n"
"s_lshl_b32 s68, s68, 9\n"
"s_add_u32 s68, s68, 22020096\n"
"s_lshl_b32 s69, s7, 11\n"
"s_sub_u32 s90, s68, s69\n"
"s_sub_u32 s90, s90, 512\n"
"s_cmp_eq_u32 s7, 3\n"
"s_cselect_b32 s89, s68, 2130706432\n"
"s_lshl_b32 s68, s8, 2\n"
"s_add_u32 s68, s68, s7\n"
"s_add_u32 s71, s71, 110592\n"
"buffer_load_dwordx4 v[160:163], v156, s[96:99], s71 offen\n"
"s_lshl_b32 s68, s68, 9\n"
"s_add_u32 s91, s68, 20971520\n"
"s_lshl_b32 s92, s9, 12\n"
"s_lshl_b32 s93, s15, 10\n"
"s_add_u32 s93, s93, 55296\n"
"s_lshl_b32 s68, s6, 7\n"
"s_add_u32 s68, s68, 23072768\n"
"s_cmp_eq_u32 s7, 0\n"
"s_cselect_b32 s94, s68, 2130706432\n"
"buffer_load_dwordx4 v[164:167], v156, s[52:55], s71 offen\n"
"s_waitcnt vmcnt(25)\n"
"v_readfirstlane_b32 s72, v152\n"
"v_readfirstlane_b32 s73, v153\n"
"s_or_b32 s72, s72, 0x40004000\n"
"s_or_b32 s73, s73, 0x40004000\n"
"s_add_u32 s72, s72, 1\n"
"s_add_u32 s73, s73, 0x1e3739b9\n"
"s_and_b32 s72, s72, 0xbfffbfff\n"
"s_and_b32 s73, s73, 0xbfffbfff\n"
"s_waitcnt vmcnt(24)\n"
"v_lshlrev_b32 v76, 16, v128\n"
"s_add_u32 s71, s71, 110592\n"
"buffer_load_dwordx4 v[168:171], v156, s[96:99], s71 offen\n"
"v_and_b32 v77, s74, v128\n"
"v_add_f32 v68, v76, v77\n"
"v_mul_f32 v69, v76, v76\n"
"v_fmac_f32 v69, v77, v77\n"
"v_lshlrev_b32 v76, 16, v129\n"
"v_and_b32 v77, s74, v129\n"
"v_add_f32 v68, v68, v76\n"
"v_fmac_f32 v69, v76, v76\n"
"v_add_f32 v68, v68, v77\n"
"buffer_load_dwordx4 v[172:175], v156, s[52:55], s71 offen\n"
"v_fmac_f32 v69, v77, v77\n"
"v_lshlrev_b32 v76, 16, v130\n"
"v_and_b32 v77, s74, v130\n"
"v_add_f32 v68, v68, v76\n"
"v_fmac_f32 v69, v76, v76\n"
"v_add_f32 v68, v68, v77\n"
"v_fmac_f32 v69, v77, v77\n"
"v_lshlrev_b32 v76, 16, v131\n"
"v_and_b32 v77, s74, v131\n"
"s_add_u32 s71, s71, 110592\n"
"buffer_load_dwordx4 v[176:179], v156, s[96:99], s71 offen\n"
"v_add_f32 v68, v68, v76\n"
"v_fmac_f32 v69, v76, v76\n"
"v_add_f32 v68, v68, v77\n"
"v_fmac_f32 v69, v77, v77\n"
"s_waitcnt vmcnt(26)\n"
"v_lshlrev_b32 v76, 16, v132\n"
"v_and_b32 v77, s74, v132\n"
"v_add_f32 v68, v68, v76\n"
"v_fmac_f32 v69, v76, v76\n"
"v_add_f32 v68, v68, v77\n"
"buffer_load_dwordx4 v[180:183], v156, s[52:55], s71 offen\n"
"v_fmac_f32 v69, v77, v77\n"
"v_lshlrev_b32 v76, 16, v133\n"
"v_and_b32 v77, s74, v133\n"
"v_add_f32 v68, v68, v76\n"
"v_fmac_f32 v69, v76, v76\n"
"v_add_f32 v68, v68, v77\n"
"v_fmac_f32 v69, v77, v77\n"
"v_lshlrev_b32 v76, 16, v134\n"
"v_and_b32 v77, s74, v134\n"
"s_add_u32 s71, s71, 110592\n"
"buffer_load_dwordx4 v[184:187], v156, s[96:99], s71 offen\n"
"v_add_f32 v68, v68, v76\n"
"v_fmac_f32 v69, v76, v76\n"
"v_add_f32 v68, v68, v77\n"
"v_fmac_f32 v69, v77, v77\n"
"v_lshlrev_b32 v76, 16, v135\n"
"v_and_b32 v77, s74, v135\n"
"v_add_f32 v68, v68, v76\n"
"v_fmac_f32 v69, v76, v76\n"
"v_add_f32 v68, v68, v77\n"
"buffer_load_dwordx4 v[188:191], v156, s[52:55], s71 offen\n"
"v_fmac_f32 v69, v77, v77\n"
"s_waitcnt vmcnt(28)\n"
"v_lshlrev_b32 v76, 16, v136\n"
"v_and_b32 v77, s74, v136\n"
"v_add_f32 v68, v68, v76\n"
"v_fmac_f32 v69, v76, v76\n"
"v_add_f32 v68, v68, v77\n"
"v_fmac_f32 v69, v77, v77\n"
"v_lshlrev_b32 v76, 16, v137\n"
"v_and_b32 v77, s74, v137\n"
"s_add_u32 s71, s71, 110592\n"
"buffer_load_dwordx4 v[192:195], v156, s[96:99], s71 offen\n"
"v_add_f32 v68, v68, v76\n"
"v_fmac_f32 v69, v76, v76\n"
"v_add_f32 v68, v68, v77\n"
"v_fmac_f32 v69, v77, v77\n"
"s_waitcnt vmcnt(28)\n"
"v_lshlrev_b32 v76, 16, v138\n"
"v_and_b32 v77, s74, v138\n"
"v_add_f32 v70, v76, v77\n"
"v_lshlrev_b32 v76, 16, v139\n"
"v_and_b32 v77, s74, v139\n"
"buffer_load_dwordx4 v[196:199], v156, s[52:55], s71 offen\n"
"v_add_f32 v70, v70, v76\n"
"v_add_f32 v70, v70, v77\n"
"v_lshlrev_b32 v76, 16, v140\n"
"v_and_b32 v77, s74, v140\n"
"v_add_f32 v70, v70, v76\n"
"v_add_f32 v70, v70, v77\n"
"v_lshlrev_b32 v76, 16, v141\n"
"v_and_b32 v77, s74, v141\n"
"v_add_f32 v70, v70, v76\n"
"s_add_u32 s71, s71, 110592\n"
"buffer_load_dwordx4 v[200:203], v156, s[96:99], s71 offen\n"
"v_add_f32 v70, v70, v77\n"
"s_waitcnt vmcnt(29)\n"
"v_lshlrev_b32 v76, 16, v142\n"
"v_and_b32 v77, s74, v142\n"
"v_add_f32 v70, v70, v76\n"
"v_add_f32 v70, v70, v77\n"
"v_lshlrev_b32 v76, 16, v143\n"
"v_and_b32 v77, s74, v143\n"
"v_add_f32 v70, v70, v76\n"
"v_add_f32 v70, v70, v77\n"
"buffer_load_dwordx4 v[204:207], v156, s[52:55], s71 offen\n"
"v_lshlrev_b32 v76, 16, v144\n"
"v_and_b32 v77, s74, v144\n"
"v_add_f32 v70, v70, v76\n"
"v_add_f32 v70, v70, v77\n"
"v_lshlrev_b32 v76, 16, v145\n"
"v_and_b32 v77, s74, v145\n"
"v_add_f32 v70, v70, v76\n"
"v_add_f32 v70, v70, v77\n"
"s_waitcnt vmcnt(29)\n"
"v_lshlrev_b32 v76, 16, v146\n"
"s_add_u32 s71, s71, 110592\n"
"buffer_load_dwordx4 v[208:211], v156, s[96:99], s71 offen\n"
"v_and_b32 v77, s74, v146\n"
"v_add_f32 v70, v70, v76\n"
"v_add_f32 v70, v70, v77\n"
"v_lshlrev_b32 v76, 16, v147\n"
"v_and_b32 v77, s74, v147\n"
"v_add_f32 v70, v70, v76\n"
"v_add_f32 v70, v70, v77\n"
"v_add_f32_dpp v68, v68, v68 row_ror:8 row_mask:0xf bank_mask:0xf\n"
"v_add_f32_dpp v69, v69, v69 row_ror:8 row_mask:0xf bank_mask:0xf\n"
"buffer_load_dwordx4 v[212:215], v156, s[52:55], s71 offen\n"
"v_add_f32_dpp v70, v70, v70 row_ror:8 row_mask:0xf bank_mask:0xf\n"
"v_add_f32_dpp v68, v68, v68 row_ror:4 row_mask:0xf bank_mask:0xf\n"
"v_add_f32_dpp v69, v69, v69 row_ror:4 row_mask:0xf bank_mask:0xf\n"
"v_add_f32_dpp v70, v70, v70 row_ror:4 row_mask:0xf bank_mask:0xf\n"
"v_add_f32_dpp v68, v68, v68 row_ror:2 row_mask:0xf bank_mask:0xf\n"
"v_add_f32_dpp v69, v69, v69 row_ror:2 row_mask:0xf bank_mask:0xf\n"
"v_add_f32_dpp v70, v70, v70 row_ror:2 row_mask:0xf bank_mask:0xf\n"
"v_add_f32_dpp v68, v68, v68 row_ror:1 row_mask:0xf bank_mask:0xf\n"
"v_add_f32_dpp v69, v69, v69 row_ror:1 row_mask:0xf bank_mask:0xf\n"
"s_add_u32 s71, s71, 110592\n"
"buffer_load_dwordx4 v[216:219], v156, s[96:99], s71 offen\n"
"v_add_f32_dpp v70, v70, v70 row_ror:1 row_mask:0xf bank_mask:0xf\n"
"v_mov_b32 v74, v68\n"
"v_mov_b32 v75, v69\n"
"v_mov_b32 v76, v70\n"
"v_permlane16_swap_b32 v68, v74\n"
"v_permlane16_swap_b32 v69, v75\n"
"v_permlane16_swap_b32 v70, v76\n"
"v_add_f32 v68, v68, v74\n"
"v_add_f32 v69, v69, v75\n"
"buffer_load_dwordx4 v[220:223], v156, s[52:55], s71 offen\n"
"v_add_f32 v70, v70, v76\n"
"v_mul_f32 v68, 0x3acccccd, v68\n"
"v_mul_f32 v69, 0x3acccccd, v69\n"
"v_mov_b32 v129, s72\n"
"v_fma_f32 v69, -v68, v68, v69\n"
"v_mov_b32 v131, s73\n"
"v_max_f32 v69, 0, v69\n"
"v_cvt_pk_bf16_f32 v68, v68, v69\n"
"v_and_b32 v69, s74, v68\n"
"s_add_u32 s71, s71, 110592\n"
"buffer_load_dwordx4 v[224:227], v156, s[96:99], s71 offen\n"
"v_lshlrev_b32 v130, 16, v68\n"
"v_add_f32 v69, 0x3727c5ac, v69\n"
"v_cvt_pk_bf16_f32 v69, v69, v69\n"
"v_and_b32 v69, s74, v69\n"
"v_sqrt_f32 v69, v69\n"
"v_and_b32 v76, 31, v27\n"
"v_cvt_pk_bf16_f32 v69, v69, v69\n"
"v_cmp_eq_u32 vcc, 0, v76\n"
"v_and_b32 v69, s74, v69\n"
"buffer_load_dwordx4 v[228:231], v156, s[52:55], s71 offen\n"
"v_lshlrev_b32 v76, 4, v154\n"
"v_rcp_f32 v128, v69\n"
"v_add_u32 v76, 20971520, v76\n"
"v_mov_b32 v77, 2130706432\n"
"v_cndmask_b32 v76, v77, v76, vcc\n"
"buffer_store_dwordx4 v[128:131], v76, s[64:67], 0 offen sc1\n"
"v_lshrrev_b32 v76, 1, v27\n"
"v_cndmask_b32 v76, v77, v76, vcc\n"
"v_cvt_pk_bf16_f32 v77, v70, v70\n"
"s_add_u32 s71, s71, 110592\n"
"buffer_load_dwordx4 v[232:235], v156, s[96:99], s71 offen\n"
"v_and_b32 v77, s74, v77\n"
"v_sub_f32 v70, v70, v77\n"
"v_cvt_pk_bf16_f32 v132, v77, v70\n"
"v_and_b32 v77, s74, v132\n"
"v_sub_f32 v70, v70, v77\n"
"v_cvt_pk_bf16_f32 v134, v70, 0\n"
"v_mov_b32 v133, s72\n"
"v_mov_b32 v135, s73\n"
"buffer_store_dwordx4 v[132:135], v76, s[64:67], s88 offen offset:0 sc1\n"
"buffer_load_dwordx4 v[236:239], v156, s[52:55], s71 offen\n"
"s_add_u32 s71, s71, 110592\n"
"buffer_load_dwordx4 v[240:243], v156, s[96:99], s71 offen\n"
"buffer_load_dwordx4 v[244:247], v156, s[52:55], s71 offen\n"
"s_add_u32 s71, s71, 110592\n"
"buffer_load_dwordx4 v[248:251], v156, s[96:99], s71 offen\n"
"buffer_load_dwordx4 v[252:255], v156, s[52:55], s71 offen\n"
"s_add_u32 s71, s71, 110592\n"
"buffer_load_dwordx4 v[96:99], v156, s[96:99], s71 offen\n"
"buffer_load_dwordx4 v[100:103], v156, s[52:55], s71 offen\n"
"v_and_b32 v68, 15, v27\n"
"v_and_b32 v69, 1, v68\n"
"v_lshlrev_b32 v69, 4, v69\n"
"v_mov_b32 v70, 0x3f80\n"
"v_lshlrev_b32 v69, v69, v70\n"
"v_lshrrev_b32 v68, 1, v68\n"
"v_cmp_eq_u32 vcc, 0, v68\n"
"s_nop 1\n"
"v_cndmask_b32 v88, 0, v69, vcc\n"
"v_cmp_eq_u32 vcc, 1, v68\n"
"s_nop 1\n"
"v_cndmask_b32 v89, 0, v69, vcc\n"
"v_cmp_eq_u32 vcc, 2, v68\n"
"s_nop 1\n"
"v_cndmask_b32 v90, 0, v69, vcc\n"
"v_cmp_eq_u32 vcc, 3, v68\n"
"s_nop 1\n"
"v_cndmask_b32 v91, 0, v69, vcc\n"
"s_waitcnt vmcnt(41)\n"
"s_nop 0\n"
"v_mfma_f32_16x16x32_bf16 v[80:83], a[200:203], v[88:91], 0\n"
"s_add_u32 m0, s10, 36864\n"
"s_nop 0\n"
"buffer_load_dwordx4 v18, s[48:51], s13 offen offset:0 lds\n"
"s_waitcnt vmcnt(41)\n"
"v_mfma_f32_16x16x32_bf16 v[84:87], a[204:207], v[88:91], 0\n"
"s_waitcnt vmcnt(40)\n"
"v_mfma_f32_16x16x32_bf16 v[80:83], a[208:211], v[88:91], v[80:83]\n"
"buffer_load_dwordx4 v19, s[48:51], s13 offen offset:1024 lds\n"
"s_waitcnt vmcnt(40)\n"
"v_mfma_f32_16x16x32_bf16 v[84:87], a[212:215], v[88:91], v[84:87]\n"
"s_waitcnt vmcnt(39)\n"
"v_mfma_f32_16x16x32_bf16 v[80:83], a[216:219], v[88:91], v[80:83]\n"
"buffer_load_dwordx4 v20, s[48:51], s13 offen offset:2048 lds\n"
"s_waitcnt vmcnt(39)\n"
"v_mfma_f32_16x16x32_bf16 v[84:87], a[220:223], v[88:91], v[84:87]\n"
"s_waitcnt vmcnt(38)\n"
"v_mfma_f32_16x16x32_bf16 v[80:83], a[224:227], v[88:91], v[80:83]\n"
"buffer_load_dwordx4 v21, s[48:51], s13 offen offset:3072 lds\n"
"s_waitcnt vmcnt(38)\n"
"v_mfma_f32_16x16x32_bf16 v[84:87], a[228:231], v[88:91], v[84:87]\n"
"s_waitcnt vmcnt(37)\n"
"v_mfma_f32_16x16x32_bf16 v[80:83], a[232:235], v[88:91], v[80:83]\n"
"s_add_u32 m0, s10, 4096\n"
"s_nop 0\n"
"buffer_load_dwordx4 v22, s[56:59], s14 offen offset:0 lds\n"
"s_waitcnt vmcnt(37)\n"
"v_mfma_f32_16x16x32_bf16 v[84:87], a[236:239], v[88:91], v[84:87]\n"
"s_waitcnt vmcnt(36)\n"
"v_mfma_f32_16x16x32_bf16 v[80:83], a[240:243], v[88:91], v[80:83]\n"
"buffer_load_dwordx4 v23, s[56:59], s14 offen offset:1024 lds\n"
"s_waitcnt vmcnt(36)\n"
"v_mfma_f32_16x16x32_bf16 v[84:87], a[244:247], v[88:91], v[84:87]\n"
"s_waitcnt vmcnt(35)\n"
"v_mfma_f32_16x16x32_bf16 v[80:83], a[248:251], v[88:91], v[80:83]\n"
"s_add_u32 s13, s13, 221184\n"
"s_add_u32 m0, s10, 28672\n"
"s_nop 0\n"
"buffer_load_dwordx4 v18, s[48:51], s13 offen offset:0 lds\n"
"s_waitcnt vmcnt(35)\n"
"v_mfma_f32_16x16x32_bf16 v[84:87], a[252:255], v[88:91], v[84:87]\n"
"s_waitcnt vmcnt(34)\n"
"v_mfma_f32_16x16x32_bf16 v[80:83], v[160:163], v[88:91], v[80:83]\n"
"buffer_load_dwordx4 v19, s[48:51], s13 offen offset:1024 lds\n"
"s_waitcnt vmcnt(34)\n"
"v_mfma_f32_16x16x32_bf16 v[84:87], v[164:167], v[88:91], v[84:87]\n"
"s_waitcnt vmcnt(33)\n"
"v_mfma_f32_16x16x32_bf16 v[80:83], v[168:171], v[88:91], v[80:83]\n"
"buffer_load_dwordx4 v20, s[48:51], s13 offen offset:2048 lds\n"
"s_waitcnt vmcnt(33)\n"
"v_mfma_f32_16x16x32_bf16 v[84:87], v[172:175], v[88:91], v[84:87]\n"
"s_waitcnt vmcnt(32)\n"
"v_mfma_f32_16x16x32_bf16 v[80:83], v[176:179], v[88:91], v[80:83]\n"
"buffer_load_dwordx4 v21, s[48:51], s13 offen offset:3072 lds\n"
"s_waitcnt vmcnt(32)\n"
"v_mfma_f32_16x16x32_bf16 v[84:87], v[180:183], v[88:91], v[84:87]\n"
"s_waitcnt vmcnt(31)\n"
"v_mfma_f32_16x16x32_bf16 v[80:83], v[184:187], v[88:91], v[80:83]\n"
"s_add_u32 s14, s14, 128\n"
"s_add_u32 m0, s10, 6144\n"
"s_nop 0\n"
"buffer_load_dwordx4 v22, s[56:59], s14 offen offset:0 lds\n"
"s_waitcnt vmcnt(31)\n"
"v_mfma_f32_16x16x32_bf16 v[84:87], v[188:191], v[88:91], v[84:87]\n"
"s_waitcnt vmcnt(30)\n"
"v_mfma_f32_16x16x32_bf16 v[80:83], v[192:195], v[88:91], v[80:83]\n"
"buffer_load_dwordx4 v23, s[56:59], s14 offen offset:1024 lds\n"
"s_waitcnt vmcnt(30)\n"
"v_mfma_f32_16x16x32_bf16 v[84:87], v[196:199], v[88:91], v[84:87]\n"
"s_waitcnt vmcnt(29)\n"
"v_mfma_f32_16x16x32_bf16 v[80:83], v[200:203], v[88:91], v[80:83]\n"
"s_add_u32 s12, s12, 128\n"
"s_add_u32 m0, s10, 24576\n"
"s_nop 0\n"
"buffer_load_dwordx4 v22, s[44:47], s12 offen offset:0 lds\n"
"s_waitcnt vmcnt(29)\n"
"v_mfma_f32_16x16x32_bf16 v[84:87], v[204:207], v[88:91], v[84:87]\n"
"s_waitcnt vmcnt(28)\n"
"v_mfma_f32_16x16x32_bf16 v[80:83], v[208:211], v[88:91], v[80:83]\n"
"buffer_load_dwordx4 v23, s[44:47], s12 offen offset:1024 lds\n"
"s_waitcnt vmcnt(28)\n"
"v_mfma_f32_16x16x32_bf16 v[84:87], v[212:215], v[88:91], v[84:87]\n"
"s_waitcnt vmcnt(27)\n"
"v_mfma_f32_16x16x32_bf16 v[80:83], v[216:219], v[88:91], v[80:83]\n"
"buffer_load_dwordx4 v24, s[44:47], s12 offen offset:2048 lds\n"
"s_waitcnt vmcnt(27)\n"
"v_mfma_f32_16x16x32_bf16 v[84:87], v[220:223], v[88:91], v[84:87]\n"
"s_waitcnt vmcnt(26)\n"
"v_mfma_f32_16x16x32_bf16 v[80:83], v[224:227], v[88:91], v[80:83]\n"
"buffer_load_dwordx4 v25, s[44:47], s12 offen offset:3072 lds\n"
"s_waitcnt vmcnt(26)\n"
"v_mfma_f32_16x16x32_bf16 v[84:87], v[228:231], v[88:91], v[84:87]\n"
"s_waitcnt vmcnt(24)\n"
"v_mfma_f32_16x16x32_bf16 v[80:83], v[232:235], v[88:91], v[80:83]\n"
"s_waitcnt vmcnt(22)\n"
"v_mfma_f32_16x16x32_bf16 v[84:87], v[236:239], v[88:91], v[84:87]\n"
"s_waitcnt vmcnt(21)\n"
"v_mfma_f32_16x16x32_bf16 v[80:83], v[240:243], v[88:91], v[80:83]\n"
"s_waitcnt vmcnt(20)\n"
"v_mfma_f32_16x16x32_bf16 v[84:87], v[244:247], v[88:91], v[84:87]\n"
"s_waitcnt vmcnt(19)\n"
"v_mfma_f32_16x16x32_bf16 v[80:83], v[248:251], v[88:91], v[80:83]\n"
"s_waitcnt vmcnt(18)\n"
"v_mfma_f32_16x16x32_bf16 v[84:87], v[252:255], v[88:91], v[84:87]\n"
"s_waitcnt vmcnt(17)\n"
"v_mfma_f32_16x16x32_bf16 v[80:83], v[96:99], v[88:91], v[80:83]\n"
"s_waitcnt vmcnt(16)\n"
"v_mfma_f32_16x16x32_bf16 v[84:87], v[100:103], v[88:91], v[84:87]\n"
"v_and_b32 v68, 7, v27\n"
"v_bfe_u32 v69, v27, 4, 1\n"
"v_lshl_add_u32 v68, v69, 5, v68\n"
"v_lshl_add_u32 v156, v68, 2, s10\n"
"s_nop 1\n"
"v_mov_b32 v160, v80\n"
"v_mov_b32 v161, v81\n"
"v_mov_b32 v162, v82\n"
"v_mov_b32 v163, v83\n"
"v_mov_b32 v164, v84\n"
"v_mov_b32 v165, v85\n"
"v_mov_b32 v166, v86\n"
"v_mov_b32 v167, v87\n"
"v_permlane32_swap_b32 v160, v80\n"
"v_permlane32_swap_b32 v161, v81\n"
"v_permlane32_swap_b32 v162, v82\n"
"v_permlane32_swap_b32 v163, v83\n"
"v_permlane32_swap_b32 v164, v84\n"
"v_permlane32_swap_b32 v165, v85\n"
"v_permlane32_swap_b32 v166, v86\n"
"v_permlane32_swap_b32 v167, v87\n"
"v_add_f32 v160, v160, v80\n"
"v_add_f32 v161, v161, v81\n"
"v_add_f32 v162, v162, v82\n"
"v_add_f32 v163, v163, v83\n"
"v_add_f32 v164, v164, v84\n"
"v_add_f32 v165, v165, v85\n"
"v_add_f32 v166, v166, v86\n"
"v_add_f32 v167, v167, v87\n"
"s_mov_b64 exec, 0xff00ff\n"
"ds_write_b32 v156, v160 offset:1024\n"
"ds_write_b32 v156, v161 offset:1056\n"
"ds_write_b32 v156, v162 offset:1088\n"
"ds_write_b32 v156, v163 offset:1120\n"
"ds_write_b32 v156, v164 offset:1280\n"
"ds_write_b32 v156, v165 offset:1312\n"
"ds_write_b32 v156, v166 offset:1344\n"
"ds_write_b32 v156, v167 offset:1376\n"
"s_mov_b64 exec, -1\n"
"s_waitcnt lgkmcnt(0)\n"
"s_barrier\n"
"s_lshl_b32 s68, s7, 8\n"
"v_lshl_add_u32 v69, v27, 2, s68\n"
"v_add_u32 v70, 81920, v69\n"
"ds_read_b32 v160, v69 offset:1024\n"
"ds_read_b32 v161, v69 offset:41984\n"
"ds_read_b32 v162, v70 offset:1024\n"
"ds_read_b32 v163, v70 offset:41984\n"
"v_lshrrev_b32 v69, 4, v27\n"
"v_lshlrev_b32 v69, 5, v69\n"
"v_bfe_u32 v70, v27, 2, 1\n"
"v_lshl_add_u32 v69, v70, 4, v69\n"
"v_bfe_u32 v70, v27, 3, 1\n"
"v_lshl_add_u32 v69, v70, 2, v69\n"
"v_and_b32 v70, 3, v27\n"
"v_add_u32 v69, v69, v70\n"
"v_lshlrev_b32 v69, 4, v69\n"
"s_lshl_b32 s69, s7, 7\n"
"s_lshl_b32 s70, s42, 11\n"
"s_add_u32 s69, s69, s70\n"
"s_add_u32 s69, s69, 21037056\n"
"s_cmp_lt_u32 s42, 27\n"
"s_cselect_b32 s68, s69, 2130706432\n"
"s_cmp_lt_u32 s7, 2\n"
"s_cselect_b32 s68, s68, 2130706432\n"
"s_waitcnt lgkmcnt(0)\n"
"v_add_f32 v160, v160, v161\n"
"v_add_f32 v162, v162, v163\n"
"v_add_f32 v160, v160, v162\n"
"v_cvt_pk_bf16_f32 v71, v160, v160\n"
"v_and_b32 v71, s74, v71\n"
"v_sub_f32 v160, v160, v71\n"
"v_cvt_pk_bf16_f32 v164, v71, v160\n"
"v_and_b32 v71, s74, v164\n"
"v_sub_f32 v160, v160, v71\n"
"v_cvt_pk_bf16_f32 v166, v160, 0\n"
"v_mov_b32 v165, s72\n"
"v_mov_b32 v167, s73\n"
"buffer_store_dwordx4 v[164:167], v69, s[64:67], s68 offen sc1\n"
"ds_read_b128 a[0:3], v14 offset:32768\n"
"ds_read_b128 a[4:7], v15 offset:32768\n"
"ds_read_b128 a[8:11], v16 offset:32768\n"
"ds_read_b128 a[12:15], v17 offset:32768\n"
"s_waitcnt vmcnt(11)\n"
"s_barrier\n"
"ds_read_b64_tr_b16 a[160:161], v2 offset:36864\n"
"ds_read_b64_tr_b16 a[162:163], v2 offset:37376\n"
"ds_read_b64_tr_b16 a[164:165], v3 offset:36864\n"
"ds_read_b64_tr_b16 a[166:167], v3 offset:37376\n"
"ds_read_b64_tr_b16 a[168:169], v4 offset:36864\n"
"ds_read_b64_tr_b16 a[170:171], v4 offset:37376\n"
"ds_read_b64_tr_b16 a[172:173], v5 offset:36864\n"
"ds_read_b64_tr_b16 a[174:175], v5 offset:37376\n"
"ds_read_b128 a[176:179], v10 offset:4096\n"
"ds_read_b128 a[180:183], v10 offset:45056\n"
"ds_read_b64_tr_b16 a[184:185], v2 offset:38912\n"
"ds_read_b64_tr_b16 a[186:187], v2 offset:39424\n"
"ds_read_b64_tr_b16 a[188:189], v3 offset:38912\n"
"ds_read_b64_tr_b16 a[190:191], v3 offset:39424\n"
"s_waitcnt lgkmcnt(6)\n"
"v_mfma_f32_32x32x16_bf16 v[192:207], a[160:163], a[0:3], 0\n"
"ds_read_b64_tr_b16 a[160:161], v4 offset:38912\n"
"ds_read_b64_tr_b16 a[162:163], v4 offset:39424\n"
"s_add_u32 s12, s12, 128\n"
"s_add_u32 m0, s10, 20480\n"
"s_nop 0\n"
"buffer_load_dwordx4 v22, s[44:47], s12 offen offset:0 lds\n"
"s_and_b32 s68, s6, 6\n"
"s_lshl_b32 s30, s68, 1\n"
"v_mfma_f32_32x32x16_bf16 v[208:223], a[164:167], a[0:3], 0\n"
"ds_read_b64_tr_b16 a[164:165], v5 offset:38912\n"
"ds_read_b64_tr_b16 a[166:167], v5 offset:39424\n"
"s_lshr_b32 s68, s6, 6\n"
"s_add_u32 s30, s30, s68\n"
"s_bfe_u32 s31, s6, 0x30003\n"
"s_and_b32 s68, s6, 1\n"
"s_lshl_b32 s68, s68, 2\n"
"s_add_u32 s68, s68, s7\n"
"v_mfma_f32_32x32x16_bf16 v[224:239], a[168:171], a[0:3], 0\n"
"ds_read_b128 a[168:171], v11 offset:4096\n"
"buffer_load_dwordx4 v23, s[44:47], s12 offen offset:1024 lds\n"
"s_cmp_ge_u32 s68, 6\n"
"s_cselect_b32 s37, 11, 12\n"
"s_mul_i32 s32, s68, 49152\n"
"s_mul_i32 s68, s68, 96\n"
"v_mfma_f32_32x32x16_bf16 v[240:255], a[172:175], a[0:3], 0\n"
"ds_read_b128 a[172:175], v11 offset:45056\n"
"s_add_u32 s68, s68, s31\n"
"s_mul_i32 s68, s68, 5120\n"
"s_add_u32 s68, s68, 16777216\n"
"s_add_u32 s76, s38, s68\n"
"s_addc_u32 s77, s39, 0\n"
"s_and_b32 s77, s77, 0xffff\n"
"s_mul_i32 s78, s37, 40960\n"
"s_waitcnt lgkmcnt(6)\n"
"v_mfma_f32_32x32x16_bf16 v[96:111], a[0:3], a[176:179], 0\n"
"ds_read_b64_tr_b16 a[176:177], v6 offset:36864\n"
"ds_read_b64_tr_b16 a[178:179], v6 offset:37376\n"
"buffer_load_dwordx4 v24, s[44:47], s12 offen offset:2048 lds\n"
"s_sub_u32 s78, s78, 35840\n"
"s_mov_b32 s79, 0x20000\n"
"s_lshl_b32 s68, s6, 2\n"
"s_add_u32 s68, s68, s7\n"
"v_mfma_f32_32x32x16_bf16 v[112:127], a[0:3], a[180:183], 0\n"
"ds_read_b64_tr_b16 a[180:181], v7 offset:36864\n"
"ds_read_b64_tr_b16 a[182:183], v7 offset:37376\n"
"s_mul_i32 s34, s68, 5120\n"
"s_xor_b32 s68, s68, 4\n"
"s_mul_i32 s35, s68, 5120\n"
"s_add_u32 s34, s34, 24117248\n"
"s_add_u32 s35, s35, 24117248\n"
"s_lshl_b32 s36, s8, 4\n"
"v_mfma_f32_32x32x16_bf16 v[192:207], a[184:187], a[4:7], v[192:207]\n"
"ds_read_b64_tr_b16 a[184:185], v8 offset:36864\n"
"ds_read_b64_tr_b16 a[186:187], v8 offset:37376\n"
"buffer_load_dwordx4 v25, s[44:47], s12 offen offset:3072 lds\n"
"s_add_u32 s36, s36, s9\n"
"s_lshl_b32 s36, s36, 4\n"
"s_add_u32 s36, s36, s7\n"
"s_lshl_b32 s36, s36, 3\n"
"v_mfma_f32_32x32x16_bf16 v[208:223], a[188:191], a[4:7], v[208:223]\n"
"s_waitcnt lgkmcnt(6)\n"
"ds_read_b64_tr_b16 a[188:189], v9 offset:36864\n"
"ds_read_b64_tr_b16 a[190:191], v9 offset:37376\n"
"s_add_u32 m0, s10, 0\n"
"s_add_u32 s36, s36, 23134208\n"
"s_and_b32 s68, s6, 1\n"
"s_xor_b32 s68, s68, 0\n"
"s_lshl_b32 s69, s30, 1\n"
"s_add_u32 s68, s68, s69\n"
"v_mfma_f32_32x32x16_bf16 v[224:239], a[160:163], a[4:7], v[224:239]\n"
"ds_read_b128 a[160:163], v12 offset:4096\n"
"buffer_load_dwordx4 v22, s[60:63], 0 offen offset:0 lds\n"
"s_mul_i32 s68, s68, 393216\n"
"s_add_u32 s68, s68, s32\n"
"s_add_u32 s80, s38, s68\n"
"s_addc_u32 s81, s39, 0\n"
"v_mfma_f32_32x32x16_bf16 v[240:255], a[164:167], a[4:7], v[240:255]\n"
"ds_read_b128 a[164:167], v12 offset:45056\n"
"s_and_b32 s81, s81, 0xffff\n"
"s_lshl_b32 s82, s37, 12\n"
"s_mov_b32 s83, 0x20000\n"
"s_and_b32 s68, s6, 1\n"
"s_xor_b32 s68, s68, 1\n"
"s_lshl_b32 s69, s30, 1\n"
"s_add_u32 s68, s68, s69\n"
"v_mfma_f32_32x32x16_bf16 v[96:111], a[4:7], a[168:171], v[96:111]\n"
"ds_read_b64_tr_b16 a[168:169], v6 offset:38912\n"
"ds_read_b64_tr_b16 a[170:171], v6 offset:39424\n"
"buffer_load_dwordx4 v23, s[60:63], 0 offen offset:1024 lds\n"
"s_mul_i32 s68, s68, 393216\n"
"s_add_u32 s68, s68, s32\n"
"s_add_u32 s84, s38, s68\n"
"s_addc_u32 s85, s39, 0\n"
"v_mfma_f32_32x32x16_bf16 v[112:127], a[4:7], a[172:175], v[112:127]\n"
"s_waitcnt lgkmcnt(4)\n"
"ds_read_b64_tr_b16 a[172:173], v7 offset:38912\n"
"ds_read_b64_tr_b16 a[174:175], v7 offset:39424\n"
"s_and_b32 s85, s85, 0xffff\n"
"s_lshl_b32 s86, s37, 12\n"
"s_mov_b32 s87, 0x20000\n"
"v_lshrrev_b32 v68, 4, v27\n"
"v_bfe_u32 v69, v27, 2, 2\n"
"v_and_b32 v70, 3, v27\n"
"v_lshl_add_u32 v69, v68, 3, v69\n"
"v_mfma_f32_32x32x16_bf16 v[192:207], a[176:179], a[8:11], v[192:207]\n"
"ds_read_b64_tr_b16 a[176:177], v8 offset:38912\n"
"ds_read_b64_tr_b16 a[178:179], v8 offset:39424\n"
"buffer_load_dwordx4 v24, s[60:63], 0 offen offset:2048 lds\n"
"v_mul_u32_u24 v34, 72, v69\n"
"v_lshl_add_u32 v34, v70, 3, v34\n"
"s_add_u32 s68, s10, 2048\n"
"v_add_u32 v34, s68, v34\n"
"v_bfe_u32 v71, v69, 1, 3\n"
"v_mfma_f32_32x32x16_bf16 v[208:223], a[180:183], a[8:11], v[208:223]\n"
"ds_read_b64_tr_b16 a[180:181], v9 offset:38912\n"
"ds_read_b64_tr_b16 a[182:183], v9 offset:39424\n"
"v_lshlrev_b32 v71, 4, v71\n"
"v_lshl_add_u32 v69, v69, 7, s10\n"
"v_lshlrev_b32 v70, 3, v70\n"
"v_xor_b32 v70, v70, v71\n"
"v_add_u32 v40, v69, v70\n"
"v_xor_b32 v41, 32, v40\n"
"v_xor_b32 v42, 64, v40\n"
"v_xor_b32 v43, 96, v40\n"
"v_and_b32 v70, 15, v27\n"
"v_mfma_f32_32x32x16_bf16 v[224:239], a[184:187], a[8:11], v[224:239]\n"
"ds_read_b128 a[184:187], v13 offset:4096\n"
"buffer_load_dwordx4 v25, s[60:63], 0 offen offset:3072 lds\n"
"v_lshrrev_b32 v71, 1, v70\n"
"v_xor_b32 v71, v71, v68\n"
"v_lshl_add_u32 v70, v70, 7, s10\n"
"v_lshl_add_u32 v36, v71, 4, v70\n"
"v_xor_b32 v37, 64, v36\n"
"v_mfma_f32_32x32x16_bf16 v[240:255], a[188:191], a[8:11], v[240:255]\n"
"s_waitcnt lgkmcnt(5)\n"
"ds_read_b128 a[188:191], v13 offset:45056\n"
"s_add_u32 s13, s13, 221184\n"
"s_add_u32 m0, s10, 32768\n"
"v_mfma_f32_32x32x16_bf16 v[96:111], a[8:11], a[160:163], v[96:111]\n"
"s_waitcnt vmcnt(13)\n"
"s_barrier\n"
"ds_read_b64_tr_b16 a[160:161], v2 offset:28672\n"
"ds_read_b64_tr_b16 a[162:163], v2 offset:29184\n"
"buffer_load_dwordx4 v18, s[48:51], s13 offen offset:0 lds\n"
"v_mfma_f32_32x32x16_bf16 v[112:127], a[8:11], a[164:167], v[112:127]\n"
"ds_read_b64_tr_b16 a[164:165], v3 offset:28672\n"
"ds_read_b64_tr_b16 a[166:167], v3 offset:29184\n"
"v_mfma_f32_32x32x16_bf16 v[192:207], a[168:171], a[12:15], v[192:207]\n"
"s_waitcnt vmcnt(10)\n"
"ds_read_b64_tr_b16 a[168:169], v4 offset:28672\n"
"ds_read_b64_tr_b16 a[170:171], v4 offset:29184\n"
"ds_read_b128 a[16:19], v14 offset:24576\n"
"buffer_load_dwordx4 v19, s[48:51], s13 offen offset:1024 lds\n"
"v_mfma_f32_32x32x16_bf16 v[208:223], a[172:175], a[12:15], v[208:223]\n"
"s_waitcnt lgkmcnt(7)\n"
"ds_read_b64_tr_b16 a[172:173], v5 offset:28672\n"
"ds_read_b64_tr_b16 a[174:175], v5 offset:29184\n"
"ds_read_b128 a[20:23], v15 offset:24576\n"
"v_mfma_f32_32x32x16_bf16 v[224:239], a[176:179], a[12:15], v[224:239]\n"
"ds_read_b128 a[176:179], v10 offset:6144\n"
"ds_read_b128 a[24:27], v16 offset:24576\n"
"buffer_load_dwordx4 v20, s[48:51], s13 offen offset:2048 lds\n"
"v_mfma_f32_32x32x16_bf16 v[240:255], a[180:183], a[12:15], v[240:255]\n"
"ds_read_b128 a[180:183], v10 offset:47104\n"
"ds_read_b128 a[28:31], v17 offset:24576\n"
"v_mfma_f32_32x32x16_bf16 v[96:111], a[12:15], a[184:187], v[96:111]\n"
"ds_read_b64_tr_b16 a[184:185], v2 offset:30720\n"
"ds_read_b64_tr_b16 a[186:187], v2 offset:31232\n"
"buffer_load_dwordx4 v21, s[48:51], s13 offen offset:3072 lds\n"
"v_mfma_f32_32x32x16_bf16 v[112:127], a[12:15], a[188:191], v[112:127]\n"
"s_waitcnt lgkmcnt(9)\n"
"ds_read_b64_tr_b16 a[188:189], v3 offset:30720\n"
"ds_read_b64_tr_b16 a[190:191], v3 offset:31232\n"
"s_add_u32 s14, s14, 128\n"
"s_add_u32 m0, s10, 8192\n"
"v_mfma_f32_32x32x16_bf16 v[192:207], a[160:163], a[16:19], v[192:207]\n"
"ds_read_b64_tr_b16 a[160:161], v4 offset:30720\n"
"ds_read_b64_tr_b16 a[162:163], v4 offset:31232\n"
"buffer_load_dwordx4 v22, s[56:59], s14 offen offset:0 lds\n"
"v_mfma_f32_32x32x16_bf16 v[208:223], a[164:167], a[16:19], v[208:223]\n"
"ds_read_b64_tr_b16 a[164:165], v5 offset:30720\n"
"ds_read_b64_tr_b16 a[166:167], v5 offset:31232\n"
"v_mfma_f32_32x32x16_bf16 v[224:239], a[168:171], a[16:19], v[224:239]\n"
"ds_read_b128 a[168:171], v11 offset:6144\n"
"buffer_load_dwordx4 v23, s[56:59], s14 offen offset:1024 lds\n"
"s_waitcnt lgkmcnt(14)\n"
"v_mfma_f32_32x32x16_bf16 v[240:255], a[172:175], a[16:19], v[240:255]\n"
"s_waitcnt lgkmcnt(5)\n"
"ds_read_b128 a[172:175], v11 offset:47104\n"
"s_add_u32 s12, s12, 128\n"
"s_add_u32 m0, s10, 24576\n"
"v_mfma_f32_32x32x16_bf16 v[96:111], a[16:19], a[176:179], v[96:111]\n"
"s_barrier\n"
"ds_read_b64_tr_b16 a[176:177], v6 offset:28672\n"
"ds_read_b64_tr_b16 a[178:179], v6 offset:29184\n"
"buffer_load_dwordx4 v22, s[44:47], s12 offen offset:0 lds\n"
"v_mfma_f32_32x32x16_bf16 v[112:127], a[16:19], a[180:183], v[112:127]\n"
"ds_read_b64_tr_b16 a[180:181], v7 offset:28672\n"
"ds_read_b64_tr_b16 a[182:183], v7 offset:29184\n"
"v_mfma_f32_32x32x16_bf16 v[192:207], a[184:187], a[20:23], v[192:207]\n"
"ds_read_b64_tr_b16 a[184:185], v8 offset:28672\n"
"ds_read_b64_tr_b16 a[186:187], v8 offset:29184\n"
"buffer_load_dwordx4 v23, s[44:47], s12 offen offset:1024 lds\n"
"v_mfma_f32_32x32x16_bf16 v[208:223], a[188:191], a[20:23], v[208:223]\n"
"s_waitcnt lgkmcnt(6)\n"
"ds_read_b64_tr_b16 a[188:189], v9 offset:28672\n"
"ds_read_b64_tr_b16 a[190:191], v9 offset:29184\n"
"v_mfma_f32_32x32x16_bf16 v[224:239], a[160:163], a[20:23], v[224:239]\n"
"ds_read_b128 a[160:163], v12 offset:6144\n"
"buffer_load_dwordx4 v24, s[44:47], s12 offen offset:2048 lds\n"
"v_mfma_f32_32x32x16_bf16 v[240:255], a[164:167], a[20:23], v[240:255]\n"
"ds_read_b128 a[164:167], v12 offset:47104\n"
"v_mfma_f32_32x32x16_bf16 v[96:111], a[20:23], a[168:171], v[96:111]\n"
"ds_read_b64_tr_b16 a[168:169], v6 offset:30720\n"
"ds_read_b64_tr_b16 a[170:171], v6 offset:31232\n"
"buffer_load_dwordx4 v25, s[44:47], s12 offen offset:3072 lds\n"
"v_mfma_f32_32x32x16_bf16 v[112:127], a[20:23], a[172:175], v[112:127]\n"
"s_waitcnt lgkmcnt(4)\n"
"ds_read_b64_tr_b16 a[172:173], v7 offset:30720\n"
"ds_read_b64_tr_b16 a[174:175], v7 offset:31232\n"
"s_add_u32 s13, s13, 221184\n"
"s_add_u32 m0, s10, 36864\n"
"v_mfma_f32_32x32x16_bf16 v[192:207], a[176:179], a[24:27], v[192:207]\n"
"ds_read_b64_tr_b16 a[176:177], v8 offset:30720\n"
"ds_read_b64_tr_b16 a[178:179], v8 offset:31232\n"
"buffer_load_dwordx4 v18, s[48:51], s13 offen offset:0 lds\n"
"v_mfma_f32_32x32x16_bf16 v[208:223], a[180:183], a[24:27], v[208:223]\n"
"ds_read_b64_tr_b16 a[180:181], v9 offset:30720\n"
"ds_read_b64_tr_b16 a[182:183], v9 offset:31232\n"
"v_mfma_f32_32x32x16_bf16 v[224:239], a[184:187], a[24:27], v[224:239]\n"
"ds_read_b128 a[184:187], v13 offset:6144\n"
"buffer_load_dwordx4 v19, s[48:51], s13 offen offset:1024 lds\n"
"v_mfma_f32_32x32x16_bf16 v[240:255], a[188:191], a[24:27], v[240:255]\n"
"s_waitcnt vmcnt(16) lgkmcnt(5)\n"
"ds_read_b128 a[188:191], v13 offset:47104\n"
"ds_read_b128 a[32:35], v14 offset:20480\n"
"v_mfma_f32_32x32x16_bf16 v[96:111], a[24:27], a[160:163], v[96:111]\n"
"s_waitcnt vmcnt(6)\n"
"s_barrier\n"
"ds_read_b64_tr_b16 a[160:161], v2 offset:32768\n"
"ds_read_b64_tr_b16 a[162:163], v2 offset:33280\n"
"ds_read_b128 a[36:39], v15 offset:20480\n"
"buffer_load_dwordx4 v20, s[48:51], s13 offen offset:2048 lds\n"
"v_mfma_f32_32x32x16_bf16 v[112:127], a[24:27], a[164:167], v[112:127]\n"
"ds_read_b64_tr_b16 a[164:165], v3 offset:32768\n"
"ds_read_b64_tr_b16 a[166:167], v3 offset:33280\n"
"ds_read_b128 a[40:43], v16 offset:20480\n"
"s_cmp_eq_u32 s75, 0\n"
"s_cbranch_scc1 K1X_rtc\n"
"ds_read_b64_tr_b16 v[128:129], v40 offset:0\n"
"ds_read_b64_tr_b16 v[130:131], v41 offset:512\n"
"ds_read_b64_tr_b16 v[132:133], v41 offset:0\n"
"ds_read_b64_tr_b16 v[134:135], v40 offset:512\n"
"ds_read_b64_tr_b16 v[136:137], v42 offset:0\n"
"ds_read_b64_tr_b16 v[138:139], v43 offset:512\n"
"ds_read_b64_tr_b16 v[140:141], v43 offset:0\n"
"ds_read_b64_tr_b16 v[142:143], v42 offset:512\n"
"s_branch K1X_rtd\n"
"K1X_rtc:\n"
"ds_read_b128 v[128:131], v36 offset:0\n"
"ds_read_b128 v[132:135], v36 offset:2048\n"
"ds_read_b128 v[136:139], v37 offset:0\n"
"ds_read_b128 v[140:143], v37 offset:2048\n"
"K1X_rtd:\n"
"v_mfma_f32_32x32x16_bf16 v[192:207], a[168:171], a[28:31], v[192:207]\n"
"ds_read_b64_tr_b16 a[168:169], v4 offset:32768\n"
"ds_read_b64_tr_b16 a[170:171], v4 offset:33280\n"
"ds_read_b128 a[44:47], v17 offset:20480\n"
"buffer_load_dwordx4 v21, s[48:51], s13 offen offset:3072 lds\n"
"v_mfma_f32_32x32x16_bf16 v[208:223], a[172:175], a[28:31], v[208:223]\n"
"s_waitcnt lgkmcnt(14)\n"
"ds_read_b64_tr_b16 a[172:173], v5 offset:32768\n"
"ds_read_b64_tr_b16 a[174:175], v5 offset:33280\n"
"s_add_u32 s14, s14, 128\n"
"s_add_u32 m0, s10, 4096\n"
"v_mfma_f32_32x32x16_bf16 v[224:239], a[176:179], a[28:31], v[224:239]\n"
"ds_read_b128 a[176:179], v10 offset:8192\n"
"buffer_load_dwordx4 v22, s[56:59], s14 offen offset:0 lds\n"
"v_mfma_f32_32x32x16_bf16 v[240:255], a[180:183], a[28:31], v[240:255]\n"
"ds_read_b128 a[180:183], v10 offset:49152\n"
"s_mov_b32 s16, 0\n"
"s_mov_b32 s17, -1\n"
"v_lshrrev_b32 v68, 5, v27\n"
"s_lshl_b32 s68, s7, 2\n"
"v_add_u32 v69, s68, v68\n"
"v_cmp_gt_u32 s[18:19], s8, v69\n"
"v_add_u32 v69, 2, v69\n"
"v_cmp_gt_u32 s[20:21], s8, v69\n"
"v_and_b32 v70, 31, v27\n"
"v_mfma_f32_32x32x16_bf16 v[96:111], a[28:31], a[184:187], v[96:111]\n"
"ds_read_b64_tr_b16 a[184:185], v2 offset:34816\n"
"ds_read_b64_tr_b16 a[186:187], v2 offset:35328\n"
"buffer_load_dwordx4 v23, s[56:59], s14 offen offset:1024 lds\n"
"s_lshl_b32 s68, s7, 9\n"
"v_lshl_add_u32 v29, v27, 3, s68\n"
"v_lshlrev_b32 v30, 3, v70\n"
"v_mfma_f32_32x32x16_bf16 v[112:127], a[28:31], a[188:191], v[112:127]\n"
"s_waitcnt lgkmcnt(4)\n"
"ds_read_b64_tr_b16 a[188:189], v3 offset:34816\n"
"ds_read_b64_tr_b16 a[190:191], v3 offset:35328\n"
"s_add_u32 s12, s12, 128\n"
"s_add_u32 m0, s10, 20480\n"
"buffer_store_dwordx4 v[128:131], v28, s[64:67], s28 offen offset:0 sc1\n"
"v_add_u32 v30, 256, v30\n"
"v_lshlrev_b32 v32, 6, v70\n"
"v_mfma_f32_32x32x16_bf16 v[192:207], a[160:163], a[32:35], v[192:207]\n"
"ds_read_b64_tr_b16 a[160:161], v4 offset:34816\n"
"ds_read_b64_tr_b16 a[162:163], v4 offset:35328\n"
"buffer_load_dwordx4 v22, s[44:47], s12 offen offset:0 lds\n"
"v_add_u32 v32, 122880, v32\n"
"s_lshl_b32 s68, s7, 4\n"
"v_lshl_add_u32 v31, v68, 3, v32\n"
"v_add_u32 v31, s68, v31\n"
"v_mfma_f32_32x32x16_bf16 v[208:223], a[164:167], a[32:35], v[208:223]\n"
"ds_read_b64_tr_b16 a[164:165], v5 offset:34816\n"
"ds_read_b64_tr_b16 a[166:167], v5 offset:35328\n"
"s_add_u32 s68, s10, 2048\n"
"v_mul_u32_u24 v33, 72, v70\n"
"v_lshl_add_u32 v33, v68, 3, v33\n"
"v_add_u32 v33, s68, v33\n"
"v_lshl_add_u32 v71, v70, 4, s89\n"
"v_mov_b32 v72, 2130706432\n"
"v_cndmask_b32 v35, v72, v71, s[16:17]\n"
"v_bfe_u32 v69, v27, 4, 1\n"
"v_mfma_f32_32x32x16_bf16 v[224:239], a[168:171], a[32:35], v[224:239]\n"
"ds_read_b128 a[168:171], v11 offset:8192\n"
"buffer_load_dwordx4 v23, s[44:47], s12 offen offset:1024 lds\n"
"v_lshlrev_b32 v69, 10, v69\n"
"v_lshl_add_u32 v69, v68, 8, v69\n"
"v_and_b32 v70, 15, v27\n"
"v_lshl_add_u32 v26, v70, 4, v69\n"
"v_and_b32 v68, 31, v27\n"
"v_lshrrev_b32 v69, 5, v27\n"
"v_mfma_f32_32x32x16_bf16 v[240:255], a[172:175], a[32:35], v[240:255]\n"
"s_waitcnt lgkmcnt(5)\n"
"ds_read_b128 a[172:175], v11 offset:49152\n"
"v_lshl_add_u32 v68, v68, 4, s90\n"
"v_lshlrev_b32 v69, 9, v69\n"
"v_sub_u32 v68, v68, v69\n"
"v_mov_b32 v69, 2130706432\n"
"v_mfma_f32_32x32x16_bf16 v[96:111], a[32:35], a[176:179], v[96:111]\n"
"s_barrier\n"
"ds_read_b64_tr_b16 a[176:177], v6 offset:32768\n"
"ds_read_b64_tr_b16 a[178:179], v6 offset:33280\n"
"buffer_load_dwordx4 v24, s[44:47], s12 offen offset:2048 lds\n"
"buffer_store_dwordx4 v[132:135], v28, s[64:67], s28 offen offset:1024 sc1\n"
"v_cndmask_b32 v38, v69, v68, s[18:19]\n"
"v_mfma_f32_32x32x16_bf16 v[112:127], a[32:35], a[180:183], v[112:127]\n"
"ds_read_b64_tr_b16 a[180:181], v7 offset:32768\n"
"ds_read_b64_tr_b16 a[182:183], v7 offset:33280\n"
"v_subrev_u32 v68, 1024, v68\n"
"v_cndmask_b32 v39, v69, v68, s[20:21]\n"
"v_lshlrev_b32 v70, 16, v150\n"
"v_lshlrev_b32 v68, 16, v148\n"
"v_mul_f32 v70, 0x3fb8aa3b, v70\n"
"v_lshlrev_b32 v69, 16, v149\n"
"v_exp_f32 v70, v70\n"
"v_lshrrev_b32 v73, 5, v27\n"
"v_mfma_f32_32x32x16_bf16 v[192:207], a[184:187], a[36:39], v[192:207]\n"
"ds_read_b64_tr_b16 a[184:185], v8 offset:32768\n"
"ds_read_b64_tr_b16 a[186:187], v8 offset:33280\n"
"buffer_load_dwordx4 v25, s[44:47], s12 offen offset:3072 lds\n"
"v_cvt_pk_bf16_f32 v70, v70, v70\n"
"v_cvt_f32_u32 v73, v73\n"
"v_lshlrev_b32 v70, 16, v70\n"
"v_mul_f32 v71, v70, v69\n"
"v_mul_f32 v70, v70, v68\n"
"v_mfma_f32_32x32x16_bf16 v[208:223], a[188:191], a[36:39], v[208:223]\n"
"s_waitcnt lgkmcnt(6)\n"
"ds_read_b64_tr_b16 a[188:189], v9 offset:32768\n"
"ds_read_b64_tr_b16 a[190:191], v9 offset:33280\n"
"s_add_u32 s13, s13, 221184\n"
"s_add_u32 m0, s10, 28672\n"
"v_mul_f32 v71, 0x3e22f983, v71\n"
"v_mul_f32 v70, 0x3fb8aa3b, v70\n"
"v_mul_f32 v74, 0x3f800000, v70\n"
"v_mul_f32 v75, 0x3f800000, v71\n"
"v_mfma_f32_32x32x16_bf16 v[224:239], a[160:163], a[36:39], v[224:239]\n"
"ds_read_b128 a[160:163], v12 offset:8192\n"
"buffer_load_dwordx4 v18, s[48:51], s13 offen offset:0 lds\n"
"v_exp_f32 v74, v74\n"
"v_fract_f32 v75, v75\n"
"v_cos_f32 v44, v75\n"
"v_mfma_f32_32x32x16_bf16 v[240:255], a[164:167], a[36:39], v[240:255]\n"
"ds_read_b128 a[164:167], v12 offset:49152\n"
"v_sin_f32 v45, v75\n"
"v_mul_f32 v44, v44, v74\n"
"v_mul_f32 v45, v45, v74\n"
"v_mfma_f32_32x32x16_bf16 v[96:111], a[36:39], a[168:171], v[96:111]\n"
"ds_read_b64_tr_b16 a[168:169], v6 offset:34816\n"
"ds_read_b64_tr_b16 a[170:171], v6 offset:35328\n"
"buffer_load_dwordx4 v19, s[48:51], s13 offen offset:1024 lds\n"
"buffer_store_dwordx4 v[136:139], v28, s[64:67], s29 offen offset:0 sc1\n"
"v_mul_f32 v72, 0x41800000, v73\n"
"v_mul_f32 v74, v72, v70\n"
"v_mfma_f32_32x32x16_bf16 v[112:127], a[36:39], a[172:175], v[112:127]\n"
"s_waitcnt vmcnt(15) lgkmcnt(4)\n"
"ds_read_b64_tr_b16 a[172:173], v7 offset:34816\n"
"ds_read_b64_tr_b16 a[174:175], v7 offset:35328\n"
"ds_read_b128 a[48:51], v14 offset:24576\n"
"v_mul_f32 v75, v72, v71\n"
"v_exp_f32 v74, v74\n"
"v_fract_f32 v75, v75\n"
"v_cos_f32 v46, v75\n"
"v_mfma_f32_32x32x16_bf16 v[192:207], a[176:179], a[40:43], v[192:207]\n"
"ds_read_b64_tr_b16 a[176:177], v8 offset:34816\n"
"ds_read_b64_tr_b16 a[178:179], v8 offset:35328\n"
"ds_read_b128 a[52:55], v15 offset:24576\n"
"buffer_load_dwordx4 v20, s[48:51], s13 offen offset:2048 lds\n"
"v_sin_f32 v47, v75\n"
"v_mul_f32 v46, v46, v74\n"
"v_mul_f32 v47, v47, v74\n"
"v_mfma_f32_32x32x16_bf16 v[208:223], a[180:183], a[40:43], v[208:223]\n"
"ds_read_b64_tr_b16 a[180:181], v9 offset:34816\n"
"ds_read_b64_tr_b16 a[182:183], v9 offset:35328\n"
"ds_read_b128 a[56:59], v16 offset:24576\n"
"v_mul_f32 v74, 0x42000000, v70\n"
"v_mul_f32 v75, 0x42000000, v71\n"
"v_exp_f32 v74, v74\n"
"v_fract_f32 v75, v75\n"
"v_cos_f32 v54, v75\n"
"v_mfma_f32_32x32x16_bf16 v[224:239], a[184:187], a[40:43], v[224:239]\n"
"ds_read_b128 a[184:187], v13 offset:8192\n"
"ds_read_b128 a[60:63], v17 offset:24576\n"
"buffer_load_dwordx4 v21, s[48:51], s13 offen offset:3072 lds\n"
"v_sin_f32 v55, v75\n"
"v_mul_f32 v54, v54, v74\n"
"v_mul_f32 v55, v55, v74\n"
"v_mul_f32 v74, v55, v55\n"
"v_mfma_f32_32x32x16_bf16 v[240:255], a[188:191], a[40:43], v[240:255]\n"
"s_waitcnt lgkmcnt(9)\n"
"ds_read_b128 a[188:191], v13 offset:49152\n"
"s_add_u32 s14, s14, 128\n"
"s_add_u32 m0, s10, 2048\n"
"v_mul_f32 v75, v54, v55\n"
"v_fma_f32 v74, v54, v54, -v74\n"
"v_add_f32 v75, v75, v75\n"
"s_cmp_eq_u32 s7, 3\n"
"s_cselect_b32 s68, 1.0, 0\n"
"v_mfma_f32_32x32x16_bf16 v[96:111], a[40:43], a[160:163], v[96:111]\n"
"s_waitcnt vmcnt(11)\n"
"s_barrier\n"
"ds_read_b64_tr_b16 a[160:161], v2 offset:36864\n"
"ds_read_b64_tr_b16 a[162:163], v2 offset:37376\n"
"buffer_load_dwordx4 v22, s[56:59], s14 offen offset:0 lds\n"
"s_cmp_eq_u32 s7, 2\n"
"s_cselect_b32 s69, 1.0, 0\n"
"v_mfma_f32_32x32x16_bf16 v[112:127], a[40:43], a[164:167], v[112:127]\n"
"ds_read_b64_tr_b16 a[164:165], v3 offset:36864\n"
"ds_read_b64_tr_b16 a[166:167], v3 offset:37376\n"
"buffer_store_dwordx4 v[140:143], v28, s[64:67], s29 offen offset:1024 sc1\n"
"s_cmp_eq_u32 s7, 1\n"
"s_cselect_b32 s70, 1.0, 0\n"
"v_mov_b32 v52, s68\n"
"v_mov_b32 v53, 0\n"
"v_mov_b32 v50, s69\n"
"v_mfma_f32_32x32x16_bf16 v[192:207], a[168:171], a[44:47], v[192:207]\n"
"ds_read_b64_tr_b16 a[168:169], v4 offset:36864\n"
"ds_read_b64_tr_b16 a[170:171], v4 offset:37376\n"
"buffer_load_dwordx4 v23, s[56:59], s14 offen offset:1024 lds\n"
"v_mul_f32 v51, s68, v55\n"
"v_fmac_f32 v50, s68, v54\n"
"v_mov_b32 v48, s70\n"
"v_mul_f32 v49, s69, v55\n"
"v_fmac_f32 v48, s69, v54\n"
"v_mfma_f32_32x32x16_bf16 v[208:223], a[172:175], a[44:47], v[208:223]\n"
"s_waitcnt lgkmcnt(6)\n"
"ds_read_b64_tr_b16 a[172:173], v5 offset:36864\n"
"ds_read_b64_tr_b16 a[174:175], v5 offset:37376\n"
"s_add_u32 s12, s12, 128\n"
"s_add_u32 m0, s10, 24576\n"
"v_fmac_f32 v49, s68, v75\n"
"v_fmac_f32 v48, s68, v74\n"
"s_lshl_b32 s68, s7, 5\n"
"v_cvt_f32_u32 v72, s68\n"
"v_fmac_f32 v72, 0x41800000, v73\n"
"v_mfma_f32_32x32x16_bf16 v[224:239], a[176:179], a[44:47], v[224:239]\n"
"ds_read_b128 a[176:179], v10 offset:4096\n"
"buffer_load_dwordx4 v22, s[44:47], s12 offen offset:0 lds\n"
"v_mul_f32 v74, v72, v70\n"
"v_mul_f32 v75, v72, v71\n"
"v_exp_f32 v74, v74\n"
"v_fract_f32 v75, v75\n"
"v_mfma_f32_32x32x16_bf16 v[240:255], a[180:183], a[44:47], v[240:255]\n"
"ds_read_b128 a[180:183], v10 offset:45056\n"
"v_cos_f32 v56, v75\n"
"v_sin_f32 v57, v75\n"
"v_mul_f32 v56, v56, v74\n"
"v_mul_f32 v57, v57, v74\n"
"s_lshl_b32 s68, s7, 9\n"
"s_add_u32 s68, s68, 0\n"
"v_cvt_f32_u32 v72, s68\n"
"v_mfma_f32_32x32x16_bf16 v[96:111], a[44:47], a[184:187], v[96:111]\n"
"ds_read_b64_tr_b16 a[184:185], v2 offset:38912\n"
"ds_read_b64_tr_b16 a[186:187], v2 offset:39424\n"
"buffer_load_dwordx4 v23, s[44:47], s12 offen offset:1024 lds\n"
"v_fmac_f32 v72, 0x43000000, v73\n"
"v_mul_f32 v74, v72, v70\n"
"v_mul_f32 v75, v72, v71\n"
"v_exp_f32 v74, v74\n"
"v_mfma_f32_32x32x16_bf16 v[112:127], a[44:47], a[188:191], v[112:127]\n"
"s_waitcnt lgkmcnt(4)\n"
"ds_read_b64_tr_b16 a[188:189], v3 offset:38912\n"
"ds_read_b64_tr_b16 a[190:191], v3 offset:39424\n"
"v_fract_f32 v75, v75\n"
"v_cos_f32 v58, v75\n"
"v_sin_f32 v59, v75\n"
"v_cndmask_b32 v74, 0, v74, s[18:19]\n"
"v_mul_f32 v58, v58, v74\n"
"v_mul_f32 v59, v59, v74\n"
"v_mfma_f32_32x32x16_bf16 v[192:207], a[160:163], a[48:51], v[192:207]\n"
"ds_read_b64_tr_b16 a[160:161], v4 offset:38912\n"
"ds_read_b64_tr_b16 a[162:163], v4 offset:39424\n"
"buffer_load_dwordx4 v24, s[44:47], s12 offen offset:2048 lds\n"
"s_lshl_b32 s68, s7, 9\n"
"s_add_u32 s68, s68, 256\n"
"v_cvt_f32_u32 v72, s68\n"
"v_fmac_f32 v72, 0x43000000, v73\n"
"v_mfma_f32_32x32x16_bf16 v[208:223], a[164:167], a[48:51], v[208:223]\n"
"ds_read_b64_tr_b16 a[164:165], v5 offset:38912\n"
"ds_read_b64_tr_b16 a[166:167], v5 offset:39424\n"
"v_mul_f32 v74, v72, v70\n"
"v_mul_f32 v75, v72, v71\n"
"v_exp_f32 v74, v74\n"
"v_fract_f32 v75, v75\n"
"v_cos_f32 v60, v75\n"
"v_sin_f32 v61, v75\n"
"v_mfma_f32_32x32x16_bf16 v[224:239], a[168:171], a[48:51], v[224:239]\n"
"ds_read_b128 a[168:171], v11 offset:4096\n"
"buffer_load_dwordx4 v25, s[44:47], s12 offen offset:3072 lds\n"
"v_cndmask_b32 v74, 0, v74, s[20:21]\n"
"v_mul_f32 v60, v60, v74\n"
"v_mul_f32 v61, v61, v74\n"
"v_mul_f32 v71, v68, v68\n"
"v_add_f32 v70, -1.0, v44\n"
"v_fmac_f32 v71, v69, v69\n"
"v_mfma_f32_32x32x16_bf16 v[240:255], a[172:175], a[48:51], v[240:255]\n"
"s_waitcnt lgkmcnt(5)\n"
"ds_read_b128 a[172:175], v11 offset:45056\n"
"s_add_u32 s13, s13, 221184\n"
"s_add_u32 m0, s10, 16384\n"
"v_mul_f32 v62, v70, v68\n"
"v_rcp_f32 v71, v71\n"
"v_mul_f32 v63, v45, v68\n"
"v_fmac_f32 v62, v45, v69\n"
"v_fma_f32 v63, -v70, v69, v63\n"
"v_mfma_f32_32x32x16_bf16 v[96:111], a[48:51], a[176:179], v[96:111]\n"
"s_barrier\n"
"ds_read_b64_tr_b16 a[176:177], v6 offset:36864\n"
"ds_read_b64_tr_b16 a[178:179], v6 offset:37376\n"
"buffer_load_dwordx4 v18, s[48:51], s13 offen offset:0 lds\n"
"v_add_f32 v71, v71, v71\n"
"v_mul_f32 v62, v62, v71\n"
"v_mul_f32 v63, v63, v71\n"
"v_mfma_f32_32x32x16_bf16 v[112:127], a[48:51], a[180:183], v[112:127]\n"
"ds_read_b64_tr_b16 a[180:181], v7 offset:36864\n"
"ds_read_b64_tr_b16 a[182:183], v7 offset:37376\n"
"v_mfma_f32_32x32x16_bf16 v[192:207], a[184:187], a[52:55], v[192:207]\n"
"ds_read_b64_tr_b16 a[184:185], v8 offset:36864\n"
"ds_read_b64_tr_b16 a[186:187], v8 offset:37376\n"
"buffer_load_dwordx4 v19, s[48:51], s13 offen offset:1024 lds\n"
"v_mfma_f32_32x32x16_bf16 v[208:223], a[188:191], a[52:55], v[208:223]\n"
"s_waitcnt vmcnt(14) lgkmcnt(6)\n"
"ds_read_b64_tr_b16 a[188:189], v9 offset:36864\n"
"ds_read_b64_tr_b16 a[190:191], v9 offset:37376\n"
"ds_read_b128 a[64:67], v14 offset:20480\n"
"v_mfma_f32_32x32x16_bf16 v[224:239], a[160:163], a[52:55], v[224:239]\n"
"ds_read_b128 a[160:163], v12 offset:4096\n"
"ds_read_b128 a[68:71], v15 offset:20480\n"
"buffer_load_dwordx4 v20, s[48:51], s13 offen offset:2048 lds\n"
"v_mfma_f32_32x32x16_bf16 v[240:255], a[164:167], a[52:55], v[240:255]\n"
"ds_read_b128 a[164:167], v12 offset:45056\n"
"ds_read_b128 a[72:75], v16 offset:20480\n"
"v_mfma_f32_32x32x16_bf16 v[96:111], a[52:55], a[168:171], v[96:111]\n"
"ds_read_b64_tr_b16 a[168:169], v6 offset:38912\n"
"ds_read_b64_tr_b16 a[170:171], v6 offset:39424\n"
"ds_read_b128 a[76:79], v17 offset:20480\n"
"buffer_load_dwordx4 v21, s[48:51], s13 offen offset:3072 lds\n"
"v_mfma_f32_32x32x16_bf16 v[112:127], a[52:55], a[172:175], v[112:127]\n"
"s_waitcnt lgkmcnt(8)\n"
"ds_read_b64_tr_b16 a[172:173], v7 offset:38912\n"
"ds_read_b64_tr_b16 a[174:175], v7 offset:39424\n"
"s_add_u32 s14, s14, 128\n"
"s_add_u32 m0, s10, 6144\n"
"v_mfma_f32_32x32x16_bf16 v[192:207], a[176:179], a[56:59], v[192:207]\n"
"ds_read_b64_tr_b16 a[176:177], v8 offset:38912\n"
"ds_read_b64_tr_b16 a[178:179], v8 offset:39424\n"
"buffer_load_dwordx4 v22, s[56:59], s14 offen offset:0 lds\n"
"v_mfma_f32_32x32x16_bf16 v[208:223], a[180:183], a[56:59], v[208:223]\n"
"ds_read_b64_tr_b16 a[180:181], v9 offset:38912\n"
"ds_read_b64_tr_b16 a[182:183], v9 offset:39424\n"
"v_mfma_f32_32x32x16_bf16 v[224:239], a[184:187], a[56:59], v[224:239]\n"
"ds_read_b128 a[184:187], v13 offset:4096\n"
"buffer_load_dwordx4 v23, s[56:59], s14 offen offset:1024 lds\n"
"v_mfma_f32_32x32x16_bf16 v[240:255], a[188:191], a[56:59], v[240:255]\n"
"s_waitcnt lgkmcnt(5)\n"
"ds_read_b128 a[188:191], v13 offset:45056\n"
"s_add_u32 s12, s12, 128\n"
"s_add_u32 m0, s10, 32768\n"
"v_mfma_f32_32x32x16_bf16 v[96:111], a[56:59], a[160:163], v[96:111]\n"
"s_waitcnt vmcnt(10)\n"
"s_barrier\n"
"ds_read_b64_tr_b16 a[160:161], v2 offset:28672\n"
"ds_read_b64_tr_b16 a[162:163], v2 offset:29184\n"
"buffer_load_dwordx4 v22, s[44:47], s12 offen offset:0 lds\n"
"v_mov_b32 v68, s72\n"
"v_mov_b32 v69, s73\n"
"v_mov_b32 v70, 0\n"
"v_mfma_f32_32x32x16_bf16 v[112:127], a[56:59], a[164:167], v[112:127]\n"
"ds_read_b64_tr_b16 a[164:165], v3 offset:28672\n"
"ds_read_b64_tr_b16 a[166:167], v3 offset:29184\n"
"buffer_store_dwordx2 v[68:69], v70, s[64:67], s94 offen sc1\n"
"v_mov_b32 v68, s72\n"
"v_mov_b32 v69, s73\n"
"v_mov_b32 v70, 0\n"
"v_mfma_f32_32x32x16_bf16 v[192:207], a[168:171], a[60:63], v[192:207]\n"
"ds_read_b64_tr_b16 a[168:169], v4 offset:28672\n"
"ds_read_b64_tr_b16 a[170:171], v4 offset:29184\n"
"buffer_load_dwordx4 v23, s[44:47], s12 offen offset:1024 lds\n"
"buffer_store_dwordx2 v[68:69], v70, s[64:67], s36 offen sc1\n"
"v_mov_b32 v68, s72\n"
"v_mov_b32 v69, s73\n"
"v_mfma_f32_32x32x16_bf16 v[208:223], a[172:175], a[60:63], v[208:223]\n"
"s_waitcnt lgkmcnt(6)\n"
"ds_read_b64_tr_b16 a[172:173], v5 offset:28672\n"
"ds_read_b64_tr_b16 a[174:175], v5 offset:29184\n"
"v_mov_b32 v70, 32768\n"
"v_mfma_f32_32x32x16_bf16 v[224:239], a[176:179], a[60:63], v[224:239]\n"
"ds_read_b128 a[176:179], v10 offset:2048\n"
"buffer_load_dwordx4 v24, s[44:47], s12 offen offset:2048 lds\n"
"buffer_store_dwordx2 v[68:69], v70, s[64:67], s36 offen sc1\n"
"v_mfma_f32_32x32x16_bf16 v[240:255], a[180:183], a[60:63], v[240:255]\n"
"ds_read_b128 a[180:183], v10 offset:43008\n"
"v_mfma_f32_32x32x16_bf16 v[96:111], a[60:63], a[184:187], v[96:111]\n"
"ds_read_b64_tr_b16 a[184:185], v2 offset:30720\n"
"ds_read_b64_tr_b16 a[186:187], v2 offset:31232\n"
"buffer_load_dwordx4 v25, s[44:47], s12 offen offset:3072 lds\n"
"v_mfma_f32_32x32x16_bf16 v[112:127], a[60:63], a[188:191], v[112:127]\n"
"s_waitcnt lgkmcnt(4)\n"
"ds_read_b64_tr_b16 a[188:189], v3 offset:30720\n"
"ds_read_b64_tr_b16 a[190:191], v3 offset:31232\n"
"s_add_u32 s13, s13, 221184\n"
"s_add_u32 m0, s10, 20480\n"
"v_mfma_f32_32x32x16_bf16 v[192:207], a[160:163], a[64:67], v[192:207]\n"
"ds_read_b64_tr_b16 a[160:161], v4 offset:30720\n"
"ds_read_b64_tr_b16 a[162:163], v4 offset:31232\n"
"buffer_load_dwordx4 v18, s[48:51], s13 offen offset:0 lds\n"
"v_mfma_f32_32x32x16_bf16 v[208:223], a[164:167], a[64:67], v[208:223]\n"
"ds_read_b64_tr_b16 a[164:165], v5 offset:30720\n"
"ds_read_b64_tr_b16 a[166:167], v5 offset:31232\n"
"v_mfma_f32_32x32x16_bf16 v[224:239], a[168:171], a[64:67], v[224:239]\n"
"s_waitcnt vmcnt(14)\n"
"ds_read_b128 a[168:171], v11 offset:2048\n"
"ds_read_b128 a[80:83], v14 offset:24576\n"
"buffer_load_dwordx4 v19, s[48:51], s13 offen offset:1024 lds\n"
"v_mfma_f32_32x32x16_bf16 v[240:255], a[172:175], a[64:67], v[240:255]\n"
"s_waitcnt lgkmcnt(6)\n"
"ds_read_b128 a[172:175], v11 offset:43008\n"
"ds_read_b128 a[84:87], v15 offset:24576\n"
"v_mfma_f32_32x32x16_bf16 v[96:111], a[64:67], a[176:179], v[96:111]\n"
"s_barrier\n"
"ds_read_b64_tr_b16 a[176:177], v6 offset:28672\n"
"ds_read_b64_tr_b16 a[178:179], v6 offset:29184\n"
"ds_read_b128 a[88:91], v16 offset:24576\n"
"buffer_load_dwordx4 v20, s[48:51], s13 offen offset:2048 lds\n"
"v_mfma_f32_32x32x16_bf16 v[112:127], a[64:67], a[180:183], v[112:127]\n"
"ds_read_b64_tr_b16 a[180:181], v7 offset:28672\n"
"ds_read_b64_tr_b16 a[182:183], v7 offset:29184\n"
"ds_read_b128 a[92:95], v17 offset:24576\n"
"v_mfma_f32_32x32x16_bf16 v[192:207], a[184:187], a[68:71], v[192:207]\n"
"ds_read_b64_tr_b16 a[184:185], v8 offset:28672\n"
"ds_read_b64_tr_b16 a[186:187], v8 offset:29184\n"
"buffer_load_dwordx4 v21, s[48:51], s13 offen offset:3072 lds\n"
"v_mfma_f32_32x32x16_bf16 v[208:223], a[188:191], a[68:71], v[208:223]\n"
"s_waitcnt lgkmcnt(9)\n"
"ds_read_b64_tr_b16 a[188:189], v9 offset:28672\n"
"ds_read_b64_tr_b16 a[190:191], v9 offset:29184\n"
"s_add_u32 s14, s14, 128\n"
"s_add_u32 m0, s10, 4096\n"
"v_mfma_f32_32x32x16_bf16 v[224:239], a[160:163], a[68:71], v[224:239]\n"
"ds_read_b128 a[160:163], v12 offset:2048\n"
"buffer_load_dwordx4 v22, s[56:59], s14 offen offset:0 lds\n"
"v_mfma_f32_32x32x16_bf16 v[240:255], a[164:167], a[68:71], v[240:255]\n"
"ds_read_b128 a[164:167], v12 offset:43008\n"
"v_mfma_f32_32x32x16_bf16 v[96:111], a[68:71], a[168:171], v[96:111]\n"
"ds_read_b64_tr_b16 a[168:169], v6 offset:30720\n"
"ds_read_b64_tr_b16 a[170:171], v6 offset:31232\n"
"buffer_load_dwordx4 v23, s[56:59], s14 offen offset:1024 lds\n"
"v_mfma_f32_32x32x16_bf16 v[112:127], a[68:71], a[172:175], v[112:127]\n"
"s_waitcnt lgkmcnt(4)\n"
"ds_read_b64_tr_b16 a[172:173], v7 offset:30720\n"
"ds_read_b64_tr_b16 a[174:175], v7 offset:31232\n"
"s_add_u32 s12, s12, 128\n"
"s_add_u32 m0, s10, 36864\n"
"v_mfma_f32_32x32x16_bf16 v[192:207], a[176:179], a[72:75], v[192:207]\n"
"ds_read_b64_tr_b16 a[176:177], v8 offset:30720\n"
"ds_read_b64_tr_b16 a[178:179], v8 offset:31232\n"
"buffer_load_dwordx4 v22, s[44:47], s12 offen offset:0 lds\n"
"v_mfma_f32_32x32x16_bf16 v[208:223], a[180:183], a[72:75], v[208:223]\n"
"ds_read_b64_tr_b16 a[180:181], v9 offset:30720\n"
"ds_read_b64_tr_b16 a[182:183], v9 offset:31232\n"
"v_mfma_f32_32x32x16_bf16 v[224:239], a[184:187], a[72:75], v[224:239]\n"
"ds_read_b128 a[184:187], v13 offset:2048\n"
"buffer_load_dwordx4 v23, s[44:47], s12 offen offset:1024 lds\n"
"v_mfma_f32_32x32x16_bf16 v[240:255], a[188:191], a[72:75], v[240:255]\n"
"s_waitcnt lgkmcnt(5)\n"
"ds_read_b128 a[188:191], v13 offset:43008\n"
"v_mfma_f32_32x32x16_bf16 v[96:111], a[72:75], a[160:163], v[96:111]\n"
"s_waitcnt vmcnt(15)\n"
"s_barrier\n"
"ds_read_b64_tr_b16 a[160:161], v2 offset:16384\n"
"ds_read_b64_tr_b16 a[162:163], v2 offset:16896\n"
"buffer_load_dwordx4 v24, s[44:47], s12 offen offset:2048 lds\n"
"v_mfma_f32_32x32x16_bf16 v[112:127], a[72:75], a[164:167], v[112:127]\n"
"ds_read_b64_tr_b16 a[164:165], v3 offset:16384\n"
"ds_read_b64_tr_b16 a[166:167], v3 offset:16896\n"
"v_mfma_f32_32x32x16_bf16 v[192:207], a[168:171], a[76:79], v[192:207]\n"
"ds_read_b64_tr_b16 a[168:169], v4 offset:16384\n"
"ds_read_b64_tr_b16 a[170:171], v4 offset:16896\n"
"buffer_load_dwordx4 v25, s[44:47], s12 offen offset:3072 lds\n"
"v_mfma_f32_32x32x16_bf16 v[208:223], a[172:175], a[76:79], v[208:223]\n"
"s_waitcnt lgkmcnt(6)\n"
"ds_read_b64_tr_b16 a[172:173], v5 offset:16384\n"
"ds_read_b64_tr_b16 a[174:175], v5 offset:16896\n"
"s_add_u32 s12, s12, 128\n"
"s_add_u32 m0, s10, 24576\n"
"v_mfma_f32_32x32x16_bf16 v[224:239], a[176:179], a[76:79], v[224:239]\n"
"ds_read_b128 a[176:179], v10 offset:6144\n"
"buffer_load_dwordx4 v22, s[44:47], s12 offen offset:0 lds\n"
"v_mfma_f32_32x32x16_bf16 v[240:255], a[180:183], a[76:79], v[240:255]\n"
"ds_read_b128 a[180:183], v10 offset:47104\n"
"v_mfma_f32_32x32x16_bf16 v[96:111], a[76:79], a[184:187], v[96:111]\n"
"s_waitcnt vmcnt(11)\n"
"ds_read_b64_tr_b16 a[184:185], v2 offset:18432\n"
"ds_read_b64_tr_b16 a[186:187], v2 offset:18944\n"
"ds_read_b128 a[96:99], v14 offset:32768\n"
"buffer_load_dwordx4 v23, s[44:47], s12 offen offset:1024 lds\n"
"v_mfma_f32_32x32x16_bf16 v[112:127], a[76:79], a[188:191], v[112:127]\n"
"s_waitcnt lgkmcnt(5)\n"
"ds_read_b64_tr_b16 a[188:189], v3 offset:18432\n"
"ds_read_b64_tr_b16 a[190:191], v3 offset:18944\n"
"ds_read_b128 a[100:103], v15 offset:32768\n"
"v_mfma_f32_32x32x16_bf16 v[192:207], a[160:163], a[80:83], v[192:207]\n"
"ds_read_b64_tr_b16 a[160:161], v4 offset:18432\n"
"ds_read_b64_tr_b16 a[162:163], v4 offset:18944\n"
"ds_read_b128 a[104:107], v16 offset:32768\n"
"buffer_load_dwordx4 v24, s[44:47], s12 offen offset:2048 lds\n"
"v_mfma_f32_32x32x16_bf16 v[208:223], a[164:167], a[80:83], v[208:223]\n"
"ds_read_b64_tr_b16 a[164:165], v5 offset:18432\n"
"ds_read_b64_tr_b16 a[166:167], v5 offset:18944\n"
"ds_read_b128 a[108:111], v17 offset:32768\n"
"v_mfma_f32_32x32x16_bf16 v[224:239], a[168:171], a[80:83], v[224:239]\n"
"ds_read_b128 a[168:171], v11 offset:6144\n"
"buffer_load_dwordx4 v25, s[44:47], s12 offen offset:3072 lds\n"
"v_mfma_f32_32x32x16_bf16 v[240:255], a[172:175], a[80:83], v[240:255]\n"
"s_waitcnt lgkmcnt(8)\n"
"ds_read_b128 a[172:175], v11 offset:47104\n"
"s_add_u32 s13, s13, 221184\n"
"s_add_u32 m0, s10, 12288\n"
"v_mfma_f32_32x32x16_bf16 v[96:111], a[80:83], a[176:179], v[96:111]\n"
"s_barrier\n"
"ds_read_b64_tr_b16 a[176:177], v6 offset:16384\n"
"ds_read_b64_tr_b16 a[178:179], v6 offset:16896\n"
"buffer_load_dwordx4 v18, s[48:51], s13 offen offset:0 lds\n"
"v_mfma_f32_32x32x16_bf16 v[112:127], a[80:83], a[180:183], v[112:127]\n"
"ds_read_b64_tr_b16 a[180:181], v7 offset:16384\n"
"ds_read_b64_tr_b16 a[182:183], v7 offset:16896\n"
"v_mfma_f32_32x32x16_bf16 v[192:207], a[184:187], a[84:87], v[192:207]\n"
"ds_read_b64_tr_b16 a[184:185], v8 offset:16384\n"
"ds_read_b64_tr_b16 a[186:187], v8 offset:16896\n"
"buffer_load_dwordx4 v19, s[48:51], s13 offen offset:1024 lds\n"
"v_mfma_f32_32x32x16_bf16 v[208:223], a[188:191], a[84:87], v[208:223]\n"
"s_waitcnt lgkmcnt(6)\n"
"ds_read_b64_tr_b16 a[188:189], v9 offset:16384\n"
"ds_read_b64_tr_b16 a[190:191], v9 offset:16896\n"
"v_mfma_f32_32x32x16_bf16 v[224:239], a[160:163], a[84:87], v[224:239]\n"
"ds_read_b128 a[160:163], v12 offset:6144\n"
"buffer_load_dwordx4 v20, s[48:51], s13 offen offset:2048 lds\n"
"v_lshl_add_u32 v78, v1, 4, s91\n"
"v_add_u32 v79, 21037056, v28\n"
"v_mfma_f32_32x32x16_bf16 v[240:255], a[164:167], a[84:87], v[240:255]\n"
"ds_read_b128 a[164:167], v12 offset:47104\n"
"v_mfma_f32_32x32x16_bf16 v[96:111], a[84:87], a[168:171], v[96:111]\n"
"ds_read_b64_tr_b16 a[168:169], v6 offset:18432\n"
"ds_read_b64_tr_b16 a[170:171], v6 offset:18944\n"
"buffer_load_dwordx4 v21, s[48:51], s13 offen offset:3072 lds\n"
"buffer_load_dwordx4 v[64:67], v78, s[64:67], 0 offen offset:0 sc1\n"
"v_mfma_f32_32x32x16_bf16 v[112:127], a[84:87], a[172:175], v[112:127]\n"
"s_waitcnt lgkmcnt(4)\n"
"ds_read_b64_tr_b16 a[172:173], v7 offset:18432\n"
"ds_read_b64_tr_b16 a[174:175], v7 offset:18944\n"
"s_add_u32 s14, s14, 128\n"
"s_add_u32 m0, s10, 2048\n"
"v_mfma_f32_32x32x16_bf16 v[192:207], a[176:179], a[88:91], v[192:207]\n"
"ds_read_b64_tr_b16 a[176:177], v8 offset:18432\n"
"ds_read_b64_tr_b16 a[178:179], v8 offset:18944\n"
"buffer_load_dwordx4 v22, s[56:59], s14 offen offset:0 lds\n"
"buffer_load_dwordx4 v[88:91], v79, s[64:67], s93 offen offset:0 sc1\n"
"v_mfma_f32_32x32x16_bf16 v[208:223], a[180:183], a[88:91], v[208:223]\n"
"ds_read_b64_tr_b16 a[180:181], v9 offset:18432\n"
"ds_read_b64_tr_b16 a[182:183], v9 offset:18944\n"
"v_mfma_f32_32x32x16_bf16 v[224:239], a[184:187], a[88:91], v[224:239]\n"
"ds_read_b128 a[184:187], v13 offset:6144\n"
"buffer_load_dwordx4 v23, s[56:59], s14 offen offset:1024 lds\n"
"v_mfma_f32_32x32x16_bf16 v[240:255], a[188:191], a[88:91], v[240:255]\n"
"s_waitcnt lgkmcnt(5)\n"
"ds_read_b128 a[188:191], v13 offset:47104\n"
"s_add_u32 s12, s12, 128\n"
"s_add_u32 m0, s10, 32768\n"
"v_mfma_f32_32x32x16_bf16 v[96:111], a[88:91], a[160:163], v[96:111]\n"
"s_waitcnt vmcnt(16)\n"
"s_barrier\n"
"ds_read_b64_tr_b16 a[160:161], v2 offset:20480\n"
"ds_read_b64_tr_b16 a[162:163], v2 offset:20992\n"
"buffer_load_dwordx4 v22, s[44:47], s12 offen offset:0 lds\n"
"v_mfma_f32_32x32x16_bf16 v[112:127], a[88:91], a[164:167], v[112:127]\n"
"ds_read_b64_tr_b16 a[164:165], v3 offset:20480\n"
"ds_read_b64_tr_b16 a[166:167], v3 offset:20992\n"
"v_mfma_f32_32x32x16_bf16 v[192:207], a[168:171], a[92:95], v[192:207]\n"
"s_waitcnt vmcnt(13)\n"
"ds_read_b64_tr_b16 a[168:169], v4 offset:20480\n"
"ds_read_b64_tr_b16 a[170:171], v4 offset:20992\n"
"ds_read_b128 a[112:115], v14 offset:36864\n"
"buffer_load_dwordx4 v23, s[44:47], s12 offen offset:1024 lds\n"
"v_mfma_f32_32x32x16_bf16 v[208:223], a[172:175], a[92:95], v[208:223]\n"
"s_waitcnt lgkmcnt(7)\n"
"ds_read_b64_tr_b16 a[172:173], v5 offset:20480\n"
"ds_read_b64_tr_b16 a[174:175], v5 offset:20992\n"
"ds_read_b128 a[116:119], v15 offset:36864\n"
"v_mfma_f32_32x32x16_bf16 v[224:239], a[176:179], a[92:95], v[224:239]\n"
"ds_read_b128 a[176:179], v10 offset:4096\n"
"ds_read_b128 a[120:123], v16 offset:36864\n"
"buffer_load_dwordx4 v24, s[44:47], s12 offen offset:2048 lds\n"
"v_mfma_f32_32x32x16_bf16 v[240:255], a[180:183], a[92:95], v[240:255]\n"
"ds_read_b128 a[180:183], v10 offset:45056\n"
"ds_read_b128 a[124:127], v17 offset:36864\n"
"v_mfma_f32_32x32x16_bf16 v[96:111], a[92:95], a[184:187], v[96:111]\n"
"ds_read_b64_tr_b16 a[184:185], v2 offset:22528\n"
"ds_read_b64_tr_b16 a[186:187], v2 offset:23040\n"
"buffer_load_dwordx4 v25, s[44:47], s12 offen offset:3072 lds\n"
"v_mfma_f32_32x32x16_bf16 v[112:127], a[92:95], a[188:191], v[112:127]\n"
"s_waitcnt lgkmcnt(7)\n"
"ds_read_b64_tr_b16 a[188:189], v3 offset:22528\n"
"ds_read_b64_tr_b16 a[190:191], v3 offset:23040\n"
"s_add_u32 s14, s14, 128\n"
"s_add_u32 m0, s10, 8192\n"
"v_mfma_f32_32x32x16_bf16 v[192:207], a[160:163], a[96:99], v[192:207]\n"
"ds_read_b64_tr_b16 a[160:161], v4 offset:22528\n"
"ds_read_b64_tr_b16 a[162:163], v4 offset:23040\n"
"buffer_load_dwordx4 v22, s[56:59], s14 offen offset:0 lds\n"
"v_mfma_f32_32x32x16_bf16 v[208:223], a[164:167], a[96:99], v[208:223]\n"
"ds_read_b64_tr_b16 a[164:165], v5 offset:22528\n"
"ds_read_b64_tr_b16 a[166:167], v5 offset:23040\n"
"v_mfma_f32_32x32x16_bf16 v[224:239], a[168:171], a[96:99], v[224:239]\n"
"ds_read_b128 a[168:171], v11 offset:4096\n"
"buffer_load_dwordx4 v23, s[56:59], s14 offen offset:1024 lds\n"
"v_mfma_f32_32x32x16_bf16 v[240:255], a[172:175], a[96:99], v[240:255]\n"
"s_waitcnt lgkmcnt(5)\n"
"ds_read_b128 a[172:175], v11 offset:45056\n"
"s_add_u32 s14, s14, 128\n"
"s_add_u32 m0, s10, 10240\n"
"v_mfma_f32_32x32x16_bf16 v[96:111], a[96:99], a[176:179], v[96:111]\n"
"s_barrier\n"
"ds_read_b64_tr_b16 a[176:177], v6 offset:20480\n"
"ds_read_b64_tr_b16 a[178:179], v6 offset:20992\n"
"buffer_load_dwordx4 v22, s[56:59], s14 offen offset:0 lds\n"
"v_mfma_f32_32x32x16_bf16 v[112:127], a[96:99], a[180:183], v[112:127]\n"
"ds_read_b64_tr_b16 a[180:181], v7 offset:20480\n"
"ds_read_b64_tr_b16 a[182:183], v7 offset:20992\n"
"v_mfma_f32_32x32x16_bf16 v[192:207], a[184:187], a[100:103], v[192:207]\n"
"ds_read_b64_tr_b16 a[184:185], v8 offset:20480\n"
"ds_read_b64_tr_b16 a[186:187], v8 offset:20992\n"
"buffer_load_dwordx4 v23, s[56:59], s14 offen offset:1024 lds\n"
"v_mfma_f32_32x32x16_bf16 v[208:223], a[188:191], a[100:103], v[208:223]\n"
"s_waitcnt lgkmcnt(6)\n"
"ds_read_b64_tr_b16 a[188:189], v9 offset:20480\n"
"ds_read_b64_tr_b16 a[190:191], v9 offset:20992\n"
"s_add_u32 s13, s13, 221184\n"
"s_add_u32 m0, s10, 36864\n"
"v_mfma_f32_32x32x16_bf16 v[224:239], a[160:163], a[100:103], v[224:239]\n"
"ds_read_b128 a[160:163], v12 offset:4096\n"
"buffer_load_dwordx4 v18, s[48:51], s13 offen offset:0 lds\n"
"v_mfma_f32_32x32x16_bf16 v[240:255], a[164:167], a[100:103], v[240:255]\n"
"ds_read_b128 a[164:167], v12 offset:45056\n"
"v_mfma_f32_32x32x16_bf16 v[96:111], a[100:103], a[168:171], v[96:111]\n"
"ds_read_b64_tr_b16 a[168:169], v6 offset:22528\n"
"ds_read_b64_tr_b16 a[170:171], v6 offset:23040\n"
"buffer_load_dwordx4 v19, s[48:51], s13 offen offset:1024 lds\n"
"v_mfma_f32_32x32x16_bf16 v[112:127], a[100:103], a[172:175], v[112:127]\n"
"s_waitcnt lgkmcnt(4)\n"
"ds_read_b64_tr_b16 a[172:173], v7 offset:22528\n"
"ds_read_b64_tr_b16 a[174:175], v7 offset:23040\n"
"v_mfma_f32_32x32x16_bf16 v[192:207], a[176:179], a[104:107], v[192:207]\n"
"ds_read_b64_tr_b16 a[176:177], v8 offset:22528\n"
"ds_read_b64_tr_b16 a[178:179], v8 offset:23040\n"
"buffer_load_dwordx4 v20, s[48:51], s13 offen offset:2048 lds\n"
"v_mfma_f32_32x32x16_bf16 v[208:223], a[180:183], a[104:107], v[208:223]\n"
"ds_read_b64_tr_b16 a[180:181], v9 offset:22528\n"
"ds_read_b64_tr_b16 a[182:183], v9 offset:23040\n"
"v_mfma_f32_32x32x16_bf16 v[224:239], a[184:187], a[104:107], v[224:239]\n"
"s_waitcnt vmcnt(19)\n"
"ds_read_b128 a[184:187], v13 offset:4096\n"
"ds_read_b128 a[128:131], v14 offset:24576\n"
"buffer_load_dwordx4 v21, s[48:51], s13 offen offset:3072 lds\n"
"v_mfma_f32_32x32x16_bf16 v[240:255], a[188:191], a[104:107], v[240:255]\n"
"s_waitcnt lgkmcnt(6)\n"
"ds_read_b128 a[188:191], v13 offset:45056\n"
"ds_read_b128 a[132:135], v15 offset:24576\n"
"s_add_u32 s13, s13, 221184\n"
"s_add_u32 m0, s10, 28672\n"
"v_mfma_f32_32x32x16_bf16 v[96:111], a[104:107], a[160:163], v[96:111]\n"
"s_waitcnt vmcnt(12)\n"
"s_barrier\n"
"ds_read_b64_tr_b16 a[160:161], v2 offset:12288\n"
"ds_read_b64_tr_b16 a[162:163], v2 offset:12800\n"
"ds_read_b128 a[136:139], v16 offset:24576\n"
"buffer_load_dwordx4 v18, s[48:51], s13 offen offset:0 lds\n"
"v_mfma_f32_32x32x16_bf16 v[112:127], a[104:107], a[164:167], v[112:127]\n"
"ds_read_b64_tr_b16 a[164:165], v3 offset:12288\n"
"ds_read_b64_tr_b16 a[166:167], v3 offset:12800\n"
"ds_read_b128 a[140:143], v17 offset:24576\n"
"v_mfma_f32_32x32x16_bf16 v[192:207], a[168:171], a[108:111], v[192:207]\n"
"ds_read_b64_tr_b16 a[168:169], v4 offset:12288\n"
"ds_read_b64_tr_b16 a[170:171], v4 offset:12800\n"
"buffer_load_dwordx4 v19, s[48:51], s13 offen offset:1024 lds\n"
"v_mfma_f32_32x32x16_bf16 v[208:223], a[172:175], a[108:111], v[208:223]\n"
"s_waitcnt lgkmcnt(9)\n"
"ds_read_b64_tr_b16 a[172:173], v5 offset:12288\n"
"ds_read_b64_tr_b16 a[174:175], v5 offset:12800\n"
"v_mfma_f32_32x32x16_bf16 v[224:239], a[176:179], a[108:111], v[224:239]\n"
"ds_read_b128 a[176:179], v10 offset:2048\n"
"buffer_load_dwordx4 v20, s[48:51], s13 offen offset:2048 lds\n"
"v_mfma_f32_32x32x16_bf16 v[240:255], a[180:183], a[108:111], v[240:255]\n"
"ds_read_b128 a[180:183], v10 offset:43008\n"
"v_mfma_f32_32x32x16_bf16 v[96:111], a[108:111], a[184:187], v[96:111]\n"
"ds_read_b64_tr_b16 a[184:185], v2 offset:14336\n"
"ds_read_b64_tr_b16 a[186:187], v2 offset:14848\n"
"buffer_load_dwordx4 v21, s[48:51], s13 offen offset:3072 lds\n"
"v_mfma_f32_32x32x16_bf16 v[112:127], a[108:111], a[188:191], v[112:127]\n"
"s_waitcnt lgkmcnt(4)\n"
"ds_read_b64_tr_b16 a[188:189], v3 offset:14336\n"
"ds_read_b64_tr_b16 a[190:191], v3 offset:14848\n"
"v_mfma_f32_32x32x16_bf16 v[192:207], a[160:163], a[112:115], v[192:207]\n"
"ds_read_b64_tr_b16 a[160:161], v4 offset:14336\n"
"ds_read_b64_tr_b16 a[162:163], v4 offset:14848\n"
"v_mfma_f32_32x32x16_bf16 v[208:223], a[164:167], a[112:115], v[208:223]\n"
"ds_read_b64_tr_b16 a[164:165], v5 offset:14336\n"
"ds_read_b64_tr_b16 a[166:167], v5 offset:14848\n"
"v_mfma_f32_32x32x16_bf16 v[224:239], a[168:171], a[112:115], v[224:239]\n"
"ds_read_b128 a[168:171], v11 offset:2048\n"
"v_mfma_f32_32x32x16_bf16 v[240:255], a[172:175], a[112:115], v[240:255]\n"
"s_waitcnt lgkmcnt(5)\n"
"ds_read_b128 a[172:175], v11 offset:43008\n"
"v_mfma_f32_32x32x16_bf16 v[96:111], a[112:115], a[176:179], v[96:111]\n"
"s_barrier\n"
"ds_read_b64_tr_b16 a[176:177], v6 offset:12288\n"
"ds_read_b64_tr_b16 a[178:179], v6 offset:12800\n"
"v_mfma_f32_32x32x16_bf16 v[112:127], a[112:115], a[180:183], v[112:127]\n"
"ds_read_b64_tr_b16 a[180:181], v7 offset:12288\n"
"ds_read_b64_tr_b16 a[182:183], v7 offset:12800\n"
"v_mfma_f32_32x32x16_bf16 v[192:207], a[184:187], a[116:119], v[192:207]\n"
"ds_read_b64_tr_b16 a[184:185], v8 offset:12288\n"
"ds_read_b64_tr_b16 a[186:187], v8 offset:12800\n"
"v_mfma_f32_32x32x16_bf16 v[208:223], a[188:191], a[116:119], v[208:223]\n"
"s_waitcnt lgkmcnt(6)\n"
"ds_read_b64_tr_b16 a[188:189], v9 offset:12288\n"
"ds_read_b64_tr_b16 a[190:191], v9 offset:12800\n"
"v_mfma_f32_32x32x16_bf16 v[224:239], a[160:163], a[116:119], v[224:239]\n"
"ds_read_b128 a[160:163], v12 offset:2048\n"
"v_mfma_f32_32x32x16_bf16 v[240:255], a[164:167], a[116:119], v[240:255]\n"
"ds_read_b128 a[164:167], v12 offset:43008\n"
"v_mfma_f32_32x32x16_bf16 v[96:111], a[116:119], a[168:171], v[96:111]\n"
"s_waitcnt vmcnt(12)\n"
"ds_read_b64_tr_b16 a[168:169], v6 offset:14336\n"
"ds_read_b64_tr_b16 a[170:171], v6 offset:14848\n"
"ds_read_b128 a[144:147], v14 offset:32768\n"
"v_mfma_f32_32x32x16_bf16 v[112:127], a[116:119], a[172:175], v[112:127]\n"
"s_waitcnt lgkmcnt(5)\n"
"ds_read_b64_tr_b16 a[172:173], v7 offset:14336\n"
"ds_read_b64_tr_b16 a[174:175], v7 offset:14848\n"
"ds_read_b128 a[148:151], v15 offset:32768\n"
"v_mfma_f32_32x32x16_bf16 v[192:207], a[176:179], a[120:123], v[192:207]\n"
"ds_read_b64_tr_b16 a[176:177], v8 offset:14336\n"
"ds_read_b64_tr_b16 a[178:179], v8 offset:14848\n"
"ds_read_b128 a[152:155], v16 offset:32768\n"
"v_mfma_f32_32x32x16_bf16 v[208:223], a[180:183], a[120:123], v[208:223]\n"
"ds_read_b64_tr_b16 a[180:181], v9 offset:14336\n"
"ds_read_b64_tr_b16 a[182:183], v9 offset:14848\n"
"ds_read_b128 a[156:159], v17 offset:32768\n"
"v_mfma_f32_32x32x16_bf16 v[224:239], a[184:187], a[120:123], v[224:239]\n"
"ds_read_b128 a[184:187], v13 offset:2048\n"
"v_mfma_f32_32x32x16_bf16 v[240:255], a[188:191], a[120:123], v[240:255]\n"
"s_waitcnt lgkmcnt(8)\n"
"ds_read_b128 a[188:191], v13 offset:43008\n"
"v_mfma_f32_32x32x16_bf16 v[96:111], a[120:123], a[160:163], v[96:111]\n"
"s_waitcnt vmcnt(8)\n"
"s_barrier\n"
"ds_read_b128 a[160:163], v10 offset:8192\n"
"v_xor_b32 v69, s72, v65\n"
"v_xor_b32 v70, s73, v67\n"
"v_or3_b32 v68, v69, v70, 0\n"
"v_xor_b32 v69, s72, v89\n"
"v_xor_b32 v70, s73, v91\n"
"v_or3_b32 v68, v69, v70, v68\n"
"v_cmp_ne_u32 vcc, 0, v68\n"
"s_nop 0\n"
"s_cmp_lg_u64 vcc, 0\n"
"s_cbranch_scc1 K1X_l_slow\n"
"K1X_l_back:\n"
"v_mfma_f32_32x32x16_bf16 v[112:127], a[120:123], a[164:167], v[112:127]\n"
"ds_read_b128 a[164:167], v10 offset:49152\n"
"s_lshl_b32 s68, s7, 8\n"
"s_add_u32 s68, s68, 81920\n"
"v_and_b32 v69, 0x1ff, v28\n"
"v_cvt_pk_bf16_f32 v68, -v66, -v66\n"
"v_lshrrev_b32 v70, 5, v27\n"
"v_lshl_add_u32 v14, v70, 6, s68\n"
"v_cndmask_b32 v40, v68, 0, s[16:17]\n"
"v_mov_b32 v41, 0\n"
"v_and_b32 v42, 0xffff, v40\n"
"v_mfma_f32_32x32x16_bf16 v[192:207], a[168:171], a[124:127], v[192:207]\n"
"ds_read_b128 a[168:171], v11 offset:8192\n"
"v_mov_b32 v43, 0\n"
"v_cndmask_b32 v68, 0, v68, s[16:17]\n"
"v_accvgpr_write_b32 a197, 0\n"
"v_accvgpr_write_b32 a196, v68\n"
"v_and_b32 v68, 0xffff, v68\n"
"v_accvgpr_write_b32 a199, 0\n"
"v_accvgpr_write_b32 a198, v68\n"
"s_mov_b32 s69, m0\n"
"s_mov_b32 m0, s68\n"
"s_nop 0\n"
"buffer_load_dword v69, s[64:67], s91 offen sc1 lds\n"
"s_mov_b32 m0, s69\n"
"v_mfma_f32_32x32x16_bf16 v[208:223], a[172:175], a[124:127], v[208:223]\n"
"s_waitcnt lgkmcnt(3)\n"
"ds_read_b128 a[172:175], v11 offset:49152\n"
"v_mul_f32 v37, v64, v64\n"
"v_mul_f32 v36, 0xbfb8aa3b, v64\n"
"v_rcp_f32 v37, v37\n"
"v_mfma_f32_32x32x16_bf16 v[224:239], a[176:179], a[124:127], v[224:239]\n"
"ds_read_b128 a[176:179], v12 offset:8192\n"
"v_mfma_f32_32x32x16_bf16 v[240:255], a[180:183], a[124:127], v[240:255]\n"
"ds_read_b128 a[180:183], v12 offset:49152\n"
"buffer_load_dwordx4 v[80:83], v79, s[64:67], s92 offen offset:0 sc1\n"
"v_mfma_f32_32x32x16_bf16 v[96:111], a[124:127], a[184:187], v[96:111]\n"
"ds_read_b128 a[184:187], v13 offset:8192\n"
"v_mfma_f32_32x32x16_bf16 v[112:127], a[124:127], a[188:191], v[112:127]\n"
"s_waitcnt lgkmcnt(3)\n"
"ds_read_b128 a[188:191], v13 offset:49152\n"
"buffer_load_dwordx4 v[84:87], v79, s[64:67], s92 offen offset:1024 sc1\n"
"v_mfma_f32_32x32x16_bf16 v[96:111], a[128:131], a[160:163], v[96:111]\n"
"ds_read_b128 a[160:163], v10 offset:10240\n"
"v_mfma_f32_32x32x16_bf16 v[112:127], a[128:131], a[164:167], v[112:127]\n"
"ds_read_b128 a[164:167], v10 offset:51200\n"
"v_mfma_f32_32x32x16_bf16 v[96:111], a[132:135], a[168:171], v[96:111]\n"
"ds_read_b128 a[168:171], v11 offset:10240\n"
"v_mfma_f32_32x32x16_bf16 v[112:127], a[132:135], a[172:175], v[112:127]\n"
"s_waitcnt lgkmcnt(3)\n"
"ds_read_b128 a[172:175], v11 offset:51200\n"
"v_mfma_f32_32x32x16_bf16 v[96:111], a[136:139], a[176:179], v[96:111]\n"
"ds_read_b128 a[176:179], v12 offset:10240\n"
"v_mfma_f32_32x32x16_bf16 v[112:127], a[136:139], a[180:183], v[112:127]\n"
"ds_read_b128 a[180:183], v12 offset:51200\n"
"v_mfma_f32_32x32x16_bf16 v[96:111], a[140:143], a[184:187], v[96:111]\n"
"ds_read_b128 a[184:187], v13 offset:10240\n"
"v_mfma_f32_32x32x16_bf16 v[112:127], a[140:143], a[188:191], v[112:127]\n"
"s_waitcnt lgkmcnt(3)\n"
"ds_read_b128 a[188:191], v13 offset:51200\n"
"v_mfma_f32_32x32x16_bf16 v[96:111], a[144:147], a[160:163], v[96:111]\n"
"s_waitcnt vmcnt(7)\n"
"s_barrier\n"
"ds_read_b64_tr_b16 a[160:161], v2 offset:36864\n"
"ds_read_b64_tr_b16 a[162:163], v2 offset:37376\n"
"v_mfma_f32_32x32x16_bf16 v[112:127], a[144:147], a[164:167], v[112:127]\n"
"ds_read_b64_tr_b16 a[164:165], v3 offset:36864\n"
"ds_read_b64_tr_b16 a[166:167], v3 offset:37376\n"
"v_mfma_f32_32x32x16_bf16 v[96:111], a[148:151], a[168:171], v[96:111]\n"
"ds_read_b64_tr_b16 a[168:169], v4 offset:36864\n"
"ds_read_b64_tr_b16 a[170:171], v4 offset:37376\n"
"v_mfma_f32_32x32x16_bf16 v[112:127], a[148:151], a[172:175], v[112:127]\n"
"s_waitcnt lgkmcnt(6)\n"
"ds_read_b64_tr_b16 a[172:173], v5 offset:36864\n"
"ds_read_b64_tr_b16 a[174:175], v5 offset:37376\n"
"v_mfma_f32_32x32x16_bf16 v[96:111], a[152:155], a[176:179], v[96:111]\n"
"ds_read_b64_tr_b16 a[176:177], v2 offset:38912\n"
"ds_read_b64_tr_b16 a[178:179], v2 offset:39424\n"
"v_mfma_f32_32x32x16_bf16 v[112:127], a[152:155], a[180:183], v[112:127]\n"
"ds_read_b64_tr_b16 a[180:181], v3 offset:38912\n"
"ds_read_b64_tr_b16 a[182:183], v3 offset:39424\n"
"v_mfma_f32_32x32x16_bf16 v[96:111], a[156:159], a[184:187], v[96:111]\n"
"ds_read_b64_tr_b16 a[184:185], v4 offset:38912\n"
"ds_read_b64_tr_b16 a[186:187], v4 offset:39424\n"
"v_mfma_f32_32x32x16_bf16 v[112:127], a[156:159], a[188:191], v[112:127]\n"
"ds_read_b64_tr_b16 a[188:189], v5 offset:38912\n"
"ds_read_b64_tr_b16 a[190:191], v5 offset:39424\n"
"v_mfma_f32_32x32x16_bf16 v[96:111], v[40:43], v[88:91], v[96:111]\n"
"v_mfma_f32_32x32x16_bf16 v[112:127], a[196:199], v[88:91], v[112:127]\n"
"s_waitcnt lgkmcnt(8)\n"
"v_mfma_f32_32x32x16_bf16 v[192:207], a[160:163], a[128:131], v[192:207]\n"
"ds_read_b64_tr_b16 a[160:161], v6 offset:36864\n"
"ds_read_b64_tr_b16 a[162:163], v6 offset:37376\n"
"v_mfma_f32_32x32x16_bf16 v[208:223], a[164:167], a[128:131], v[208:223]\n"
"ds_read_b64_tr_b16 a[164:165], v7 offset:36864\n"
"ds_read_b64_tr_b16 a[166:167], v7 offset:37376\n"
"v_mfma_f32_32x32x16_bf16 v[224:239], a[168:171], a[128:131], v[224:239]\n"
"ds_read_b64_tr_b16 a[168:169], v8 offset:36864\n"
"ds_read_b64_tr_b16 a[170:171], v8 offset:37376\n"
"s_waitcnt vmcnt(2)\n"
"ds_read_b128 v[68:71], v14 offset:0\n"
"ds_read_b128 v[72:75], v14 offset:16\n"
"v_mfma_f32_32x32x16_bf16 v[240:255], a[172:175], a[128:131], v[240:255]\n"
"s_waitcnt lgkmcnt(8)\n"
"ds_read_b64_tr_b16 a[172:173], v9 offset:36864\n"
"ds_read_b64_tr_b16 a[174:175], v9 offset:37376\n"
"v_mfma_f32_32x32x16_bf16 v[192:207], a[176:179], a[132:135], v[192:207]\n"
"ds_read_b64_tr_b16 a[176:177], v6 offset:38912\n"
"ds_read_b64_tr_b16 a[178:179], v6 offset:39424\n"
"s_waitcnt lgkmcnt(4)\n"
"v_mul_f32 v96, v96, v68\n"
"v_mul_f32 v112, v112, v68\n"
"v_mul_f32 v97, v97, v69\n"
"v_mul_f32 v113, v113, v69\n"
"v_mul_f32 v98, v98, v70\n"
"v_mul_f32 v114, v114, v70\n"
"v_mul_f32 v99, v99, v71\n"
"v_mul_f32 v115, v115, v71\n"
"v_mul_f32 v100, v100, v72\n"
"v_mfma_f32_32x32x16_bf16 v[208:223], a[180:183], a[132:135], v[208:223]\n"
"ds_read_b64_tr_b16 a[180:181], v7 offset:38912\n"
"ds_read_b64_tr_b16 a[182:183], v7 offset:39424\n"
"v_mul_f32 v116, v116, v72\n"
"v_mul_f32 v101, v101, v73\n"
"v_mul_f32 v117, v117, v73\n"
"v_mul_f32 v102, v102, v74\n"
"v_mul_f32 v118, v118, v74\n"
"v_mul_f32 v103, v103, v75\n"
"v_mul_f32 v119, v119, v75\n"
"v_mfma_f32_32x32x16_bf16 v[224:239], a[184:187], a[132:135], v[224:239]\n"
"ds_read_b64_tr_b16 a[184:185], v8 offset:38912\n"
"ds_read_b64_tr_b16 a[186:187], v8 offset:39424\n"
"ds_read_b128 v[68:71], v14 offset:32\n"
"ds_read_b128 v[72:75], v14 offset:48\n"
"v_mfma_f32_32x32x16_bf16 v[240:255], a[188:191], a[132:135], v[240:255]\n"
"ds_read_b64_tr_b16 a[188:189], v9 offset:38912\n"
"ds_read_b64_tr_b16 a[190:191], v9 offset:39424\n"
"v_mfma_f32_32x32x16_bf16 v[192:207], a[160:163], a[136:139], v[192:207]\n"
"s_barrier\n"
"ds_read_b64_tr_b16 a[160:161], v2 offset:28672\n"
"ds_read_b64_tr_b16 a[162:163], v2 offset:29184\n"
"s_waitcnt lgkmcnt(4)\n"
"v_mul_f32 v104, v104, v68\n"
"v_mul_f32 v120, v120, v68\n"
"v_mul_f32 v105, v105, v69\n"
"v_mul_f32 v121, v121, v69\n"
"v_mul_f32 v106, v106, v70\n"
"v_mul_f32 v122, v122, v70\n"
"v_mfma_f32_32x32x16_bf16 v[208:223], a[164:167], a[136:139], v[208:223]\n"
"ds_read_b64_tr_b16 a[164:165], v3 offset:28672\n"
"ds_read_b64_tr_b16 a[166:167], v3 offset:29184\n"
"v_mul_f32 v107, v107, v71\n"
"v_mul_f32 v123, v123, v71\n"
"v_mul_f32 v108, v108, v72\n"
"v_mul_f32 v124, v124, v72\n"
"v_mul_f32 v109, v109, v73\n"
"v_mul_f32 v125, v125, v73\n"
"v_mul_f32 v110, v110, v74\n"
"v_mul_f32 v126, v126, v74\n"
"v_mul_f32 v111, v111, v75\n"
"v_mfma_f32_32x32x16_bf16 v[224:239], a[168:171], a[136:139], v[224:239]\n"
"ds_read_b64_tr_b16 a[168:169], v4 offset:28672\n"
"ds_read_b64_tr_b16 a[170:171], v4 offset:29184\n"
"v_mul_f32 v127, v127, v75\n"
"v_fma_f32 v70, v44, v96, v97\n"
"v_fma_f32 v71, v44, v112, v113\n"
"v_fma_f32 v70, -v45, v112, v70\n"
"v_fma_f32 v71, v45, v96, v71\n"
"v_fma_f32 v68, v44, v70, v98\n"
"v_fma_f32 v69, v44, v71, v114\n"
"v_fma_f32 v68, -v45, v71, v68\n"
"v_fma_f32 v69, v45, v70, v69\n"
"v_mfma_f32_32x32x16_bf16 v[240:255], a[172:175], a[136:139], v[240:255]\n"
"ds_read_b64_tr_b16 a[172:173], v5 offset:28672\n"
"ds_read_b64_tr_b16 a[174:175], v5 offset:29184\n"
"v_fma_f32 v70, v44, v68, v99\n"
"v_fma_f32 v71, v44, v69, v115\n"
"v_fma_f32 v70, -v45, v69, v70\n"
"v_fma_f32 v71, v45, v68, v71\n"
"v_fma_f32 v68, v44, v70, v100\n"
"v_fma_f32 v69, v44, v71, v116\n"
"v_fma_f32 v68, -v45, v71, v68\n"
"v_fma_f32 v69, v45, v70, v69\n"
"v_mfma_f32_32x32x16_bf16 v[192:207], a[176:179], a[140:143], v[192:207]\n"
"ds_read_b64_tr_b16 a[176:177], v2 offset:30720\n"
"ds_read_b64_tr_b16 a[178:179], v2 offset:31232\n"
"v_fma_f32 v70, v44, v68, v101\n"
"v_fma_f32 v71, v44, v69, v117\n"
"v_fma_f32 v70, -v45, v69, v70\n"
"v_fma_f32 v71, v45, v68, v71\n"
"v_fma_f32 v68, v44, v70, v102\n"
"v_fma_f32 v69, v44, v71, v118\n"
"v_fma_f32 v68, -v45, v71, v68\n"
"v_fma_f32 v69, v45, v70, v69\n"
"v_fma_f32 v70, v44, v68, v103\n"
"v_mfma_f32_32x32x16_bf16 v[208:223], a[180:183], a[140:143], v[208:223]\n"
"ds_read_b64_tr_b16 a[180:181], v3 offset:30720\n"
"ds_read_b64_tr_b16 a[182:183], v3 offset:31232\n"
"v_fma_f32 v71, v44, v69, v119\n"
"v_fma_f32 v70, -v45, v69, v70\n"
"v_fma_f32 v71, v45, v68, v71\n"
"v_fma_f32 v68, v44, v70, v104\n"
"v_fma_f32 v69, v44, v71, v120\n"
"v_fma_f32 v68, -v45, v71, v68\n"
"v_fma_f32 v69, v45, v70, v69\n"
"v_fma_f32 v70, v44, v68, v105\n"
"v_fma_f32 v71, v44, v69, v121\n"
"v_mfma_f32_32x32x16_bf16 v[224:239], a[184:187], a[140:143], v[224:239]\n"
"ds_read_b64_tr_b16 a[184:185], v4 offset:30720\n"
"ds_read_b64_tr_b16 a[186:187], v4 offset:31232\n"
"v_fma_f32 v70, -v45, v69, v70\n"
"v_fma_f32 v71, v45, v68, v71\n"
"v_fma_f32 v68, v44, v70, v106\n"
"v_fma_f32 v69, v44, v71, v122\n"
"v_fma_f32 v68, -v45, v71, v68\n"
"v_fma_f32 v69, v45, v70, v69\n"
"v_fma_f32 v70, v44, v68, v107\n"
"v_fma_f32 v71, v44, v69, v123\n"
"v_fma_f32 v70, -v45, v69, v70\n"
"s_waitcnt lgkmcnt(14)\n"
"v_mfma_f32_32x32x16_bf16 v[240:255], a[188:191], a[140:143], v[240:255]\n"
"s_waitcnt lgkmcnt(6)\n"
"ds_read_b64_tr_b16 a[188:189], v5 offset:30720\n"
"ds_read_b64_tr_b16 a[190:191], v5 offset:31232\n"
"v_fma_f32 v71, v45, v68, v71\n"
"v_fma_f32 v68, v44, v70, v108\n"
"v_fma_f32 v69, v44, v71, v124\n"
"v_fma_f32 v68, -v45, v71, v68\n"
"v_fma_f32 v69, v45, v70, v69\n"
"v_fma_f32 v70, v44, v68, v109\n"
"v_fma_f32 v71, v44, v69, v125\n"
"v_fma_f32 v70, -v45, v69, v70\n"
"v_mfma_f32_32x32x16_bf16 v[192:207], a[160:163], a[144:147], v[192:207]\n"
"ds_read_b64_tr_b16 a[160:161], v6 offset:28672\n"
"ds_read_b64_tr_b16 a[162:163], v6 offset:29184\n"
"v_fma_f32 v71, v45, v68, v71\n"
"v_fma_f32 v68, v44, v70, v110\n"
"v_fma_f32 v69, v44, v71, v126\n"
"v_fma_f32 v68, -v45, v71, v68\n"
"v_fma_f32 v69, v45, v70, v69\n"
"v_fma_f32 v70, v44, v68, v111\n"
"v_fma_f32 v71, v44, v69, v127\n"
"v_fma_f32 v70, -v45, v69, v70\n"
"v_fma_f32 v71, v45, v68, v71\n"
"v_mfma_f32_32x32x16_bf16 v[208:223], a[164:167], a[144:147], v[208:223]\n"
"ds_read_b64_tr_b16 a[164:165], v7 offset:28672\n"
"ds_read_b64_tr_b16 a[166:167], v7 offset:29184\n"
"v_mov_b32 v68, v70\n"
"v_mov_b32 v69, v71\n"
"s_nop 0\n"
"v_permlane32_swap_b32 v70, v68\n"
"v_permlane32_swap_b32 v71, v69\n"
"v_fma_f32 v66, v46, v70, v68\n"
"v_fma_f32 v67, v46, v71, v69\n"
"v_fma_f32 v66, -v47, v71, v66\n"
"v_mfma_f32_32x32x16_bf16 v[224:239], a[168:171], a[144:147], v[224:239]\n"
"ds_read_b64_tr_b16 a[168:169], v8 offset:28672\n"
"ds_read_b64_tr_b16 a[170:171], v8 offset:29184\n"
"v_fma_f32 v67, v47, v70, v67\n"
"v_cndmask_b32 v64, 0, v70, s[16:17]\n"
"v_cndmask_b32 v65, 0, v71, s[16:17]\n"
"v_mfma_f32_32x32x16_bf16 v[240:255], a[172:175], a[144:147], v[240:255]\n"
"s_waitcnt lgkmcnt(6)\n"
"ds_read_b64_tr_b16 a[172:173], v9 offset:28672\n"
"ds_read_b64_tr_b16 a[174:175], v9 offset:29184\n"
"ds_write_b64 v29, v[66:67] offset:0\n"
"s_waitcnt vmcnt(0)\n"
"v_xor_b32 v69, s72, v81\n"
"v_xor_b32 v70, s73, v83\n"
"v_or3_b32 v68, v69, v70, 0\n"
"v_xor_b32 v69, s72, v85\n"
"v_xor_b32 v70, s73, v87\n"
"v_or3_b32 v68, v69, v70, v68\n"
"v_cmp_ne_u32 vcc, 0, v68\n"
"s_nop 0\n"
"s_cmp_lg_u64 vcc, 0\n"
"s_cbranch_scc1 K1X_m_slow\n"
"K1X_m_back:\n"
"v_mfma_f32_32x32x16_bf16 v[192:207], a[176:179], a[148:151], v[192:207]\n"
"ds_read_b64_tr_b16 a[176:177], v6 offset:30720\n"
"ds_read_b64_tr_b16 a[178:179], v6 offset:31232\n"
"v_mfma_f32_32x32x16_bf16 v[208:223], a[180:183], a[148:151], v[208:223]\n"
"ds_read_b64_tr_b16 a[180:181], v7 offset:30720\n"
"ds_read_b64_tr_b16 a[182:183], v7 offset:31232\n"
"v_mfma_f32_32x32x16_bf16 v[224:239], a[184:187], a[148:151], v[224:239]\n"
"ds_read_b64_tr_b16 a[184:185], v8 offset:30720\n"
"ds_read_b64_tr_b16 a[186:187], v8 offset:31232\n"
"v_mfma_f32_32x32x16_bf16 v[240:255], a[188:191], a[148:151], v[240:255]\n"
"s_waitcnt lgkmcnt(7)\n"
"ds_read_b64_tr_b16 a[188:189], v9 offset:30720\n"
"ds_read_b64_tr_b16 a[190:191], v9 offset:31232\n"
"v_mfma_f32_32x32x16_bf16 v[192:207], a[160:163], a[152:155], v[192:207]\n"
"v_mfma_f32_32x32x16_bf16 v[208:223], a[164:167], a[152:155], v[208:223]\n"
"v_mfma_f32_32x32x16_bf16 v[224:239], a[168:171], a[152:155], v[224:239]\n"
"v_mfma_f32_32x32x16_bf16 v[240:255], a[172:175], a[152:155], v[240:255]\n"
"s_waitcnt lgkmcnt(0)\n"
"v_mfma_f32_32x32x16_bf16 v[192:207], a[176:179], a[156:159], v[192:207]\n"
"v_mfma_f32_32x32x16_bf16 v[208:223], a[180:183], a[156:159], v[208:223]\n"
"v_mfma_f32_32x32x16_bf16 v[224:239], a[184:187], a[156:159], v[224:239]\n"
"v_mfma_f32_32x32x16_bf16 v[240:255], a[188:191], a[156:159], v[240:255]\n"
"v_mfma_f32_32x32x16_bf16 v[192:207], v[80:83], v[40:43], v[192:207]\n"
"s_barrier\n"
"ds_read_b64 v[68:69], v30 offset:0\n"
"ds_read_b64 v[70:71], v30 offset:512\n"
"ds_read_b64 v[72:73], v30 offset:1024\n"
"v_mov_b32 v77, s72\n"
"v_mov_b32 v79, s73\n"
"s_mov_b32 s70, 0\n"
"v_mfma_f32_32x32x16_bf16 v[208:223], v[80:83], a[196:199], v[208:223]\n"
"v_mfma_f32_32x32x16_bf16 v[224:239], v[84:87], v[40:43], v[224:239]\n"
"s_waitcnt lgkmcnt(0)\n"
"v_mul_f32 v74, v48, v68\n"
"v_mul_f32 v75, v48, v69\n"
"v_fma_f32 v74, -v49, v69, v74\n"
"v_fma_f32 v75, v49, v68, v75\n"
"v_fma_f32 v68, v50, v70, v74\n"
"v_fma_f32 v69, v50, v71, v75\n"
"v_fma_f32 v68, -v51, v71, v68\n"
"v_fma_f32 v69, v51, v70, v69\n"
"v_fma_f32 v74, v52, v72, v68\n"
"v_fma_f32 v75, v52, v73, v69\n"
"v_fma_f32 v74, -v53, v73, v74\n"
"v_mfma_f32_32x32x16_bf16 v[240:255], v[84:87], a[196:199], v[240:255]\n"
"v_fma_f32 v75, v53, v72, v75\n"
"v_fma_f32 v76, v54, v74, v66\n"
"v_fma_f32 v78, v54, v75, v67\n"
"v_fma_f32 v76, -v55, v75, v76\n"
"v_fma_f32 v78, v55, v74, v78\n"
"K1X_c3:\n"
"buffer_store_dwordx4 v[76:79], v35, s[64:67], s70 offen sc1\n"
"v_fma_f32 v66, v46, v74, v64\n"
"v_fma_f32 v67, v46, v75, v65\n"
"v_fma_f32 v66, -v47, v75, v66\n"
"v_fma_f32 v67, v47, v74, v67\n"
"s_bfe_u32 s26, s100, 0x80000\n"
"s_lshl_b32 s26, s26, 12\n"
"s_add_u32 s26, s26, s43\n"
"s_bfe_u32 s27, s100, 0x80008\n"
"s_lshl_b32 s27, s27, 12\n"
"s_add_u32 s27, s27, s43\n"
"v_mul_f32 v76, v36, v192\n"
"v_mul_f32 v77, v36, v193\n"
"v_exp_f32 v76, v76\n"
"v_exp_f32 v77, v77\n"
"v_mul_f32 v192, v192, v196\n"
"v_mul_f32 v193, v193, v197\n"
"v_fma_f32 v76, v76, v37, v37\n"
"v_fma_f32 v77, v77, v37, v37\n"
"v_rcp_f32 v76, v76\n"
"v_rcp_f32 v77, v77\n"
"v_mul_f32 v192, v192, v76\n"
"v_mul_f32 v193, v193, v77\n"
"v_mul_f32 v76, v36, v194\n"
"v_mul_f32 v77, v36, v195\n"
"v_exp_f32 v76, v76\n"
"v_exp_f32 v77, v77\n"
"v_mul_f32 v194, v194, v198\n"
"v_mul_f32 v195, v195, v199\n"
"v_fma_f32 v76, v76, v37, v37\n"
"v_fma_f32 v77, v77, v37, v37\n"
"v_rcp_f32 v76, v76\n"
"v_rcp_f32 v77, v77\n"
"v_mul_f32 v194, v194, v76\n"
"v_mul_f32 v195, v195, v77\n"
"v_cvt_pk_bf16_f32 v196, v192, v193\n"
"v_cvt_pk_bf16_f32 v197, v194, v195\n"
"v_mul_f32 v76, v36, v200\n"
"v_mul_f32 v77, v36, v201\n"
"v_exp_f32 v76, v76\n"
"v_exp_f32 v77, v77\n"
"v_mul_f32 v200, v200, v204\n"
"v_mul_f32 v201, v201, v205\n"
"v_fma_f32 v76, v76, v37, v37\n"
"v_fma_f32 v77, v77, v37, v37\n"
"v_rcp_f32 v76, v76\n"
"v_rcp_f32 v77, v77\n"
"v_mul_f32 v200, v200, v76\n"
"v_mul_f32 v201, v201, v77\n"
"v_cvt_pk_bf16_f32 v198, v200, v201\n"
"v_mul_f32 v76, v36, v202\n"
"v_mul_f32 v77, v36, v203\n"
"v_exp_f32 v76, v76\n"
"v_exp_f32 v77, v77\n"
"v_mul_f32 v202, v202, v206\n"
"v_mul_f32 v203, v203, v207\n"
"v_fma_f32 v76, v76, v37, v37\n"
"v_fma_f32 v77, v77, v37, v37\n"
"v_rcp_f32 v76, v76\n"
"v_rcp_f32 v77, v77\n"
"v_mul_f32 v202, v202, v76\n"
"v_mul_f32 v203, v203, v77\n"
"v_cvt_pk_bf16_f32 v199, v202, v203\n"
"buffer_store_dwordx4 v[196:199], v26, s[64:67], s26 offen offset:0 sc1\n"
"v_mul_f32 v76, v36, v208\n"
"v_mul_f32 v77, v36, v209\n"
"v_exp_f32 v76, v76\n"
"v_exp_f32 v77, v77\n"
"v_mul_f32 v208, v208, v212\n"
"v_mul_f32 v209, v209, v213\n"
"v_fma_f32 v76, v76, v37, v37\n"
"v_fma_f32 v77, v77, v37, v37\n"
"v_rcp_f32 v76, v76\n"
"v_rcp_f32 v77, v77\n"
"v_mul_f32 v208, v208, v76\n"
"v_mul_f32 v209, v209, v77\n"
"v_mul_f32 v76, v36, v210\n"
"v_mul_f32 v77, v36, v211\n"
"v_exp_f32 v76, v76\n"
"v_exp_f32 v77, v77\n"
"v_mul_f32 v210, v210, v214\n"
"v_mul_f32 v211, v211, v215\n"
"v_fma_f32 v76, v76, v37, v37\n"
"v_fma_f32 v77, v77, v37, v37\n"
"v_rcp_f32 v76, v76\n"
"v_rcp_f32 v77, v77\n"
"v_mul_f32 v210, v210, v76\n"
"v_mul_f32 v211, v211, v77\n"
"v_cvt_pk_bf16_f32 v212, v208, v209\n"
"v_cvt_pk_bf16_f32 v213, v210, v211\n"
"v_mul_f32 v76, v36, v216\n"
"v_mul_f32 v77, v36, v217\n"
"v_exp_f32 v76, v76\n"
"v_exp_f32 v77, v77\n"
"v_mul_f32 v216, v216, v220\n"
"v_mul_f32 v217, v217, v221\n"
"v_fma_f32 v76, v76, v37, v37\n"
"v_fma_f32 v77, v77, v37, v37\n"
"v_rcp_f32 v76, v76\n"
"v_rcp_f32 v77, v77\n"
"v_mul_f32 v216, v216, v76\n"
"v_mul_f32 v217, v217, v77\n"
"v_cvt_pk_bf16_f32 v214, v216, v217\n"
"v_mul_f32 v76, v36, v218\n"
"v_mul_f32 v77, v36, v219\n"
"v_exp_f32 v76, v76\n"
"v_exp_f32 v77, v77\n"
"v_mul_f32 v218, v218, v222\n"
"v_mul_f32 v219, v219, v223\n"
"v_fma_f32 v76, v76, v37, v37\n"
"v_fma_f32 v77, v77, v37, v37\n"
"v_rcp_f32 v76, v76\n"
"v_rcp_f32 v77, v77\n"
"v_mul_f32 v218, v218, v76\n"
"v_mul_f32 v219, v219, v77\n"
"v_cvt_pk_bf16_f32 v215, v218, v219\n"
"buffer_store_dwordx4 v[212:215], v26, s[64:67], s26 offen offset:512 sc1\n"
"v_mul_f32 v76, v36, v224\n"
"v_mul_f32 v77, v36, v225\n"
"v_exp_f32 v76, v76\n"
"v_exp_f32 v77, v77\n"
"v_mul_f32 v224, v224, v228\n"
"v_mul_f32 v225, v225, v229\n"
"v_fma_f32 v76, v76, v37, v37\n"
"v_fma_f32 v77, v77, v37, v37\n"
"v_rcp_f32 v76, v76\n"
"v_rcp_f32 v77, v77\n"
"v_mul_f32 v224, v224, v76\n"
"v_mul_f32 v225, v225, v77\n"
"v_mul_f32 v76, v36, v226\n"
"v_mul_f32 v77, v36, v227\n"
"v_exp_f32 v76, v76\n"
"v_exp_f32 v77, v77\n"
"v_mul_f32 v226, v226, v230\n"
"v_mul_f32 v227, v227, v231\n"
"v_fma_f32 v76, v76, v37, v37\n"
"v_fma_f32 v77, v77, v37, v37\n"
"v_rcp_f32 v76, v76\n"
"v_rcp_f32 v77, v77\n"
"v_mul_f32 v226, v226, v76\n"
"v_mul_f32 v227, v227, v77\n"
"v_cvt_pk_bf16_f32 v228, v224, v225\n"
"v_cvt_pk_bf16_f32 v229, v226, v227\n"
"v_mul_f32 v76, v36, v232\n"
"v_mul_f32 v77, v36, v233\n"
"v_exp_f32 v76, v76\n"
"v_exp_f32 v77, v77\n"
"v_mul_f32 v232, v232, v236\n"
"v_mul_f32 v233, v233, v237\n"
"v_fma_f32 v76, v76, v37, v37\n"
"v_fma_f32 v77, v77, v37, v37\n"
"v_rcp_f32 v76, v76\n"
"v_rcp_f32 v77, v77\n"
"v_mul_f32 v232, v232, v76\n"
"v_mul_f32 v233, v233, v77\n"
"v_cvt_pk_bf16_f32 v230, v232, v233\n"
"v_mul_f32 v76, v36, v234\n"
"v_mul_f32 v77, v36, v235\n"
"v_exp_f32 v76, v76\n"
"v_exp_f32 v77, v77\n"
"v_mul_f32 v234, v234, v238\n"
"v_mul_f32 v235, v235, v239\n"
"v_fma_f32 v76, v76, v37, v37\n"
"v_fma_f32 v77, v77, v37, v37\n"
"v_rcp_f32 v76, v76\n"
"v_rcp_f32 v77, v77\n"
"v_mul_f32 v234, v234, v76\n"
"v_mul_f32 v235, v235, v77\n"
"buffer_load_dwordx4 v[68:71], v38, s[64:67], 0 offen sc1\n"
"buffer_load_dwordx4 v[72:75], v39, s[64:67], 0 offen sc1\n"
"s_lshl_b32 s68, s7, 13\n"
"s_add_u32 s68, s68, 23072768\n"
"v_lshl_add_u32 v24, v27, 7, s68\n"
"buffer_load_dwordx2 v[22:23], v24, s[64:67], 0 offen sc1\n"
"v_add_u32 v25, 0x400, v28\n"
"v_cvt_pk_bf16_f32 v231, v234, v235\n"
"buffer_store_dwordx4 v[228:231], v26, s[64:67], s27 offen offset:0 sc1\n"
"v_mul_f32 v76, v36, v240\n"
"v_mul_f32 v77, v36, v241\n"
"v_exp_f32 v76, v76\n"
"v_exp_f32 v77, v77\n"
"v_mul_f32 v240, v240, v244\n"
"v_mul_f32 v241, v241, v245\n"
"v_fma_f32 v76, v76, v37, v37\n"
"v_fma_f32 v77, v77, v37, v37\n"
"v_rcp_f32 v76, v76\n"
"v_rcp_f32 v77, v77\n"
"v_mul_f32 v240, v240, v76\n"
"v_mul_f32 v241, v241, v77\n"
"v_mul_f32 v76, v36, v242\n"
"v_mul_f32 v77, v36, v243\n"
"v_exp_f32 v76, v76\n"
"v_exp_f32 v77, v77\n"
"v_mul_f32 v242, v242, v246\n"
"v_mul_f32 v243, v243, v247\n"
"v_fma_f32 v76, v76, v37, v37\n"
"v_fma_f32 v77, v77, v37, v37\n"
"v_rcp_f32 v76, v76\n"
"v_rcp_f32 v77, v77\n"
"v_mul_f32 v242, v242, v76\n"
"v_mul_f32 v243, v243, v77\n"
"v_cvt_pk_bf16_f32 v244, v240, v241\n"
"v_cvt_pk_bf16_f32 v245, v242, v243\n"
"v_mul_f32 v76, v36, v248\n"
"v_mul_f32 v77, v36, v249\n"
"v_exp_f32 v76, v76\n"
"v_exp_f32 v77, v77\n"
"v_mul_f32 v248, v248, v252\n"
"v_mul_f32 v249, v249, v253\n"
"v_fma_f32 v76, v76, v37, v37\n"
"v_fma_f32 v77, v77, v37, v37\n"
"v_rcp_f32 v76, v76\n"
"v_rcp_f32 v77, v77\n"
"v_mul_f32 v248, v248, v76\n"
"v_mul_f32 v249, v249, v77\n"
"v_cvt_pk_bf16_f32 v246, v248, v249\n"
"v_mul_f32 v76, v36, v250\n"
"v_mul_f32 v77, v36, v251\n"
"v_exp_f32 v76, v76\n"
"v_exp_f32 v77, v77\n"
"v_mul_f32 v250, v250, v254\n"
"v_mul_f32 v251, v251, v255\n"
"v_fma_f32 v76, v76, v37, v37\n"
"v_fma_f32 v77, v77, v37, v37\n"
"v_rcp_f32 v76, v76\n"
"v_rcp_f32 v77, v77\n"
"v_mul_f32 v250, v250, v76\n"
"v_mul_f32 v251, v251, v77\n"
"v_cvt_pk_bf16_f32 v247, v250, v251\n"
"buffer_store_dwordx4 v[244:247], v26, s[64:67], s27 offen offset:512 sc1\n"
"s_waitcnt vmcnt(3)\n"
"v_xor_b32 v76, s72, v69\n"
"v_xor_b32 v77, s73, v71\n"
"v_or_b32 v76, v76, v77\n"
"v_cmp_ne_u32 s[22:23], 0, v76\n"
"v_xor_b32 v76, s72, v73\n"
"v_xor_b32 v77, s73, v75\n"
"v_or_b32 v76, v76, v77\n"
"v_cmp_ne_u32 s[24:25], 0, v76\n"
"s_and_b64 s[22:23], s[22:23], s[18:19]\n"
"s_and_b64 s[24:25], s[24:25], s[20:21]\n"
"s_or_b64 s[22:23], s[22:23], s[24:25]\n"
"s_cbranch_scc1 K1X_g0_slow\n"
"K1X_g0_back:\n"
"v_mul_f32 v76, v58, v68\n"
"v_mul_f32 v77, v58, v70\n"
"v_fma_f32 v76, -v59, v70, v76\n"
"v_fmac_f32 v77, v59, v68\n"
"v_fmac_f32 v76, v60, v72\n"
"v_fmac_f32 v77, v60, v74\n"
"v_fma_f32 v76, -v61, v74, v76\n"
"v_fmac_f32 v77, v61, v72\n"
"ds_write_b64 v31, v[76:77] offset:0\n"
"s_waitcnt vmcnt(2)\n"
"v_xor_b32 v22, s72, v22\n"
"v_xor_b32 v23, s73, v23\n"
"v_or_b32 v22, v22, v23\n"
"v_cmp_ne_u32 vcc, 0, v22\n"
"s_nop 0\n"
"s_cmp_lg_u64 vcc, 0\n"
"s_cbranch_scc1 K1X_s_slow\n"
"K1X_s_back:\n"
"s_waitcnt lgkmcnt(0)\n"
"s_barrier\n"
"ds_read_b128 v[68:71], v32 offset:0\n"
"ds_read_b128 v[72:75], v32 offset:16\n"
"s_waitcnt lgkmcnt(0)\n"
"v_add_f32 v68, v68, v70\n"
"v_add_f32 v72, v72, v74\n"
"v_add_f32 v69, v69, v71\n"
"v_add_f32 v73, v73, v75\n"
"v_add_f32 v76, v68, v72\n"
"v_add_f32 v77, v69, v73\n"
"ds_read_b128 v[68:71], v32 offset:32\n"
"ds_read_b128 v[72:75], v32 offset:48\n"
"s_waitcnt lgkmcnt(0)\n"
"v_add_f32 v68, v68, v70\n"
"v_add_f32 v72, v72, v74\n"
"v_add_f32 v69, v69, v71\n"
"v_add_f32 v73, v73, v75\n"
"v_add_f32 v68, v68, v72\n"
"v_add_f32 v76, v76, v68\n"
"s_mov_b32 s33, 0\n"
"buffer_load_dwordx4 a[200:203], v28, s[76:79], s33 offen offset:0 sc1\n"
"v_add_f32 v69, v69, v73\n"
"v_add_f32 v77, v77, v69\n"
"v_fma_f32 v68, v56, v76, v66\n"
"v_fma_f32 v69, v56, v77, v67\n"
"v_fma_f32 v68, -v57, v77, v68\n"
"v_fma_f32 v69, v57, v76, v69\n"
"v_fma_f32 v70, v44, v68, v96\n"
"v_fma_f32 v71, v44, v69, v112\n"
"v_fma_f32 v70, -v45, v69, v70\n"
"v_fma_f32 v71, v45, v68, v71\n"
"v_mul_f32 v96, v62, v70\n"
"v_mul_f32 v112, v62, v71\n"
"v_fma_f32 v96, -v63, v71, v96\n"
"v_fma_f32 v112, -v63, v70, -v112\n"
"v_fma_f32 v68, v44, v70, v97\n"
"v_fma_f32 v69, v44, v71, v113\n"
"buffer_load_dwordx4 a[204:207], v28, s[76:79], s33 offen offset:1024 sc1\n"
"v_fma_f32 v68, -v45, v71, v68\n"
"v_fma_f32 v69, v45, v70, v69\n"
"v_mul_f32 v97, v62, v68\n"
"v_mul_f32 v113, v62, v69\n"
"v_fma_f32 v97, -v63, v69, v97\n"
"v_fma_f32 v113, -v63, v68, -v113\n"
"v_fma_f32 v70, v44, v68, v98\n"
"v_fma_f32 v71, v44, v69, v114\n"
"v_fma_f32 v70, -v45, v69, v70\n"
"v_fma_f32 v71, v45, v68, v71\n"
"v_mul_f32 v98, v62, v70\n"
"v_mul_f32 v114, v62, v71\n"
"v_fma_f32 v98, -v63, v71, v98\n"
"v_fma_f32 v114, -v63, v70, -v114\n"
"v_fma_f32 v68, v44, v70, v99\n"
"v_fma_f32 v69, v44, v71, v115\n"
"buffer_load_dwordx4 a[208:211], v28, s[76:79], s33 offen offset:2048 sc1\n"
"v_fma_f32 v68, -v45, v71, v68\n"
"v_fma_f32 v69, v45, v70, v69\n"
"v_mul_f32 v99, v62, v68\n"
"v_mul_f32 v115, v62, v69\n"
"v_fma_f32 v99, -v63, v69, v99\n"
"v_fma_f32 v115, -v63, v68, -v115\n"
"v_cvt_pk_bf16_f32 v96, v96, v97\n"
"v_cvt_pk_bf16_f32 v97, v98, v99\n"
"ds_write_b64 v33, v[96:97] offset:0\n"
"v_cvt_pk_bf16_f32 v112, v112, v113\n"
"v_cvt_pk_bf16_f32 v113, v114, v115\n"
"ds_write_b64 v33, v[112:113] offset:2304\n"
"v_fma_f32 v70, v44, v68, v100\n"
"v_fma_f32 v71, v44, v69, v116\n"
"v_fma_f32 v70, -v45, v69, v70\n"
"v_fma_f32 v71, v45, v68, v71\n"
"buffer_load_dwordx4 a[212:215], v28, s[76:79], s33 offen offset:3072 sc1\n"
"v_mul_f32 v100, v62, v70\n"
"v_mul_f32 v116, v62, v71\n"
"v_fma_f32 v100, -v63, v71, v100\n"
"v_fma_f32 v116, -v63, v70, -v116\n"
"v_fma_f32 v68, v44, v70, v101\n"
"v_fma_f32 v69, v44, v71, v117\n"
"v_fma_f32 v68, -v45, v71, v68\n"
"v_fma_f32 v69, v45, v70, v69\n"
"v_mul_f32 v101, v62, v68\n"
"v_mul_f32 v117, v62, v69\n"
"v_fma_f32 v101, -v63, v69, v101\n"
"v_fma_f32 v117, -v63, v68, -v117\n"
"v_fma_f32 v70, v44, v68, v102\n"
"v_fma_f32 v71, v44, v69, v118\n"
"v_fma_f32 v70, -v45, v69, v70\n"
"v_fma_f32 v71, v45, v68, v71\n"
"buffer_load_dwordx4 a[216:219], v25, s[76:79], s33 offen offset:3072 sc1\n"
"v_mul_f32 v102, v62, v70\n"
"v_mul_f32 v118, v62, v71\n"
"v_fma_f32 v102, -v63, v71, v102\n"
"v_fma_f32 v118, -v63, v70, -v118\n"
"v_fma_f32 v68, v44, v70, v103\n"
"v_fma_f32 v69, v44, v71, v119\n"
"v_fma_f32 v68, -v45, v71, v68\n"
"v_fma_f32 v69, v45, v70, v69\n"
"v_mul_f32 v103, v62, v68\n"
"v_mul_f32 v119, v62, v69\n"
"v_fma_f32 v103, -v63, v69, v103\n"
"v_fma_f32 v119, -v63, v68, -v119\n"
"v_cvt_pk_bf16_f32 v100, v100, v101\n"
"v_cvt_pk_bf16_f32 v101, v102, v103\n"
"ds_write_b64 v33, v[100:101] offset:16\n"
"v_cvt_pk_bf16_f32 v116, v116, v117\n"
"s_mov_b32 s33, 40960\n"
"buffer_load_dwordx4 a[220:223], v28, s[76:79], s33 offen offset:0 sc1\n"
"v_cvt_pk_bf16_f32 v117, v118, v119\n"
"ds_write_b64 v33, v[116:117] offset:2320\n"
"v_fma_f32 v70, v44, v68, v104\n"
"v_fma_f32 v71, v44, v69, v120\n"
"v_fma_f32 v70, -v45, v69, v70\n"
"v_fma_f32 v71, v45, v68, v71\n"
"v_mul_f32 v104, v62, v70\n"
"v_mul_f32 v120, v62, v71\n"
"v_fma_f32 v104, -v63, v71, v104\n"
"v_fma_f32 v120, -v63, v70, -v120\n"
"v_fma_f32 v68, v44, v70, v105\n"
"v_fma_f32 v69, v44, v71, v121\n"
"v_fma_f32 v68, -v45, v71, v68\n"
"v_fma_f32 v69, v45, v70, v69\n"
"v_mul_f32 v105, v62, v68\n"
"v_mul_f32 v121, v62, v69\n"
"buffer_load_dwordx4 a[224:227], v28, s[76:79], s33 offen offset:1024 sc1\n"
"v_fma_f32 v105, -v63, v69, v105\n"
"v_fma_f32 v121, -v63, v68, -v121\n"
"v_fma_f32 v70, v44, v68, v106\n"
"v_fma_f32 v71, v44, v69, v122\n"
"v_fma_f32 v70, -v45, v69, v70\n"
"v_fma_f32 v71, v45, v68, v71\n"
"v_mul_f32 v106, v62, v70\n"
"v_mul_f32 v122, v62, v71\n"
"v_fma_f32 v106, -v63, v71, v106\n"
"v_fma_f32 v122, -v63, v70, -v122\n"
"v_fma_f32 v68, v44, v70, v107\n"
"v_fma_f32 v69, v44, v71, v123\n"
"v_fma_f32 v68, -v45, v71, v68\n"
"v_fma_f32 v69, v45, v70, v69\n"
"v_mul_f32 v107, v62, v68\n"
"v_mul_f32 v123, v62, v69\n"
"buffer_load_dwordx4 a[228:231], v28, s[76:79], s33 offen offset:2048 sc1\n"
"v_fma_f32 v107, -v63, v69, v107\n"
"v_fma_f32 v123, -v63, v68, -v123\n"
"v_cvt_pk_bf16_f32 v104, v104, v105\n"
"v_cvt_pk_bf16_f32 v105, v106, v107\n"
"ds_write_b64 v33, v[104:105] offset:32\n"
"v_cvt_pk_bf16_f32 v120, v120, v121\n"
"v_cvt_pk_bf16_f32 v121, v122, v123\n"
"ds_write_b64 v33, v[120:121] offset:2336\n"
"v_fma_f32 v70, v44, v68, v108\n"
"v_fma_f32 v71, v44, v69, v124\n"
"v_fma_f32 v70, -v45, v69, v70\n"
"v_fma_f32 v71, v45, v68, v71\n"
"v_mul_f32 v108, v62, v70\n"
"v_mul_f32 v124, v62, v71\n"
"v_fma_f32 v108, -v63, v71, v108\n"
"v_fma_f32 v124, -v63, v70, -v124\n"
"buffer_load_dwordx4 a[232:235], v28, s[76:79], s33 offen offset:3072 sc1\n"
"v_fma_f32 v68, v44, v70, v109\n"
"v_fma_f32 v69, v44, v71, v125\n"
"v_fma_f32 v68, -v45, v71, v68\n"
"v_fma_f32 v69, v45, v70, v69\n"
"v_mul_f32 v109, v62, v68\n"
"v_mul_f32 v125, v62, v69\n"
"v_fma_f32 v109, -v63, v69, v109\n"
"v_fma_f32 v125, -v63, v68, -v125\n"
"v_fma_f32 v70, v44, v68, v110\n"
"v_fma_f32 v71, v44, v69, v126\n"
"v_fma_f32 v70, -v45, v69, v70\n"
"v_fma_f32 v71, v45, v68, v71\n"
"v_mul_f32 v110, v62, v70\n"
"v_mul_f32 v126, v62, v71\n"
"v_fma_f32 v110, -v63, v71, v110\n"
"v_fma_f32 v126, -v63, v70, -v126\n"
"buffer_load_dwordx4 a[236:239], v25, s[76:79], s33 offen offset:3072 sc1\n"
"v_fma_f32 v68, v44, v70, v111\n"
"v_fma_f32 v69, v44, v71, v127\n"
"v_fma_f32 v68, -v45, v71, v68\n"
"v_fma_f32 v69, v45, v70, v69\n"
"v_mul_f32 v111, v62, v68\n"
"v_mul_f32 v127, v62, v69\n"
"v_fma_f32 v111, -v63, v69, v111\n"
"v_fma_f32 v127, -v63, v68, -v127\n"
"v_cvt_pk_bf16_f32 v108, v108, v109\n"
"v_cvt_pk_bf16_f32 v109, v110, v111\n"
"ds_write_b64 v33, v[108:109] offset:48\n"
"v_cvt_pk_bf16_f32 v124, v124, v125\n"
"v_cvt_pk_bf16_f32 v125, v126, v127\n"
"ds_write_b64 v33, v[124:125] offset:2352\n"
"ds_read_b64_tr_b16 v[96:97], v34 offset:0\n"
"ds_read_b64_tr_b16 v[98:99], v34 offset:288\n"
"s_mov_b32 s33, 81920\n"
"buffer_load_dwordx4 a[240:243], v28, s[76:79], s33 offen offset:0 sc1\n"
"ds_read_b64_tr_b16 v[100:101], v34 offset:32\n"
"ds_read_b64_tr_b16 v[102:103], v34 offset:320\n"
"ds_read_b64_tr_b16 v[104:105], v34 offset:2304\n"
"ds_read_b64_tr_b16 v[106:107], v34 offset:2592\n"
"ds_read_b64_tr_b16 v[108:109], v34 offset:2336\n"
"ds_read_b64_tr_b16 v[110:111], v34 offset:2624\n"
"s_bfe_u32 s26, s101, 0x80000\n"
"s_lshl_b32 s26, s26, 12\n"
"s_add_u32 s26, s26, s43\n"
"s_bfe_u32 s27, s101, 0x80008\n"
"s_lshl_b32 s27, s27, 12\n"
"s_add_u32 s27, s27, s43\n"
"s_waitcnt lgkmcnt(0)\n"
"buffer_store_dwordx4 v[96:99], v28, s[64:67], s26 offen offset:0 sc1\n"
"buffer_store_dwordx4 v[100:103], v28, s[64:67], s26 offen offset:1024 sc1\n"
"buffer_store_dwordx4 v[104:107], v28, s[64:67], s27 offen offset:0 sc1\n"
"buffer_store_dwordx4 v[108:111], v28, s[64:67], s27 offen offset:1024 sc1\n"
"buffer_load_dwordx4 a[244:247], v28, s[76:79], s33 offen offset:1024 sc1\n"
"buffer_load_dwordx4 a[248:251], v28, s[76:79], s33 offen offset:2048 sc1\n"
"buffer_load_dwordx4 a[252:255], v28, s[76:79], s33 offen offset:3072 sc1\n"
"buffer_load_dwordx4 a[192:195], v25, s[76:79], s33 offen offset:3072 sc1\n"
"s_mov_b32 s33, 122880\n"
"buffer_load_dwordx4 a[196:199], v28, s[76:79], s33 offen offset:0 sc1\n"
"buffer_load_dwordx4 v[80:83], v28, s[76:79], s33 offen offset:1024 sc1\n"
"s_waitcnt vmcnt(6)\n"
"v_mov_b32 v40, s72\n"
"v_mov_b32 v41, s73\n"
"v_mov_b32 v42, 65536\n"
"buffer_store_dwordx2 v[40:41], v42, s[64:67], s36 offen sc1\n"
"s_lshl_b32 s68, s30, 11\n"
"s_add_u32 s68, s68, 23134208\n"
"v_and_b32 v3, 3, v27\n"
"v_lshl_add_u32 v3, v3, 3, s68\n"
"v_lshrrev_b32 v42, 2, v27\n"
"v_lshl_add_u32 v3, v42, 7, v3\n"
"s_mov_b32 s70, 0\n"
"buffer_load_dwordx2 v[42:43], v3, s[64:67], s70 offen sc1\n"
"buffer_load_dwordx4 v[84:87], v28, s[76:79], s33 offen offset:2048 sc1\n"
"buffer_load_dwordx4 v[88:91], v28, s[76:79], s33 offen offset:3072 sc1\n"
"buffer_load_dwordx4 v[92:95], v25, s[76:79], s33 offen offset:3072 sc1\n"
"s_mov_b32 s33, 163840\n"
"buffer_load_dwordx4 v[224:227], v28, s[76:79], s33 offen offset:0 sc1\n"
"buffer_load_dwordx4 v[228:231], v28, s[76:79], s33 offen offset:1024 sc1\n"
"buffer_load_dwordx4 v[232:235], v28, s[76:79], s33 offen offset:2048 sc1\n"
"buffer_load_dwordx4 v[236:239], v28, s[76:79], s33 offen offset:3072 sc1\n"
"buffer_load_dwordx4 v[240:243], v25, s[76:79], s33 offen offset:3072 sc1\n"
"s_mov_b32 s33, 204800\n"
"buffer_load_dwordx4 v[244:247], v28, s[76:79], s33 offen offset:0 sc1\n"
"buffer_load_dwordx4 v[248:251], v28, s[76:79], s33 offen offset:1024 sc1\n"
"buffer_load_dwordx4 v[252:255], v28, s[76:79], s33 offen offset:2048 sc1\n"
"buffer_load_dwordx4 v[128:131], v28, s[76:79], s33 offen offset:3072 sc1\n"
"buffer_load_dwordx4 v[132:135], v25, s[76:79], s33 offen offset:3072 sc1\n"
"s_mov_b32 s33, 245760\n"
"buffer_load_dwordx4 v[136:139], v28, s[76:79], s33 offen offset:0 sc1\n"
"buffer_load_dwordx4 v[140:143], v28, s[76:79], s33 offen offset:1024 sc1\n"
"buffer_load_dwordx4 v[144:147], v28, s[76:79], s33 offen offset:2048 sc1\n"
"buffer_load_dwordx4 v[148:151], v28, s[76:79], s33 offen offset:3072 sc1\n"
"buffer_load_dwordx4 v[152:155], v25, s[76:79], s33 offen offset:3072 sc1\n"
"s_mov_b32 s33, 286720\n"
"buffer_load_dwordx4 v[156:159], v28, s[76:79], s33 offen offset:0 sc1\n"
"buffer_load_dwordx4 v[160:163], v28, s[76:79], s33 offen offset:1024 sc1\n"
"buffer_load_dwordx4 v[164:167], v28, s[76:79], s33 offen offset:2048 sc1\n"
"buffer_load_dwordx4 v[168:171], v28, s[76:79], s33 offen offset:3072 sc1\n"
"buffer_load_dwordx4 v[172:175], v25, s[76:79], s33 offen offset:3072 sc1\n"
"s_waitcnt vmcnt(23)\n"
"v_xor_b32 v42, s72, v42\n"
"v_xor_b32 v43, s73, v43\n"
"v_or_b32 v42, v42, v43\n"
"v_cmp_ne_u32 vcc, 0, v42\n"
"s_nop 0\n"
"s_cmp_lg_u64 vcc, 0\n"
"s_cbranch_scc1 K1X_r0_slow\n"
"K1X_r0_back:\n"
"s_mov_b32 s32, 0\n"
"buffer_load_dwordx4 a[160:163], v28, s[84:87], s32 offen offset:0 sc1\n"
"buffer_load_dwordx4 a[164:167], v28, s[84:87], s32 offen offset:1024 sc1\n"
"buffer_load_dwordx4 a[168:171], v28, s[84:87], s32 offen offset:2048 sc1\n"
"buffer_load_dwordx4 a[172:175], v28, s[84:87], s32 offen offset:3072 sc1\n"
"s_mov_b32 s32, 4096\n"
"buffer_load_dwordx4 a[176:179], v28, s[84:87], s32 offen offset:0 sc1\n"
"buffer_load_dwordx4 a[180:183], v28, s[84:87], s32 offen offset:1024 sc1\n"
"buffer_load_dwordx4 a[184:187], v28, s[84:87], s32 offen offset:2048 sc1\n"
"buffer_load_dwordx4 a[188:191], v28, s[84:87], s32 offen offset:3072 sc1\n"
"s_mov_b32 s32, 8192\n"
"buffer_load_dwordx4 v[192:195], v28, s[84:87], s32 offen offset:0 sc1\n"
"buffer_load_dwordx4 v[196:199], v28, s[84:87], s32 offen offset:1024 sc1\n"
"buffer_load_dwordx4 v[200:203], v28, s[84:87], s32 offen offset:2048 sc1\n"
"buffer_load_dwordx4 v[204:207], v28, s[84:87], s32 offen offset:3072 sc1\n"
"s_mov_b32 s32, 0\n"
"buffer_load_dwordx4 v[208:211], v28, s[80:83], s32 offen offset:0 sc1\n"
"buffer_load_dwordx4 v[212:215], v28, s[80:83], s32 offen offset:1024 sc1\n"
"buffer_load_dwordx4 v[216:219], v28, s[80:83], s32 offen offset:2048 sc1\n"
"buffer_load_dwordx4 v[220:223], v28, s[80:83], s32 offen offset:3072 sc1\n"
"K1X_c4:\n"
"s_waitcnt vmcnt(15)\n"
"v_mfma_f32_16x16x32_bf16 a[80:83], a[200:203], a[160:163], 0\n"
"s_waitcnt vmcnt(14)\n"
"v_mfma_f32_16x16x32_bf16 a[84:87], a[200:203], a[164:167], 0\n"
"s_mov_b32 s32, 4096\n"
"buffer_load_dwordx4 v[176:179], v28, s[80:83], s32 offen offset:0 sc1\n"
"s_waitcnt vmcnt(14)\n"
"v_mfma_f32_16x16x32_bf16 a[88:91], a[200:203], a[168:171], 0\n"
"s_waitcnt vmcnt(13)\n"
"v_mfma_f32_16x16x32_bf16 a[92:95], a[200:203], a[172:175], 0\n"
"buffer_load_dwordx4 v[180:183], v28, s[80:83], s32 offen offset:1024 sc1\n"
"v_mfma_f32_16x16x32_bf16 a[96:99], a[204:207], a[160:163], 0\n"
"v_mfma_f32_16x16x32_bf16 a[100:103], a[204:207], a[164:167], 0\n"
"buffer_load_dwordx4 v[184:187], v28, s[80:83], s32 offen offset:2048 sc1\n"
"v_mfma_f32_16x16x32_bf16 a[104:107], a[204:207], a[168:171], 0\n"
"v_mfma_f32_16x16x32_bf16 a[108:111], a[204:207], a[172:175], 0\n"
"v_mfma_f32_16x16x32_bf16 a[112:115], a[208:211], a[160:163], 0\n"
"v_mfma_f32_16x16x32_bf16 a[116:119], a[208:211], a[164:167], 0\n"
"buffer_load_dwordx4 v[188:191], v28, s[80:83], s32 offen offset:3072 sc1\n"
"v_mfma_f32_16x16x32_bf16 a[120:123], a[208:211], a[168:171], 0\n"
"v_mfma_f32_16x16x32_bf16 a[124:127], a[208:211], a[172:175], 0\n"
"s_mov_b32 s32, 8192\n"
"buffer_load_dwordx4 v[116:119], v28, s[80:83], s32 offen offset:0 sc1\n"
"v_mfma_f32_16x16x32_bf16 a[128:131], a[212:215], a[160:163], 0\n"
"v_mfma_f32_16x16x32_bf16 a[132:135], a[212:215], a[164:167], 0\n"
"buffer_load_dwordx4 v[120:123], v28, s[80:83], s32 offen offset:1024 sc1\n"
"v_mfma_f32_16x16x32_bf16 a[136:139], a[212:215], a[168:171], 0\n"
"v_mfma_f32_16x16x32_bf16 a[140:143], a[212:215], a[172:175], 0\n"
"v_mfma_f32_16x16x32_bf16 a[144:147], a[216:219], a[160:163], 0\n"
"s_waitcnt vmcnt(14)\n"
"v_mfma_f32_16x16x32_bf16 a[148:151], a[216:219], a[164:167], 0\n"
"buffer_load_dwordx4 v[124:127], v28, s[80:83], s32 offen offset:2048 sc1\n"
"v_mfma_f32_16x16x32_bf16 a[152:155], a[216:219], a[168:171], 0\n"
"v_mfma_f32_16x16x32_bf16 a[156:159], a[216:219], a[172:175], 0\n"
"v_mfma_f32_16x16x32_bf16 a[80:83], a[220:223], a[176:179], a[80:83]\n"
"v_mfma_f32_16x16x32_bf16 a[84:87], a[220:223], a[180:183], a[84:87]\n"
"buffer_load_dwordx4 v[44:47], v28, s[80:83], s32 offen offset:3072 sc1\n"
"v_mfma_f32_16x16x32_bf16 a[88:91], a[220:223], a[184:187], a[88:91]\n"
"v_mfma_f32_16x16x32_bf16 a[92:95], a[220:223], a[188:191], a[92:95]\n"
"s_mov_b32 s33, 327680\n"
"buffer_load_dwordx4 v[48:51], v28, s[76:79], s33 offen offset:0 sc1\n"
"v_mfma_f32_16x16x32_bf16 a[96:99], a[224:227], a[176:179], a[96:99]\n"
"v_mfma_f32_16x16x32_bf16 a[100:103], a[224:227], a[180:183], a[100:103]\n"
"buffer_load_dwordx4 v[52:55], v28, s[76:79], s33 offen offset:1024 sc1\n"
"v_mfma_f32_16x16x32_bf16 a[104:107], a[224:227], a[184:187], a[104:107]\n"
"v_mfma_f32_16x16x32_bf16 a[108:111], a[224:227], a[188:191], a[108:111]\n"
"v_mfma_f32_16x16x32_bf16 a[112:115], a[228:231], a[176:179], a[112:115]\n"
"v_mfma_f32_16x16x32_bf16 a[116:119], a[228:231], a[180:183], a[116:119]\n"
"buffer_load_dwordx4 v[56:59], v28, s[76:79], s33 offen offset:2048 sc1\n"
"v_mfma_f32_16x16x32_bf16 a[120:123], a[228:231], a[184:187], a[120:123]\n"
"v_mfma_f32_16x16x32_bf16 a[124:127], a[228:231], a[188:191], a[124:127]\n"
"buffer_load_dwordx4 v[60:63], v28, s[76:79], s33 offen offset:3072 sc1\n"
"v_mfma_f32_16x16x32_bf16 a[128:131], a[232:235], a[176:179], a[128:131]\n"
"v_mfma_f32_16x16x32_bf16 a[132:135], a[232:235], a[180:183], a[132:135]\n"
"buffer_load_dwordx4 v[64:67], v25, s[76:79], s33 offen offset:3072 sc1\n"
"v_mfma_f32_16x16x32_bf16 a[136:139], a[232:235], a[184:187], a[136:139]\n"
"v_mfma_f32_16x16x32_bf16 a[140:143], a[232:235], a[188:191], a[140:143]\n"
"v_mfma_f32_16x16x32_bf16 a[144:147], a[236:239], a[176:179], a[144:147]\n"
"s_waitcnt vmcnt(17)\n"
"v_mfma_f32_16x16x32_bf16 a[148:151], a[236:239], a[180:183], a[148:151]\n"
"s_mov_b32 s33, 368640\n"
"buffer_load_dwordx4 v[68:71], v28, s[76:79], s33 offen offset:0 sc1\n"
"v_mfma_f32_16x16x32_bf16 a[152:155], a[236:239], a[184:187], a[152:155]\n"
"v_mfma_f32_16x16x32_bf16 a[156:159], a[236:239], a[188:191], a[156:159]\n"
"v_mfma_f32_16x16x32_bf16 a[80:83], a[240:243], v[192:195], a[80:83]\n"
"v_mfma_f32_16x16x32_bf16 a[84:87], a[240:243], v[196:199], a[84:87]\n"
"buffer_load_dwordx4 v[72:75], v28, s[76:79], s33 offen offset:1024 sc1\n"
"v_mfma_f32_16x16x32_bf16 a[88:91], a[240:243], v[200:203], a[88:91]\n"
"v_mfma_f32_16x16x32_bf16 a[92:95], a[240:243], v[204:207], a[92:95]\n"
"buffer_load_dwordx4 a[160:163], v28, s[76:79], s33 offen offset:2048 sc1\n"
"v_mfma_f32_16x16x32_bf16 a[96:99], a[244:247], v[192:195], a[96:99]\n"
"v_mfma_f32_16x16x32_bf16 a[100:103], a[244:247], v[196:199], a[100:103]\n"
"buffer_load_dwordx4 a[164:167], v28, s[76:79], s33 offen offset:3072 sc1\n"
"v_mfma_f32_16x16x32_bf16 a[104:107], a[244:247], v[200:203], a[104:107]\n"
"s_load_dwordx2 s[20:21], s[4:5], 0x0\n"
"s_load_dwordx4 s[88:91], s[4:5], 0x60\n"
"v_mfma_f32_16x16x32_bf16 a[108:111], a[244:247], v[204:207], a[108:111]\n"
"s_mov_b32 s70, 32768\n"
"buffer_load_dwordx2 v[42:43], v3, s[64:67], s70 offen sc1\n"
"v_mfma_f32_16x16x32_bf16 a[112:115], a[248:251], v[192:195], a[112:115]\n"
"s_and_b32 s68, s6, 1\n"
"s_lshl_b32 s69, s30, 1\n"
"v_mfma_f32_16x16x32_bf16 a[116:119], a[248:251], v[196:199], a[116:119]\n"
"buffer_load_dwordx4 a[168:171], v25, s[76:79], s33 offen offset:3072 sc1\n"
"v_mfma_f32_16x16x32_bf16 a[120:123], a[248:251], v[200:203], a[120:123]\n"
"s_add_u32 s68, s68, s69\n"
"s_lshl_b32 s68, s68, 2\n"
"v_mfma_f32_16x16x32_bf16 a[124:127], a[248:251], v[204:207], a[124:127]\n"
"s_mov_b32 s33, 409600\n"
"buffer_load_dwordx4 a[172:175], v28, s[76:79], s33 offen offset:0 sc1\n"
"v_mfma_f32_16x16x32_bf16 a[128:131], a[252:255], v[192:195], a[128:131]\n"
"s_add_u32 s68, s68, s7\n"
"s_lshl_b32 s100, s68, 8\n"
"v_mfma_f32_16x16x32_bf16 a[132:135], a[252:255], v[196:199], a[132:135]\n"
"buffer_load_dwordx4 a[176:179], v28, s[76:79], s33 offen offset:1024 sc1\n"
"v_mfma_f32_16x16x32_bf16 a[136:139], a[252:255], v[200:203], a[136:139]\n"
"s_mul_i32 s69, s68, 20480\n"
"s_mul_i32 s68, s31, 160\n"
"v_mfma_f32_16x16x32_bf16 a[140:143], a[252:255], v[204:207], a[140:143]\n"
"s_add_u32 s69, s69, s68\n"
"v_mfma_f32_16x16x32_bf16 a[144:147], a[192:195], v[192:195], a[144:147]\n"
"s_waitcnt vmcnt(21)\n"
"v_mfma_f32_16x16x32_bf16 a[148:151], a[192:195], v[196:199], a[148:151]\n"
"buffer_load_dwordx4 a[180:183], v28, s[76:79], s33 offen offset:2048 sc1\n"
"v_mfma_f32_16x16x32_bf16 a[152:155], a[192:195], v[200:203], a[152:155]\n"
"v_mfma_f32_16x16x32_bf16 a[156:159], a[192:195], v[204:207], a[156:159]\n"
"v_mfma_f32_16x16x32_bf16 a[0:3], a[200:203], v[208:211], 0\n"
"v_mfma_f32_16x16x32_bf16 a[4:7], a[200:203], v[212:215], 0\n"
"buffer_load_dwordx4 a[184:187], v28, s[76:79], s33 offen offset:3072 sc1\n"
"v_mfma_f32_16x16x32_bf16 a[8:11], a[200:203], v[216:219], 0\n"
"v_mfma_f32_16x16x32_bf16 a[12:15], a[200:203], v[220:223], 0\n"
"buffer_load_dwordx4 a[188:191], v25, s[76:79], s33 offen offset:3072 sc1\n"
"s_waitcnt lgkmcnt(0)\n"
"v_mfma_f32_16x16x32_bf16 a[16:19], a[204:207], v[208:211], 0\n"
"s_add_u32 s48, s20, s69\n"
"s_addc_u32 s49, s21, 0\n"
"v_mfma_f32_16x16x32_bf16 a[20:23], a[204:207], v[212:215], 0\n"
"s_mov_b32 s33, 450560\n"
"buffer_load_dwordx4 v[192:195], v28, s[76:79], s33 offen offset:0 sc1\n"
"v_mfma_f32_16x16x32_bf16 a[24:27], a[204:207], v[216:219], 0\n"
"s_and_b32 s49, s49, 0xffff\n"
"s_mov_b32 s50, 19360\n"
"v_mfma_f32_16x16x32_bf16 a[28:31], a[204:207], v[220:223], 0\n"
"s_mov_b32 s51, 0x20000\n"
"s_lshl_b32 s69, s69, 1\n"
"v_mfma_f32_16x16x32_bf16 a[32:35], a[208:211], v[208:211], 0\n"
"s_add_u32 s52, s90, s69\n"
"s_addc_u32 s53, s91, 0\n"
"v_mfma_f32_16x16x32_bf16 a[36:39], a[208:211], v[212:215], 0\n"
"buffer_load_dwordx4 v[196:199], v28, s[76:79], s33 offen offset:1024 sc1\n"
"v_mfma_f32_16x16x32_bf16 a[40:43], a[208:211], v[216:219], 0\n"
"s_and_b32 s53, s53, 0xffff\n"
"s_mov_b32 s54, 38720\n"
"v_mfma_f32_16x16x32_bf16 a[44:47], a[208:211], v[220:223], 0\n"
"buffer_load_dwordx4 v[200:203], v28, s[76:79], s33 offen offset:2048 sc1\n"
"v_mfma_f32_16x16x32_bf16 a[48:51], a[212:215], v[208:211], 0\n"
"s_mov_b32 s55, 0x20000\n"
"s_add_u32 s24, s88, s68\n"
"s_addc_u32 s25, s89, 0\n"
"v_mfma_f32_16x16x32_bf16 a[52:55], a[212:215], v[212:215], 0\n"
"buffer_load_dwordx4 v[204:207], v28, s[76:79], s33 offen offset:3072 sc1\n"
"v_mfma_f32_16x16x32_bf16 a[56:59], a[212:215], v[216:219], 0\n"
"s_and_b32 s25, s25, 0xffff\n"
"s_mov_b32 s26, 160\n"
"v_mfma_f32_16x16x32_bf16 a[60:63], a[212:215], v[220:223], 0\n"
"s_mov_b32 s27, 0x20000\n"
"s_add_u32 s69, s100, 20971520\n"
"v_mfma_f32_16x16x32_bf16 a[64:67], a[216:219], v[208:211], 0\n"
"s_waitcnt vmcnt(24)\n"
"s_add_u32 s16, s38, s69\n"
"s_addc_u32 s17, s39, 0\n"
"v_mfma_f32_16x16x32_bf16 a[68:71], a[216:219], v[212:215], 0\n"
"buffer_load_dwordx4 a[200:203], v25, s[76:79], s33 offen offset:3072 sc1\n"
"v_mfma_f32_16x16x32_bf16 a[72:75], a[216:219], v[216:219], 0\n"
"s_and_b32 s17, s17, 0xffff\n"
"s_mov_b32 s18, 256\n"
"v_mfma_f32_16x16x32_bf16 a[76:79], a[216:219], v[220:223], 0\n"
"s_barrier\n"
"s_mov_b32 s19, 0x20000\n"
"v_mfma_f32_16x16x32_bf16 a[0:3], a[220:223], v[176:179], a[0:3]\n"
"s_waitcnt vmcnt(11)\n"
"v_xor_b32 v42, s72, v42\n"
"v_xor_b32 v43, s73, v43\n"
"v_or_b32 v42, v42, v43\n"
"v_cmp_ne_u32 vcc, 0, v42\n"
"s_nop 0\n"
"s_cmp_lg_u64 vcc, 0\n"
"s_cbranch_scc1 K1X_r1_slow\n"
"K1X_r1_back:\n"
"v_mfma_f32_16x16x32_bf16 a[4:7], a[220:223], v[180:183], a[4:7]\n"
"s_mov_b32 s32, 12288\n"
"buffer_load_dwordx4 a[204:207], v28, s[84:87], s32 offen offset:0 sc1\n"
"v_mfma_f32_16x16x32_bf16 a[8:11], a[220:223], v[184:187], a[8:11]\n"
"v_and_b32 v76, 3, v27\n"
"v_bfe_u32 v77, v27, 3, 1\n"
"v_lshl_add_u32 v76, v77, 2, v76\n"
"v_mfma_f32_16x16x32_bf16 a[12:15], a[220:223], v[188:191], a[12:15]\n"
"v_bfe_u32 v77, v27, 2, 1\n"
"v_lshl_add_u32 v76, v77, 4, v76\n"
"v_mul_u32_u24 v76, 320, v76\n"
"v_mfma_f32_16x16x32_bf16 a[16:19], a[224:227], v[176:179], a[16:19]\n"
"v_bfe_u32 v78, v27, 1, 2\n"
"v_lshrrev_b32 v77, 4, v27\n"
"v_xor_b32 v78, v78, v77\n"
"v_mfma_f32_16x16x32_bf16 a[20:23], a[224:227], v[180:183], a[20:23]\n"
"buffer_load_dwordx4 a[208:211], v28, s[84:87], s32 offen offset:1024 sc1\n"
"v_mfma_f32_16x16x32_bf16 a[24:27], a[224:227], v[184:187], a[24:27]\n"
"v_lshl_add_u32 v76, v78, 4, v76\n"
"s_mul_i32 s68, s7, 20480\n"
"v_mfma_f32_16x16x32_bf16 a[28:31], a[224:227], v[188:191], a[28:31]\n"
"v_add_u32 v2, s68, v76\n"
"v_add_u32 v1, 81920, v2\n"
"v_mfma_f32_16x16x32_bf16 a[32:35], a[228:231], v[176:179], a[32:35]\n"
"s_mul_i32 s68, s7, 5120\n"
"s_and_b32 s69, s7, 1\n"
"v_mfma_f32_16x16x32_bf16 a[36:39], a[228:231], v[180:183], a[36:39]\n"
"buffer_load_dwordx4 a[212:215], v28, s[84:87], s32 offen offset:2048 sc1\n"
"v_mfma_f32_16x16x32_bf16 a[40:43], a[228:231], v[184:187], a[40:43]\n"
"s_lshl_b32 s69, s69, 1\n"
"v_add_u32 v76, 0, v27\n"
"v_mfma_f32_16x16x32_bf16 a[44:47], a[228:231], v[188:191], a[44:47]\n"
"v_mul_u32_u24 v77, 3277, v76\n"
"v_lshrrev_b32 v77, 16, v77\n"
"v_mul_u32_u24 v78, 20, v77\n"
"v_mfma_f32_16x16x32_bf16 a[48:51], a[232:235], v[176:179], a[48:51]\n"
"v_sub_u32 v78, v76, v78\n"
"v_mul_u32_u24 v76, 1280, v77\n"
"v_lshl_add_u32 v10, v78, 3, v76\n"
"v_mfma_f32_16x16x32_bf16 a[52:55], a[232:235], v[180:183], a[52:55]\n"
"buffer_load_dwordx4 a[216:219], v28, s[84:87], s32 offen offset:3072 sc1\n"
"v_mfma_f32_16x16x32_bf16 a[56:59], a[232:235], v[184:187], a[56:59]\n"
"v_bfe_u32 v76, v77, 1, 1\n"
"v_or_b32 v76, s69, v76\n"
"v_xor_b32 v78, v78, v76\n"
"v_mfma_f32_16x16x32_bf16 a[60:63], a[232:235], v[188:191], a[60:63]\n"
"v_mul_u32_u24 v76, 320, v77\n"
"v_lshl_add_u32 v76, v78, 4, v76\n"
"v_add_u32 v4, s68, v76\n"
"v_mfma_f32_16x16x32_bf16 a[64:67], a[236:239], v[176:179], a[64:67]\n"
"v_add_u32 v76, 64, v27\n"
"v_mul_u32_u24 v77, 3277, v76\n"
"v_mfma_f32_16x16x32_bf16 a[68:71], a[236:239], v[180:183], a[68:71]\n"
"s_mov_b32 s32, 16384\n"
"buffer_load_dwordx4 v[208:211], v28, s[84:87], s32 offen offset:0 sc1\n"
"v_mfma_f32_16x16x32_bf16 a[72:75], a[236:239], v[184:187], a[72:75]\n"
"v_lshrrev_b32 v77, 16, v77\n"
"v_mul_u32_u24 v78, 20, v77\n"
"v_sub_u32 v78, v76, v78\n"
"v_mfma_f32_16x16x32_bf16 a[76:79], a[236:239], v[188:191], a[76:79]\n"
"v_mul_u32_u24 v76, 1280, v77\n"
"v_lshl_add_u32 v11, v78, 3, v76\n"
"v_bfe_u32 v76, v77, 1, 1\n"
"v_mfma_f32_16x16x32_bf16 a[0:3], a[240:243], v[116:119], a[0:3]\n"
"v_or_b32 v76, s69, v76\n"
"v_xor_b32 v78, v78, v76\n"
"v_mul_u32_u24 v76, 320, v77\n"
"v_mfma_f32_16x16x32_bf16 a[4:7], a[240:243], v[120:123], a[4:7]\n"
"buffer_load_dwordx4 v[212:215], v28, s[84:87], s32 offen offset:1024 sc1\n"
"v_mfma_f32_16x16x32_bf16 a[8:11], a[240:243], v[124:127], a[8:11]\n"
"v_lshl_add_u32 v76, v78, 4, v76\n"
"v_add_u32 v5, s68, v76\n"
"v_add_u32 v76, 128, v27\n"
"v_mfma_f32_16x16x32_bf16 a[12:15], a[240:243], v[44:47], a[12:15]\n"
"v_mul_u32_u24 v77, 3277, v76\n"
"v_lshrrev_b32 v77, 16, v77\n"
"v_mul_u32_u24 v78, 20, v77\n"
"v_mfma_f32_16x16x32_bf16 a[16:19], a[244:247], v[116:119], a[16:19]\n"
"v_sub_u32 v78, v76, v78\n"
"v_mul_u32_u24 v76, 1280, v77\n"
"v_lshl_add_u32 v12, v78, 3, v76\n"
"v_mfma_f32_16x16x32_bf16 a[20:23], a[244:247], v[120:123], a[20:23]\n"
"buffer_load_dwordx4 v[216:219], v28, s[84:87], s32 offen offset:2048 sc1\n"
"v_mfma_f32_16x16x32_bf16 a[24:27], a[244:247], v[124:127], a[24:27]\n"
"v_bfe_u32 v76, v77, 1, 1\n"
"v_or_b32 v76, s69, v76\n"
"v_xor_b32 v78, v78, v76\n"
"v_mfma_f32_16x16x32_bf16 a[28:31], a[244:247], v[44:47], a[28:31]\n"
"v_mul_u32_u24 v76, 320, v77\n"
"v_lshl_add_u32 v76, v78, 4, v76\n"
"v_add_u32 v6, s68, v76\n"
"v_mfma_f32_16x16x32_bf16 a[32:35], a[248:251], v[116:119], a[32:35]\n"
"v_add_u32 v76, 192, v27\n"
"v_mul_u32_u24 v77, 3277, v76\n"
"v_lshrrev_b32 v77, 16, v77\n"
"v_mfma_f32_16x16x32_bf16 a[36:39], a[248:251], v[120:123], a[36:39]\n"
"buffer_load_dwordx4 v[220:223], v28, s[84:87], s32 offen offset:3072 sc1\n"
"v_mfma_f32_16x16x32_bf16 a[40:43], a[248:251], v[124:127], a[40:43]\n"
"v_mul_u32_u24 v78, 20, v77\n"
"v_sub_u32 v78, v76, v78\n"
"v_mul_u32_u24 v76, 1280, v77\n"
"v_mfma_f32_16x16x32_bf16 a[44:47], a[248:251], v[44:47], a[44:47]\n"
"v_lshl_add_u32 v13, v78, 3, v76\n"
"v_bfe_u32 v76, v77, 1, 1\n"
"v_or_b32 v76, s69, v76\n"
"v_mfma_f32_16x16x32_bf16 a[48:51], a[252:255], v[116:119], a[48:51]\n"
"v_xor_b32 v78, v78, v76\n"
"v_mul_u32_u24 v76, 320, v77\n"
"v_lshl_add_u32 v76, v78, 4, v76\n"
"v_mfma_f32_16x16x32_bf16 a[52:55], a[252:255], v[120:123], a[52:55]\n"
"s_mov_b32 s32, 20480\n"
"buffer_load_dwordx4 a[220:223], v28, s[84:87], s32 offen offset:0 sc1\n"
"v_mfma_f32_16x16x32_bf16 a[56:59], a[252:255], v[124:127], a[56:59]\n"
"v_add_u32 v7, s68, v76\n"
"v_add_u32 v76, 256, v27\n"
"v_mul_u32_u24 v77, 3277, v76\n"
"v_mfma_f32_16x16x32_bf16 a[60:63], a[252:255], v[44:47], a[60:63]\n"
"v_lshrrev_b32 v77, 16, v77\n"
"v_mul_u32_u24 v78, 20, v77\n"
"v_sub_u32 v78, v76, v78\n"
"v_mfma_f32_16x16x32_bf16 a[64:67], a[192:195], v[116:119], a[64:67]\n"
"s_waitcnt vmcnt(5)\n"
"v_mul_u32_u24 v76, 1280, v77\n"
"v_lshl_add_u32 v14, v78, 3, v76\n"
"v_mfma_f32_16x16x32_bf16 a[68:71], a[192:195], v[120:123], a[68:71]\n"
"buffer_load_dwordx4 a[224:227], v28, s[84:87], s32 offen offset:1024 sc1\n"
"v_mfma_f32_16x16x32_bf16 a[72:75], a[192:195], v[124:127], a[72:75]\n"
"v_bfe_u32 v76, v77, 1, 1\n"
"v_or_b32 v76, s69, v76\n"
"v_xor_b32 v78, v78, v76\n"
"v_mfma_f32_16x16x32_bf16 a[76:79], a[192:195], v[44:47], a[76:79]\n"
"v_mul_u32_u24 v76, 320, v77\n"
"v_lshl_add_u32 v76, v78, 4, v76\n"
"v_add_u32 v8, s68, v76\n"
"v_mfma_f32_16x16x32_bf16 a[80:83], a[196:199], a[204:207], a[80:83]\n"
"s_and_b32 s68, s72, 0xfff\n"
"v_mov_b32 v20, s68\n"
"v_mfma_f32_16x16x32_bf16 a[84:87], a[196:199], a[208:211], a[84:87]\n"
"buffer_load_dwordx4 a[228:231], v28, s[84:87], s32 offen offset:2048 sc1\n"
"v_mfma_f32_16x16x32_bf16 a[88:91], a[196:199], a[212:215], a[88:91]\n"
"v_mov_b32 v21, 0xfffff000\n"
"v_add_u32 v4, 81920, v4\n"
"v_add_u32 v5, 81920, v5\n"
"v_mfma_f32_16x16x32_bf16 a[92:95], a[196:199], a[216:219], a[92:95]\n"
"v_add_u32 v6, 81920, v6\n"
"v_add_u32 v7, 81920, v7\n"
"v_add_u32 v8, 81920, v8\n"
"v_mfma_f32_16x16x32_bf16 a[96:99], v[80:83], a[204:207], a[96:99]\n"
"v_mfma_f32_16x16x32_bf16 a[100:103], v[80:83], a[208:211], a[100:103]\n"
"buffer_load_dwordx4 a[232:235], v28, s[84:87], s32 offen offset:3072 sc1\n"
"v_mfma_f32_16x16x32_bf16 a[104:107], v[80:83], a[212:215], a[104:107]\n"
"v_mfma_f32_16x16x32_bf16 a[108:111], v[80:83], a[216:219], a[108:111]\n"
"v_mfma_f32_16x16x32_bf16 a[112:115], v[84:87], a[204:207], a[112:115]\n"
"v_mfma_f32_16x16x32_bf16 a[116:119], v[84:87], a[208:211], a[116:119]\n"
"s_mov_b32 s32, 24576\n"
"buffer_load_dwordx4 a[236:239], v28, s[84:87], s32 offen offset:0 sc1\n"
"v_mfma_f32_16x16x32_bf16 a[120:123], v[84:87], a[212:215], a[120:123]\n"
"v_mfma_f32_16x16x32_bf16 a[124:127], v[84:87], a[216:219], a[124:127]\n"
"v_mfma_f32_16x16x32_bf16 a[128:131], v[88:91], a[204:207], a[128:131]\n"
"v_mfma_f32_16x16x32_bf16 a[132:135], v[88:91], a[208:211], a[132:135]\n"
"buffer_load_dwordx4 v[176:179], v28, s[84:87], s32 offen offset:1024 sc1\n"
"v_mfma_f32_16x16x32_bf16 a[136:139], v[88:91], a[212:215], a[136:139]\n"
"v_mfma_f32_16x16x32_bf16 a[140:143], v[88:91], a[216:219], a[140:143]\n"
"v_mfma_f32_16x16x32_bf16 a[144:147], v[92:95], a[204:207], a[144:147]\n"
"s_waitcnt vmcnt(6)\n"
"v_mfma_f32_16x16x32_bf16 a[148:151], v[92:95], a[208:211], a[148:151]\n"
"buffer_load_dwordx4 v[180:183], v28, s[84:87], s32 offen offset:2048 sc1\n"
"v_mfma_f32_16x16x32_bf16 a[152:155], v[92:95], a[212:215], a[152:155]\n"
"v_mfma_f32_16x16x32_bf16 a[156:159], v[92:95], a[216:219], a[156:159]\n"
"v_mfma_f32_16x16x32_bf16 a[80:83], v[224:227], v[208:211], a[80:83]\n"
"v_mfma_f32_16x16x32_bf16 a[84:87], v[224:227], v[212:215], a[84:87]\n"
"buffer_load_dwordx4 v[184:187], v28, s[84:87], s32 offen offset:3072 sc1\n"
"v_mfma_f32_16x16x32_bf16 a[88:91], v[224:227], v[216:219], a[88:91]\n"
"v_mfma_f32_16x16x32_bf16 a[92:95], v[224:227], v[220:223], a[92:95]\n"
"v_mfma_f32_16x16x32_bf16 a[96:99], v[228:231], v[208:211], a[96:99]\n"
"v_mfma_f32_16x16x32_bf16 a[100:103], v[228:231], v[212:215], a[100:103]\n"
"s_mov_b32 s32, 28672\n"
"buffer_load_dwordx4 v[188:191], v28, s[84:87], s32 offen offset:0 sc1\n"
"v_mfma_f32_16x16x32_bf16 a[104:107], v[228:231], v[216:219], a[104:107]\n"
"v_mfma_f32_16x16x32_bf16 a[108:111], v[228:231], v[220:223], a[108:111]\n"
"s_mov_b32 s70, 65536\n"
"buffer_load_dwordx2 v[42:43], v3, s[64:67], s70 offen sc1\n"
"v_mfma_f32_16x16x32_bf16 a[112:115], v[232:235], v[208:211], a[112:115]\n"
"v_mfma_f32_16x16x32_bf16 a[116:119], v[232:235], v[212:215], a[116:119]\n"
"buffer_load_dwordx4 a[240:243], v28, s[84:87], s32 offen offset:1024 sc1\n"
"v_mfma_f32_16x16x32_bf16 a[120:123], v[232:235], v[216:219], a[120:123]\n"
"v_mfma_f32_16x16x32_bf16 a[124:127], v[232:235], v[220:223], a[124:127]\n"
"v_mfma_f32_16x16x32_bf16 a[128:131], v[236:239], v[208:211], a[128:131]\n"
"v_mfma_f32_16x16x32_bf16 a[132:135], v[236:239], v[212:215], a[132:135]\n"
"buffer_load_dwordx4 a[244:247], v28, s[84:87], s32 offen offset:2048 sc1\n"
"v_mfma_f32_16x16x32_bf16 a[136:139], v[236:239], v[216:219], a[136:139]\n"
"v_mfma_f32_16x16x32_bf16 a[140:143], v[236:239], v[220:223], a[140:143]\n"
"v_mfma_f32_16x16x32_bf16 a[144:147], v[240:243], v[208:211], a[144:147]\n"
"s_waitcnt vmcnt(8)\n"
"v_mfma_f32_16x16x32_bf16 a[148:151], v[240:243], v[212:215], a[148:151]\n"
"buffer_load_dwordx4 a[248:251], v28, s[84:87], s32 offen offset:3072 sc1\n"
"v_mfma_f32_16x16x32_bf16 a[152:155], v[240:243], v[216:219], a[152:155]\n"
"v_mfma_f32_16x16x32_bf16 a[156:159], v[240:243], v[220:223], a[156:159]\n"
"v_mfma_f32_16x16x32_bf16 a[80:83], v[244:247], a[220:223], a[80:83]\n"
"v_mfma_f32_16x16x32_bf16 a[84:87], v[244:247], a[224:227], a[84:87]\n"
"s_mov_b32 s32, 32768\n"
"buffer_load_dwordx4 a[252:255], v28, s[84:87], s32 offen offset:0 sc1\n"
"v_mfma_f32_16x16x32_bf16 a[88:91], v[244:247], a[228:231], a[88:91]\n"
"v_mfma_f32_16x16x32_bf16 a[92:95], v[244:247], a[232:235], a[92:95]\n"
"v_mfma_f32_16x16x32_bf16 a[96:99], v[248:251], a[220:223], a[96:99]\n"
"v_mfma_f32_16x16x32_bf16 a[100:103], v[248:251], a[224:227], a[100:103]\n"
"buffer_load_dwordx4 a[192:195], v28, s[84:87], s32 offen offset:1024 sc1\n"
"v_mfma_f32_16x16x32_bf16 a[104:107], v[248:251], a[228:231], a[104:107]\n"
"v_mfma_f32_16x16x32_bf16 a[108:111], v[248:251], a[232:235], a[108:111]\n"
"v_mfma_f32_16x16x32_bf16 a[112:115], v[252:255], a[220:223], a[112:115]\n"
"v_mfma_f32_16x16x32_bf16 a[116:119], v[252:255], a[224:227], a[116:119]\n"
"buffer_load_dwordx4 v[116:119], v28, s[84:87], s32 offen offset:2048 sc1\n"
"v_mfma_f32_16x16x32_bf16 a[120:123], v[252:255], a[228:231], a[120:123]\n"
"v_mfma_f32_16x16x32_bf16 a[124:127], v[252:255], a[232:235], a[124:127]\n"
"v_mfma_f32_16x16x32_bf16 a[128:131], v[128:131], a[220:223], a[128:131]\n"
"v_mfma_f32_16x16x32_bf16 a[132:135], v[128:131], a[224:227], a[132:135]\n"
"buffer_load_dwordx4 v[120:123], v28, s[84:87], s32 offen offset:3072 sc1\n"
"v_mfma_f32_16x16x32_bf16 a[136:139], v[128:131], a[228:231], a[136:139]\n"
"v_mfma_f32_16x16x32_bf16 a[140:143], v[128:131], a[232:235], a[140:143]\n"
"v_mfma_f32_16x16x32_bf16 a[144:147], v[132:135], a[220:223], a[144:147]\n"
"s_waitcnt vmcnt(9)\n"
"v_mfma_f32_16x16x32_bf16 a[148:151], v[132:135], a[224:227], a[148:151]\n"
"v_mfma_f32_16x16x32_bf16 a[152:155], v[132:135], a[228:231], a[152:155]\n"
"v_mfma_f32_16x16x32_bf16 a[156:159], v[132:135], a[232:235], a[156:159]\n"
"v_mfma_f32_16x16x32_bf16 a[80:83], v[136:139], a[236:239], a[80:83]\n"
"s_waitcnt vmcnt(7)\n"
"v_xor_b32 v42, s72, v42\n"
"v_xor_b32 v43, s73, v43\n"
"v_or_b32 v42, v42, v43\n"
"v_cmp_ne_u32 vcc, 0, v42\n"
"s_nop 0\n"
"s_cmp_lg_u64 vcc, 0\n"
"s_cbranch_scc1 K1X_r2_slow\n"
"K1X_r2_back:\n"
"v_mfma_f32_16x16x32_bf16 a[84:87], v[136:139], v[176:179], a[84:87]\n"
"s_mov_b32 s32, 36864\n"
"buffer_load_dwordx4 v[124:127], v28, s[84:87], s32 offen offset:0 sc1\n"
"v_mfma_f32_16x16x32_bf16 a[88:91], v[136:139], v[180:183], a[88:91]\n"
"v_mfma_f32_16x16x32_bf16 a[92:95], v[136:139], v[184:187], a[92:95]\n"
"v_mfma_f32_16x16x32_bf16 a[96:99], v[140:143], a[236:239], a[96:99]\n"
"v_mfma_f32_16x16x32_bf16 a[100:103], v[140:143], v[176:179], a[100:103]\n"
"buffer_load_dwordx4 v[44:47], v28, s[84:87], s32 offen offset:1024 sc1\n"
"v_mfma_f32_16x16x32_bf16 a[104:107], v[140:143], v[180:183], a[104:107]\n"
"v_mfma_f32_16x16x32_bf16 a[108:111], v[140:143], v[184:187], a[108:111]\n"
"v_mfma_f32_16x16x32_bf16 a[112:115], v[144:147], a[236:239], a[112:115]\n"
"v_mfma_f32_16x16x32_bf16 a[116:119], v[144:147], v[176:179], a[116:119]\n"
"buffer_load_dwordx4 a[204:207], v28, s[84:87], s32 offen offset:2048 sc1\n"
"v_mfma_f32_16x16x32_bf16 a[120:123], v[144:147], v[180:183], a[120:123]\n"
"v_mfma_f32_16x16x32_bf16 a[124:127], v[144:147], v[184:187], a[124:127]\n"
"v_mfma_f32_16x16x32_bf16 a[128:131], v[148:151], a[236:239], a[128:131]\n"
"v_mfma_f32_16x16x32_bf16 a[132:135], v[148:151], v[176:179], a[132:135]\n"
"buffer_load_dwordx4 a[208:211], v28, s[84:87], s32 offen offset:3072 sc1\n"
"v_mfma_f32_16x16x32_bf16 a[136:139], v[148:151], v[180:183], a[136:139]\n"
"v_mfma_f32_16x16x32_bf16 a[140:143], v[148:151], v[184:187], a[140:143]\n"
"v_mfma_f32_16x16x32_bf16 a[144:147], v[152:155], a[236:239], a[144:147]\n"
"s_waitcnt vmcnt(8)\n"
"v_mfma_f32_16x16x32_bf16 a[148:151], v[152:155], v[176:179], a[148:151]\n"
"s_mov_b32 s32, 40960\n"
"buffer_load_dwordx4 a[212:215], v28, s[84:87], s32 offen offset:0 sc1\n"
"v_mfma_f32_16x16x32_bf16 a[152:155], v[152:155], v[180:183], a[152:155]\n"
"v_mfma_f32_16x16x32_bf16 a[156:159], v[152:155], v[184:187], a[156:159]\n"
"v_mfma_f32_16x16x32_bf16 a[80:83], v[156:159], v[188:191], a[80:83]\n"
"v_mfma_f32_16x16x32_bf16 a[84:87], v[156:159], a[240:243], a[84:87]\n"
"buffer_load_dwordx4 a[216:219], v28, s[84:87], s32 offen offset:1024 sc1\n"
"v_mfma_f32_16x16x32_bf16 a[88:91], v[156:159], a[244:247], a[88:91]\n"
"v_mfma_f32_16x16x32_bf16 a[92:95], v[156:159], a[248:251], a[92:95]\n"
"v_mfma_f32_16x16x32_bf16 a[96:99], v[160:163], v[188:191], a[96:99]\n"
"v_mfma_f32_16x16x32_bf16 a[100:103], v[160:163], a[240:243], a[100:103]\n"
"buffer_load_dwordx4 v[208:211], v28, s[84:87], s32 offen offset:2048 sc1\n"
"v_mfma_f32_16x16x32_bf16 a[104:107], v[160:163], a[244:247], a[104:107]\n"
"v_mfma_f32_16x16x32_bf16 a[108:111], v[160:163], a[248:251], a[108:111]\n"
"v_mfma_f32_16x16x32_bf16 a[112:115], v[164:167], v[188:191], a[112:115]\n"
"v_mfma_f32_16x16x32_bf16 a[116:119], v[164:167], a[240:243], a[116:119]\n"
"buffer_load_dwordx4 v[212:215], v28, s[84:87], s32 offen offset:3072 sc1\n"
"v_mfma_f32_16x16x32_bf16 a[120:123], v[164:167], a[244:247], a[120:123]\n"
"v_mfma_f32_16x16x32_bf16 a[124:127], v[164:167], a[248:251], a[124:127]\n"
"v_mfma_f32_16x16x32_bf16 a[128:131], v[168:171], v[188:191], a[128:131]\n"
"v_mfma_f32_16x16x32_bf16 a[132:135], v[168:171], a[240:243], a[132:135]\n"
"s_mov_b32 s32, 45056\n"
"buffer_load_dwordx4 v[216:219], v28, s[84:87], s32 offen offset:0 sc1\n"
"v_mfma_f32_16x16x32_bf16 a[136:139], v[168:171], a[244:247], a[136:139]\n"
"v_mfma_f32_16x16x32_bf16 a[140:143], v[168:171], a[248:251], a[140:143]\n"
"v_mfma_f32_16x16x32_bf16 a[144:147], v[172:175], v[188:191], a[144:147]\n"
"s_waitcnt vmcnt(9)\n"
"v_mfma_f32_16x16x32_bf16 a[148:151], v[172:175], a[240:243], a[148:151]\n"
"buffer_load_dwordx4 v[220:223], v28, s[84:87], s32 offen offset:1024 sc1\n"
"v_mfma_f32_16x16x32_bf16 a[152:155], v[172:175], a[244:247], a[152:155]\n"
"v_mfma_f32_16x16x32_bf16 a[156:159], v[172:175], a[248:251], a[156:159]\n"
"v_mfma_f32_16x16x32_bf16 a[80:83], v[48:51], a[252:255], a[80:83]\n"
"v_mfma_f32_16x16x32_bf16 a[84:87], v[48:51], a[192:195], a[84:87]\n"
"buffer_load_dwordx4 a[220:223], v28, s[84:87], s32 offen offset:2048 sc1\n"
"v_mfma_f32_16x16x32_bf16 a[88:91], v[48:51], v[116:119], a[88:91]\n"
"v_mfma_f32_16x16x32_bf16 a[92:95], v[48:51], v[120:123], a[92:95]\n"
"v_mfma_f32_16x16x32_bf16 a[96:99], v[52:55], a[252:255], a[96:99]\n"
"v_mfma_f32_16x16x32_bf16 a[100:103], v[52:55], a[192:195], a[100:103]\n"
"buffer_load_dwordx4 a[224:227], v28, s[84:87], s32 offen offset:3072 sc1\n"
"v_mfma_f32_16x16x32_bf16 a[104:107], v[52:55], v[116:119], a[104:107]\n"
"v_mfma_f32_16x16x32_bf16 a[108:111], v[52:55], v[120:123], a[108:111]\n"
"v_mfma_f32_16x16x32_bf16 a[112:115], v[56:59], a[252:255], a[112:115]\n"
"v_mfma_f32_16x16x32_bf16 a[116:119], v[56:59], a[192:195], a[116:119]\n"
"s_mov_b32 s32, 12288\n"
"buffer_load_dwordx4 a[228:231], v28, s[80:83], s32 offen offset:0 sc1\n"
"v_mfma_f32_16x16x32_bf16 a[120:123], v[56:59], v[116:119], a[120:123]\n"
"v_mfma_f32_16x16x32_bf16 a[124:127], v[56:59], v[120:123], a[124:127]\n"
"v_mfma_f32_16x16x32_bf16 a[128:131], v[60:63], a[252:255], a[128:131]\n"
"v_mfma_f32_16x16x32_bf16 a[132:135], v[60:63], a[192:195], a[132:135]\n"
"buffer_load_dwordx4 a[232:235], v28, s[80:83], s32 offen offset:1024 sc1\n"
"v_mfma_f32_16x16x32_bf16 a[136:139], v[60:63], v[116:119], a[136:139]\n"
"v_mfma_f32_16x16x32_bf16 a[140:143], v[60:63], v[120:123], a[140:143]\n"
"v_mfma_f32_16x16x32_bf16 a[144:147], v[64:67], a[252:255], a[144:147]\n"
"s_waitcnt vmcnt(10)\n"
"v_mfma_f32_16x16x32_bf16 a[148:151], v[64:67], a[192:195], a[148:151]\n"
"buffer_load_dwordx4 a[236:239], v28, s[80:83], s32 offen offset:2048 sc1\n"
"v_mfma_f32_16x16x32_bf16 a[152:155], v[64:67], v[116:119], a[152:155]\n"
"s_waitcnt vmcnt(3)\n"
"v_mfma_f32_16x16x32_bf16 a[156:159], v[64:67], v[120:123], a[156:159]\n"
"v_mfma_f32_16x16x32_bf16 a[80:83], v[68:71], v[124:127], a[80:83]\n"
"v_mfma_f32_16x16x32_bf16 a[84:87], v[68:71], v[44:47], a[84:87]\n"
"buffer_load_dwordx4 v[176:179], v28, s[80:83], s32 offen offset:3072 sc1\n"
"v_mfma_f32_16x16x32_bf16 a[88:91], v[68:71], a[204:207], a[88:91]\n"
"v_mfma_f32_16x16x32_bf16 a[92:95], v[68:71], a[208:211], a[92:95]\n"
"v_mfma_f32_16x16x32_bf16 a[80:83], a[172:175], a[212:215], a[80:83]\n"
"v_mfma_f32_16x16x32_bf16 a[84:87], a[172:175], a[216:219], a[84:87]\n"
"s_mov_b32 s32, 16384\n"
"buffer_load_dwordx4 v[180:183], v28, s[80:83], s32 offen offset:0 sc1\n"
"v_mfma_f32_16x16x32_bf16 a[88:91], a[172:175], v[208:211], a[88:91]\n"
"v_mfma_f32_16x16x32_bf16 a[92:95], a[172:175], v[212:215], a[92:95]\n"
"v_mfma_f32_16x16x32_bf16 a[80:83], v[192:195], v[216:219], a[80:83]\n"
"v_mfma_f32_16x16x32_bf16 a[84:87], v[192:195], v[220:223], a[84:87]\n"
"buffer_load_dwordx4 v[184:187], v28, s[80:83], s32 offen offset:1024 sc1\n"
"v_mfma_f32_16x16x32_bf16 a[88:91], v[192:195], a[220:223], a[88:91]\n"
"v_mfma_f32_16x16x32_bf16 a[92:95], v[192:195], a[224:227], a[92:95]\n"
"v_mfma_f32_16x16x32_bf16 a[96:99], v[72:75], v[124:127], a[96:99]\n"
"v_mfma_f32_16x16x32_bf16 a[100:103], v[72:75], v[44:47], a[100:103]\n"
"buffer_load_dwordx4 v[188:191], v28, s[80:83], s32 offen offset:2048 sc1\n"
"v_mfma_f32_16x16x32_bf16 a[104:107], v[72:75], a[204:207], a[104:107]\n"
"ds_write_b128 v1, a[80:83] offset:0\n"
"v_mfma_f32_16x16x32_bf16 a[108:111], v[72:75], a[208:211], a[108:111]\n"
"v_mfma_f32_16x16x32_bf16 a[96:99], a[176:179], a[212:215], a[96:99]\n"
"v_mfma_f32_16x16x32_bf16 a[100:103], a[176:179], a[216:219], a[100:103]\n"
"buffer_load_dwordx4 a[240:243], v28, s[80:83], s32 offen offset:3072 sc1\n"
"ds_write_b128 v1, a[84:87] offset:2560\n"
"v_mfma_f32_16x16x32_bf16 a[104:107], a[176:179], v[208:211], a[104:107]\n"
"v_mfma_f32_16x16x32_bf16 a[108:111], a[176:179], v[212:215], a[108:111]\n"
"v_mfma_f32_16x16x32_bf16 a[96:99], v[196:199], v[216:219], a[96:99]\n"
"ds_write_b128 v1, a[88:91] offset:10240\n"
"v_mfma_f32_16x16x32_bf16 a[100:103], v[196:199], v[220:223], a[100:103]\n"
"s_mov_b32 s32, 20480\n"
"buffer_load_dwordx4 a[244:247], v28, s[80:83], s32 offen offset:0 sc1\n"
"v_mfma_f32_16x16x32_bf16 a[104:107], v[196:199], a[220:223], a[104:107]\n"
"v_mfma_f32_16x16x32_bf16 a[108:111], v[196:199], a[224:227], a[108:111]\n"
"ds_write_b128 v1, a[92:95] offset:12800\n"
"v_mfma_f32_16x16x32_bf16 a[112:115], a[160:163], v[124:127], a[112:115]\n"
"v_mfma_f32_16x16x32_bf16 a[116:119], a[160:163], v[44:47], a[116:119]\n"
"buffer_load_dwordx4 a[248:251], v28, s[80:83], s32 offen offset:1024 sc1\n"
"v_mfma_f32_16x16x32_bf16 a[120:123], a[160:163], a[204:207], a[120:123]\n"
"ds_write_b128 v1, a[96:99] offset:64\n"
"v_mfma_f32_16x16x32_bf16 a[124:127], a[160:163], a[208:211], a[124:127]\n"
"v_mfma_f32_16x16x32_bf16 a[112:115], a[180:183], a[212:215], a[112:115]\n"
"v_mfma_f32_16x16x32_bf16 a[116:119], a[180:183], a[216:219], a[116:119]\n"
"buffer_load_dwordx4 a[252:255], v28, s[80:83], s32 offen offset:2048 sc1\n"
"ds_write_b128 v1, a[100:103] offset:2624\n"
"v_mfma_f32_16x16x32_bf16 a[120:123], a[180:183], v[208:211], a[120:123]\n"
"v_mfma_f32_16x16x32_bf16 a[124:127], a[180:183], v[212:215], a[124:127]\n"
"v_mfma_f32_16x16x32_bf16 a[112:115], v[200:203], v[216:219], a[112:115]\n"
"ds_write_b128 v1, a[104:107] offset:10304\n"
"v_mfma_f32_16x16x32_bf16 a[116:119], v[200:203], v[220:223], a[116:119]\n"
"buffer_load_dwordx4 a[192:195], v28, s[80:83], s32 offen offset:3072 sc1\n"
"v_mfma_f32_16x16x32_bf16 a[120:123], v[200:203], a[220:223], a[120:123]\n"
"v_mfma_f32_16x16x32_bf16 a[124:127], v[200:203], a[224:227], a[124:127]\n"
"ds_write_b128 v1, a[108:111] offset:12864\n"
"v_mfma_f32_16x16x32_bf16 a[128:131], a[164:167], v[124:127], a[128:131]\n"
"v_mfma_f32_16x16x32_bf16 a[132:135], a[164:167], v[44:47], a[132:135]\n"
"s_mov_b32 s32, 24576\n"
"buffer_load_dwordx4 v[116:119], v28, s[80:83], s32 offen offset:0 sc1\n"
"v_mfma_f32_16x16x32_bf16 a[136:139], a[164:167], a[204:207], a[136:139]\n"
"ds_write_b128 v1, a[112:115] offset:128\n"
"v_mfma_f32_16x16x32_bf16 a[140:143], a[164:167], a[208:211], a[140:143]\n"
"v_mfma_f32_16x16x32_bf16 a[128:131], a[184:187], a[212:215], a[128:131]\n"
"v_mfma_f32_16x16x32_bf16 a[132:135], a[184:187], a[216:219], a[132:135]\n"
"buffer_load_dwordx4 v[120:123], v28, s[80:83], s32 offen offset:1024 sc1\n"
"ds_write_b128 v1, a[116:119] offset:2688\n"
"v_mfma_f32_16x16x32_bf16 a[136:139], a[184:187], v[208:211], a[136:139]\n"
"v_mfma_f32_16x16x32_bf16 a[140:143], a[184:187], v[212:215], a[140:143]\n"
"v_mfma_f32_16x16x32_bf16 a[128:131], v[204:207], v[216:219], a[128:131]\n"
"ds_write_b128 v1, a[120:123] offset:10368\n"
"v_mfma_f32_16x16x32_bf16 a[132:135], v[204:207], v[220:223], a[132:135]\n"
"v_mfma_f32_16x16x32_bf16 a[136:139], v[204:207], a[220:223], a[136:139]\n"
"v_mfma_f32_16x16x32_bf16 a[140:143], v[204:207], a[224:227], a[140:143]\n"
"ds_write_b128 v1, a[124:127] offset:12928\n"
"v_mfma_f32_16x16x32_bf16 a[144:147], a[168:171], v[124:127], a[144:147]\n"
"v_mfma_f32_16x16x32_bf16 a[148:151], a[168:171], v[44:47], a[148:151]\n"
"v_mfma_f32_16x16x32_bf16 a[152:155], a[168:171], a[204:207], a[152:155]\n"
"ds_write_b128 v1, a[128:131] offset:192\n"
"v_mfma_f32_16x16x32_bf16 a[156:159], a[168:171], a[208:211], a[156:159]\n"
"v_mfma_f32_16x16x32_bf16 a[144:147], a[188:191], a[212:215], a[144:147]\n"
"v_mfma_f32_16x16x32_bf16 a[148:151], a[188:191], a[216:219], a[148:151]\n"
"ds_write_b128 v1, a[132:135] offset:2752\n"
"v_mfma_f32_16x16x32_bf16 a[152:155], a[188:191], v[208:211], a[152:155]\n"
"v_mfma_f32_16x16x32_bf16 a[156:159], a[188:191], v[212:215], a[156:159]\n"
"v_mfma_f32_16x16x32_bf16 a[144:147], a[200:203], v[216:219], a[144:147]\n"
"s_waitcnt vmcnt(10)\n"
"ds_write_b128 v1, a[136:139] offset:10432\n"
"v_mfma_f32_16x16x32_bf16 a[148:151], a[200:203], v[220:223], a[148:151]\n"
"v_mfma_f32_16x16x32_bf16 a[152:155], a[200:203], a[220:223], a[152:155]\n"
"v_mfma_f32_16x16x32_bf16 a[156:159], a[200:203], a[224:227], a[156:159]\n"
"ds_write_b128 v1, a[140:143] offset:12992\n"
"v_mfma_f32_16x16x32_bf16 a[0:3], a[196:199], a[228:231], a[0:3]\n"
"v_mfma_f32_16x16x32_bf16 a[4:7], a[196:199], a[232:235], a[4:7]\n"
"buffer_load_dwordx4 v[124:127], v28, s[80:83], s32 offen offset:2048 sc1\n"
"v_mfma_f32_16x16x32_bf16 a[8:11], a[196:199], a[236:239], a[8:11]\n"
"ds_write_b128 v1, a[144:147] offset:256\n"
"v_mfma_f32_16x16x32_bf16 a[12:15], a[196:199], v[176:179], a[12:15]\n"
"v_mfma_f32_16x16x32_bf16 a[16:19], v[80:83], a[228:231], a[16:19]\n"
"v_mfma_f32_16x16x32_bf16 a[20:23], v[80:83], a[232:235], a[20:23]\n"
"buffer_load_dwordx4 v[44:47], v28, s[80:83], s32 offen offset:3072 sc1\n"
"ds_write_b128 v1, a[148:151] offset:2816\n"
"v_mfma_f32_16x16x32_bf16 a[24:27], v[80:83], a[236:239], a[24:27]\n"
"v_mfma_f32_16x16x32_bf16 a[28:31], v[80:83], v[176:179], a[28:31]\n"
"v_mfma_f32_16x16x32_bf16 a[32:35], v[84:87], a[228:231], a[32:35]\n"
"ds_write_b128 v1, a[152:155] offset:10496\n"
"v_mfma_f32_16x16x32_bf16 a[36:39], v[84:87], a[232:235], a[36:39]\n"
"s_mov_b32 s32, 28672\n"
"buffer_load_dwordx4 a[204:207], v28, s[80:83], s32 offen offset:0 sc1\n"
"v_mfma_f32_16x16x32_bf16 a[40:43], v[84:87], a[236:239], a[40:43]\n"
"v_mfma_f32_16x16x32_bf16 a[44:47], v[84:87], v[176:179], a[44:47]\n"
"ds_write_b128 v1, a[156:159] offset:13056\n"
"v_mfma_f32_16x16x32_bf16 a[48:51], v[88:91], a[228:231], a[48:51]\n"
"v_mfma_f32_16x16x32_bf16 a[52:55], v[88:91], a[232:235], a[52:55]\n"
"s_waitcnt lgkmcnt(0)\n"
"s_barrier\n"
"buffer_load_dwordx4 a[208:211], v28, s[80:83], s32 offen offset:1024 sc1\n"
"v_mfma_f32_16x16x32_bf16 a[56:59], v[88:91], a[236:239], a[56:59]\n"
"ds_read_b128 v[30:33], v4 offset:0\n"
"ds_read_b128 v[34:37], v4 offset:20480\n"
"ds_read_b128 v[38:41], v4 offset:40960\n"
"v_mfma_f32_16x16x32_bf16 a[60:63], v[88:91], v[176:179], a[60:63]\n"
"ds_read_b128 v[76:79], v4 offset:61440\n"
"ds_read_b128 v[96:99], v5 offset:0\n"
"ds_read_b128 v[100:103], v5 offset:20480\n"
"v_mfma_f32_16x16x32_bf16 a[64:67], v[92:95], a[228:231], a[64:67]\n"
"s_waitcnt vmcnt(10)\n"
"ds_read_b128 v[104:107], v5 offset:40960\n"
"ds_read_b128 v[108:111], v5 offset:61440\n"
"v_mfma_f32_16x16x32_bf16 a[68:71], v[92:95], a[232:235], a[68:71]\n"
"buffer_load_dwordx4 a[212:215], v28, s[80:83], s32 offen offset:2048 sc1\n"
"v_mfma_f32_16x16x32_bf16 a[72:75], v[92:95], a[236:239], a[72:75]\n"
"v_mfma_f32_16x16x32_bf16 a[76:79], v[92:95], v[176:179], a[76:79]\n"
"s_waitcnt lgkmcnt(4)\n"
"v_add_f32 v30, v30, v34\n"
"v_add_f32 v38, v38, v76\n"
"v_add_f32 v31, v31, v35\n"
"v_mfma_f32_16x16x32_bf16 a[0:3], v[224:227], v[180:183], a[0:3]\n"
"v_add_f32 v39, v39, v77\n"
"v_add_f32 v32, v32, v36\n"
"v_add_f32 v40, v40, v78\n"
"v_mfma_f32_16x16x32_bf16 a[4:7], v[224:227], v[184:187], a[4:7]\n"
"buffer_load_dwordx4 a[216:219], v28, s[80:83], s32 offen offset:3072 sc1\n"
"v_mfma_f32_16x16x32_bf16 a[8:11], v[224:227], v[188:191], a[8:11]\n"
"v_add_f32 v33, v33, v37\n"
"v_add_f32 v41, v41, v79\n"
"v_add_f32 v30, v30, v38\n"
"v_mfma_f32_16x16x32_bf16 a[12:15], v[224:227], a[240:243], a[12:15]\n"
"v_add_f32 v31, v31, v39\n"
"v_add_f32 v32, v32, v40\n"
"v_add_f32 v33, v33, v41\n"
"v_mfma_f32_16x16x32_bf16 a[16:19], v[228:231], v[180:183], a[16:19]\n"
"s_add_u32 s69, s34, 0\n"
"v_and_or_b32 v16, v30, v21, v20\n"
"v_mfma_f32_16x16x32_bf16 a[20:23], v[228:231], v[184:187], a[20:23]\n"
"s_mov_b32 s32, 32768\n"
"buffer_load_dwordx4 v[208:211], v28, s[80:83], s32 offen offset:0 sc1\n"
"v_mfma_f32_16x16x32_bf16 a[24:27], v[228:231], v[188:191], a[24:27]\n"
"v_and_or_b32 v17, v31, v21, v20\n"
"v_and_or_b32 v18, v32, v21, v20\n"
"v_and_or_b32 v19, v33, v21, v20\n"
"v_mfma_f32_16x16x32_bf16 a[28:31], v[228:231], a[240:243], a[28:31]\n"
"buffer_store_dwordx4 v[16:19], v28, s[64:67], s69 offen offset:0 sc1\n"
"v_mfma_f32_16x16x32_bf16 a[32:35], v[232:235], v[180:183], a[32:35]\n"
"ds_read_b128 v[30:33], v6 offset:0\n"
"ds_read_b128 v[34:37], v6 offset:20480\n"
"ds_read_b128 v[38:41], v6 offset:40960\n"
"v_mfma_f32_16x16x32_bf16 a[36:39], v[232:235], v[184:187], a[36:39]\n"
"buffer_load_dwordx4 v[212:215], v28, s[80:83], s32 offen offset:1024 sc1\n"
"v_mfma_f32_16x16x32_bf16 a[40:43], v[232:235], v[188:191], a[40:43]\n"
"ds_read_b128 v[76:79], v6 offset:61440\n"
"v_mfma_f32_16x16x32_bf16 a[44:47], v[232:235], a[240:243], a[44:47]\n"
"v_mfma_f32_16x16x32_bf16 a[48:51], v[236:239], v[180:183], a[48:51]\n"
"v_mfma_f32_16x16x32_bf16 a[52:55], v[236:239], v[184:187], a[52:55]\n"
"buffer_load_dwordx4 v[216:219], v28, s[80:83], s32 offen offset:2048 sc1\n"
"v_mfma_f32_16x16x32_bf16 a[56:59], v[236:239], v[188:191], a[56:59]\n"
"v_mfma_f32_16x16x32_bf16 a[60:63], v[236:239], a[240:243], a[60:63]\n"
"v_mfma_f32_16x16x32_bf16 a[64:67], v[240:243], v[180:183], a[64:67]\n"
"s_waitcnt vmcnt(12)\n"
"v_mfma_f32_16x16x32_bf16 a[68:71], v[240:243], v[184:187], a[68:71]\n"
"buffer_load_dwordx4 v[220:223], v28, s[80:83], s32 offen offset:3072 sc1\n"
"v_mfma_f32_16x16x32_bf16 a[72:75], v[240:243], v[188:191], a[72:75]\n"
"v_mfma_f32_16x16x32_bf16 a[76:79], v[240:243], a[240:243], a[76:79]\n"
"v_mfma_f32_16x16x32_bf16 a[0:3], v[244:247], a[244:247], a[0:3]\n"
"v_mfma_f32_16x16x32_bf16 a[4:7], v[244:247], a[248:251], a[4:7]\n"
"s_mov_b32 s32, 36864\n"
"buffer_load_dwordx4 a[220:223], v28, s[80:83], s32 offen offset:0 sc1\n"
"v_mfma_f32_16x16x32_bf16 a[8:11], v[244:247], a[252:255], a[8:11]\n"
"s_waitcnt lgkmcnt(4)\n"
"v_add_f32 v96, v96, v100\n"
"v_add_f32 v104, v104, v108\n"
"v_add_f32 v97, v97, v101\n"
"v_mfma_f32_16x16x32_bf16 a[12:15], v[244:247], a[192:195], a[12:15]\n"
"v_add_f32 v105, v105, v109\n"
"v_add_f32 v98, v98, v102\n"
"v_add_f32 v106, v106, v110\n"
"v_mfma_f32_16x16x32_bf16 a[16:19], v[248:251], a[244:247], a[16:19]\n"
"v_add_f32 v99, v99, v103\n"
"v_add_f32 v107, v107, v111\n"
"v_add_f32 v96, v96, v104\n"
"v_mfma_f32_16x16x32_bf16 a[20:23], v[248:251], a[248:251], a[20:23]\n"
"buffer_load_dwordx4 a[224:227], v28, s[80:83], s32 offen offset:1024 sc1\n"
"v_mfma_f32_16x16x32_bf16 a[24:27], v[248:251], a[252:255], a[24:27]\n"
"v_add_f32 v97, v97, v105\n"
"v_add_f32 v98, v98, v106\n"
"v_add_f32 v99, v99, v107\n"
"v_mfma_f32_16x16x32_bf16 a[28:31], v[248:251], a[192:195], a[28:31]\n"
"v_and_or_b32 v16, v96, v21, v20\n"
"v_and_or_b32 v17, v97, v21, v20\n"
"v_and_or_b32 v18, v98, v21, v20\n"
"v_mfma_f32_16x16x32_bf16 a[32:35], v[252:255], a[244:247], a[32:35]\n"
"v_and_or_b32 v19, v99, v21, v20\n"
"v_mfma_f32_16x16x32_bf16 a[36:39], v[252:255], a[248:251], a[36:39]\n"
"buffer_load_dwordx4 a[196:199], v28, s[80:83], s32 offen offset:2048 sc1\n"
"v_mfma_f32_16x16x32_bf16 a[40:43], v[252:255], a[252:255], a[40:43]\n"
"buffer_store_dwordx4 v[16:19], v28, s[64:67], s69 offen offset:1024 sc1\n"
"v_mfma_f32_16x16x32_bf16 a[44:47], v[252:255], a[192:195], a[44:47]\n"
"ds_read_b128 v[96:99], v7 offset:0\n"
"ds_read_b128 v[100:103], v7 offset:20480\n"
"ds_read_b128 v[104:107], v7 offset:40960\n"
"v_mfma_f32_16x16x32_bf16 a[48:51], v[128:131], a[244:247], a[48:51]\n"
"ds_read_b128 v[108:111], v7 offset:61440\n"
"v_mfma_f32_16x16x32_bf16 a[52:55], v[128:131], a[248:251], a[52:55]\n"
"buffer_load_dwordx4 v[80:83], v28, s[80:83], s32 offen offset:3072 sc1\n"
"v_mfma_f32_16x16x32_bf16 a[56:59], v[128:131], a[252:255], a[56:59]\n"
"v_mfma_f32_16x16x32_bf16 a[60:63], v[128:131], a[192:195], a[60:63]\n"
"v_mfma_f32_16x16x32_bf16 a[64:67], v[132:135], a[244:247], a[64:67]\n"
"s_waitcnt vmcnt(14)\n"
"v_mfma_f32_16x16x32_bf16 a[68:71], v[132:135], a[248:251], a[68:71]\n"
"s_mov_b32 s32, 40960\n"
"buffer_load_dwordx4 v[84:87], v28, s[80:83], s32 offen offset:0 sc1\n"
"v_mfma_f32_16x16x32_bf16 a[72:75], v[132:135], a[252:255], a[72:75]\n"
"v_mfma_f32_16x16x32_bf16 a[76:79], v[132:135], a[192:195], a[76:79]\n"
"v_mfma_f32_16x16x32_bf16 a[0:3], v[136:139], v[116:119], a[0:3]\n"
"v_mfma_f32_16x16x32_bf16 a[4:7], v[136:139], v[120:123], a[4:7]\n"
"buffer_load_dwordx4 v[88:91], v28, s[80:83], s32 offen offset:1024 sc1\n"
"v_mfma_f32_16x16x32_bf16 a[8:11], v[136:139], v[124:127], a[8:11]\n"
"v_mfma_f32_16x16x32_bf16 a[12:15], v[136:139], v[44:47], a[12:15]\n"
"s_waitcnt lgkmcnt(4)\n"
"v_add_f32 v30, v30, v34\n"
"v_add_f32 v38, v38, v76\n"
"v_add_f32 v31, v31, v35\n"
"v_mfma_f32_16x16x32_bf16 a[16:19], v[140:143], v[116:119], a[16:19]\n"
"v_add_f32 v39, v39, v77\n"
"v_add_f32 v32, v32, v36\n"
"v_add_f32 v40, v40, v78\n"
"v_mfma_f32_16x16x32_bf16 a[20:23], v[140:143], v[120:123], a[20:23]\n"
"buffer_load_dwordx4 v[92:95], v28, s[80:83], s32 offen offset:2048 sc1\n"
"v_mfma_f32_16x16x32_bf16 a[24:27], v[140:143], v[124:127], a[24:27]\n"
"v_add_f32 v33, v33, v37\n"
"v_add_f32 v41, v41, v79\n"
"v_add_f32 v30, v30, v38\n"
"v_mfma_f32_16x16x32_bf16 a[28:31], v[140:143], v[44:47], a[28:31]\n"
"v_add_f32 v31, v31, v39\n"
"v_add_f32 v32, v32, v40\n"
"v_add_f32 v33, v33, v41\n"
"v_mfma_f32_16x16x32_bf16 a[32:35], v[144:147], v[116:119], a[32:35]\n"
"v_and_or_b32 v16, v30, v21, v20\n"
"v_and_or_b32 v17, v31, v21, v20\n"
"v_and_or_b32 v18, v32, v21, v20\n"
"v_mfma_f32_16x16x32_bf16 a[36:39], v[144:147], v[120:123], a[36:39]\n"
"buffer_load_dwordx4 a[228:231], v28, s[80:83], s32 offen offset:3072 sc1\n"
"v_mfma_f32_16x16x32_bf16 a[40:43], v[144:147], v[124:127], a[40:43]\n"
"v_and_or_b32 v19, v33, v21, v20\n"
"v_mfma_f32_16x16x32_bf16 a[44:47], v[144:147], v[44:47], a[44:47]\n"
"buffer_store_dwordx4 v[16:19], v28, s[64:67], s69 offen offset:2048 sc1\n"
"v_mfma_f32_16x16x32_bf16 a[48:51], v[148:151], v[116:119], a[48:51]\n"
"ds_read_b128 v[30:33], v8 offset:0\n"
"ds_read_b128 v[34:37], v8 offset:20480\n"
"ds_read_b128 v[38:41], v8 offset:40960\n"
"v_mfma_f32_16x16x32_bf16 a[52:55], v[148:151], v[120:123], a[52:55]\n"
"s_mov_b32 s32, 45056\n"
"buffer_load_dwordx4 a[232:235], v28, s[80:83], s32 offen offset:0 sc1\n"
"v_mfma_f32_16x16x32_bf16 a[56:59], v[148:151], v[124:127], a[56:59]\n"
"ds_read_b128 v[76:79], v8 offset:61440\n"
"v_mfma_f32_16x16x32_bf16 a[60:63], v[148:151], v[44:47], a[60:63]\n"
"v_mfma_f32_16x16x32_bf16 a[64:67], v[152:155], v[116:119], a[64:67]\n"
"s_waitcnt vmcnt(16)\n"
"v_mfma_f32_16x16x32_bf16 a[68:71], v[152:155], v[120:123], a[68:71]\n"
"buffer_load_dwordx4 a[236:239], v28, s[80:83], s32 offen offset:1024 sc1\n"
"v_mfma_f32_16x16x32_bf16 a[72:75], v[152:155], v[124:127], a[72:75]\n"
"v_mfma_f32_16x16x32_bf16 a[76:79], v[152:155], v[44:47], a[76:79]\n"
"v_mfma_f32_16x16x32_bf16 a[0:3], v[156:159], a[204:207], a[0:3]\n"
"v_mfma_f32_16x16x32_bf16 a[4:7], v[156:159], a[208:211], a[4:7]\n"
"buffer_load_dwordx4 v[176:179], v28, s[80:83], s32 offen offset:2048 sc1\n"
"v_mfma_f32_16x16x32_bf16 a[8:11], v[156:159], a[212:215], a[8:11]\n"
"v_mfma_f32_16x16x32_bf16 a[12:15], v[156:159], a[216:219], a[12:15]\n"
"v_mfma_f32_16x16x32_bf16 a[16:19], v[160:163], a[204:207], a[16:19]\n"
"v_mfma_f32_16x16x32_bf16 a[20:23], v[160:163], a[208:211], a[20:23]\n"
"buffer_load_dwordx4 v[180:183], v28, s[80:83], s32 offen offset:3072 sc1\n"
"v_mfma_f32_16x16x32_bf16 a[24:27], v[160:163], a[212:215], a[24:27]\n"
"s_waitcnt lgkmcnt(4)\n"
"v_add_f32 v96, v96, v100\n"
"v_add_f32 v104, v104, v108\n"
"v_add_f32 v97, v97, v101\n"
"v_mfma_f32_16x16x32_bf16 a[28:31], v[160:163], a[216:219], a[28:31]\n"
"v_add_f32 v105, v105, v109\n"
"v_add_f32 v98, v98, v102\n"
"v_add_f32 v106, v106, v110\n"
"v_mfma_f32_16x16x32_bf16 a[32:35], v[164:167], a[204:207], a[32:35]\n"
"v_add_f32 v99, v99, v103\n"
"v_add_f32 v107, v107, v111\n"
"v_add_f32 v96, v96, v104\n"
"v_mfma_f32_16x16x32_bf16 a[36:39], v[164:167], a[208:211], a[36:39]\n"
"v_add_f32 v97, v97, v105\n"
"v_add_f32 v98, v98, v106\n"
"v_add_f32 v99, v99, v107\n"
"v_mfma_f32_16x16x32_bf16 a[40:43], v[164:167], a[212:215], a[40:43]\n"
"v_and_or_b32 v16, v96, v21, v20\n"
"v_and_or_b32 v17, v97, v21, v20\n"
"v_and_or_b32 v18, v98, v21, v20\n"
"v_mfma_f32_16x16x32_bf16 a[44:47], v[164:167], a[216:219], a[44:47]\n"
"v_and_or_b32 v19, v99, v21, v20\n"
"v_mfma_f32_16x16x32_bf16 a[48:51], v[168:171], a[204:207], a[48:51]\n"
"buffer_store_dwordx4 v[16:19], v28, s[64:67], s69 offen offset:3072 sc1\n"
"v_mfma_f32_16x16x32_bf16 a[52:55], v[168:171], a[208:211], a[52:55]\n"
"v_mfma_f32_16x16x32_bf16 a[56:59], v[168:171], a[212:215], a[56:59]\n"
"v_mfma_f32_16x16x32_bf16 a[60:63], v[168:171], a[216:219], a[60:63]\n"
"v_mfma_f32_16x16x32_bf16 a[64:67], v[172:175], a[204:207], a[64:67]\n"
"s_waitcnt vmcnt(15)\n"
"v_mfma_f32_16x16x32_bf16 a[68:71], v[172:175], a[208:211], a[68:71]\n"
"v_mfma_f32_16x16x32_bf16 a[72:75], v[172:175], a[212:215], a[72:75]\n"
"v_mfma_f32_16x16x32_bf16 a[76:79], v[172:175], a[216:219], a[76:79]\n"
"v_mfma_f32_16x16x32_bf16 a[0:3], v[48:51], v[208:211], a[0:3]\n"
"v_mfma_f32_16x16x32_bf16 a[4:7], v[48:51], v[212:215], a[4:7]\n"
"v_mfma_f32_16x16x32_bf16 a[8:11], v[48:51], v[216:219], a[8:11]\n"
"v_mfma_f32_16x16x32_bf16 a[12:15], v[48:51], v[220:223], a[12:15]\n"
"v_mfma_f32_16x16x32_bf16 a[16:19], v[52:55], v[208:211], a[16:19]\n"
"v_mfma_f32_16x16x32_bf16 a[20:23], v[52:55], v[212:215], a[20:23]\n"
"v_mfma_f32_16x16x32_bf16 a[24:27], v[52:55], v[216:219], a[24:27]\n"
"v_mfma_f32_16x16x32_bf16 a[28:31], v[52:55], v[220:223], a[28:31]\n"
"s_waitcnt lgkmcnt(0)\n"
"v_add_f32 v30, v30, v34\n"
"v_add_f32 v38, v38, v76\n"
"v_add_f32 v31, v31, v35\n"
"v_mfma_f32_16x16x32_bf16 a[32:35], v[56:59], v[208:211], a[32:35]\n"
"v_add_f32 v39, v39, v77\n"
"v_add_f32 v32, v32, v36\n"
"v_add_f32 v40, v40, v78\n"
"v_mfma_f32_16x16x32_bf16 a[36:39], v[56:59], v[212:215], a[36:39]\n"
"v_add_f32 v33, v33, v37\n"
"v_add_f32 v41, v41, v79\n"
"v_add_f32 v30, v30, v38\n"
"v_mfma_f32_16x16x32_bf16 a[40:43], v[56:59], v[216:219], a[40:43]\n"
"v_add_f32 v31, v31, v39\n"
"v_add_f32 v32, v32, v40\n"
"v_add_f32 v33, v33, v41\n"
"v_mfma_f32_16x16x32_bf16 a[44:47], v[56:59], v[220:223], a[44:47]\n"
"s_add_u32 s69, s34, 4096\n"
"v_and_or_b32 v16, v30, v21, v20\n"
"v_mfma_f32_16x16x32_bf16 a[48:51], v[60:63], v[208:211], a[48:51]\n"
"v_and_or_b32 v17, v31, v21, v20\n"
"v_and_or_b32 v18, v32, v21, v20\n"
"v_and_or_b32 v19, v33, v21, v20\n"
"v_mfma_f32_16x16x32_bf16 a[52:55], v[60:63], v[212:215], a[52:55]\n"
"buffer_store_dwordx4 v[16:19], v28, s[64:67], s69 offen offset:0 sc1\n"
"v_mfma_f32_16x16x32_bf16 a[56:59], v[60:63], v[216:219], a[56:59]\n"
"v_subrev_u32 v4, 81920, v4\n"
"v_subrev_u32 v5, 81920, v5\n"
"v_subrev_u32 v6, 81920, v6\n"
"v_mfma_f32_16x16x32_bf16 a[60:63], v[60:63], v[220:223], a[60:63]\n"
"v_subrev_u32 v7, 81920, v7\n"
"v_subrev_u32 v8, 81920, v8\n"
"v_mfma_f32_16x16x32_bf16 a[64:67], v[64:67], v[208:211], a[64:67]\n"
"s_waitcnt vmcnt(11)\n"
"buffer_load_dword v224, v10, s[48:51], 0 offen offset:0\n"
"v_mfma_f32_16x16x32_bf16 a[68:71], v[64:67], v[212:215], a[68:71]\n"
"buffer_load_dword v225, v10, s[48:51], 0 offen offset:4\n"
"v_mfma_f32_16x16x32_bf16 a[72:75], v[64:67], v[216:219], a[72:75]\n"
"s_waitcnt vmcnt(4)\n"
"v_mul_u32_u24 v9, 52, v10\n"
"v_mfma_f32_16x16x32_bf16 a[76:79], v[64:67], v[220:223], a[76:79]\n"
"v_lshrrev_b32 v9, 16, v9\n"
"v_mul_u32_u24 v15, 1280, v9\n"
"v_sub_u32 v15, v10, v15\n"
"v_mfma_f32_16x16x32_bf16 a[0:3], v[68:71], a[220:223], a[0:3]\n"
"v_lshlrev_b32 v9, 4, v9\n"
"v_mfma_f32_16x16x32_bf16 a[4:7], v[68:71], a[224:227], a[4:7]\n"
"buffer_load_dword v234, v15, s[24:27], 0 offen offset:0\n"
"v_mfma_f32_16x16x32_bf16 a[8:11], v[68:71], a[196:199], a[8:11]\n"
"buffer_load_dword v235, v15, s[24:27], 0 offen offset:4\n"
"v_mfma_f32_16x16x32_bf16 a[12:15], v[68:71], v[80:83], a[12:15]\n"
"buffer_load_dword v244, v9, s[16:19], 0 offen offset:0 sc1\n"
"v_mfma_f32_16x16x32_bf16 a[0:3], a[172:175], v[84:87], a[0:3]\n"
"buffer_load_dword v245, v9, s[16:19], 0 offen offset:8 sc1\n"
"v_mfma_f32_16x16x32_bf16 a[4:7], a[172:175], v[88:91], a[4:7]\n"
"buffer_load_dword v226, v11, s[48:51], 0 offen offset:0\n"
"v_mfma_f32_16x16x32_bf16 a[8:11], a[172:175], v[92:95], a[8:11]\n"
"buffer_load_dword v227, v11, s[48:51], 0 offen offset:4\n"
"v_mfma_f32_16x16x32_bf16 a[12:15], a[172:175], a[228:231], a[12:15]\n"
"v_mul_u32_u24 v9, 52, v11\n"
"v_lshrrev_b32 v9, 16, v9\n"
"v_mul_u32_u24 v15, 1280, v9\n"
"v_mfma_f32_16x16x32_bf16 a[0:3], v[192:195], a[232:235], a[0:3]\n"
"v_sub_u32 v15, v11, v15\n"
"v_lshlrev_b32 v9, 4, v9\n"
"v_mfma_f32_16x16x32_bf16 a[4:7], v[192:195], a[236:239], a[4:7]\n"
"buffer_load_dword v236, v15, s[24:27], 0 offen offset:0\n"
"v_mfma_f32_16x16x32_bf16 a[8:11], v[192:195], v[176:179], a[8:11]\n"
"buffer_load_dword v237, v15, s[24:27], 0 offen offset:4\n"
"v_mfma_f32_16x16x32_bf16 a[12:15], v[192:195], v[180:183], a[12:15]\n"
"buffer_load_dword v246, v9, s[16:19], 0 offen offset:0 sc1\n"
"v_mfma_f32_16x16x32_bf16 a[16:19], v[72:75], a[220:223], a[16:19]\n"
"buffer_load_dword v247, v9, s[16:19], 0 offen offset:8 sc1\n"
"v_mfma_f32_16x16x32_bf16 a[20:23], v[72:75], a[224:227], a[20:23]\n"
"buffer_load_dword v228, v12, s[48:51], 0 offen offset:0\n"
"v_mfma_f32_16x16x32_bf16 a[24:27], v[72:75], a[196:199], a[24:27]\n"
"ds_write_b128 v2, a[0:3] offset:0\n"
"buffer_load_dword v229, v12, s[48:51], 0 offen offset:4\n"
"v_mfma_f32_16x16x32_bf16 a[28:31], v[72:75], v[80:83], a[28:31]\n"
"v_mul_u32_u24 v9, 52, v12\n"
"v_lshrrev_b32 v9, 16, v9\n"
"v_mul_u32_u24 v15, 1280, v9\n"
"v_mfma_f32_16x16x32_bf16 a[16:19], a[176:179], v[84:87], a[16:19]\n"
"v_sub_u32 v15, v12, v15\n"
"v_lshlrev_b32 v9, 4, v9\n"
"v_mfma_f32_16x16x32_bf16 a[20:23], a[176:179], v[88:91], a[20:23]\n"
"ds_write_b128 v2, a[4:7] offset:2560\n"
"buffer_load_dword v238, v15, s[24:27], 0 offen offset:0\n"
"v_mfma_f32_16x16x32_bf16 a[24:27], a[176:179], v[92:95], a[24:27]\n"
"buffer_load_dword v239, v15, s[24:27], 0 offen offset:4\n"
"v_mfma_f32_16x16x32_bf16 a[28:31], a[176:179], a[228:231], a[28:31]\n"
"buffer_load_dword v248, v9, s[16:19], 0 offen offset:0 sc1\n"
"v_mfma_f32_16x16x32_bf16 a[16:19], v[196:199], a[232:235], a[16:19]\n"
"ds_write_b128 v2, a[8:11] offset:10240\n"
"buffer_load_dword v249, v9, s[16:19], 0 offen offset:8 sc1\n"
"v_mfma_f32_16x16x32_bf16 a[20:23], v[196:199], a[236:239], a[20:23]\n"
"buffer_load_dword v230, v13, s[48:51], 0 offen offset:0\n"
"v_mfma_f32_16x16x32_bf16 a[24:27], v[196:199], v[176:179], a[24:27]\n"
"buffer_load_dword v231, v13, s[48:51], 0 offen offset:4\n"
"v_mfma_f32_16x16x32_bf16 a[28:31], v[196:199], v[180:183], a[28:31]\n"
"ds_write_b128 v2, a[12:15] offset:12800\n"
"v_mul_u32_u24 v9, 52, v13\n"
"v_lshrrev_b32 v9, 16, v9\n"
"v_mfma_f32_16x16x32_bf16 a[32:35], a[160:163], a[220:223], a[32:35]\n"
"v_mul_u32_u24 v15, 1280, v9\n"
"v_sub_u32 v15, v13, v15\n"
"v_lshlrev_b32 v9, 4, v9\n"
"v_mfma_f32_16x16x32_bf16 a[36:39], a[160:163], a[224:227], a[36:39]\n"
"buffer_load_dword v240, v15, s[24:27], 0 offen offset:0\n"
"v_mfma_f32_16x16x32_bf16 a[40:43], a[160:163], a[196:199], a[40:43]\n"
"ds_write_b128 v2, a[16:19] offset:64\n"
"buffer_load_dword v241, v15, s[24:27], 0 offen offset:4\n"
"v_mfma_f32_16x16x32_bf16 a[44:47], a[160:163], v[80:83], a[44:47]\n"
"buffer_load_dword v250, v9, s[16:19], 0 offen offset:0 sc1\n"
"v_mfma_f32_16x16x32_bf16 a[32:35], a[180:183], v[84:87], a[32:35]\n"
"buffer_load_dword v251, v9, s[16:19], 0 offen offset:8 sc1\n"
"v_mfma_f32_16x16x32_bf16 a[36:39], a[180:183], v[88:91], a[36:39]\n"
"ds_write_b128 v2, a[20:23] offset:2624\n"
"buffer_load_dword v232, v14, s[48:51], 0 offen offset:0\n"
"v_mfma_f32_16x16x32_bf16 a[40:43], a[180:183], v[92:95], a[40:43]\n"
"buffer_load_dword v233, v14, s[48:51], 0 offen offset:4\n"
"v_mfma_f32_16x16x32_bf16 a[44:47], a[180:183], a[228:231], a[44:47]\n"
"v_mul_u32_u24 v9, 52, v14\n"
"v_lshrrev_b32 v9, 16, v9\n"
"v_mul_u32_u24 v15, 1280, v9\n"
"v_mfma_f32_16x16x32_bf16 a[32:35], v[200:203], a[232:235], a[32:35]\n"
"ds_write_b128 v2, a[24:27] offset:10304\n"
"v_sub_u32 v15, v14, v15\n"
"v_lshlrev_b32 v9, 4, v9\n"
"v_mfma_f32_16x16x32_bf16 a[36:39], v[200:203], a[236:239], a[36:39]\n"
"buffer_load_dword v242, v15, s[24:27], 0 offen offset:0\n"
"v_mfma_f32_16x16x32_bf16 a[40:43], v[200:203], v[176:179], a[40:43]\n"
"buffer_load_dword v243, v15, s[24:27], 0 offen offset:4\n"
"v_mfma_f32_16x16x32_bf16 a[44:47], v[200:203], v[180:183], a[44:47]\n"
"ds_write_b128 v2, a[28:31] offset:12864\n"
"buffer_load_dword v252, v9, s[16:19], 0 offen offset:0 sc1\n"
"v_mfma_f32_16x16x32_bf16 a[48:51], a[164:167], a[220:223], a[48:51]\n"
"buffer_load_dword v253, v9, s[16:19], 0 offen offset:8 sc1\n"
"v_mfma_f32_16x16x32_bf16 a[52:55], a[164:167], a[224:227], a[52:55]\n"
"v_mfma_f32_16x16x32_bf16 a[56:59], a[164:167], a[196:199], a[56:59]\n"
"ds_write_b128 v2, a[32:35] offset:128\n"
"v_mfma_f32_16x16x32_bf16 a[60:63], a[164:167], v[80:83], a[60:63]\n"
"v_mfma_f32_16x16x32_bf16 a[48:51], a[184:187], v[84:87], a[48:51]\n"
"v_mfma_f32_16x16x32_bf16 a[52:55], a[184:187], v[88:91], a[52:55]\n"
"ds_write_b128 v2, a[36:39] offset:2688\n"
"v_mfma_f32_16x16x32_bf16 a[56:59], a[184:187], v[92:95], a[56:59]\n"
"v_mfma_f32_16x16x32_bf16 a[60:63], a[184:187], a[228:231], a[60:63]\n"
"v_mfma_f32_16x16x32_bf16 a[48:51], v[204:207], a[232:235], a[48:51]\n"
"ds_write_b128 v2, a[40:43] offset:10368\n"
"v_mfma_f32_16x16x32_bf16 a[52:55], v[204:207], a[236:239], a[52:55]\n"
"v_mfma_f32_16x16x32_bf16 a[56:59], v[204:207], v[176:179], a[56:59]\n"
"v_mfma_f32_16x16x32_bf16 a[60:63], v[204:207], v[180:183], a[60:63]\n"
"ds_write_b128 v2, a[44:47] offset:12928\n"
"v_mfma_f32_16x16x32_bf16 a[64:67], a[168:171], a[220:223], a[64:67]\n"
"v_mfma_f32_16x16x32_bf16 a[68:71], a[168:171], a[224:227], a[68:71]\n"
"v_mfma_f32_16x16x32_bf16 a[72:75], a[168:171], a[196:199], a[72:75]\n"
"ds_write_b128 v2, a[48:51] offset:192\n"
"v_mfma_f32_16x16x32_bf16 a[76:79], a[168:171], v[80:83], a[76:79]\n"
"v_mfma_f32_16x16x32_bf16 a[64:67], a[188:191], v[84:87], a[64:67]\n"
"s_add_u32 s68, s35, 0\n"
"v_mfma_f32_16x16x32_bf16 a[68:71], a[188:191], v[88:91], a[68:71]\n"
"ds_write_b128 v2, a[52:55] offset:2752\n"
"buffer_load_dwordx4 v[96:99], v28, s[64:67], s68 offen offset:0 sc1\n"
"v_mfma_f32_16x16x32_bf16 a[72:75], a[188:191], v[92:95], a[72:75]\n"
"buffer_load_dwordx4 v[100:103], v28, s[64:67], s68 offen offset:1024 sc1\n"
"v_mfma_f32_16x16x32_bf16 a[76:79], a[188:191], a[228:231], a[76:79]\n"
"buffer_load_dwordx4 v[104:107], v28, s[64:67], s68 offen offset:2048 sc1\n"
"v_mfma_f32_16x16x32_bf16 a[64:67], a[200:203], a[232:235], a[64:67]\n"
"ds_write_b128 v2, a[56:59] offset:10432\n"
"buffer_load_dwordx4 v[108:111], v28, s[64:67], s68 offen offset:3072 sc1\n"
"v_mfma_f32_16x16x32_bf16 a[68:71], a[200:203], a[236:239], a[68:71]\n"
"s_add_u32 s68, s35, 4096\n"
"v_mfma_f32_16x16x32_bf16 a[72:75], a[200:203], v[176:179], a[72:75]\n"
"buffer_load_dwordx4 v[112:115], v28, s[64:67], s68 offen offset:0 sc1\n"
"v_mfma_f32_16x16x32_bf16 a[76:79], a[200:203], v[180:183], a[76:79]\n"
"ds_write_b128 v2, a[60:63] offset:12992\n"
"ds_write_b128 v2, a[64:67] offset:256\n"
"s_nop 1\n"
"ds_write_b128 v2, a[68:71] offset:2816\n"
"s_nop 0\n"
"ds_write_b128 v2, a[72:75] offset:10496\n"
"s_nop 0\n"
"ds_write_b128 v2, a[76:79] offset:13056\n"
"s_waitcnt vmcnt(5)\n"
"v_mul_f32 v245, -v245, v244\n"
"v_mul_f32 v247, -v247, v246\n"
"v_mul_f32 v249, -v249, v248\n"
"v_mul_f32 v251, -v251, v250\n"
"v_mul_f32 v253, -v253, v252\n"
"s_waitcnt lgkmcnt(0)\n"
"s_barrier\n"
"ds_read_b128 v[128:131], v4 offset:0\n"
"ds_read_b128 v[132:135], v4 offset:20480\n"
"ds_read_b128 v[136:139], v4 offset:40960\n"
"ds_read_b128 v[140:143], v4 offset:61440\n"
"ds_read_b128 v[144:147], v5 offset:0\n"
"ds_read_b128 v[148:151], v5 offset:20480\n"
"ds_read_b128 v[152:155], v5 offset:40960\n"
"ds_read_b128 v[156:159], v5 offset:61440\n"
"ds_read_b128 v[160:163], v6 offset:0\n"
"ds_read_b128 v[164:167], v6 offset:20480\n"
"ds_read_b128 v[168:171], v6 offset:40960\n"
"ds_read_b128 v[172:175], v6 offset:61440\n"
"ds_read_b128 v[176:179], v7 offset:0\n"
"ds_read_b128 v[180:183], v7 offset:20480\n"
"ds_read_b128 v[184:187], v7 offset:40960\n"
"ds_read_b128 v[188:191], v7 offset:61440\n"
"ds_read_b128 v[192:195], v8 offset:0\n"
"ds_read_b128 v[196:199], v8 offset:20480\n"
"ds_read_b128 v[200:203], v8 offset:40960\n"
"ds_read_b128 v[204:207], v8 offset:61440\n"
"s_waitcnt vmcnt(0)\n"
"v_or3_b32 v76, v96, v97, v96\n"
"v_bitop3_b32 v77, v96, v97, v96 bitop3:0x80\n"
"v_or3_b32 v76, v98, v99, v76\n"
"v_bitop3_b32 v77, v98, v99, v77 bitop3:0x80\n"
"v_or3_b32 v76, v100, v101, v76\n"
"v_bitop3_b32 v77, v100, v101, v77 bitop3:0x80\n"
"v_or3_b32 v76, v102, v103, v76\n"
"v_bitop3_b32 v77, v102, v103, v77 bitop3:0x80\n"
"v_or3_b32 v76, v104, v105, v76\n"
"v_bitop3_b32 v77, v104, v105, v77 bitop3:0x80\n"
"v_or3_b32 v76, v106, v107, v76\n"
"v_bitop3_b32 v77, v106, v107, v77 bitop3:0x80\n"
"v_or3_b32 v76, v108, v109, v76\n"
"v_bitop3_b32 v77, v108, v109, v77 bitop3:0x80\n"
"v_or3_b32 v76, v110, v111, v76\n"
"v_bitop3_b32 v77, v110, v111, v77 bitop3:0x80\n"
"v_or3_b32 v76, v112, v113, v76\n"
"v_bitop3_b32 v77, v112, v113, v77 bitop3:0x80\n"
"v_or3_b32 v76, v114, v115, v76\n"
"v_bitop3_b32 v77, v114, v115, v77 bitop3:0x80\n"
"v_xor_b32 v76, v76, v20\n"
"v_xor_b32 v77, v77, v20\n"
"v_or_b32 v76, v76, v77\n"
"v_and_b32 v76, 0xfff, v76\n"
"v_cmp_ne_u32 vcc, 0, v76\n"
"s_nop 0\n"
"s_cmp_lg_u64 vcc, 0\n"
"s_cbranch_scc1 K1X_x_slow\n"
"K1X_x_back:\n"
"s_waitcnt lgkmcnt(15)\n"
"v_pk_add_f32 v[128:129], v[128:129], v[132:133]\n"
"v_pk_add_f32 v[136:137], v[136:137], v[140:141]\n"
"v_pk_add_f32 v[130:131], v[130:131], v[134:135]\n"
"v_pk_add_f32 v[138:139], v[138:139], v[142:143]\n"
"v_lshlrev_b32 v132, 16, v224\n"
"v_and_b32 v133, s74, v224\n"
"v_lshlrev_b32 v140, 16, v234\n"
"v_and_b32 v141, s74, v234\n"
"v_lshlrev_b32 v134, 16, v225\n"
"v_and_b32 v135, s74, v225\n"
"v_lshlrev_b32 v142, 16, v235\n"
"v_and_b32 v143, s74, v235\n"
"v_pk_add_f32 v[128:129], v[128:129], v[136:137]\n"
"v_pk_add_f32 v[130:131], v[130:131], v[138:139]\n"
"v_and_b32 v96, v21, v96\n"
"v_and_b32 v97, v21, v97\n"
"v_and_b32 v98, v21, v98\n"
"v_and_b32 v99, v21, v99\n"
"v_pk_add_f32 v[136:137], v[132:133], v[96:97]\n"
"v_pk_add_f32 v[138:139], v[134:135], v[98:99]\n"
"v_pk_fma_f32 v[132:133], v[132:133], v[244:245], v[244:245] op_sel:[0,0,1] op_sel_hi:[1,0,1]\n"
"v_pk_add_f32 v[128:129], v[128:129], v[136:137]\n"
"v_pk_fma_f32 v[134:135], v[134:135], v[244:245], v[244:245] op_sel:[0,0,1] op_sel_hi:[1,0,1]\n"
"v_pk_add_f32 v[130:131], v[130:131], v[138:139]\n"
"v_lshlrev_b32 v76, 1, v10\n"
"v_pk_fma_f32 v[128:129], v[132:133], v[140:141], v[128:129]\n"
"v_pk_fma_f32 v[130:131], v[134:135], v[142:143], v[130:131]\n"
"buffer_store_dwordx4 v[128:131], v76, s[52:55], 0 offen sc1\n"
"s_waitcnt lgkmcnt(12)\n"
"v_pk_add_f32 v[144:145], v[144:145], v[148:149]\n"
"v_pk_add_f32 v[152:153], v[152:153], v[156:157]\n"
"v_pk_add_f32 v[146:147], v[146:147], v[150:151]\n"
"v_pk_add_f32 v[154:155], v[154:155], v[158:159]\n"
"v_lshlrev_b32 v148, 16, v226\n"
"v_and_b32 v149, s74, v226\n"
"v_lshlrev_b32 v156, 16, v236\n"
"v_and_b32 v157, s74, v236\n"
"v_lshlrev_b32 v150, 16, v227\n"
"v_and_b32 v151, s74, v227\n"
"v_lshlrev_b32 v158, 16, v237\n"
"v_and_b32 v159, s74, v237\n"
"v_pk_add_f32 v[144:145], v[144:145], v[152:153]\n"
"v_pk_add_f32 v[146:147], v[146:147], v[154:155]\n"
"v_and_b32 v100, v21, v100\n"
"v_and_b32 v101, v21, v101\n"
"v_and_b32 v102, v21, v102\n"
"v_and_b32 v103, v21, v103\n"
"v_pk_add_f32 v[152:153], v[148:149], v[100:101]\n"
"v_pk_add_f32 v[154:155], v[150:151], v[102:103]\n"
"v_pk_fma_f32 v[148:149], v[148:149], v[246:247], v[246:247] op_sel:[0,0,1] op_sel_hi:[1,0,1]\n"
"v_pk_add_f32 v[144:145], v[144:145], v[152:153]\n"
"v_pk_fma_f32 v[150:151], v[150:151], v[246:247], v[246:247] op_sel:[0,0,1] op_sel_hi:[1,0,1]\n"
"v_pk_add_f32 v[146:147], v[146:147], v[154:155]\n"
"v_lshlrev_b32 v76, 1, v11\n"
"v_pk_fma_f32 v[144:145], v[148:149], v[156:157], v[144:145]\n"
"v_pk_fma_f32 v[146:147], v[150:151], v[158:159], v[146:147]\n"
"buffer_store_dwordx4 v[144:147], v76, s[52:55], 0 offen sc1\n"
"s_waitcnt lgkmcnt(8)\n"
"v_pk_add_f32 v[160:161], v[160:161], v[164:165]\n"
"v_pk_add_f32 v[168:169], v[168:169], v[172:173]\n"
"v_pk_add_f32 v[162:163], v[162:163], v[166:167]\n"
"v_pk_add_f32 v[170:171], v[170:171], v[174:175]\n"
"v_lshlrev_b32 v164, 16, v228\n"
"v_and_b32 v165, s74, v228\n"
"v_lshlrev_b32 v172, 16, v238\n"
"v_and_b32 v173, s74, v238\n"
"v_lshlrev_b32 v166, 16, v229\n"
"v_and_b32 v167, s74, v229\n"
"v_lshlrev_b32 v174, 16, v239\n"
"v_and_b32 v175, s74, v239\n"
"v_pk_add_f32 v[160:161], v[160:161], v[168:169]\n"
"v_pk_add_f32 v[162:163], v[162:163], v[170:171]\n"
"v_and_b32 v104, v21, v104\n"
"v_and_b32 v105, v21, v105\n"
"v_and_b32 v106, v21, v106\n"
"v_and_b32 v107, v21, v107\n"
"v_pk_add_f32 v[168:169], v[164:165], v[104:105]\n"
"v_pk_add_f32 v[170:171], v[166:167], v[106:107]\n"
"v_pk_fma_f32 v[164:165], v[164:165], v[248:249], v[248:249] op_sel:[0,0,1] op_sel_hi:[1,0,1]\n"
"v_pk_add_f32 v[160:161], v[160:161], v[168:169]\n"
"v_pk_fma_f32 v[166:167], v[166:167], v[248:249], v[248:249] op_sel:[0,0,1] op_sel_hi:[1,0,1]\n"
"v_pk_add_f32 v[162:163], v[162:163], v[170:171]\n"
"v_lshlrev_b32 v76, 1, v12\n"
"v_pk_fma_f32 v[160:161], v[164:165], v[172:173], v[160:161]\n"
"v_pk_fma_f32 v[162:163], v[166:167], v[174:175], v[162:163]\n"
"buffer_store_dwordx4 v[160:163], v76, s[52:55], 0 offen sc1\n"
"s_waitcnt lgkmcnt(4)\n"
"v_pk_add_f32 v[176:177], v[176:177], v[180:181]\n"
"v_pk_add_f32 v[184:185], v[184:185], v[188:189]\n"
"v_pk_add_f32 v[178:179], v[178:179], v[182:183]\n"
"v_pk_add_f32 v[186:187], v[186:187], v[190:191]\n"
"v_lshlrev_b32 v180, 16, v230\n"
"v_and_b32 v181, s74, v230\n"
"v_lshlrev_b32 v188, 16, v240\n"
"v_and_b32 v189, s74, v240\n"
"v_lshlrev_b32 v182, 16, v231\n"
"v_and_b32 v183, s74, v231\n"
"v_lshlrev_b32 v190, 16, v241\n"
"v_and_b32 v191, s74, v241\n"
"v_pk_add_f32 v[176:177], v[176:177], v[184:185]\n"
"v_pk_add_f32 v[178:179], v[178:179], v[186:187]\n"
"v_and_b32 v108, v21, v108\n"
"v_and_b32 v109, v21, v109\n"
"v_and_b32 v110, v21, v110\n"
"v_and_b32 v111, v21, v111\n"
"v_pk_add_f32 v[184:185], v[180:181], v[108:109]\n"
"v_pk_add_f32 v[186:187], v[182:183], v[110:111]\n"
"v_pk_fma_f32 v[180:181], v[180:181], v[250:251], v[250:251] op_sel:[0,0,1] op_sel_hi:[1,0,1]\n"
"v_pk_add_f32 v[176:177], v[176:177], v[184:185]\n"
"v_pk_fma_f32 v[182:183], v[182:183], v[250:251], v[250:251] op_sel:[0,0,1] op_sel_hi:[1,0,1]\n"
"v_pk_add_f32 v[178:179], v[178:179], v[186:187]\n"
"v_lshlrev_b32 v76, 1, v13\n"
"v_pk_fma_f32 v[176:177], v[180:181], v[188:189], v[176:177]\n"
"v_pk_fma_f32 v[178:179], v[182:183], v[190:191], v[178:179]\n"
"buffer_store_dwordx4 v[176:179], v76, s[52:55], 0 offen sc1\n"
"s_waitcnt lgkmcnt(0)\n"
"v_pk_add_f32 v[192:193], v[192:193], v[196:197]\n"
"v_pk_add_f32 v[200:201], v[200:201], v[204:205]\n"
"v_pk_add_f32 v[194:195], v[194:195], v[198:199]\n"
"v_pk_add_f32 v[202:203], v[202:203], v[206:207]\n"
"v_lshlrev_b32 v196, 16, v232\n"
"v_and_b32 v197, s74, v232\n"
"v_lshlrev_b32 v204, 16, v242\n"
"v_and_b32 v205, s74, v242\n"
"v_lshlrev_b32 v198, 16, v233\n"
"v_and_b32 v199, s74, v233\n"
"v_lshlrev_b32 v206, 16, v243\n"
"v_and_b32 v207, s74, v243\n"
"v_pk_add_f32 v[192:193], v[192:193], v[200:201]\n"
"v_pk_add_f32 v[194:195], v[194:195], v[202:203]\n"
"v_and_b32 v112, v21, v112\n"
"v_and_b32 v113, v21, v113\n"
"v_and_b32 v114, v21, v114\n"
"v_and_b32 v115, v21, v115\n"
"v_pk_add_f32 v[200:201], v[196:197], v[112:113]\n"
"v_pk_add_f32 v[202:203], v[198:199], v[114:115]\n"
"v_pk_fma_f32 v[196:197], v[196:197], v[252:253], v[252:253] op_sel:[0,0,1] op_sel_hi:[1,0,1]\n"
"v_pk_add_f32 v[192:193], v[192:193], v[200:201]\n"
"v_pk_fma_f32 v[198:199], v[198:199], v[252:253], v[252:253] op_sel:[0,0,1] op_sel_hi:[1,0,1]\n"
"v_pk_add_f32 v[194:195], v[194:195], v[202:203]\n"
"v_lshlrev_b32 v76, 1, v14\n"
"v_pk_fma_f32 v[192:193], v[196:197], v[204:205], v[192:193]\n"
"v_pk_fma_f32 v[194:195], v[198:199], v[206:207], v[194:195]\n"
"buffer_store_dwordx4 v[192:195], v76, s[52:55], 0 offen sc1\n"
"s_endpgm\n"
"K1S:\n"
"s_nop 4\n"
"s_mul_i32 s15, s9, 3\n"
"s_sub_u32 s15, s15, 28\n"
"s_bitcmp1_b32 s7, 0\n"
"s_cselect_b32 s56, s24, s22\n"
"s_cselect_b32 s57, s25, s23\n"
"s_and_b32 s57, s57, 0xffff\n"
"s_mov_b32 s58, 819200\n"
"s_mov_b32 s59, 0x20000\n"
"s_lshl_b32 s14, s15, 1\n"
"s_lshr_b32 s68, s7, 1\n"
"s_add_u32 s14, s14, s68\n"
"s_mul_i32 s14, s14, 20480\n"
"s_add_u32 s48, s14, 40960\n"
"s_add_u32 s49, s14, 81920\n"
"s_lshl_b32 s68, s8, 1\n"
"s_add_u32 s68, s68, s9\n"
"s_add_u32 s68, s68, 18\n"
"s_lshr_b32 s42, s68, 1\n"
"s_and_b32 s96, s68, 1\n"
"s_cselect_b32 s52, s34, s32\n"
"s_cselect_b32 s53, s35, s33\n"
"s_and_b32 s53, s53, 0xffff\n"
"s_cmp_lt_u32 s42, 27\n"
"s_cselect_b32 s54, 2211840, 0\n"
"s_mov_b32 s55, 0x20000\n"
"s_mov_b64 s[64:65], s[38:39]\n"
"s_and_b32 s65, s65, 0xffff\n"
"s_mov_b32 s66, 35651584\n"
"s_mov_b32 s67, 0x20000\n"
"v_mov_b32 v71, 23068672\n"
"buffer_load_dword v146, v71, s[64:67], 0 offen offset:0 sc1\n"
"buffer_load_dword v147, v71, s[64:67], 0 offen offset:4 sc1\n"
"s_lshl_b32 s68, s8, 4\n"
"s_add_u32 s68, s68, s9\n"
"s_lshl_b32 s68, s68, 2\n"
"s_add_u32 s68, s68, s7\n"
"s_mul_i32 s69, s68, 2560\n"
"v_lshrrev_b32 v71, 5, v27\n"
"v_and_b32 v73, 31, v27\n"
"v_mul_u32_u24 v74, 1280, v71\n"
"v_add_u32 v74, s69, v74\n"
"v_lshl_add_u32 v75, v73, 4, v74\n"
"v_lshl_add_u32 v74, v73, 3, v74\n"
"global_load_dwordx4 v[96:99], v75, s[16:17]\n"
"global_load_dwordx4 v[100:103], v75, s[16:17] offset:512\n"
"global_load_dwordx2 v[104:105], v74, s[16:17] offset:1024\n"
"s_lshl_b32 s68, s68, 1\n"
"v_add_u32 v148, s68, v71\n"
"s_bitcmp1_b32 s8, 2\n"
"s_cselect_b32 s60, s24, s22\n"
"s_cselect_b32 s61, s25, s23\n"
"s_and_b32 s68, s8, 3\n"
"s_lshl_b32 s68, s68, 2\n"
"s_add_u32 s68, s68, s7\n"
"s_lshl_b32 s69, s15, 4\n"
"s_add_u32 s68, s68, s69\n"
"s_mul_i32 s68, s68, 2560\n"
"v_mul_u32_u24 v74, 1280, v71\n"
"v_add_u32 v74, s68, v74\n"
"v_lshl_add_u32 v75, v73, 4, v74\n"
"v_lshl_add_u32 v74, v73, 3, v74\n"
"global_load_dwordx4 v[106:109], v75, s[60:61]\n"
"global_load_dwordx4 v[110:113], v75, s[60:61] offset:512\n"
"global_load_dwordx2 v[114:115], v74, s[60:61] offset:1024\n"
"v_add_u32 v75, 40960, v75\n"
"v_add_u32 v74, 40960, v74\n"
"global_load_dwordx4 v[116:119], v75, s[60:61]\n"
"global_load_dwordx4 v[120:123], v75, s[60:61] offset:512\n"
"global_load_dwordx2 v[124:125], v74, s[60:61] offset:1024\n"
"v_add_u32 v75, 40960, v75\n"
"v_add_u32 v74, 40960, v74\n"
"global_load_dwordx4 v[126:129], v75, s[60:61]\n"
"global_load_dwordx4 v[130:133], v75, s[60:61] offset:512\n"
"global_load_dwordx2 v[134:135], v74, s[60:61] offset:1024\n"
"s_lshl_b32 s69, s15, 6\n"
"v_lshl_add_u32 v74, v73, 1, s69\n"
"global_load_ushort v136, v74, s[18:19] offset:0\n"
"global_load_ushort v137, v74, s[20:21] offset:0\n"
"global_load_ushort v138, v74, s[30:31] offset:0\n"
"global_load_ushort v139, v74, s[18:19] offset:64\n"
"global_load_ushort v140, v74, s[20:21] offset:64\n"
"global_load_ushort v141, v74, s[30:31] offset:64\n"
"global_load_ushort v142, v74, s[18:19] offset:128\n"
"global_load_ushort v143, v74, s[20:21] offset:128\n"
"global_load_ushort v144, v74, s[30:31] offset:128\n"
"v_lshrrev_b32 v68, 3, v27\n"
"v_and_b32 v69, 7, v27\n"
"v_mul_u32_u24 v26, 3456, v68\n"
"v_lshl_add_u32 v26, v69, 4, v26\n"
"s_mul_i32 s68, s7, 27648\n"
"s_lshl_b32 s71, s42, 7\n"
"s_add_u32 s71, s71, s68\n"
"v_lshrrev_b32 v68, 5, v27\n"
"buffer_load_dwordx4 v[160:163], v26, s[52:55], s71 offen\n"
"v_and_b32 v69, 3, v27\n"
"v_bfe_u32 v70, v27, 3, 2\n"
"v_lshl_add_u32 v69, v70, 2, v69\n"
"v_bfe_u32 v70, v27, 2, 1\n"
"v_lshl_add_u32 v1, v70, 4, v69\n"
"v_lshrrev_b32 v69, 1, v69\n"
"v_xor_b32 v69, v69, v68\n"
"v_lshlrev_b32 v69, 4, v69\n"
"v_lshl_add_u32 v69, v1, 7, v69\n"
"v_add_u32 v14, s10, v69\n"
"v_xor_b32 v15, 32, v14\n"
"v_xor_b32 v16, 64, v14\n"
"v_xor_b32 v17, 96, v14\n"
"v_bfe_u32 v69, v27, 1, 3\n"
"s_add_u32 s71, s71, 110592\n"
"buffer_load_dwordx4 v[164:167], v26, s[52:55], s71 offen\n"
"v_xor_b32 v69, v69, v68\n"
"v_and_b32 v70, 15, v27\n"
"v_lshlrev_b32 v70, 7, v70\n"
"v_lshl_add_u32 v70, v69, 4, v70\n"
"v_bfe_u32 v69, v27, 4, 1\n"
"v_mul_u32_u24 v69, 81920, v69\n"
"v_add_u32 v10, v69, v70\n"
"v_xor_b32 v11, 32, v10\n"
"v_xor_b32 v12, 64, v10\n"
"v_xor_b32 v13, 96, v10\n"
"v_add_u32 v152, 40960, v10\n"
"v_add_u32 v153, 40960, v11\n"
"v_add_u32 v154, 40960, v12\n"
"v_add_u32 v155, 40960, v13\n"
"s_add_u32 s71, s71, 110592\n"
"buffer_load_dwordx4 v[168:171], v26, s[52:55], s71 offen\n"
"v_lshlrev_b32 v28, 4, v27\n"
"s_mov_b32 s74, 0xffff0000\n"
"s_lshl_b32 s69, s6, 2\n"
"s_add_u32 s69, s69, s7\n"
"s_cmp_lt_u32 s69, 740\n"
"s_cselect_b32 s26, s26, s28\n"
"s_cselect_b32 s27, s27, s29\n"
"s_cselect_b32 s70, 0, 200\n"
"s_cselect_b32 s29, 0, 8\n"
"s_add_u32 s70, s70, 540\n"
"s_cmp_lt_u32 s69, 540\n"
"s_cselect_b32 s26, s36, s26\n"
"s_cselect_b32 s27, s37, s27\n"
"s_cselect_b32 s70, 0, s70\n"
"s_cselect_b32 s75, 1, 0\n"
"s_add_u32 s71, s71, 110592\n"
"buffer_load_dwordx4 v[172:175], v26, s[52:55], s71 offen\n"
"s_sub_u32 s70, s69, s70\n"
"s_mul_i32 s68, s70, 6554\n"
"s_lshr_b32 s68, s68, 16\n"
"s_mul_i32 s43, s68, 10\n"
"s_sub_u32 s70, s70, s43\n"
"s_cmp_lt_u32 s69, 940\n"
"s_cselect_b32 s28, 39808, 0\n"
"s_lshl_b32 s69, s70, 2\n"
"s_add_u32 s43, s68, 40\n"
"s_cmp_eq_u32 s75, 1\n"
"s_cselect_b32 s43, s43, s69\n"
"s_and_b32 s69, s43, 0x7c\n"
"s_add_u32 s69, s69, 0xf0\n"
"s_load_dword s11, s[4:5], s69\n"
"s_add_u32 s71, s71, 110592\n"
"buffer_load_dwordx4 v[176:179], v26, s[52:55], s71 offen\n"
"s_mov_b32 s40, s68\n"
"s_mov_b32 s36, s70\n"
"s_waitcnt lgkmcnt(0)\n"
"s_mul_i32 s68, s40, 320\n"
"s_add_u32 s68, s68, s36\n"
"s_lshl_b32 s68, s68, 7\n"
"s_add_u32 s60, s26, s68\n"
"s_addc_u32 s61, s27, 0\n"
"s_mov_b32 s62, s28\n"
"s_and_b32 s61, s61, 0xffff\n"
"s_mov_b32 s63, 0x20000\n"
"s_and_b32 s68, s43, 3\n"
"s_lshl_b32 s68, s68, 3\n"
"s_add_u32 s68, s68, s29\n"
"s_lshr_b32 s11, s11, s68\n"
"s_add_u32 s71, s71, 110592\n"
"buffer_load_dwordx4 v[180:183], v26, s[52:55], s71 offen\n"
"s_and_b32 s28, s11, 0xff\n"
"s_bfe_u32 s29, s11, 0x80010\n"
"s_mul_i32 s28, s28, 40\n"
"s_mul_i32 s29, s29, 40\n"
"s_lshl_b32 s68, s36, 2\n"
"s_lshl_b32 s69, s40, 1\n"
"s_add_u32 s70, s28, 2\n"
"s_cmp_eq_u32 s75, 1\n"
"s_cselect_b32 s68, s68, s69\n"
"s_cselect_b32 s29, s70, s29\n"
"s_add_u32 s28, s28, s68\n"
"s_add_u32 s29, s29, s68\n"
"s_cmp_eq_u32 s62, 0\n"
"s_cselect_b32 s68, 2130706432, 0\n"
"s_add_u32 s71, s71, 110592\n"
"buffer_load_dwordx4 v[184:187], v26, s[52:55], s71 offen\n"
"s_add_u32 s68, s68, 16777216\n"
"s_lshl_b32 s28, s28, 10\n"
"s_lshl_b32 s29, s29, 10\n"
"s_add_u32 s28, s28, s68\n"
"s_add_u32 s29, s29, s68\n"
"s_lshl_b32 s68, s15, 3\n"
"s_and_b32 s69, s8, 7\n"
"s_add_u32 s68, s68, s69\n"
"s_lshl_b32 s68, s68, 2\n"
"s_add_u32 s68, s68, s7\n"
"s_lshl_b32 s68, s68, 5\n"
"s_add_u32 s68, s68, 21092352\n"
"s_cmp_lt_u32 s8, 8\n"
"s_cselect_b32 s88, s68, 2130706432\n"
"s_add_u32 s71, s71, 110592\n"
"buffer_load_dwordx4 v[188:191], v26, s[52:55], s71 offen\n"
"s_lshl_b32 s43, s8, 1\n"
"s_lshr_b32 s68, s7, 1\n"
"s_add_u32 s43, s43, s68\n"
"s_mul_i32 s43, s43, 393216\n"
"s_and_b32 s68, s7, 1\n"
"s_lshl_b32 s68, s68, 11\n"
"s_add_u32 s43, s43, s68\n"
"s_lshl_b32 s68, s15, 4\n"
"s_add_u32 s68, s68, s8\n"
"s_lshl_b32 s68, s68, 9\n"
"s_add_u32 s68, s68, 22020096\n"
"s_lshl_b32 s69, s7, 11\n"
"s_sub_u32 s90, s68, s69\n"
"s_sub_u32 s90, s90, 512\n"
"s_add_u32 s71, s71, 110592\n"
"buffer_load_dwordx4 v[192:195], v26, s[52:55], s71 offen\n"
"s_cmp_eq_u32 s7, 3\n"
"s_cselect_b32 s89, s68, 2130706432\n"
"s_lshl_b32 s68, s8, 2\n"
"s_add_u32 s68, s68, s7\n"
"s_lshl_b32 s68, s68, 9\n"
"s_add_u32 s91, s68, 20971520\n"
"s_lshl_b32 s92, s9, 12\n"
"s_lshl_b32 s93, s15, 10\n"
"s_add_u32 s93, s93, 55296\n"
"s_lshl_b32 s68, s6, 7\n"
"s_add_u32 s68, s68, 23072768\n"
"s_cmp_eq_u32 s7, 0\n"
"s_cselect_b32 s94, s68, 2130706432\n"
"s_waitcnt vmcnt(30)\n"
"v_readfirstlane_b32 s72, v146\n"
"s_add_u32 s71, s71, 110592\n"
"buffer_load_dwordx4 v[196:199], v26, s[52:55], s71 offen\n"
"v_readfirstlane_b32 s73, v147\n"
"s_or_b32 s72, s72, 0x40004000\n"
"s_or_b32 s73, s73, 0x40004000\n"
"s_add_u32 s72, s72, 1\n"
"s_add_u32 s73, s73, 0x1e3739b9\n"
"s_and_b32 s72, s72, 0xbfffbfff\n"
"s_and_b32 s73, s73, 0xbfffbfff\n"
"s_waitcnt vmcnt(30)\n"
"v_lshlrev_b32 v76, 16, v96\n"
"v_and_b32 v77, s74, v96\n"
"v_add_f32 v68, v76, v77\n"
"v_mul_f32 v69, v76, v76\n"
"v_fmac_f32 v69, v77, v77\n"
"v_lshlrev_b32 v76, 16, v97\n"
"v_and_b32 v77, s74, v97\n"
"s_add_u32 s71, s71, 110592\n"
"buffer_load_dwordx4 v[200:203], v26, s[52:55], s71 offen\n"
"v_add_f32 v68, v68, v76\n"
"v_fmac_f32 v69, v76, v76\n"
"v_add_f32 v68, v68, v77\n"
"v_fmac_f32 v69, v77, v77\n"
"v_lshlrev_b32 v76, 16, v98\n"
"v_and_b32 v77, s74, v98\n"
"v_add_f32 v68, v68, v76\n"
"v_fmac_f32 v69, v76, v76\n"
"v_add_f32 v68, v68, v77\n"
"v_fmac_f32 v69, v77, v77\n"
"v_lshlrev_b32 v76, 16, v99\n"
"v_and_b32 v77, s74, v99\n"
"v_add_f32 v68, v68, v76\n"
"v_fmac_f32 v69, v76, v76\n"
"s_add_u32 s71, s71, 110592\n"
"buffer_load_dwordx4 v[204:207], v26, s[52:55], s71 offen\n"
"v_add_f32 v68, v68, v77\n"
"v_fmac_f32 v69, v77, v77\n"
"s_waitcnt vmcnt(31)\n"
"v_lshlrev_b32 v76, 16, v100\n"
"v_and_b32 v77, s74, v100\n"
"v_add_f32 v68, v68, v76\n"
"v_fmac_f32 v69, v76, v76\n"
"v_add_f32 v68, v68, v77\n"
"v_fmac_f32 v69, v77, v77\n"
"v_lshlrev_b32 v76, 16, v101\n"
"v_and_b32 v77, s74, v101\n"
"v_add_f32 v68, v68, v76\n"
"v_fmac_f32 v69, v76, v76\n"
"v_add_f32 v68, v68, v77\n"
"v_fmac_f32 v69, v77, v77\n"
"s_add_u32 s71, s71, 110592\n"
"buffer_load_dwordx4 v[208:211], v26, s[52:55], s71 offen\n"
"v_lshlrev_b32 v76, 16, v102\n"
"v_and_b32 v77, s74, v102\n"
"v_add_f32 v68, v68, v76\n"
"v_fmac_f32 v69, v76, v76\n"
"v_add_f32 v68, v68, v77\n"
"v_fmac_f32 v69, v77, v77\n"
"v_lshlrev_b32 v76, 16, v103\n"
"v_and_b32 v77, s74, v103\n"
"v_add_f32 v68, v68, v76\n"
"v_fmac_f32 v69, v76, v76\n"
"v_add_f32 v68, v68, v77\n"
"v_fmac_f32 v69, v77, v77\n"
"s_waitcnt vmcnt(31)\n"
"v_lshlrev_b32 v76, 16, v104\n"
"v_and_b32 v77, s74, v104\n"
"s_add_u32 s71, s71, 110592\n"
"buffer_load_dwordx4 v[212:215], v26, s[52:55], s71 offen\n"
"v_add_f32 v68, v68, v76\n"
"v_fmac_f32 v69, v76, v76\n"
"v_add_f32 v68, v68, v77\n"
"v_fmac_f32 v69, v77, v77\n"
"v_lshlrev_b32 v76, 16, v105\n"
"v_and_b32 v77, s74, v105\n"
"v_add_f32 v68, v68, v76\n"
"v_fmac_f32 v69, v76, v76\n"
"v_add_f32 v68, v68, v77\n"
"v_fmac_f32 v69, v77, v77\n"
"s_waitcnt vmcnt(31)\n"
"v_lshlrev_b32 v76, 16, v106\n"
"v_and_b32 v77, s74, v106\n"
"v_add_f32 v70, v76, v77\n"
"v_lshlrev_b32 v76, 16, v107\n"
"s_add_u32 s71, s71, 110592\n"
"buffer_load_dwordx4 v[216:219], v26, s[52:55], s71 offen\n"
"v_and_b32 v77, s74, v107\n"
"v_add_f32 v70, v70, v76\n"
"v_add_f32 v70, v70, v77\n"
"v_lshlrev_b32 v76, 16, v108\n"
"v_and_b32 v77, s74, v108\n"
"v_add_f32 v70, v70, v76\n"
"v_add_f32 v70, v70, v77\n"
"v_lshlrev_b32 v76, 16, v109\n"
"v_and_b32 v77, s74, v109\n"
"v_add_f32 v70, v70, v76\n"
"v_add_f32 v70, v70, v77\n"
"s_waitcnt vmcnt(31)\n"
"v_lshlrev_b32 v76, 16, v110\n"
"v_and_b32 v77, s74, v110\n"
"v_add_f32 v70, v70, v76\n"
"s_add_u32 s71, s71, 110592\n"
"buffer_load_dwordx4 v[220:223], v26, s[52:55], s71 offen\n"
"v_add_f32 v70, v70, v77\n"
"v_lshlrev_b32 v76, 16, v111\n"
"v_and_b32 v77, s74, v111\n"
"v_add_f32 v70, v70, v76\n"
"v_add_f32 v70, v70, v77\n"
"v_lshlrev_b32 v76, 16, v112\n"
"v_and_b32 v77, s74, v112\n"
"v_add_f32 v70, v70, v76\n"
"v_add_f32 v70, v70, v77\n"
"v_lshlrev_b32 v76, 16, v113\n"
"v_and_b32 v77, s74, v113\n"
"v_add_f32 v70, v70, v76\n"
"v_add_f32 v70, v70, v77\n"
"s_waitcnt vmcnt(31)\n"
"v_lshlrev_b32 v76, 16, v114\n"
"s_add_u32 s71, s71, 110592\n"
"buffer_load_dwordx4 v[224:227], v26, s[52:55], s71 offen\n"
"v_and_b32 v77, s74, v114\n"
"v_add_f32 v70, v70, v76\n"
"v_add_f32 v70, v70, v77\n"
"v_lshlrev_b32 v76, 16, v115\n"
"v_and_b32 v77, s74, v115\n"
"v_add_f32 v70, v70, v76\n"
"v_add_f32 v70, v70, v77\n"
"s_waitcnt vmcnt(31)\n"
"v_lshlrev_b32 v76, 16, v116\n"
"v_and_b32 v77, s74, v116\n"
"v_add_f32 v71, v76, v77\n"
"v_lshlrev_b32 v76, 16, v117\n"
"v_and_b32 v77, s74, v117\n"
"v_add_f32 v71, v71, v76\n"
"v_add_f32 v71, v71, v77\n"
"s_add_u32 s71, s71, 110592\n"
"buffer_load_dwordx4 v[228:231], v26, s[52:55], s71 offen\n"
"v_lshlrev_b32 v76, 16, v118\n"
"v_and_b32 v77, s74, v118\n"
"v_add_f32 v71, v71, v76\n"
"v_add_f32 v71, v71, v77\n"
"v_lshlrev_b32 v76, 16, v119\n"
"v_and_b32 v77, s74, v119\n"
"v_add_f32 v71, v71, v76\n"
"v_add_f32 v71, v71, v77\n"
"s_waitcnt vmcnt(31)\n"
"v_lshlrev_b32 v76, 16, v120\n"
"v_and_b32 v77, s74, v120\n"
"v_add_f32 v71, v71, v76\n"
"v_add_f32 v71, v71, v77\n"
"v_lshlrev_b32 v76, 16, v121\n"
"v_and_b32 v77, s74, v121\n"
"s_add_u32 s71, s71, 110592\n"
"buffer_load_dwordx4 v[232:235], v26, s[52:55], s71 offen\n"
"v_add_f32 v71, v71, v76\n"
"v_add_f32 v71, v71, v77\n"
"v_lshlrev_b32 v76, 16, v122\n"
"v_and_b32 v77, s74, v122\n"
"v_add_f32 v71, v71, v76\n"
"v_add_f32 v71, v71, v77\n"
"v_lshlrev_b32 v76, 16, v123\n"
"v_and_b32 v77, s74, v123\n"
"v_add_f32 v71, v71, v76\n"
"v_add_f32 v71, v71, v77\n"
"s_waitcnt vmcnt(31)\n"
"v_lshlrev_b32 v76, 16, v124\n"
"v_and_b32 v77, s74, v124\n"
"v_add_f32 v71, v71, v76\n"
"v_add_f32 v71, v71, v77\n"
"s_add_u32 s71, s71, 110592\n"
"buffer_load_dwordx4 v[236:239], v26, s[52:55], s71 offen\n"
"v_lshlrev_b32 v76, 16, v125\n"
"v_and_b32 v77, s74, v125\n"
"v_add_f32 v71, v71, v76\n"
"v_add_f32 v71, v71, v77\n"
"s_waitcnt vmcnt(31)\n"
"v_lshlrev_b32 v76, 16, v126\n"
"v_and_b32 v77, s74, v126\n"
"v_add_f32 v72, v76, v77\n"
"v_lshlrev_b32 v76, 16, v127\n"
"v_and_b32 v77, s74, v127\n"
"v_add_f32 v72, v72, v76\n"
"v_add_f32 v72, v72, v77\n"
"v_lshlrev_b32 v76, 16, v128\n"
"v_and_b32 v77, s74, v128\n"
"v_add_f32 v72, v72, v76\n"
"v_add_f32 v72, v72, v77\n"
"v_lshlrev_b32 v76, 16, v129\n"
"v_and_b32 v77, s74, v129\n"
"v_add_f32 v72, v72, v76\n"
"v_add_f32 v72, v72, v77\n"
"s_waitcnt vmcnt(30)\n"
"v_lshlrev_b32 v76, 16, v130\n"
"v_and_b32 v77, s74, v130\n"
"v_add_f32 v72, v72, v76\n"
"v_add_f32 v72, v72, v77\n"
"v_lshlrev_b32 v76, 16, v131\n"
"v_and_b32 v77, s74, v131\n"
"v_add_f32 v72, v72, v76\n"
"v_add_f32 v72, v72, v77\n"
"v_lshlrev_b32 v76, 16, v132\n"
"v_and_b32 v77, s74, v132\n"
"v_add_f32 v72, v72, v76\n"
"v_add_f32 v72, v72, v77\n"
"v_lshlrev_b32 v76, 16, v133\n"
"v_and_b32 v77, s74, v133\n"
"v_add_f32 v72, v72, v76\n"
"v_add_f32 v72, v72, v77\n"
"s_waitcnt vmcnt(29)\n"
"v_lshlrev_b32 v76, 16, v134\n"
"v_and_b32 v77, s74, v134\n"
"v_add_f32 v72, v72, v76\n"
"v_add_f32 v72, v72, v77\n"
"v_lshlrev_b32 v76, 16, v135\n"
"v_and_b32 v77, s74, v135\n"
"v_add_f32 v72, v72, v76\n"
"v_add_f32 v72, v72, v77\n"
"v_add_f32_dpp v68, v68, v68 row_ror:8 row_mask:0xf bank_mask:0xf\n"
"v_add_f32_dpp v69, v69, v69 row_ror:8 row_mask:0xf bank_mask:0xf\n"
"v_add_f32_dpp v70, v70, v70 row_ror:8 row_mask:0xf bank_mask:0xf\n"
"v_add_f32_dpp v71, v71, v71 row_ror:8 row_mask:0xf bank_mask:0xf\n"
"v_add_f32_dpp v72, v72, v72 row_ror:8 row_mask:0xf bank_mask:0xf\n"
"v_add_f32_dpp v68, v68, v68 row_ror:4 row_mask:0xf bank_mask:0xf\n"
"v_add_f32_dpp v69, v69, v69 row_ror:4 row_mask:0xf bank_mask:0xf\n"
"v_add_f32_dpp v70, v70, v70 row_ror:4 row_mask:0xf bank_mask:0xf\n"
"v_add_f32_dpp v71, v71, v71 row_ror:4 row_mask:0xf bank_mask:0xf\n"
"v_add_f32_dpp v72, v72, v72 row_ror:4 row_mask:0xf bank_mask:0xf\n"
"v_add_f32_dpp v68, v68, v68 row_ror:2 row_mask:0xf bank_mask:0xf\n"
"v_add_f32_dpp v69, v69, v69 row_ror:2 row_mask:0xf bank_mask:0xf\n"
"v_add_f32_dpp v70, v70, v70 row_ror:2 row_mask:0xf bank_mask:0xf\n"
"v_add_f32_dpp v71, v71, v71 row_ror:2 row_mask:0xf bank_mask:0xf\n"
"v_add_f32_dpp v72, v72, v72 row_ror:2 row_mask:0xf bank_mask:0xf\n"
"v_add_f32_dpp v68, v68, v68 row_ror:1 row_mask:0xf bank_mask:0xf\n"
"v_add_f32_dpp v69, v69, v69 row_ror:1 row_mask:0xf bank_mask:0xf\n"
"v_add_f32_dpp v70, v70, v70 row_ror:1 row_mask:0xf bank_mask:0xf\n"
"v_add_f32_dpp v71, v71, v71 row_ror:1 row_mask:0xf bank_mask:0xf\n"
"v_add_f32_dpp v72, v72, v72 row_ror:1 row_mask:0xf bank_mask:0xf\n"
"v_mov_b32 v74, v68\n"
"v_mov_b32 v75, v69\n"
"v_mov_b32 v76, v70\n"
"v_mov_b32 v77, v71\n"
"v_mov_b32 v78, v72\n"
"v_permlane16_swap_b32 v68, v74\n"
"v_permlane16_swap_b32 v69, v75\n"
"v_permlane16_swap_b32 v70, v76\n"
"v_permlane16_swap_b32 v71, v77\n"
"v_permlane16_swap_b32 v72, v78\n"
"v_add_f32 v68, v68, v74\n"
"v_add_f32 v69, v69, v75\n"
"v_add_f32 v70, v70, v76\n"
"v_add_f32 v71, v71, v77\n"
"v_add_f32 v72, v72, v78\n"
"v_mul_f32 v68, 0x3acccccd, v68\n"
"v_mul_f32 v69, 0x3acccccd, v69\n"
"v_mov_b32 v97, s72\n"
"v_fma_f32 v69, -v68, v68, v69\n"
"v_mov_b32 v99, s73\n"
"v_max_f32 v69, 0, v69\n"
"v_cvt_pk_bf16_f32 v68, v68, v69\n"
"v_and_b32 v69, s74, v68\n"
"v_lshlrev_b32 v98, 16, v68\n"
"v_add_f32 v69, 0x3727c5ac, v69\n"
"v_cvt_pk_bf16_f32 v69, v69, v69\n"
"v_and_b32 v69, s74, v69\n"
"v_sqrt_f32 v69, v69\n"
"v_and_b32 v76, 31, v27\n"
"v_cvt_pk_bf16_f32 v69, v69, v69\n"
"v_cmp_eq_u32 vcc, 0, v76\n"
"v_and_b32 v69, s74, v69\n"
"v_lshlrev_b32 v76, 4, v148\n"
"v_rcp_f32 v96, v69\n"
"v_add_u32 v76, 20971520, v76\n"
"v_mov_b32 v77, 2130706432\n"
"v_cndmask_b32 v76, v77, v76, vcc\n"
"buffer_store_dwordx4 v[96:99], v76, s[64:67], 0 offen sc1\n"
"v_lshrrev_b32 v76, 1, v27\n"
"v_cndmask_b32 v76, v77, v76, vcc\n"
"v_cvt_pk_bf16_f32 v77, v70, v70\n"
"v_and_b32 v77, s74, v77\n"
"v_sub_f32 v70, v70, v77\n"
"v_cvt_pk_bf16_f32 v100, v77, v70\n"
"v_and_b32 v77, s74, v100\n"
"v_sub_f32 v70, v70, v77\n"
"v_cvt_pk_bf16_f32 v102, v70, 0\n"
"v_mov_b32 v101, s72\n"
"v_mov_b32 v103, s73\n"
"buffer_store_dwordx4 v[100:103], v76, s[64:67], s88 offen offset:0 sc1\n"
"v_cvt_pk_bf16_f32 v77, v71, v71\n"
"v_and_b32 v77, s74, v77\n"
"v_sub_f32 v71, v71, v77\n"
"v_cvt_pk_bf16_f32 v100, v77, v71\n"
"v_and_b32 v77, s74, v100\n"
"v_sub_f32 v71, v71, v77\n"
"v_cvt_pk_bf16_f32 v102, v71, 0\n"
"v_mov_b32 v101, s72\n"
"v_mov_b32 v103, s73\n"
"buffer_store_dwordx4 v[100:103], v76, s[64:67], s88 offen offset:1024 sc1\n"
"v_cvt_pk_bf16_f32 v77, v72, v72\n"
"v_and_b32 v77, s74, v77\n"
"v_sub_f32 v72, v72, v77\n"
"v_cvt_pk_bf16_f32 v100, v77, v72\n"
"v_and_b32 v77, s74, v100\n"
"v_sub_f32 v72, v72, v77\n"
"v_cvt_pk_bf16_f32 v102, v72, 0\n"
"v_mov_b32 v101, s72\n"
"v_mov_b32 v103, s73\n"
"buffer_store_dwordx4 v[100:103], v76, s[64:67], s88 offen offset:2048 sc1\n"
"v_and_b32 v68, 15, v27\n"
"v_and_b32 v69, 1, v68\n"
"v_lshlrev_b32 v69, 4, v69\n"
"v_mov_b32 v70, 0x3f80\n"
"v_lshlrev_b32 v69, v69, v70\n"
"v_lshrrev_b32 v68, 1, v68\n"
"v_cmp_eq_u32 vcc, 0, v68\n"
"s_nop 1\n"
"v_cndmask_b32 v84, 0, v69, vcc\n"
"v_cmp_eq_u32 vcc, 1, v68\n"
"s_nop 1\n"
"v_cndmask_b32 v85, 0, v69, vcc\n"
"v_cmp_eq_u32 vcc, 2, v68\n"
"s_nop 1\n"
"v_cndmask_b32 v86, 0, v69, vcc\n"
"v_cmp_eq_u32 vcc, 3, v68\n"
"s_nop 1\n"
"v_cndmask_b32 v87, 0, v69, vcc\n"
"s_waitcnt vmcnt(23)\n"
"s_nop 0\n"
"v_mfma_f32_16x16x32_bf16 v[80:83], v[160:163], v[84:87], 0\n"
"s_add_u32 m0, s10, 4096\n"
"s_nop 0\n"
"buffer_load_dwordx4 v22, s[56:59], s14 offen offset:0 lds\n"
"s_waitcnt vmcnt(23)\n"
"v_mfma_f32_16x16x32_bf16 v[80:83], v[164:167], v[84:87], v[80:83]\n"
"buffer_load_dwordx4 v23, s[56:59], s14 offen offset:1024 lds\n"
"s_waitcnt vmcnt(23)\n"
"v_mfma_f32_16x16x32_bf16 v[80:83], v[168:171], v[84:87], v[80:83]\n"
"s_add_u32 m0, s10, 6144\n"
"s_nop 0\n"
"buffer_load_dwordx4 v22, s[56:59], s48 offen offset:0 lds\n"
"s_waitcnt vmcnt(23)\n"
"v_mfma_f32_16x16x32_bf16 v[80:83], v[172:175], v[84:87], v[80:83]\n"
"buffer_load_dwordx4 v23, s[56:59], s48 offen offset:1024 lds\n"
"s_waitcnt vmcnt(23)\n"
"v_mfma_f32_16x16x32_bf16 v[80:83], v[176:179], v[84:87], v[80:83]\n"
"s_add_u32 m0, s10, 8192\n"
"s_nop 0\n"
"buffer_load_dwordx4 v22, s[56:59], s49 offen offset:0 lds\n"
"s_waitcnt vmcnt(23)\n"
"v_mfma_f32_16x16x32_bf16 v[80:83], v[180:183], v[84:87], v[80:83]\n"
"buffer_load_dwordx4 v23, s[56:59], s49 offen offset:1024 lds\n"
"s_waitcnt vmcnt(23)\n"
"v_mfma_f32_16x16x32_bf16 v[80:83], v[184:187], v[84:87], v[80:83]\n"
"s_add_u32 s14, s14, 128\n"
"s_add_u32 m0, s10, 10240\n"
"s_nop 0\n"
"buffer_load_dwordx4 v22, s[56:59], s14 offen offset:0 lds\n"
"s_waitcnt vmcnt(23)\n"
"v_mfma_f32_16x16x32_bf16 v[80:83], v[188:191], v[84:87], v[80:83]\n"
"buffer_load_dwordx4 v23, s[56:59], s14 offen offset:1024 lds\n"
"s_waitcnt vmcnt(23)\n"
"v_mfma_f32_16x16x32_bf16 v[80:83], v[192:195], v[84:87], v[80:83]\n"
"s_add_u32 s48, s48, 128\n"
"s_add_u32 m0, s10, 12288\n"
"s_nop 0\n"
"buffer_load_dwordx4 v22, s[56:59], s48 offen offset:0 lds\n"
"s_waitcnt vmcnt(23)\n"
"v_mfma_f32_16x16x32_bf16 v[80:83], v[196:199], v[84:87], v[80:83]\n"
"buffer_load_dwordx4 v23, s[56:59], s48 offen offset:1024 lds\n"
"s_waitcnt vmcnt(23)\n"
"v_mfma_f32_16x16x32_bf16 v[80:83], v[200:203], v[84:87], v[80:83]\n"
"s_add_u32 s49, s49, 128\n"
"s_add_u32 m0, s10, 14336\n"
"s_nop 0\n"
"buffer_load_dwordx4 v22, s[56:59], s49 offen offset:0 lds\n"
"s_waitcnt vmcnt(23)\n"
"v_mfma_f32_16x16x32_bf16 v[80:83], v[204:207], v[84:87], v[80:83]\n"
"buffer_load_dwordx4 v23, s[56:59], s49 offen offset:1024 lds\n"
"s_waitcnt vmcnt(23)\n"
"v_mfma_f32_16x16x32_bf16 v[80:83], v[208:211], v[84:87], v[80:83]\n"
"s_add_u32 s12, s12, 128\n"
"s_add_u32 m0, s10, 28672\n"
"s_nop 0\n"
"buffer_load_dwordx4 v22, s[44:47], s12 offen offset:0 lds\n"
"s_waitcnt vmcnt(23)\n"
"v_mfma_f32_16x16x32_bf16 v[80:83], v[212:215], v[84:87], v[80:83]\n"
"buffer_load_dwordx4 v23, s[44:47], s12 offen offset:1024 lds\n"
"s_waitcnt vmcnt(23)\n"
"v_mfma_f32_16x16x32_bf16 v[80:83], v[216:219], v[84:87], v[80:83]\n"
"buffer_load_dwordx4 v24, s[44:47], s12 offen offset:2048 lds\n"
"s_waitcnt vmcnt(23)\n"
"v_mfma_f32_16x16x32_bf16 v[80:83], v[220:223], v[84:87], v[80:83]\n"
"buffer_load_dwordx4 v25, s[44:47], s12 offen offset:3072 lds\n"
"s_waitcnt vmcnt(23)\n"
"v_mfma_f32_16x16x32_bf16 v[80:83], v[224:227], v[84:87], v[80:83]\n"
"s_waitcnt vmcnt(22)\n"
"v_mfma_f32_16x16x32_bf16 v[80:83], v[228:231], v[84:87], v[80:83]\n"
"s_waitcnt vmcnt(21)\n"
"v_mfma_f32_16x16x32_bf16 v[80:83], v[232:235], v[84:87], v[80:83]\n"
"s_waitcnt vmcnt(20)\n"
"v_mfma_f32_16x16x32_bf16 v[80:83], v[236:239], v[84:87], v[80:83]\n"
"v_and_b32 v68, 7, v27\n"
"v_bfe_u32 v69, v27, 4, 1\n"
"v_lshl_add_u32 v68, v69, 5, v68\n"
"v_lshl_add_u32 v26, v68, 2, s10\n"
"s_nop 3\n"
"v_mov_b32 v160, v80\n"
"v_mov_b32 v161, v81\n"
"v_mov_b32 v162, v82\n"
"v_mov_b32 v163, v83\n"
"v_permlane32_swap_b32 v160, v80\n"
"v_permlane32_swap_b32 v161, v81\n"
"v_permlane32_swap_b32 v162, v82\n"
"v_permlane32_swap_b32 v163, v83\n"
"v_add_f32 v160, v160, v80\n"
"v_add_f32 v161, v161, v81\n"
"v_add_f32 v162, v162, v82\n"
"v_add_f32 v163, v163, v83\n"
"s_mov_b64 exec, 0xff00ff\n"
"ds_write_b32 v26, v160 offset:1024\n"
"ds_write_b32 v26, v161 offset:1056\n"
"ds_write_b32 v26, v162 offset:1088\n"
"ds_write_b32 v26, v163 offset:1120\n"
"s_mov_b64 exec, -1\n"
"s_waitcnt lgkmcnt(0)\n"
"s_barrier\n"
"s_lshl_b32 s68, s7, 8\n"
"v_lshl_add_u32 v69, v27, 2, s68\n"
"v_add_u32 v70, 81920, v69\n"
"ds_read_b32 v160, v69 offset:1024\n"
"ds_read_b32 v161, v69 offset:41984\n"
"ds_read_b32 v162, v70 offset:1024\n"
"ds_read_b32 v163, v70 offset:41984\n"
"v_lshrrev_b32 v69, 4, v27\n"
"v_lshlrev_b32 v69, 5, v69\n"
"v_bfe_u32 v70, v27, 2, 1\n"
"v_lshl_add_u32 v69, v70, 4, v69\n"
"v_bfe_u32 v70, v27, 3, 1\n"
"v_lshl_add_u32 v69, v70, 2, v69\n"
"v_and_b32 v70, 3, v27\n"
"v_add_u32 v69, v69, v70\n"
"v_lshlrev_b32 v69, 4, v69\n"
"s_lshl_b32 s69, s96, 7\n"
"s_lshl_b32 s70, s42, 11\n"
"s_add_u32 s69, s69, s70\n"
"s_add_u32 s69, s69, 21037056\n"
"s_cmp_lt_u32 s42, 27\n"
"s_cselect_b32 s68, s69, 2130706432\n"
"s_cmp_lt_u32 s7, 1\n"
"s_cselect_b32 s68, s68, 2130706432\n"
"s_waitcnt lgkmcnt(0)\n"
"v_add_f32 v160, v160, v161\n"
"v_add_f32 v162, v162, v163\n"
"v_add_f32 v160, v160, v162\n"
"v_cvt_pk_bf16_f32 v71, v160, v160\n"
"v_and_b32 v71, s74, v71\n"
"v_sub_f32 v160, v160, v71\n"
"v_cvt_pk_bf16_f32 v164, v71, v160\n"
"v_and_b32 v71, s74, v164\n"
"v_sub_f32 v160, v160, v71\n"
"v_cvt_pk_bf16_f32 v166, v160, 0\n"
"v_mov_b32 v165, s72\n"
"v_mov_b32 v167, s73\n"
"buffer_store_dwordx4 v[164:167], v69, s[64:67], s68 offen sc1\n"
"ds_read_b128 a[0:3], v14 offset:32768\n"
"ds_read_b128 a[4:7], v15 offset:32768\n"
"ds_read_b128 a[8:11], v16 offset:32768\n"
"ds_read_b128 a[12:15], v17 offset:32768\n"
"s_waitcnt vmcnt(11)\n"
"s_barrier\n"
"ds_read_b128 a[160:163], v10 offset:4096\n"
"ds_read_b128 a[164:167], v152 offset:4096\n"
"ds_read_b128 a[168:171], v10 offset:6144\n"
"ds_read_b128 a[172:175], v152 offset:6144\n"
"ds_read_b128 a[176:179], v10 offset:8192\n"
"ds_read_b128 a[180:183], v152 offset:8192\n"
"ds_read_b128 a[184:187], v11 offset:4096\n"
"ds_read_b128 a[188:191], v153 offset:4096\n"
"s_waitcnt lgkmcnt(4)\n"
"v_mfma_f32_32x32x16_bf16 v[160:175], a[0:3], a[160:163], 0\n"
"ds_read_b128 a[160:163], v11 offset:6144\n"
"s_add_u32 m0, s10, 0\n"
"s_nop 0\n"
"buffer_load_dwordx4 v22, s[60:63], 0 offen offset:0 lds\n"
"s_and_b32 s68, s6, 6\n"
"s_lshl_b32 s30, s68, 1\n"
"s_lshr_b32 s68, s6, 6\n"
"v_mfma_f32_32x32x16_bf16 v[176:191], a[0:3], a[164:167], 0\n"
"ds_read_b128 a[164:167], v153 offset:6144\n"
"s_add_u32 s30, s30, s68\n"
"s_bfe_u32 s31, s6, 0x30003\n"
"s_and_b32 s68, s6, 1\n"
"s_lshl_b32 s68, s68, 2\n"
"s_add_u32 s68, s68, s7\n"
"s_cmp_ge_u32 s68, 6\n"
"s_cselect_b32 s37, 11, 12\n"
"v_mfma_f32_32x32x16_bf16 v[192:207], a[0:3], a[168:171], 0\n"
"ds_read_b128 a[168:171], v11 offset:8192\n"
"buffer_load_dwordx4 v23, s[60:63], 0 offen offset:1024 lds\n"
"s_mul_i32 s32, s68, 49152\n"
"s_mul_i32 s68, s68, 96\n"
"s_add_u32 s68, s68, s31\n"
"s_mul_i32 s68, s68, 5120\n"
"v_mfma_f32_32x32x16_bf16 v[208:223], a[0:3], a[172:175], 0\n"
"ds_read_b128 a[172:175], v153 offset:8192\n"
"s_add_u32 s68, s68, 16777216\n"
"s_add_u32 s76, s38, s68\n"
"s_addc_u32 s77, s39, 0\n"
"s_and_b32 s77, s77, 0xffff\n"
"s_mul_i32 s78, s37, 40960\n"
"s_sub_u32 s78, s78, 35840\n"
"s_waitcnt lgkmcnt(4)\n"
"v_mfma_f32_32x32x16_bf16 v[224:239], a[0:3], a[176:179], 0\n"
"ds_read_b128 a[176:179], v12 offset:4096\n"
"buffer_load_dwordx4 v24, s[60:63], 0 offen offset:2048 lds\n"
"s_mov_b32 s79, 0x20000\n"
"s_lshl_b32 s68, s6, 2\n"
"s_add_u32 s68, s68, s7\n"
"s_mul_i32 s34, s68, 5120\n"
"v_mfma_f32_32x32x16_bf16 v[240:255], a[0:3], a[180:183], 0\n"
"ds_read_b128 a[180:183], v154 offset:4096\n"
"s_xor_b32 s68, s68, 4\n"
"s_mul_i32 s35, s68, 5120\n"
"s_add_u32 s34, s34, 24117248\n"
"s_add_u32 s35, s35, 24117248\n"
"s_lshl_b32 s36, s8, 4\n"
"s_add_u32 s36, s36, s9\n"
"v_mfma_f32_32x32x16_bf16 v[160:175], a[4:7], a[184:187], v[160:175]\n"
"ds_read_b128 a[184:187], v12 offset:6144\n"
"buffer_load_dwordx4 v25, s[60:63], 0 offen offset:3072 lds\n"
"s_lshl_b32 s36, s36, 4\n"
"s_add_u32 s36, s36, s7\n"
"s_lshl_b32 s36, s36, 3\n"
"s_add_u32 s36, s36, 23134208\n"
"v_mfma_f32_32x32x16_bf16 v[176:191], a[4:7], a[188:191], v[176:191]\n"
"s_waitcnt lgkmcnt(3)\n"
"ds_read_b128 a[188:191], v154 offset:6144\n"
"s_add_u32 s12, s12, 128\n"
"s_add_u32 m0, s10, 32768\n"
"s_and_b32 s68, s6, 1\n"
"s_xor_b32 s68, s68, 0\n"
"s_lshl_b32 s69, s30, 1\n"
"s_add_u32 s68, s68, s69\n"
"v_mfma_f32_32x32x16_bf16 v[192:207], a[4:7], a[160:163], v[192:207]\n"
"ds_read_b128 a[160:163], v12 offset:8192\n"
"buffer_load_dwordx4 v22, s[44:47], s12 offen offset:0 lds\n"
"s_mul_i32 s68, s68, 393216\n"
"s_add_u32 s68, s68, s32\n"
"s_add_u32 s80, s38, s68\n"
"s_addc_u32 s81, s39, 0\n"
"v_mfma_f32_32x32x16_bf16 v[208:223], a[4:7], a[164:167], v[208:223]\n"
"ds_read_b128 a[164:167], v154 offset:8192\n"
"s_and_b32 s81, s81, 0xffff\n"
"s_lshl_b32 s82, s37, 12\n"
"s_mov_b32 s83, 0x20000\n"
"s_and_b32 s68, s6, 1\n"
"s_xor_b32 s68, s68, 1\n"
"s_lshl_b32 s69, s30, 1\n"
"v_mfma_f32_32x32x16_bf16 v[224:239], a[4:7], a[168:171], v[224:239]\n"
"ds_read_b128 a[168:171], v13 offset:4096\n"
"buffer_load_dwordx4 v23, s[44:47], s12 offen offset:1024 lds\n"
"s_add_u32 s68, s68, s69\n"
"s_mul_i32 s68, s68, 393216\n"
"s_add_u32 s68, s68, s32\n"
"s_add_u32 s84, s38, s68\n"
"s_addc_u32 s85, s39, 0\n"
"v_mfma_f32_32x32x16_bf16 v[240:255], a[4:7], a[172:175], v[240:255]\n"
"s_waitcnt lgkmcnt(3)\n"
"ds_read_b128 a[172:175], v155 offset:4096\n"
"s_and_b32 s85, s85, 0xffff\n"
"s_lshl_b32 s86, s37, 12\n"
"s_mov_b32 s87, 0x20000\n"
"v_lshrrev_b32 v68, 4, v27\n"
"v_bfe_u32 v69, v27, 2, 2\n"
"v_and_b32 v70, 3, v27\n"
"v_lshl_add_u32 v69, v68, 3, v69\n"
"v_mfma_f32_32x32x16_bf16 v[160:175], a[8:11], a[176:179], v[160:175]\n"
"ds_read_b128 a[176:179], v13 offset:6144\n"
"buffer_load_dwordx4 v24, s[44:47], s12 offen offset:2048 lds\n"
"v_mul_u32_u24 v34, 72, v69\n"
"v_lshl_add_u32 v34, v70, 3, v34\n"
"s_add_u32 s68, s10, 2048\n"
"v_add_u32 v34, s68, v34\n"
"v_bfe_u32 v71, v69, 1, 3\n"
"v_mfma_f32_32x32x16_bf16 v[176:191], a[8:11], a[180:183], v[176:191]\n"
"ds_read_b128 a[180:183], v155 offset:6144\n"
"v_lshlrev_b32 v71, 4, v71\n"
"v_lshl_add_u32 v69, v69, 7, s10\n"
"v_lshlrev_b32 v70, 3, v70\n"
"v_xor_b32 v70, v70, v71\n"
"v_add_u32 v40, v69, v70\n"
"v_xor_b32 v41, 32, v40\n"
"v_xor_b32 v42, 64, v40\n"
"v_xor_b32 v43, 96, v40\n"
"v_and_b32 v70, 15, v27\n"
"v_mfma_f32_32x32x16_bf16 v[192:207], a[8:11], a[184:187], v[192:207]\n"
"ds_read_b128 a[184:187], v13 offset:8192\n"
"buffer_load_dwordx4 v25, s[44:47], s12 offen offset:3072 lds\n"
"v_lshrrev_b32 v71, 1, v70\n"
"v_xor_b32 v71, v71, v68\n"
"v_lshl_add_u32 v70, v70, 7, s10\n"
"v_lshl_add_u32 v36, v71, 4, v70\n"
"v_xor_b32 v37, 64, v36\n"
"v_mfma_f32_32x32x16_bf16 v[208:223], a[8:11], a[188:191], v[208:223]\n"
"s_waitcnt lgkmcnt(3)\n"
"ds_read_b128 a[188:191], v155 offset:8192\n"
"s_add_u32 s14, s14, 128\n"
"s_add_u32 m0, s10, 16384\n"
"v_mfma_f32_32x32x16_bf16 v[224:239], a[8:11], a[160:163], v[224:239]\n"
"s_waitcnt vmcnt(13)\n"
"s_barrier\n"
"ds_read_b128 a[160:163], v10 offset:10240\n"
"buffer_load_dwordx4 v22, s[56:59], s14 offen offset:0 lds\n"
"v_mfma_f32_32x32x16_bf16 v[240:255], a[8:11], a[164:167], v[240:255]\n"
"ds_read_b128 a[164:167], v152 offset:10240\n"
"v_mfma_f32_32x32x16_bf16 v[160:175], a[12:15], a[168:171], v[160:175]\n"
"s_waitcnt vmcnt(10)\n"
"ds_read_b128 a[168:171], v10 offset:12288\n"
"ds_read_b128 a[16:19], v14 offset:28672\n"
"buffer_load_dwordx4 v23, s[56:59], s14 offen offset:1024 lds\n"
"v_mfma_f32_32x32x16_bf16 v[176:191], a[12:15], a[172:175], v[176:191]\n"
"s_waitcnt lgkmcnt(4)\n"
"ds_read_b128 a[172:175], v152 offset:12288\n"
"ds_read_b128 a[20:23], v15 offset:28672\n"
"s_add_u32 s48, s48, 128\n"
"s_add_u32 m0, s10, 18432\n"
"v_mfma_f32_32x32x16_bf16 v[192:207], a[12:15], a[176:179], v[192:207]\n"
"ds_read_b128 a[176:179], v10 offset:14336\n"
"ds_read_b128 a[24:27], v16 offset:28672\n"
"buffer_load_dwordx4 v22, s[56:59], s48 offen offset:0 lds\n"
"v_mfma_f32_32x32x16_bf16 v[208:223], a[12:15], a[180:183], v[208:223]\n"
"ds_read_b128 a[180:183], v152 offset:14336\n"
"ds_read_b128 a[28:31], v17 offset:28672\n"
"v_mfma_f32_32x32x16_bf16 v[224:239], a[12:15], a[184:187], v[224:239]\n"
"ds_read_b128 a[184:187], v11 offset:10240\n"
"buffer_load_dwordx4 v23, s[56:59], s48 offen offset:1024 lds\n"
"v_mfma_f32_32x32x16_bf16 v[240:255], a[12:15], a[188:191], v[240:255]\n"
"s_waitcnt lgkmcnt(7)\n"
"ds_read_b128 a[188:191], v153 offset:10240\n"
"s_add_u32 s49, s49, 128\n"
"s_add_u32 m0, s10, 20480\n"
"v_mfma_f32_32x32x16_bf16 v[160:175], a[16:19], a[160:163], v[160:175]\n"
"ds_read_b128 a[160:163], v11 offset:12288\n"
"buffer_load_dwordx4 v22, s[56:59], s49 offen offset:0 lds\n"
"v_mfma_f32_32x32x16_bf16 v[176:191], a[16:19], a[164:167], v[176:191]\n"
"ds_read_b128 a[164:167], v153 offset:12288\n"
"v_mfma_f32_32x32x16_bf16 v[192:207], a[16:19], a[168:171], v[192:207]\n"
"ds_read_b128 a[168:171], v11 offset:14336\n"
"buffer_load_dwordx4 v23, s[56:59], s49 offen offset:1024 lds\n"
"s_waitcnt lgkmcnt(10)\n"
"v_mfma_f32_32x32x16_bf16 v[208:223], a[16:19], a[172:175], v[208:223]\n"
"s_waitcnt lgkmcnt(3)\n"
"ds_read_b128 a[172:175], v153 offset:14336\n"
"s_add_u32 s12, s12, 128\n"
"s_add_u32 m0, s10, 28672\n"
"v_mfma_f32_32x32x16_bf16 v[224:239], a[16:19], a[176:179], v[224:239]\n"
"s_barrier\n"
"ds_read_b128 a[176:179], v12 offset:10240\n"
"buffer_load_dwordx4 v22, s[44:47], s12 offen offset:0 lds\n"
"v_mfma_f32_32x32x16_bf16 v[240:255], a[16:19], a[180:183], v[240:255]\n"
"ds_read_b128 a[180:183], v154 offset:10240\n"
"v_mfma_f32_32x32x16_bf16 v[160:175], a[20:23], a[184:187], v[160:175]\n"
"ds_read_b128 a[184:187], v12 offset:12288\n"
"buffer_load_dwordx4 v23, s[44:47], s12 offen offset:1024 lds\n"
"v_mfma_f32_32x32x16_bf16 v[176:191], a[20:23], a[188:191], v[176:191]\n"
"s_waitcnt lgkmcnt(3)\n"
"ds_read_b128 a[188:191], v154 offset:12288\n"
"v_mfma_f32_32x32x16_bf16 v[192:207], a[20:23], a[160:163], v[192:207]\n"
"ds_read_b128 a[160:163], v12 offset:14336\n"
"buffer_load_dwordx4 v24, s[44:47], s12 offen offset:2048 lds\n"
"v_mfma_f32_32x32x16_bf16 v[208:223], a[20:23], a[164:167], v[208:223]\n"
"ds_read_b128 a[164:167], v154 offset:14336\n"
"v_mfma_f32_32x32x16_bf16 v[224:239], a[20:23], a[168:171], v[224:239]\n"
"ds_read_b128 a[168:171], v13 offset:10240\n"
"buffer_load_dwordx4 v25, s[44:47], s12 offen offset:3072 lds\n"
"v_mfma_f32_32x32x16_bf16 v[240:255], a[20:23], a[172:175], v[240:255]\n"
"s_waitcnt lgkmcnt(3)\n"
"ds_read_b128 a[172:175], v155 offset:10240\n"
"s_add_u32 s14, s14, 128\n"
"s_add_u32 m0, s10, 4096\n"
"v_mfma_f32_32x32x16_bf16 v[160:175], a[24:27], a[176:179], v[160:175]\n"
"ds_read_b128 a[176:179], v13 offset:12288\n"
"buffer_load_dwordx4 v22, s[56:59], s14 offen offset:0 lds\n"
"v_mfma_f32_32x32x16_bf16 v[176:191], a[24:27], a[180:183], v[176:191]\n"
"ds_read_b128 a[180:183], v155 offset:12288\n"
"v_mfma_f32_32x32x16_bf16 v[192:207], a[24:27], a[184:187], v[192:207]\n"
"ds_read_b128 a[184:187], v13 offset:14336\n"
"buffer_load_dwordx4 v23, s[56:59], s14 offen offset:1024 lds\n"
"v_mfma_f32_32x32x16_bf16 v[208:223], a[24:27], a[188:191], v[208:223]\n"
"s_waitcnt vmcnt(12) lgkmcnt(3)\n"
"ds_read_b128 a[188:191], v155 offset:14336\n"
"ds_read_b128 a[32:35], v14 offset:32768\n"
"s_add_u32 s48, s48, 128\n"
"s_add_u32 m0, s10, 6144\n"
"v_mfma_f32_32x32x16_bf16 v[224:239], a[24:27], a[160:163], v[224:239]\n"
"s_waitcnt vmcnt(6)\n"
"s_barrier\n"
"ds_read_b128 a[160:163], v10 offset:16384\n"
"ds_read_b128 a[36:39], v15 offset:32768\n"
"buffer_load_dwordx4 v22, s[56:59], s48 offen offset:0 lds\n"
"v_mfma_f32_32x32x16_bf16 v[240:255], a[24:27], a[164:167], v[240:255]\n"
"ds_read_b128 a[164:167], v152 offset:16384\n"
"ds_read_b128 a[40:43], v16 offset:32768\n"
"s_cmp_eq_u32 s75, 0\n"
"s_cbranch_scc1 K1S_rtc\n"
"ds_read_b64_tr_b16 v[80:81], v40 offset:0\n"
"ds_read_b64_tr_b16 v[82:83], v41 offset:512\n"
"ds_read_b64_tr_b16 v[84:85], v41 offset:0\n"
"ds_read_b64_tr_b16 v[86:87], v40 offset:512\n"
"ds_read_b64_tr_b16 v[88:89], v42 offset:0\n"
"ds_read_b64_tr_b16 v[90:91], v43 offset:512\n"
"ds_read_b64_tr_b16 v[92:93], v43 offset:0\n"
"ds_read_b64_tr_b16 v[94:95], v42 offset:512\n"
"s_branch K1S_rtd\n"
"K1S_rtc:\n"
"ds_read_b128 v[80:83], v36 offset:0\n"
"ds_read_b128 v[84:87], v36 offset:2048\n"
"ds_read_b128 v[88:91], v37 offset:0\n"
"ds_read_b128 v[92:95], v37 offset:2048\n"
"K1S_rtd:\n"
"v_mfma_f32_32x32x16_bf16 v[160:175], a[28:31], a[168:171], v[160:175]\n"
"ds_read_b128 a[168:171], v10 offset:18432\n"
"ds_read_b128 a[44:47], v17 offset:32768\n"
"buffer_load_dwordx4 v23, s[56:59], s48 offen offset:1024 lds\n"
"v_mfma_f32_32x32x16_bf16 v[176:191], a[28:31], a[172:175], v[176:191]\n"
"s_waitcnt lgkmcnt(11)\n"
"ds_read_b128 a[172:175], v152 offset:18432\n"
"s_add_u32 s49, s49, 128\n"
"s_add_u32 m0, s10, 8192\n"
"v_mfma_f32_32x32x16_bf16 v[192:207], a[28:31], a[176:179], v[192:207]\n"
"ds_read_b128 a[176:179], v10 offset:20480\n"
"buffer_load_dwordx4 v22, s[56:59], s49 offen offset:0 lds\n"
"v_mfma_f32_32x32x16_bf16 v[208:223], a[28:31], a[180:183], v[208:223]\n"
"ds_read_b128 a[180:183], v152 offset:20480\n"
"s_mov_b32 s16, 0\n"
"s_mov_b32 s17, -1\n"
"v_lshrrev_b32 v68, 5, v27\n"
"s_lshl_b32 s68, s7, 2\n"
"v_add_u32 v69, s68, v68\n"
"v_cmp_gt_u32 s[18:19], s8, v69\n"
"v_add_u32 v69, 2, v69\n"
"v_cmp_gt_u32 s[20:21], s8, v69\n"
"v_mfma_f32_32x32x16_bf16 v[224:239], a[28:31], a[184:187], v[224:239]\n"
"ds_read_b128 a[184:187], v11 offset:16384\n"
"buffer_load_dwordx4 v23, s[56:59], s49 offen offset:1024 lds\n"
"v_and_b32 v70, 31, v27\n"
"s_lshl_b32 s68, s7, 9\n"
"v_lshl_add_u32 v29, v27, 3, s68\n"
"v_lshlrev_b32 v30, 3, v70\n"
"v_mfma_f32_32x32x16_bf16 v[240:255], a[28:31], a[188:191], v[240:255]\n"
"s_waitcnt lgkmcnt(3)\n"
"ds_read_b128 a[188:191], v153 offset:16384\n"
"s_add_u32 s12, s12, 128\n"
"s_add_u32 m0, s10, 32768\n"
"buffer_store_dwordx4 v[80:83], v28, s[64:67], s28 offen offset:0 sc1\n"
"v_add_u32 v30, 256, v30\n"
"v_lshlrev_b32 v32, 6, v70\n"
"v_mfma_f32_32x32x16_bf16 v[160:175], a[32:35], a[160:163], v[160:175]\n"
"ds_read_b128 a[160:163], v11 offset:18432\n"
"buffer_load_dwordx4 v22, s[44:47], s12 offen offset:0 lds\n"
"v_add_u32 v32, 122880, v32\n"
"s_lshl_b32 s68, s7, 4\n"
"v_lshl_add_u32 v31, v68, 3, v32\n"
"v_add_u32 v31, s68, v31\n"
"v_mfma_f32_32x32x16_bf16 v[176:191], a[32:35], a[164:167], v[176:191]\n"
"ds_read_b128 a[164:167], v153 offset:18432\n"
"s_add_u32 s68, s10, 2048\n"
"v_mul_u32_u24 v33, 72, v70\n"
"v_lshl_add_u32 v33, v68, 3, v33\n"
"v_add_u32 v33, s68, v33\n"
"v_lshl_add_u32 v71, v70, 4, s89\n"
"v_mov_b32 v72, 2130706432\n"
"v_cndmask_b32 v35, v72, v71, s[16:17]\n"
"v_and_b32 v68, 31, v27\n"
"v_mfma_f32_32x32x16_bf16 v[192:207], a[32:35], a[168:171], v[192:207]\n"
"ds_read_b128 a[168:171], v11 offset:20480\n"
"buffer_load_dwordx4 v23, s[44:47], s12 offen offset:1024 lds\n"
"v_lshrrev_b32 v69, 5, v27\n"
"v_lshl_add_u32 v68, v68, 4, s90\n"
"v_lshlrev_b32 v69, 9, v69\n"
"v_sub_u32 v68, v68, v69\n"
"v_mov_b32 v69, 2130706432\n"
"v_mfma_f32_32x32x16_bf16 v[208:223], a[32:35], a[172:175], v[208:223]\n"
"s_waitcnt lgkmcnt(3)\n"
"ds_read_b128 a[172:175], v153 offset:20480\n"
"v_cndmask_b32 v38, v69, v68, s[18:19]\n"
"v_subrev_u32 v68, 1024, v68\n"
"v_cndmask_b32 v39, v69, v68, s[20:21]\n"
"v_add_u32 v68, 9216, v68\n"
"v_cndmask_b32 v18, v69, v68, s[18:19]\n"
"v_mfma_f32_32x32x16_bf16 v[224:239], a[32:35], a[176:179], v[224:239]\n"
"s_barrier\n"
"ds_read_b128 a[176:179], v12 offset:16384\n"
"buffer_load_dwordx4 v24, s[44:47], s12 offen offset:2048 lds\n"
"buffer_store_dwordx4 v[84:87], v28, s[64:67], s28 offen offset:1024 sc1\n"
"v_subrev_u32 v68, 1024, v68\n"
"v_mfma_f32_32x32x16_bf16 v[240:255], a[32:35], a[180:183], v[240:255]\n"
"ds_read_b128 a[180:183], v154 offset:16384\n"
"v_cndmask_b32 v19, v69, v68, s[20:21]\n"
"v_add_u32 v68, 9216, v68\n"
"v_cndmask_b32 v20, v69, v68, s[18:19]\n"
"v_subrev_u32 v68, 1024, v68\n"
"v_cndmask_b32 v21, v69, v68, s[20:21]\n"
"v_lshlrev_b32 v70, 16, v138\n"
"v_lshlrev_b32 v68, 16, v136\n"
"v_mul_f32 v70, 0x3fb8aa3b, v70\n"
"v_lshlrev_b32 v69, 16, v137\n"
"v_mfma_f32_32x32x16_bf16 v[160:175], a[36:39], a[184:187], v[160:175]\n"
"ds_read_b128 a[184:187], v12 offset:18432\n"
"buffer_load_dwordx4 v25, s[44:47], s12 offen offset:3072 lds\n"
"v_exp_f32 v70, v70\n"
"v_lshrrev_b32 v73, 5, v27\n"
"v_cvt_pk_bf16_f32 v70, v70, v70\n"
"v_cvt_f32_u32 v73, v73\n"
"v_mfma_f32_32x32x16_bf16 v[176:191], a[36:39], a[188:191], v[176:191]\n"
"s_waitcnt lgkmcnt(3)\n"
"ds_read_b128 a[188:191], v154 offset:18432\n"
"s_add_u32 s14, s14, 128\n"
"s_add_u32 m0, s10, 2048\n"
"v_lshlrev_b32 v70, 16, v70\n"
"v_mul_f32 v71, v70, v69\n"
"v_mul_f32 v70, v70, v68\n"
"v_mul_f32 v71, 0x3e22f983, v71\n"
"v_mul_f32 v70, 0x3fb8aa3b, v70\n"
"v_mfma_f32_32x32x16_bf16 v[192:207], a[36:39], a[160:163], v[192:207]\n"
"ds_read_b128 a[160:163], v12 offset:20480\n"
"buffer_load_dwordx4 v22, s[56:59], s14 offen offset:0 lds\n"
"v_mul_f32 v74, 0x3f800000, v70\n"
"v_mul_f32 v75, 0x3f800000, v71\n"
"v_exp_f32 v74, v74\n"
"v_fract_f32 v75, v75\n"
"v_mfma_f32_32x32x16_bf16 v[208:223], a[36:39], a[164:167], v[208:223]\n"
"ds_read_b128 a[164:167], v154 offset:20480\n"
"v_cos_f32 v44, v75\n"
"v_mfma_f32_32x32x16_bf16 v[224:239], a[36:39], a[168:171], v[224:239]\n"
"ds_read_b128 a[168:171], v13 offset:16384\n"
"buffer_load_dwordx4 v23, s[56:59], s14 offen offset:1024 lds\n"
"buffer_store_dwordx4 v[88:91], v28, s[64:67], s29 offen offset:0 sc1\n"
"v_sin_f32 v45, v75\n"
"v_mfma_f32_32x32x16_bf16 v[240:255], a[36:39], a[172:175], v[240:255]\n"
"s_waitcnt vmcnt(15) lgkmcnt(3)\n"
"ds_read_b128 a[172:175], v155 offset:16384\n"
"ds_read_b128 a[48:51], v14 offset:28672\n"
"s_add_u32 s48, s48, 128\n"
"s_add_u32 m0, s10, 10240\n"
"v_mul_f32 v44, v44, v74\n"
"v_mul_f32 v45, v45, v74\n"
"v_mul_f32 v72, 0x41800000, v73\n"
"v_mfma_f32_32x32x16_bf16 v[160:175], a[40:43], a[176:179], v[160:175]\n"
"ds_read_b128 a[176:179], v13 offset:18432\n"
"ds_read_b128 a[52:55], v15 offset:28672\n"
"buffer_load_dwordx4 v22, s[56:59], s48 offen offset:0 lds\n"
"v_mul_f32 v74, v72, v70\n"
"v_mul_f32 v75, v72, v71\n"
"v_exp_f32 v74, v74\n"
"v_mfma_f32_32x32x16_bf16 v[176:191], a[40:43], a[180:183], v[176:191]\n"
"ds_read_b128 a[180:183], v155 offset:18432\n"
"ds_read_b128 a[56:59], v16 offset:28672\n"
"v_fract_f32 v75, v75\n"
"v_cos_f32 v46, v75\n"
"v_sin_f32 v47, v75\n"
"v_mul_f32 v46, v46, v74\n"
"v_mul_f32 v47, v47, v74\n"
"v_mul_f32 v74, 0x42000000, v70\n"
"v_mfma_f32_32x32x16_bf16 v[192:207], a[40:43], a[184:187], v[192:207]\n"
"ds_read_b128 a[184:187], v13 offset:20480\n"
"ds_read_b128 a[60:63], v17 offset:28672\n"
"buffer_load_dwordx4 v23, s[56:59], s48 offen offset:1024 lds\n"
"v_mul_f32 v75, 0x42000000, v71\n"
"v_exp_f32 v74, v74\n"
"v_fract_f32 v75, v75\n"
"v_mfma_f32_32x32x16_bf16 v[208:223], a[40:43], a[188:191], v[208:223]\n"
"s_waitcnt lgkmcnt(7)\n"
"ds_read_b128 a[188:191], v155 offset:20480\n"
"s_add_u32 s49, s49, 128\n"
"s_add_u32 m0, s10, 12288\n"
"v_cos_f32 v54, v75\n"
"v_sin_f32 v55, v75\n"
"v_mul_f32 v54, v54, v74\n"
"v_mfma_f32_32x32x16_bf16 v[224:239], a[40:43], a[160:163], v[224:239]\n"
"s_waitcnt vmcnt(11)\n"
"s_barrier\n"
"ds_read_b128 a[160:163], v10 offset:4096\n"
"buffer_load_dwordx4 v22, s[56:59], s49 offen offset:0 lds\n"
"v_mul_f32 v55, v55, v74\n"
"v_mul_f32 v74, v55, v55\n"
"v_mul_f32 v75, v54, v55\n"
"v_mfma_f32_32x32x16_bf16 v[240:255], a[40:43], a[164:167], v[240:255]\n"
"ds_read_b128 a[164:167], v152 offset:4096\n"
"v_fma_f32 v74, v54, v54, -v74\n"
"v_add_f32 v75, v75, v75\n"
"v_mfma_f32_32x32x16_bf16 v[160:175], a[44:47], a[168:171], v[160:175]\n"
"ds_read_b128 a[168:171], v10 offset:6144\n"
"buffer_load_dwordx4 v23, s[56:59], s49 offen offset:1024 lds\n"
"buffer_store_dwordx4 v[92:95], v28, s[64:67], s29 offen offset:1024 sc1\n"
"s_cmp_eq_u32 s7, 3\n"
"s_cselect_b32 s68, 1.0, 0\n"
"v_mfma_f32_32x32x16_bf16 v[176:191], a[44:47], a[172:175], v[176:191]\n"
"s_waitcnt lgkmcnt(3)\n"
"ds_read_b128 a[172:175], v152 offset:6144\n"
"s_add_u32 s12, s12, 128\n"
"s_add_u32 m0, s10, 28672\n"
"s_cmp_eq_u32 s7, 2\n"
"s_cselect_b32 s69, 1.0, 0\n"
"s_cmp_eq_u32 s7, 1\n"
"s_cselect_b32 s70, 1.0, 0\n"
"v_mfma_f32_32x32x16_bf16 v[192:207], a[44:47], a[176:179], v[192:207]\n"
"ds_read_b128 a[176:179], v10 offset:8192\n"
"buffer_load_dwordx4 v22, s[44:47], s12 offen offset:0 lds\n"
"v_mov_b32 v52, s68\n"
"v_mov_b32 v53, 0\n"
"v_mov_b32 v50, s69\n"
"v_mul_f32 v51, s68, v55\n"
"v_fmac_f32 v50, s68, v54\n"
"v_mfma_f32_32x32x16_bf16 v[208:223], a[44:47], a[180:183], v[208:223]\n"
"ds_read_b128 a[180:183], v152 offset:8192\n"
"v_mov_b32 v48, s70\n"
"v_mul_f32 v49, s69, v55\n"
"v_fmac_f32 v48, s69, v54\n"
"v_fmac_f32 v49, s68, v75\n"
"v_fmac_f32 v48, s68, v74\n"
"s_lshl_b32 s68, s7, 5\n"
"v_cvt_f32_u32 v72, s68\n"
"v_fmac_f32 v72, 0x41800000, v73\n"
"v_mfma_f32_32x32x16_bf16 v[224:239], a[44:47], a[184:187], v[224:239]\n"
"ds_read_b128 a[184:187], v11 offset:4096\n"
"buffer_load_dwordx4 v23, s[44:47], s12 offen offset:1024 lds\n"
"v_mul_f32 v74, v72, v70\n"
"v_mul_f32 v75, v72, v71\n"
"v_exp_f32 v74, v74\n"
"v_fract_f32 v75, v75\n"
"v_mfma_f32_32x32x16_bf16 v[240:255], a[44:47], a[188:191], v[240:255]\n"
"s_waitcnt lgkmcnt(3)\n"
"ds_read_b128 a[188:191], v153 offset:4096\n"
"v_cos_f32 v56, v75\n"
"v_sin_f32 v57, v75\n"
"v_mul_f32 v56, v56, v74\n"
"v_mul_f32 v57, v57, v74\n"
"s_lshl_b32 s68, s7, 9\n"
"v_mfma_f32_32x32x16_bf16 v[160:175], a[48:51], a[160:163], v[160:175]\n"
"ds_read_b128 a[160:163], v11 offset:6144\n"
"buffer_load_dwordx4 v24, s[44:47], s12 offen offset:2048 lds\n"
"s_add_u32 s68, s68, 0\n"
"v_cvt_f32_u32 v72, s68\n"
"v_fmac_f32 v72, 0x43000000, v73\n"
"v_mul_f32 v74, v72, v70\n"
"v_mul_f32 v75, v72, v71\n"
"v_mfma_f32_32x32x16_bf16 v[176:191], a[48:51], a[164:167], v[176:191]\n"
"ds_read_b128 a[164:167], v153 offset:6144\n"
"v_exp_f32 v74, v74\n"
"v_fract_f32 v75, v75\n"
"v_cos_f32 v58, v75\n"
"v_sin_f32 v59, v75\n"
"v_cndmask_b32 v74, 0, v74, s[18:19]\n"
"v_mul_f32 v58, v58, v74\n"
"v_mfma_f32_32x32x16_bf16 v[192:207], a[48:51], a[168:171], v[192:207]\n"
"ds_read_b128 a[168:171], v11 offset:8192\n"
"buffer_load_dwordx4 v25, s[44:47], s12 offen offset:3072 lds\n"
"v_mul_f32 v59, v59, v74\n"
"s_lshl_b32 s68, s7, 9\n"
"s_add_u32 s68, s68, 256\n"
"v_cvt_f32_u32 v72, s68\n"
"v_mfma_f32_32x32x16_bf16 v[208:223], a[48:51], a[172:175], v[208:223]\n"
"s_waitcnt lgkmcnt(3)\n"
"ds_read_b128 a[172:175], v153 offset:8192\n"
"s_add_u32 s14, s14, 128\n"
"s_add_u32 m0, s10, 14336\n"
"v_fmac_f32 v72, 0x43000000, v73\n"
"v_mul_f32 v74, v72, v70\n"
"v_mul_f32 v75, v72, v71\n"
"v_exp_f32 v74, v74\n"
"v_mfma_f32_32x32x16_bf16 v[224:239], a[48:51], a[176:179], v[224:239]\n"
"s_barrier\n"
"ds_read_b128 a[176:179], v12 offset:4096\n"
"buffer_load_dwordx4 v22, s[56:59], s14 offen offset:0 lds\n"
"v_fract_f32 v75, v75\n"
"v_cos_f32 v60, v75\n"
"v_mfma_f32_32x32x16_bf16 v[240:255], a[48:51], a[180:183], v[240:255]\n"
"ds_read_b128 a[180:183], v154 offset:4096\n"
"v_sin_f32 v61, v75\n"
"v_cndmask_b32 v74, 0, v74, s[20:21]\n"
"v_mul_f32 v60, v60, v74\n"
"v_mul_f32 v61, v61, v74\n"
"v_mul_f32 v71, v68, v68\n"
"v_add_f32 v70, -1.0, v44\n"
"v_fmac_f32 v71, v69, v69\n"
"v_mul_f32 v62, v70, v68\n"
"v_mfma_f32_32x32x16_bf16 v[160:175], a[52:55], a[184:187], v[160:175]\n"
"ds_read_b128 a[184:187], v12 offset:6144\n"
"buffer_load_dwordx4 v23, s[56:59], s14 offen offset:1024 lds\n"
"v_rcp_f32 v71, v71\n"
"v_mul_f32 v63, v45, v68\n"
"v_fmac_f32 v62, v45, v69\n"
"v_fma_f32 v63, -v70, v69, v63\n"
"v_mfma_f32_32x32x16_bf16 v[176:191], a[52:55], a[188:191], v[176:191]\n"
"s_waitcnt vmcnt(14) lgkmcnt(3)\n"
"ds_read_b128 a[188:191], v154 offset:6144\n"
"ds_read_b128 a[64:67], v14 offset:32768\n"
"s_add_u32 s48, s48, 128\n"
"s_add_u32 m0, s10, 16384\n"
"v_add_f32 v71, v71, v71\n"
"v_mul_f32 v62, v62, v71\n"
"v_mul_f32 v63, v63, v71\n"
"v_mfma_f32_32x32x16_bf16 v[192:207], a[52:55], a[160:163], v[192:207]\n"
"ds_read_b128 a[160:163], v12 offset:8192\n"
"ds_read_b128 a[68:71], v15 offset:32768\n"
"buffer_load_dwordx4 v22, s[56:59], s48 offen offset:0 lds\n"
"v_mfma_f32_32x32x16_bf16 v[208:223], a[52:55], a[164:167], v[208:223]\n"
"ds_read_b128 a[164:167], v154 offset:8192\n"
"ds_read_b128 a[72:75], v16 offset:32768\n"
"v_mfma_f32_32x32x16_bf16 v[224:239], a[52:55], a[168:171], v[224:239]\n"
"ds_read_b128 a[168:171], v13 offset:4096\n"
"ds_read_b128 a[76:79], v17 offset:32768\n"
"buffer_load_dwordx4 v23, s[56:59], s48 offen offset:1024 lds\n"
"v_mfma_f32_32x32x16_bf16 v[240:255], a[52:55], a[172:175], v[240:255]\n"
"s_waitcnt lgkmcnt(7)\n"
"ds_read_b128 a[172:175], v155 offset:4096\n"
"s_add_u32 s49, s49, 128\n"
"s_add_u32 m0, s10, 18432\n"
"v_mfma_f32_32x32x16_bf16 v[160:175], a[56:59], a[176:179], v[160:175]\n"
"ds_read_b128 a[176:179], v13 offset:6144\n"
"buffer_load_dwordx4 v22, s[56:59], s49 offen offset:0 lds\n"
"v_mfma_f32_32x32x16_bf16 v[176:191], a[56:59], a[180:183], v[176:191]\n"
"ds_read_b128 a[180:183], v155 offset:6144\n"
"v_mfma_f32_32x32x16_bf16 v[192:207], a[56:59], a[184:187], v[192:207]\n"
"ds_read_b128 a[184:187], v13 offset:8192\n"
"buffer_load_dwordx4 v23, s[56:59], s49 offen offset:1024 lds\n"
"v_mfma_f32_32x32x16_bf16 v[208:223], a[56:59], a[188:191], v[208:223]\n"
"s_waitcnt lgkmcnt(3)\n"
"ds_read_b128 a[188:191], v155 offset:8192\n"
"s_add_u32 s12, s12, 128\n"
"s_add_u32 m0, s10, 32768\n"
"v_mfma_f32_32x32x16_bf16 v[224:239], a[56:59], a[160:163], v[224:239]\n"
"s_waitcnt vmcnt(11)\n"
"s_barrier\n"
"ds_read_b128 a[160:163], v10 offset:2048\n"
"buffer_load_dwordx4 v22, s[44:47], s12 offen offset:0 lds\n"
"v_mfma_f32_32x32x16_bf16 v[240:255], a[56:59], a[164:167], v[240:255]\n"
"ds_read_b128 a[164:167], v152 offset:2048\n"
"v_mfma_f32_32x32x16_bf16 v[160:175], a[60:63], a[168:171], v[160:175]\n"
"ds_read_b128 a[168:171], v10 offset:10240\n"
"buffer_load_dwordx4 v23, s[44:47], s12 offen offset:1024 lds\n"
"v_mfma_f32_32x32x16_bf16 v[176:191], a[60:63], a[172:175], v[176:191]\n"
"s_waitcnt lgkmcnt(3)\n"
"ds_read_b128 a[172:175], v152 offset:10240\n"
"v_mfma_f32_32x32x16_bf16 v[192:207], a[60:63], a[176:179], v[192:207]\n"
"ds_read_b128 a[176:179], v10 offset:12288\n"
"buffer_load_dwordx4 v24, s[44:47], s12 offen offset:2048 lds\n"
"v_mfma_f32_32x32x16_bf16 v[208:223], a[60:63], a[180:183], v[208:223]\n"
"ds_read_b128 a[180:183], v152 offset:12288\n"
"v_mfma_f32_32x32x16_bf16 v[224:239], a[60:63], a[184:187], v[224:239]\n"
"ds_read_b128 a[184:187], v11 offset:2048\n"
"buffer_load_dwordx4 v25, s[44:47], s12 offen offset:3072 lds\n"
"v_mfma_f32_32x32x16_bf16 v[240:255], a[60:63], a[188:191], v[240:255]\n"
"s_waitcnt lgkmcnt(3)\n"
"ds_read_b128 a[188:191], v153 offset:2048\n"
"s_add_u32 s14, s14, 128\n"
"s_add_u32 m0, s10, 20480\n"
"v_mfma_f32_32x32x16_bf16 v[160:175], a[64:67], a[160:163], v[160:175]\n"
"ds_read_b128 a[160:163], v11 offset:10240\n"
"buffer_load_dwordx4 v22, s[56:59], s14 offen offset:0 lds\n"
"v_mfma_f32_32x32x16_bf16 v[176:191], a[64:67], a[164:167], v[176:191]\n"
"ds_read_b128 a[164:167], v153 offset:10240\n"
"v_mfma_f32_32x32x16_bf16 v[192:207], a[64:67], a[168:171], v[192:207]\n"
"s_waitcnt vmcnt(11)\n"
"ds_read_b128 a[168:171], v11 offset:12288\n"
"ds_read_b128 a[80:83], v14 offset:28672\n"
"buffer_load_dwordx4 v23, s[56:59], s14 offen offset:1024 lds\n"
"v_mfma_f32_32x32x16_bf16 v[208:223], a[64:67], a[172:175], v[208:223]\n"
"s_waitcnt lgkmcnt(4)\n"
"ds_read_b128 a[172:175], v153 offset:12288\n"
"ds_read_b128 a[84:87], v15 offset:28672\n"
"s_add_u32 s48, s48, 128\n"
"s_add_u32 m0, s10, 22528\n"
"v_mfma_f32_32x32x16_bf16 v[224:239], a[64:67], a[176:179], v[224:239]\n"
"s_barrier\n"
"ds_read_b128 a[176:179], v12 offset:2048\n"
"ds_read_b128 a[88:91], v16 offset:28672\n"
"buffer_load_dwordx4 v22, s[56:59], s48 offen offset:0 lds\n"
"v_mov_b32 v68, s72\n"
"v_mov_b32 v69, s73\n"
"v_mfma_f32_32x32x16_bf16 v[240:255], a[64:67], a[180:183], v[240:255]\n"
"ds_read_b128 a[180:183], v154 offset:2048\n"
"ds_read_b128 a[92:95], v17 offset:28672\n"
"v_mov_b32 v70, 0\n"
"v_mfma_f32_32x32x16_bf16 v[160:175], a[68:71], a[184:187], v[160:175]\n"
"ds_read_b128 a[184:187], v12 offset:10240\n"
"buffer_load_dwordx4 v23, s[56:59], s48 offen offset:1024 lds\n"
"buffer_store_dwordx2 v[68:69], v70, s[64:67], s94 offen sc1\n"
"v_mov_b32 v68, s72\n"
"v_mov_b32 v69, s73\n"
"v_mfma_f32_32x32x16_bf16 v[176:191], a[68:71], a[188:191], v[176:191]\n"
"s_waitcnt lgkmcnt(6)\n"
"ds_read_b128 a[188:191], v154 offset:10240\n"
"s_add_u32 s49, s49, 128\n"
"s_add_u32 m0, s10, 4096\n"
"v_mov_b32 v70, 0\n"
"v_mfma_f32_32x32x16_bf16 v[192:207], a[68:71], a[160:163], v[192:207]\n"
"ds_read_b128 a[160:163], v12 offset:12288\n"
"buffer_load_dwordx4 v22, s[56:59], s49 offen offset:0 lds\n"
"buffer_store_dwordx2 v[68:69], v70, s[64:67], s36 offen sc1\n"
"v_mov_b32 v68, s72\n"
"v_mov_b32 v69, s73\n"
"v_mfma_f32_32x32x16_bf16 v[208:223], a[68:71], a[164:167], v[208:223]\n"
"ds_read_b128 a[164:167], v154 offset:12288\n"
"v_mov_b32 v70, 32768\n"
"v_mfma_f32_32x32x16_bf16 v[224:239], a[68:71], a[168:171], v[224:239]\n"
"ds_read_b128 a[168:171], v13 offset:2048\n"
"buffer_load_dwordx4 v23, s[56:59], s49 offen offset:1024 lds\n"
"buffer_store_dwordx2 v[68:69], v70, s[64:67], s36 offen sc1\n"
"v_mfma_f32_32x32x16_bf16 v[240:255], a[68:71], a[172:175], v[240:255]\n"
"s_waitcnt lgkmcnt(3)\n"
"ds_read_b128 a[172:175], v155 offset:2048\n"
"s_add_u32 s12, s12, 128\n"
"s_add_u32 m0, s10, 28672\n"
"v_mfma_f32_32x32x16_bf16 v[160:175], a[72:75], a[176:179], v[160:175]\n"
"ds_read_b128 a[176:179], v13 offset:10240\n"
"buffer_load_dwordx4 v22, s[44:47], s12 offen offset:0 lds\n"
"v_mfma_f32_32x32x16_bf16 v[176:191], a[72:75], a[180:183], v[176:191]\n"
"ds_read_b128 a[180:183], v155 offset:10240\n"
"v_mfma_f32_32x32x16_bf16 v[192:207], a[72:75], a[184:187], v[192:207]\n"
"ds_read_b128 a[184:187], v13 offset:12288\n"
"buffer_load_dwordx4 v23, s[44:47], s12 offen offset:1024 lds\n"
"v_mfma_f32_32x32x16_bf16 v[208:223], a[72:75], a[188:191], v[208:223]\n"
"s_waitcnt lgkmcnt(3)\n"
"ds_read_b128 a[188:191], v155 offset:12288\n"
"v_mfma_f32_32x32x16_bf16 v[224:239], a[72:75], a[160:163], v[224:239]\n"
"s_waitcnt vmcnt(15)\n"
"s_barrier\n"
"ds_read_b128 a[160:163], v10 offset:14336\n"
"buffer_load_dwordx4 v24, s[44:47], s12 offen offset:2048 lds\n"
"v_mfma_f32_32x32x16_bf16 v[240:255], a[72:75], a[164:167], v[240:255]\n"
"ds_read_b128 a[164:167], v152 offset:14336\n"
"v_mfma_f32_32x32x16_bf16 v[160:175], a[76:79], a[168:171], v[160:175]\n"
"ds_read_b128 a[168:171], v10 offset:16384\n"
"buffer_load_dwordx4 v25, s[44:47], s12 offen offset:3072 lds\n"
"v_mfma_f32_32x32x16_bf16 v[176:191], a[76:79], a[172:175], v[176:191]\n"
"s_waitcnt lgkmcnt(3)\n"
"ds_read_b128 a[172:175], v152 offset:16384\n"
"s_add_u32 s14, s14, 128\n"
"s_add_u32 m0, s10, 6144\n"
"v_mfma_f32_32x32x16_bf16 v[192:207], a[76:79], a[176:179], v[192:207]\n"
"ds_read_b128 a[176:179], v10 offset:18432\n"
"buffer_load_dwordx4 v22, s[56:59], s14 offen offset:0 lds\n"
"v_mfma_f32_32x32x16_bf16 v[208:223], a[76:79], a[180:183], v[208:223]\n"
"ds_read_b128 a[180:183], v152 offset:18432\n"
"v_mfma_f32_32x32x16_bf16 v[224:239], a[76:79], a[184:187], v[224:239]\n"
"s_waitcnt vmcnt(14)\n"
"ds_read_b128 a[184:187], v11 offset:14336\n"
"ds_read_b128 a[96:99], v14 offset:32768\n"
"buffer_load_dwordx4 v23, s[56:59], s14 offen offset:1024 lds\n"
"v_mfma_f32_32x32x16_bf16 v[240:255], a[76:79], a[188:191], v[240:255]\n"
"s_waitcnt lgkmcnt(4)\n"
"ds_read_b128 a[188:191], v153 offset:14336\n"
"ds_read_b128 a[100:103], v15 offset:32768\n"
"s_add_u32 s48, s48, 128\n"
"s_add_u32 m0, s10, 8192\n"
"v_mfma_f32_32x32x16_bf16 v[160:175], a[80:83], a[160:163], v[160:175]\n"
"ds_read_b128 a[160:163], v11 offset:16384\n"
"ds_read_b128 a[104:107], v16 offset:32768\n"
"buffer_load_dwordx4 v22, s[56:59], s48 offen offset:0 lds\n"
"v_mfma_f32_32x32x16_bf16 v[176:191], a[80:83], a[164:167], v[176:191]\n"
"ds_read_b128 a[164:167], v153 offset:16384\n"
"ds_read_b128 a[108:111], v17 offset:32768\n"
"v_mfma_f32_32x32x16_bf16 v[192:207], a[80:83], a[168:171], v[192:207]\n"
"ds_read_b128 a[168:171], v11 offset:18432\n"
"buffer_load_dwordx4 v23, s[56:59], s48 offen offset:1024 lds\n"
"v_mfma_f32_32x32x16_bf16 v[208:223], a[80:83], a[172:175], v[208:223]\n"
"s_waitcnt lgkmcnt(6)\n"
"ds_read_b128 a[172:175], v153 offset:18432\n"
"s_add_u32 s49, s49, 128\n"
"s_add_u32 m0, s10, 24576\n"
"v_mfma_f32_32x32x16_bf16 v[224:239], a[80:83], a[176:179], v[224:239]\n"
"s_barrier\n"
"ds_read_b128 a[176:179], v12 offset:14336\n"
"buffer_load_dwordx4 v22, s[56:59], s49 offen offset:0 lds\n"
"v_mfma_f32_32x32x16_bf16 v[240:255], a[80:83], a[180:183], v[240:255]\n"
"ds_read_b128 a[180:183], v154 offset:14336\n"
"v_mfma_f32_32x32x16_bf16 v[160:175], a[84:87], a[184:187], v[160:175]\n"
"ds_read_b128 a[184:187], v12 offset:16384\n"
"buffer_load_dwordx4 v23, s[56:59], s49 offen offset:1024 lds\n"
"v_mfma_f32_32x32x16_bf16 v[176:191], a[84:87], a[188:191], v[176:191]\n"
"s_waitcnt lgkmcnt(3)\n"
"ds_read_b128 a[188:191], v154 offset:16384\n"
"s_add_u32 s12, s12, 128\n"
"s_add_u32 m0, s10, 32768\n"
"v_mfma_f32_32x32x16_bf16 v[192:207], a[84:87], a[160:163], v[192:207]\n"
"ds_read_b128 a[160:163], v12 offset:18432\n"
"buffer_load_dwordx4 v22, s[44:47], s12 offen offset:0 lds\n"
"v_lshl_add_u32 v78, v1, 4, s91\n"
"v_add_u32 v79, 21037056, v28\n"
"v_mfma_f32_32x32x16_bf16 v[208:223], a[84:87], a[164:167], v[208:223]\n"
"ds_read_b128 a[164:167], v154 offset:18432\n"
"v_mfma_f32_32x32x16_bf16 v[224:239], a[84:87], a[168:171], v[224:239]\n"
"ds_read_b128 a[168:171], v13 offset:14336\n"
"buffer_load_dwordx4 v23, s[44:47], s12 offen offset:1024 lds\n"
"buffer_load_dwordx4 v[64:67], v78, s[64:67], 0 offen offset:0 sc1\n"
"v_mfma_f32_32x32x16_bf16 v[240:255], a[84:87], a[172:175], v[240:255]\n"
"s_waitcnt lgkmcnt(3)\n"
"ds_read_b128 a[172:175], v155 offset:14336\n"
"v_mfma_f32_32x32x16_bf16 v[160:175], a[88:91], a[176:179], v[160:175]\n"
"ds_read_b128 a[176:179], v13 offset:16384\n"
"buffer_load_dwordx4 v24, s[44:47], s12 offen offset:2048 lds\n"
"buffer_load_dwordx4 v[80:83], v79, s[64:67], s93 offen offset:0 sc1\n"
"v_mfma_f32_32x32x16_bf16 v[176:191], a[88:91], a[180:183], v[176:191]\n"
"ds_read_b128 a[180:183], v155 offset:16384\n"
"v_mfma_f32_32x32x16_bf16 v[192:207], a[88:91], a[184:187], v[192:207]\n"
"ds_read_b128 a[184:187], v13 offset:18432\n"
"buffer_load_dwordx4 v25, s[44:47], s12 offen offset:3072 lds\n"
"buffer_load_dwordx4 v[84:87], v79, s[64:67], s93 offen offset:1024 sc1\n"
"v_mfma_f32_32x32x16_bf16 v[208:223], a[88:91], a[188:191], v[208:223]\n"
"s_waitcnt lgkmcnt(3)\n"
"ds_read_b128 a[188:191], v155 offset:18432\n"
"s_add_u32 s14, s14, 128\n"
"s_add_u32 m0, s10, 2048\n"
"v_mfma_f32_32x32x16_bf16 v[224:239], a[88:91], a[160:163], v[224:239]\n"
"s_waitcnt vmcnt(18)\n"
"s_barrier\n"
"ds_read_b128 a[160:163], v10 offset:20480\n"
"buffer_load_dwordx4 v22, s[56:59], s14 offen offset:0 lds\n"
"buffer_load_dwordx4 v[88:91], v79, s[64:67], s93 offen offset:2048 sc1\n"
"v_mfma_f32_32x32x16_bf16 v[240:255], a[88:91], a[164:167], v[240:255]\n"
"ds_read_b128 a[164:167], v152 offset:20480\n"
"v_mfma_f32_32x32x16_bf16 v[160:175], a[92:95], a[168:171], v[160:175]\n"
"s_waitcnt vmcnt(15)\n"
"ds_read_b128 a[168:171], v10 offset:22528\n"
"ds_read_b128 a[112:115], v14 offset:28672\n"
"buffer_load_dwordx4 v23, s[56:59], s14 offen offset:1024 lds\n"
"v_mfma_f32_32x32x16_bf16 v[176:191], a[92:95], a[172:175], v[176:191]\n"
"s_waitcnt lgkmcnt(4)\n"
"ds_read_b128 a[172:175], v152 offset:22528\n"
"ds_read_b128 a[116:119], v15 offset:28672\n"
"s_add_u32 s14, s14, 128\n"
"s_add_u32 m0, s10, 10240\n"
"v_mfma_f32_32x32x16_bf16 v[192:207], a[92:95], a[176:179], v[192:207]\n"
"ds_read_b128 a[176:179], v10 offset:4096\n"
"ds_read_b128 a[120:123], v16 offset:28672\n"
"buffer_load_dwordx4 v22, s[56:59], s14 offen offset:0 lds\n"
"v_mfma_f32_32x32x16_bf16 v[208:223], a[92:95], a[180:183], v[208:223]\n"
"ds_read_b128 a[180:183], v152 offset:4096\n"
"ds_read_b128 a[124:127], v17 offset:28672\n"
"v_mfma_f32_32x32x16_bf16 v[224:239], a[92:95], a[184:187], v[224:239]\n"
"ds_read_b128 a[184:187], v11 offset:20480\n"
"buffer_load_dwordx4 v23, s[56:59], s14 offen offset:1024 lds\n"
"v_mfma_f32_32x32x16_bf16 v[240:255], a[92:95], a[188:191], v[240:255]\n"
"s_waitcnt lgkmcnt(6)\n"
"ds_read_b128 a[188:191], v153 offset:20480\n"
"s_add_u32 s48, s48, 128\n"
"s_add_u32 m0, s10, 12288\n"
"v_mfma_f32_32x32x16_bf16 v[160:175], a[96:99], a[160:163], v[160:175]\n"
"ds_read_b128 a[160:163], v11 offset:22528\n"
"buffer_load_dwordx4 v22, s[56:59], s48 offen offset:0 lds\n"
"v_mfma_f32_32x32x16_bf16 v[176:191], a[96:99], a[164:167], v[176:191]\n"
"ds_read_b128 a[164:167], v153 offset:22528\n"
"v_mfma_f32_32x32x16_bf16 v[192:207], a[96:99], a[168:171], v[192:207]\n"
"ds_read_b128 a[168:171], v11 offset:4096\n"
"buffer_load_dwordx4 v23, s[56:59], s48 offen offset:1024 lds\n"
"v_mfma_f32_32x32x16_bf16 v[208:223], a[96:99], a[172:175], v[208:223]\n"
"s_waitcnt lgkmcnt(3)\n"
"ds_read_b128 a[172:175], v153 offset:4096\n"
"s_add_u32 s12, s12, 128\n"
"s_add_u32 m0, s10, 28672\n"
"v_mfma_f32_32x32x16_bf16 v[224:239], a[96:99], a[176:179], v[224:239]\n"
"s_barrier\n"
"ds_read_b128 a[176:179], v12 offset:20480\n"
"buffer_load_dwordx4 v22, s[44:47], s12 offen offset:0 lds\n"
"v_mfma_f32_32x32x16_bf16 v[240:255], a[96:99], a[180:183], v[240:255]\n"
"ds_read_b128 a[180:183], v154 offset:20480\n"
"v_mfma_f32_32x32x16_bf16 v[160:175], a[100:103], a[184:187], v[160:175]\n"
"ds_read_b128 a[184:187], v12 offset:22528\n"
"buffer_load_dwordx4 v23, s[44:47], s12 offen offset:1024 lds\n"
"v_mfma_f32_32x32x16_bf16 v[176:191], a[100:103], a[188:191], v[176:191]\n"
"s_waitcnt lgkmcnt(3)\n"
"ds_read_b128 a[188:191], v154 offset:22528\n"
"v_mfma_f32_32x32x16_bf16 v[192:207], a[100:103], a[160:163], v[192:207]\n"
"ds_read_b128 a[160:163], v12 offset:4096\n"
"buffer_load_dwordx4 v24, s[44:47], s12 offen offset:2048 lds\n"
"v_mfma_f32_32x32x16_bf16 v[208:223], a[100:103], a[164:167], v[208:223]\n"
"ds_read_b128 a[164:167], v154 offset:4096\n"
"v_mfma_f32_32x32x16_bf16 v[224:239], a[100:103], a[168:171], v[224:239]\n"
"ds_read_b128 a[168:171], v13 offset:20480\n"
"buffer_load_dwordx4 v25, s[44:47], s12 offen offset:3072 lds\n"
"v_mfma_f32_32x32x16_bf16 v[240:255], a[100:103], a[172:175], v[240:255]\n"
"s_waitcnt lgkmcnt(3)\n"
"ds_read_b128 a[172:175], v155 offset:20480\n"
"s_add_u32 s48, s48, 128\n"
"s_add_u32 m0, s10, 14336\n"
"v_mfma_f32_32x32x16_bf16 v[160:175], a[104:107], a[176:179], v[160:175]\n"
"ds_read_b128 a[176:179], v13 offset:22528\n"
"buffer_load_dwordx4 v22, s[56:59], s48 offen offset:0 lds\n"
"v_mfma_f32_32x32x16_bf16 v[176:191], a[104:107], a[180:183], v[176:191]\n"
"ds_read_b128 a[180:183], v155 offset:22528\n"
"v_mfma_f32_32x32x16_bf16 v[192:207], a[104:107], a[184:187], v[192:207]\n"
"s_waitcnt vmcnt(13)\n"
"ds_read_b128 a[184:187], v13 offset:4096\n"
"ds_read_b128 a[128:131], v14 offset:32768\n"
"buffer_load_dwordx4 v23, s[56:59], s48 offen offset:1024 lds\n"
"v_mfma_f32_32x32x16_bf16 v[208:223], a[104:107], a[188:191], v[208:223]\n"
"s_waitcnt lgkmcnt(4)\n"
"ds_read_b128 a[188:191], v155 offset:4096\n"
"ds_read_b128 a[132:135], v15 offset:32768\n"
"s_add_u32 s49, s49, 128\n"
"s_add_u32 m0, s10, 16384\n"
"v_mfma_f32_32x32x16_bf16 v[224:239], a[104:107], a[160:163], v[224:239]\n"
"s_barrier\n"
"ds_read_b128 a[160:163], v10 offset:6144\n"
"ds_read_b128 a[136:139], v16 offset:32768\n"
"buffer_load_dwordx4 v22, s[56:59], s49 offen offset:0 lds\n"
"v_mfma_f32_32x32x16_bf16 v[240:255], a[104:107], a[164:167], v[240:255]\n"
"ds_read_b128 a[164:167], v152 offset:6144\n"
"ds_read_b128 a[140:143], v17 offset:32768\n"
"v_mfma_f32_32x32x16_bf16 v[160:175], a[108:111], a[168:171], v[160:175]\n"
"ds_read_b128 a[168:171], v10 offset:8192\n"
"buffer_load_dwordx4 v23, s[56:59], s49 offen offset:1024 lds\n"
"v_mfma_f32_32x32x16_bf16 v[176:191], a[108:111], a[172:175], v[176:191]\n"
"s_waitcnt lgkmcnt(6)\n"
"ds_read_b128 a[172:175], v152 offset:8192\n"
"s_add_u32 s49, s49, 128\n"
"s_add_u32 m0, s10, 18432\n"
"v_mfma_f32_32x32x16_bf16 v[192:207], a[108:111], a[176:179], v[192:207]\n"
"ds_read_b128 a[176:179], v10 offset:24576\n"
"buffer_load_dwordx4 v22, s[56:59], s49 offen offset:0 lds\n"
"v_mfma_f32_32x32x16_bf16 v[208:223], a[108:111], a[180:183], v[208:223]\n"
"ds_read_b128 a[180:183], v152 offset:24576\n"
"v_mfma_f32_32x32x16_bf16 v[224:239], a[108:111], a[184:187], v[224:239]\n"
"ds_read_b128 a[184:187], v11 offset:6144\n"
"buffer_load_dwordx4 v23, s[56:59], s49 offen offset:1024 lds\n"
"v_mfma_f32_32x32x16_bf16 v[240:255], a[108:111], a[188:191], v[240:255]\n"
"s_waitcnt lgkmcnt(3)\n"
"ds_read_b128 a[188:191], v153 offset:6144\n"
"v_mfma_f32_32x32x16_bf16 v[160:175], a[112:115], a[160:163], v[160:175]\n"
"ds_read_b128 a[160:163], v11 offset:8192\n"
"v_mfma_f32_32x32x16_bf16 v[176:191], a[112:115], a[164:167], v[176:191]\n"
"ds_read_b128 a[164:167], v153 offset:8192\n"
"v_mfma_f32_32x32x16_bf16 v[192:207], a[112:115], a[168:171], v[192:207]\n"
"ds_read_b128 a[168:171], v11 offset:24576\n"
"v_mfma_f32_32x32x16_bf16 v[208:223], a[112:115], a[172:175], v[208:223]\n"
"s_waitcnt lgkmcnt(3)\n"
"ds_read_b128 a[172:175], v153 offset:24576\n"
"v_mfma_f32_32x32x16_bf16 v[224:239], a[112:115], a[176:179], v[224:239]\n"
"s_barrier\n"
"ds_read_b128 a[176:179], v12 offset:6144\n"
"v_mfma_f32_32x32x16_bf16 v[240:255], a[112:115], a[180:183], v[240:255]\n"
"ds_read_b128 a[180:183], v154 offset:6144\n"
"v_mfma_f32_32x32x16_bf16 v[160:175], a[116:119], a[184:187], v[160:175]\n"
"ds_read_b128 a[184:187], v12 offset:8192\n"
"v_mfma_f32_32x32x16_bf16 v[176:191], a[116:119], a[188:191], v[176:191]\n"
"s_waitcnt lgkmcnt(3)\n"
"ds_read_b128 a[188:191], v154 offset:8192\n"
"v_mfma_f32_32x32x16_bf16 v[192:207], a[116:119], a[160:163], v[192:207]\n"
"ds_read_b128 a[160:163], v12 offset:24576\n"
"v_mfma_f32_32x32x16_bf16 v[208:223], a[116:119], a[164:167], v[208:223]\n"
"ds_read_b128 a[164:167], v154 offset:24576\n"
"v_mfma_f32_32x32x16_bf16 v[224:239], a[116:119], a[168:171], v[224:239]\n"
"s_waitcnt vmcnt(6)\n"
"ds_read_b128 a[168:171], v13 offset:6144\n"
"ds_read_b128 a[144:147], v14 offset:28672\n"
"v_mfma_f32_32x32x16_bf16 v[240:255], a[116:119], a[172:175], v[240:255]\n"
"s_waitcnt lgkmcnt(4)\n"
"ds_read_b128 a[172:175], v155 offset:6144\n"
"ds_read_b128 a[148:151], v15 offset:28672\n"
"v_mfma_f32_32x32x16_bf16 v[160:175], a[120:123], a[176:179], v[160:175]\n"
"ds_read_b128 a[176:179], v13 offset:8192\n"
"ds_read_b128 a[152:155], v16 offset:28672\n"
"v_mfma_f32_32x32x16_bf16 v[176:191], a[120:123], a[180:183], v[176:191]\n"
"ds_read_b128 a[180:183], v155 offset:8192\n"
"ds_read_b128 a[156:159], v17 offset:28672\n"
"v_mfma_f32_32x32x16_bf16 v[192:207], a[120:123], a[184:187], v[192:207]\n"
"ds_read_b128 a[184:187], v13 offset:24576\n"
"v_mfma_f32_32x32x16_bf16 v[208:223], a[120:123], a[188:191], v[208:223]\n"
"s_waitcnt lgkmcnt(6)\n"
"ds_read_b128 a[188:191], v155 offset:24576\n"
"v_mfma_f32_32x32x16_bf16 v[224:239], a[120:123], a[160:163], v[224:239]\n"
"s_barrier\n"
"ds_read_b128 a[160:163], v10 offset:2048\n"
"v_xor_b32 v69, s72, v65\n"
"v_xor_b32 v70, s73, v67\n"
"v_or3_b32 v68, v69, v70, 0\n"
"v_xor_b32 v69, s72, v81\n"
"v_xor_b32 v70, s73, v83\n"
"v_or3_b32 v68, v69, v70, v68\n"
"v_mfma_f32_32x32x16_bf16 v[240:255], a[120:123], a[164:167], v[240:255]\n"
"ds_read_b128 a[164:167], v152 offset:2048\n"
"v_xor_b32 v69, s72, v85\n"
"v_xor_b32 v70, s73, v87\n"
"v_or3_b32 v68, v69, v70, v68\n"
"v_xor_b32 v69, s72, v89\n"
"v_xor_b32 v70, s73, v91\n"
"v_or3_b32 v68, v69, v70, v68\n"
"v_cmp_ne_u32 vcc, 0, v68\n"
"s_nop 0\n"
"s_cmp_lg_u64 vcc, 0\n"
"s_cbranch_scc1 K1S_l_slow\n"
"K1S_l_back:\n"
"v_mfma_f32_32x32x16_bf16 v[160:175], a[124:127], a[168:171], v[160:175]\n"
"ds_read_b128 a[168:171], v11 offset:2048\n"
"s_lshl_b32 s68, s7, 8\n"
"s_add_u32 s68, s68, 81920\n"
"v_and_b32 v69, 0x1ff, v28\n"
"v_cvt_pk_bf16_f32 v68, -v66, -v66\n"
"v_lshrrev_b32 v70, 5, v27\n"
"v_lshl_add_u32 v14, v70, 6, s68\n"
"v_cndmask_b32 v40, v68, 0, s[16:17]\n"
"v_mov_b32 v41, 0\n"
"v_mfma_f32_32x32x16_bf16 v[176:191], a[124:127], a[172:175], v[176:191]\n"
"s_waitcnt lgkmcnt(3)\n"
"ds_read_b128 a[172:175], v153 offset:2048\n"
"v_and_b32 v42, 0xffff, v40\n"
"v_mov_b32 v43, 0\n"
"v_cndmask_b32 v68, 0, v68, s[16:17]\n"
"v_accvgpr_write_b32 a197, 0\n"
"v_accvgpr_write_b32 a196, v68\n"
"v_and_b32 v68, 0xffff, v68\n"
"v_accvgpr_write_b32 a199, 0\n"
"v_accvgpr_write_b32 a198, v68\n"
"v_mfma_f32_32x32x16_bf16 v[192:207], a[124:127], a[176:179], v[192:207]\n"
"ds_read_b128 a[176:179], v12 offset:2048\n"
"s_mov_b32 s69, m0\n"
"s_mov_b32 m0, s68\n"
"s_nop 0\n"
"buffer_load_dword v69, s[64:67], s91 offen sc1 lds\n"
"s_mov_b32 m0, s69\n"
"v_mfma_f32_32x32x16_bf16 v[208:223], a[124:127], a[180:183], v[208:223]\n"
"ds_read_b128 a[180:183], v154 offset:2048\n"
"v_lshlrev_b32 v70, 16, v141\n"
"v_lshlrev_b32 v68, 16, v139\n"
"v_mul_f32 v70, 0x3fb8aa3b, v70\n"
"v_lshlrev_b32 v69, 16, v140\n"
"v_exp_f32 v70, v70\n"
"v_lshrrev_b32 v73, 5, v27\n"
"v_cvt_pk_bf16_f32 v70, v70, v70\n"
"v_cvt_f32_u32 v73, v73\n"
"v_mfma_f32_32x32x16_bf16 v[224:239], a[124:127], a[184:187], v[224:239]\n"
"ds_read_b128 a[184:187], v13 offset:2048\n"
"v_lshlrev_b32 v70, 16, v70\n"
"v_mul_f32 v71, v70, v69\n"
"v_mul_f32 v70, v70, v68\n"
"v_mul_f32 v71, 0x3e22f983, v71\n"
"v_mul_f32 v70, 0x3fb8aa3b, v70\n"
"v_mul_f32 v74, 0x3f800000, v70\n"
"v_mul_f32 v75, 0x3f800000, v71\n"
"v_exp_f32 v74, v74\n"
"v_mfma_f32_32x32x16_bf16 v[240:255], a[124:127], a[188:191], v[240:255]\n"
"s_waitcnt lgkmcnt(3)\n"
"ds_read_b128 a[188:191], v155 offset:2048\n"
"v_fract_f32 v75, v75\n"
"v_cos_f32 v96, v75\n"
"v_sin_f32 v97, v75\n"
"v_mul_f32 v96, v96, v74\n"
"v_mul_f32 v97, v97, v74\n"
"v_mul_f32 v72, 0x41800000, v73\n"
"v_mfma_f32_32x32x16_bf16 v[160:175], a[128:131], a[160:163], v[160:175]\n"
"ds_read_b128 a[160:163], v10 offset:10240\n"
"v_mul_f32 v74, v72, v70\n"
"v_mul_f32 v75, v72, v71\n"
"v_exp_f32 v74, v74\n"
"v_fract_f32 v75, v75\n"
"v_cos_f32 v98, v75\n"
"v_sin_f32 v99, v75\n"
"v_mfma_f32_32x32x16_bf16 v[176:191], a[128:131], a[164:167], v[176:191]\n"
"ds_read_b128 a[164:167], v152 offset:10240\n"
"v_mul_f32 v98, v98, v74\n"
"v_mul_f32 v99, v99, v74\n"
"v_mul_f32 v74, 0x42000000, v70\n"
"v_mul_f32 v75, 0x42000000, v71\n"
"v_exp_f32 v74, v74\n"
"v_fract_f32 v75, v75\n"
"v_cos_f32 v106, v75\n"
"v_mfma_f32_32x32x16_bf16 v[160:175], a[132:135], a[168:171], v[160:175]\n"
"ds_read_b128 a[168:171], v11 offset:10240\n"
"v_sin_f32 v107, v75\n"
"v_mul_f32 v106, v106, v74\n"
"v_mul_f32 v107, v107, v74\n"
"v_mul_f32 v74, v107, v107\n"
"v_mul_f32 v75, v106, v107\n"
"v_fma_f32 v74, v106, v106, -v74\n"
"v_add_f32 v75, v75, v75\n"
"v_mfma_f32_32x32x16_bf16 v[176:191], a[132:135], a[172:175], v[176:191]\n"
"s_waitcnt lgkmcnt(3)\n"
"ds_read_b128 a[172:175], v153 offset:10240\n"
"s_cmp_eq_u32 s7, 3\n"
"s_cselect_b32 s68, 1.0, 0\n"
"s_cmp_eq_u32 s7, 2\n"
"s_cselect_b32 s69, 1.0, 0\n"
"s_cmp_eq_u32 s7, 1\n"
"s_cselect_b32 s70, 1.0, 0\n"
"v_mfma_f32_32x32x16_bf16 v[160:175], a[136:139], a[176:179], v[160:175]\n"
"ds_read_b128 a[176:179], v12 offset:10240\n"
"v_mov_b32 v104, s68\n"
"v_mov_b32 v105, 0\n"
"v_mov_b32 v102, s69\n"
"v_mul_f32 v103, s68, v107\n"
"v_fmac_f32 v102, s68, v106\n"
"v_mov_b32 v100, s70\n"
"v_mul_f32 v101, s69, v107\n"
"v_fmac_f32 v100, s69, v106\n"
"v_fmac_f32 v101, s68, v75\n"
"v_mfma_f32_32x32x16_bf16 v[176:191], a[136:139], a[180:183], v[176:191]\n"
"ds_read_b128 a[180:183], v154 offset:10240\n"
"v_fmac_f32 v100, s68, v74\n"
"s_lshl_b32 s68, s7, 5\n"
"v_cvt_f32_u32 v72, s68\n"
"v_fmac_f32 v72, 0x41800000, v73\n"
"v_mul_f32 v74, v72, v70\n"
"v_mul_f32 v75, v72, v71\n"
"v_exp_f32 v74, v74\n"
"v_mfma_f32_32x32x16_bf16 v[160:175], a[140:143], a[184:187], v[160:175]\n"
"ds_read_b128 a[184:187], v13 offset:10240\n"
"v_fract_f32 v75, v75\n"
"v_cos_f32 v108, v75\n"
"v_sin_f32 v109, v75\n"
"v_mul_f32 v108, v108, v74\n"
"v_mul_f32 v109, v109, v74\n"
"s_lshl_b32 s68, s7, 9\n"
"v_mfma_f32_32x32x16_bf16 v[176:191], a[140:143], a[188:191], v[176:191]\n"
"s_waitcnt lgkmcnt(3)\n"
"ds_read_b128 a[188:191], v155 offset:10240\n"
"s_add_u32 s68, s68, 0\n"
"v_cvt_f32_u32 v72, s68\n"
"v_fmac_f32 v72, 0x43000000, v73\n"
"v_mul_f32 v74, v72, v70\n"
"v_mul_f32 v75, v72, v71\n"
"v_exp_f32 v74, v74\n"
"v_mfma_f32_32x32x16_bf16 v[160:175], a[144:147], a[160:163], v[160:175]\n"
"s_waitcnt vmcnt(5)\n"
"s_barrier\n"
"ds_read_b128 a[160:163], v10 offset:12288\n"
"v_fract_f32 v75, v75\n"
"v_cos_f32 v110, v75\n"
"v_sin_f32 v111, v75\n"
"v_cndmask_b32 v74, 0, v74, s[18:19]\n"
"v_mfma_f32_32x32x16_bf16 v[176:191], a[144:147], a[164:167], v[176:191]\n"
"ds_read_b128 a[164:167], v152 offset:12288\n"
"v_mul_f32 v110, v110, v74\n"
"v_mul_f32 v111, v111, v74\n"
"s_lshl_b32 s68, s7, 9\n"
"s_add_u32 s68, s68, 256\n"
"v_cvt_f32_u32 v72, s68\n"
"v_fmac_f32 v72, 0x43000000, v73\n"
"v_mul_f32 v74, v72, v70\n"
"v_mul_f32 v75, v72, v71\n"
"v_mfma_f32_32x32x16_bf16 v[160:175], a[148:151], a[168:171], v[160:175]\n"
"ds_read_b128 a[168:171], v11 offset:12288\n"
"v_exp_f32 v74, v74\n"
"v_fract_f32 v75, v75\n"
"v_cos_f32 v112, v75\n"
"v_sin_f32 v113, v75\n"
"v_cndmask_b32 v74, 0, v74, s[20:21]\n"
"v_mul_f32 v112, v112, v74\n"
"v_mfma_f32_32x32x16_bf16 v[176:191], a[148:151], a[172:175], v[176:191]\n"
"s_waitcnt lgkmcnt(3)\n"
"ds_read_b128 a[172:175], v153 offset:12288\n"
"v_mul_f32 v113, v113, v74\n"
"v_mul_f32 v71, v68, v68\n"
"v_add_f32 v70, -1.0, v96\n"
"v_fmac_f32 v71, v69, v69\n"
"v_mul_f32 v114, v70, v68\n"
"v_rcp_f32 v71, v71\n"
"v_mul_f32 v115, v97, v68\n"
"v_mfma_f32_32x32x16_bf16 v[160:175], a[152:155], a[176:179], v[160:175]\n"
"ds_read_b128 a[176:179], v12 offset:12288\n"
"v_fmac_f32 v114, v97, v69\n"
"v_fma_f32 v115, -v70, v69, v115\n"
"v_add_f32 v71, v71, v71\n"
"v_mul_f32 v114, v114, v71\n"
"v_mul_f32 v115, v115, v71\n"
"v_lshlrev_b32 v70, 16, v144\n"
"v_lshlrev_b32 v68, 16, v142\n"
"v_mul_f32 v70, 0x3fb8aa3b, v70\n"
"v_lshlrev_b32 v69, 16, v143\n"
"v_mfma_f32_32x32x16_bf16 v[176:191], a[152:155], a[180:183], v[176:191]\n"
"ds_read_b128 a[180:183], v154 offset:12288\n"
"v_exp_f32 v70, v70\n"
"v_lshrrev_b32 v73, 5, v27\n"
"v_cvt_pk_bf16_f32 v70, v70, v70\n"
"v_cvt_f32_u32 v73, v73\n"
"v_lshlrev_b32 v70, 16, v70\n"
"v_mul_f32 v71, v70, v69\n"
"v_mul_f32 v70, v70, v68\n"
"v_mul_f32 v71, 0x3e22f983, v71\n"
"v_mfma_f32_32x32x16_bf16 v[160:175], a[156:159], a[184:187], v[160:175]\n"
"ds_read_b128 a[184:187], v13 offset:12288\n"
"v_mul_f32 v70, 0x3fb8aa3b, v70\n"
"v_mul_f32 v74, 0x3f800000, v70\n"
"v_mul_f32 v75, 0x3f800000, v71\n"
"v_exp_f32 v74, v74\n"
"v_fract_f32 v75, v75\n"
"v_cos_f32 v116, v75\n"
"v_mfma_f32_32x32x16_bf16 v[176:191], a[156:159], a[188:191], v[176:191]\n"
"ds_read_b128 a[188:191], v155 offset:12288\n"
"v_sin_f32 v117, v75\n"
"v_mul_f32 v116, v116, v74\n"
"v_mul_f32 v117, v117, v74\n"
"v_mul_f32 v72, 0x41800000, v73\n"
"v_mul_f32 v74, v72, v70\n"
"v_mul_f32 v75, v72, v71\n"
"v_exp_f32 v74, v74\n"
"v_mfma_f32_32x32x16_bf16 v[160:175], v[40:43], v[80:83], v[160:175]\n"
"v_fract_f32 v75, v75\n"
"v_cos_f32 v118, v75\n"
"v_sin_f32 v119, v75\n"
"v_mul_f32 v118, v118, v74\n"
"v_mul_f32 v119, v119, v74\n"
"v_mul_f32 v74, 0x42000000, v70\n"
"v_mul_f32 v75, 0x42000000, v71\n"
"v_mfma_f32_32x32x16_bf16 v[176:191], a[196:199], v[80:83], v[176:191]\n"
"s_waitcnt lgkmcnt(4)\n"
"v_exp_f32 v74, v74\n"
"v_fract_f32 v75, v75\n"
"v_cos_f32 v126, v75\n"
"v_sin_f32 v127, v75\n"
"v_mul_f32 v126, v126, v74\n"
"v_mul_f32 v127, v127, v74\n"
"v_mfma_f32_32x32x16_bf16 v[192:207], a[128:131], a[160:163], v[192:207]\n"
"ds_read_b128 a[160:163], v10 offset:14336\n"
"v_mul_f32 v74, v127, v127\n"
"v_mul_f32 v75, v126, v127\n"
"v_fma_f32 v74, v126, v126, -v74\n"
"v_add_f32 v75, v75, v75\n"
"s_cmp_eq_u32 s7, 3\n"
"s_cselect_b32 s68, 1.0, 0\n"
"s_cmp_eq_u32 s7, 2\n"
"s_cselect_b32 s69, 1.0, 0\n"
"v_mfma_f32_32x32x16_bf16 v[208:223], a[128:131], a[164:167], v[208:223]\n"
"ds_read_b128 a[164:167], v152 offset:14336\n"
"s_cmp_eq_u32 s7, 1\n"
"s_cselect_b32 s70, 1.0, 0\n"
"v_mov_b32 v124, s68\n"
"v_mov_b32 v125, 0\n"
"v_mov_b32 v122, s69\n"
"v_mul_f32 v123, s68, v127\n"
"v_fmac_f32 v122, s68, v126\n"
"v_mov_b32 v120, s70\n"
"v_mfma_f32_32x32x16_bf16 v[192:207], a[132:135], a[168:171], v[192:207]\n"
"ds_read_b128 a[168:171], v11 offset:14336\n"
"v_mul_f32 v121, s69, v127\n"
"v_fmac_f32 v120, s69, v126\n"
"v_fmac_f32 v121, s68, v75\n"
"v_fmac_f32 v120, s68, v74\n"
"s_lshl_b32 s68, s7, 5\n"
"v_cvt_f32_u32 v72, s68\n"
"v_fmac_f32 v72, 0x41800000, v73\n"
"v_mul_f32 v74, v72, v70\n"
"v_mfma_f32_32x32x16_bf16 v[208:223], a[132:135], a[172:175], v[208:223]\n"
"s_waitcnt lgkmcnt(3)\n"
"ds_read_b128 a[172:175], v153 offset:14336\n"
"v_mul_f32 v75, v72, v71\n"
"v_exp_f32 v74, v74\n"
"v_fract_f32 v75, v75\n"
"v_cos_f32 v128, v75\n"
"v_sin_f32 v129, v75\n"
"v_mfma_f32_32x32x16_bf16 v[192:207], a[136:139], a[176:179], v[192:207]\n"
"ds_read_b128 a[176:179], v12 offset:14336\n"
"v_mul_f32 v128, v128, v74\n"
"v_mul_f32 v129, v129, v74\n"
"s_lshl_b32 s68, s7, 9\n"
"s_add_u32 s68, s68, 0\n"
"v_cvt_f32_u32 v72, s68\n"
"v_fmac_f32 v72, 0x43000000, v73\n"
"v_mul_f32 v74, v72, v70\n"
"v_mul_f32 v75, v72, v71\n"
"v_mfma_f32_32x32x16_bf16 v[208:223], a[136:139], a[180:183], v[208:223]\n"
"ds_read_b128 a[180:183], v154 offset:14336\n"
"v_exp_f32 v74, v74\n"
"v_fract_f32 v75, v75\n"
"v_cos_f32 v130, v75\n"
"v_sin_f32 v131, v75\n"
"v_cndmask_b32 v74, 0, v74, s[18:19]\n"
"v_mul_f32 v130, v130, v74\n"
"v_mfma_f32_32x32x16_bf16 v[192:207], a[140:143], a[184:187], v[192:207]\n"
"ds_read_b128 a[184:187], v13 offset:14336\n"
"v_mul_f32 v131, v131, v74\n"
"s_lshl_b32 s68, s7, 9\n"
"s_add_u32 s68, s68, 256\n"
"v_cvt_f32_u32 v72, s68\n"
"v_fmac_f32 v72, 0x43000000, v73\n"
"v_mul_f32 v74, v72, v70\n"
"v_mul_f32 v75, v72, v71\n"
"v_mfma_f32_32x32x16_bf16 v[208:223], a[140:143], a[188:191], v[208:223]\n"
"s_waitcnt lgkmcnt(3)\n"
"ds_read_b128 a[188:191], v155 offset:14336\n"
"v_exp_f32 v74, v74\n"
"v_fract_f32 v75, v75\n"
"v_cos_f32 v132, v75\n"
"v_sin_f32 v133, v75\n"
"v_cndmask_b32 v74, 0, v74, s[20:21]\n"
"v_mfma_f32_32x32x16_bf16 v[192:207], a[144:147], a[160:163], v[192:207]\n"
"s_waitcnt vmcnt(1)\n"
"s_barrier\n"
"ds_read_b128 a[160:163], v10 offset:16384\n"
"v_mul_f32 v132, v132, v74\n"
"v_mul_f32 v133, v133, v74\n"
"v_mul_f32 v71, v68, v68\n"
"v_add_f32 v70, -1.0, v116\n"
"v_fmac_f32 v71, v69, v69\n"
"v_mul_f32 v134, v70, v68\n"
"v_mfma_f32_32x32x16_bf16 v[208:223], a[144:147], a[164:167], v[208:223]\n"
"ds_read_b128 a[164:167], v152 offset:16384\n"
"v_rcp_f32 v71, v71\n"
"v_mul_f32 v135, v117, v68\n"
"v_fmac_f32 v134, v117, v69\n"
"v_fma_f32 v135, -v70, v69, v135\n"
"v_add_f32 v71, v71, v71\n"
"v_mul_f32 v134, v134, v71\n"
"v_mul_f32 v135, v135, v71\n"
"v_mfma_f32_32x32x16_bf16 v[192:207], a[148:151], a[168:171], v[192:207]\n"
"ds_read_b128 a[168:171], v11 offset:16384\n"
"s_waitcnt vmcnt(0)\n"
"ds_read_b128 v[68:71], v14 offset:0\n"
"ds_read_b128 v[72:75], v14 offset:16\n"
"v_mfma_f32_32x32x16_bf16 v[208:223], a[148:151], a[172:175], v[208:223]\n"
"s_waitcnt lgkmcnt(5)\n"
"ds_read_b128 a[172:175], v153 offset:16384\n"
"v_mfma_f32_32x32x16_bf16 v[192:207], a[152:155], a[176:179], v[192:207]\n"
"ds_read_b128 a[176:179], v12 offset:16384\n"
"s_waitcnt lgkmcnt(2)\n"
"v_mul_f32 v160, v160, v68\n"
"v_mul_f32 v176, v176, v68\n"
"v_mul_f32 v161, v161, v69\n"
"v_mul_f32 v177, v177, v69\n"
"v_mul_f32 v162, v162, v70\n"
"v_mul_f32 v178, v178, v70\n"
"v_mul_f32 v163, v163, v71\n"
"v_mul_f32 v179, v179, v71\n"
"v_mul_f32 v164, v164, v72\n"
"v_mfma_f32_32x32x16_bf16 v[208:223], a[152:155], a[180:183], v[208:223]\n"
"ds_read_b128 a[180:183], v154 offset:16384\n"
"v_mul_f32 v180, v180, v72\n"
"v_mul_f32 v165, v165, v73\n"
"v_mul_f32 v181, v181, v73\n"
"v_mul_f32 v166, v166, v74\n"
"v_mul_f32 v182, v182, v74\n"
"v_mul_f32 v167, v167, v75\n"
"v_mul_f32 v183, v183, v75\n"
"v_mfma_f32_32x32x16_bf16 v[192:207], a[156:159], a[184:187], v[192:207]\n"
"ds_read_b128 a[184:187], v13 offset:16384\n"
"ds_read_b128 v[68:71], v14 offset:32\n"
"ds_read_b128 v[72:75], v14 offset:48\n"
"v_mfma_f32_32x32x16_bf16 v[208:223], a[156:159], a[188:191], v[208:223]\n"
"ds_read_b128 a[188:191], v155 offset:16384\n"
"v_mfma_f32_32x32x16_bf16 v[192:207], v[40:43], v[84:87], v[192:207]\n"
"s_waitcnt lgkmcnt(1)\n"
"v_mul_f32 v168, v168, v68\n"
"v_mul_f32 v184, v184, v68\n"
"v_mul_f32 v169, v169, v69\n"
"v_mul_f32 v185, v185, v69\n"
"v_mul_f32 v170, v170, v70\n"
"v_mul_f32 v186, v186, v70\n"
"v_mul_f32 v171, v171, v71\n"
"v_mul_f32 v187, v187, v71\n"
"v_mul_f32 v172, v172, v72\n"
"v_mul_f32 v188, v188, v72\n"
"v_mfma_f32_32x32x16_bf16 v[208:223], a[196:199], v[84:87], v[208:223]\n"
"v_mul_f32 v173, v173, v73\n"
"v_mul_f32 v189, v189, v73\n"
"v_mul_f32 v174, v174, v74\n"
"v_mul_f32 v190, v190, v74\n"
"v_mul_f32 v175, v175, v75\n"
"v_mul_f32 v191, v191, v75\n"
"v_fma_f32 v70, v44, v160, v161\n"
"v_fma_f32 v71, v44, v176, v177\n"
"v_fma_f32 v70, -v45, v176, v70\n"
"v_mfma_f32_32x32x16_bf16 v[224:239], a[128:131], a[160:163], v[224:239]\n"
"ds_read_b128 a[160:163], v10 offset:18432\n"
"v_fma_f32 v71, v45, v160, v71\n"
"v_fma_f32 v68, v44, v70, v162\n"
"v_fma_f32 v69, v44, v71, v178\n"
"v_fma_f32 v68, -v45, v71, v68\n"
"v_fma_f32 v69, v45, v70, v69\n"
"v_fma_f32 v70, v44, v68, v163\n"
"v_fma_f32 v71, v44, v69, v179\n"
"v_fma_f32 v70, -v45, v69, v70\n"
"v_fma_f32 v71, v45, v68, v71\n"
"v_mfma_f32_32x32x16_bf16 v[240:255], a[128:131], a[164:167], v[240:255]\n"
"ds_read_b128 a[164:167], v152 offset:18432\n"
"v_fma_f32 v68, v44, v70, v164\n"
"v_fma_f32 v69, v44, v71, v180\n"
"v_fma_f32 v68, -v45, v71, v68\n"
"v_fma_f32 v69, v45, v70, v69\n"
"v_fma_f32 v70, v44, v68, v165\n"
"v_fma_f32 v71, v44, v69, v181\n"
"v_fma_f32 v70, -v45, v69, v70\n"
"v_fma_f32 v71, v45, v68, v71\n"
"v_fma_f32 v68, v44, v70, v166\n"
"v_mfma_f32_32x32x16_bf16 v[224:239], a[132:135], a[168:171], v[224:239]\n"
"ds_read_b128 a[168:171], v11 offset:18432\n"
"v_fma_f32 v69, v44, v71, v182\n"
"v_fma_f32 v68, -v45, v71, v68\n"
"v_fma_f32 v69, v45, v70, v69\n"
"v_fma_f32 v70, v44, v68, v167\n"
"v_fma_f32 v71, v44, v69, v183\n"
"v_fma_f32 v70, -v45, v69, v70\n"
"v_fma_f32 v71, v45, v68, v71\n"
"v_fma_f32 v68, v44, v70, v168\n"
"v_fma_f32 v69, v44, v71, v184\n"
"v_mfma_f32_32x32x16_bf16 v[240:255], a[132:135], a[172:175], v[240:255]\n"
"ds_read_b128 a[172:175], v153 offset:18432\n"
"v_fma_f32 v68, -v45, v71, v68\n"
"v_fma_f32 v69, v45, v70, v69\n"
"v_fma_f32 v70, v44, v68, v169\n"
"v_fma_f32 v71, v44, v69, v185\n"
"v_fma_f32 v70, -v45, v69, v70\n"
"v_fma_f32 v71, v45, v68, v71\n"
"v_fma_f32 v68, v44, v70, v170\n"
"v_fma_f32 v69, v44, v71, v186\n"
"v_mfma_f32_32x32x16_bf16 v[224:239], a[136:139], a[176:179], v[224:239]\n"
"ds_read_b128 a[176:179], v12 offset:18432\n"
"v_fma_f32 v68, -v45, v71, v68\n"
"v_fma_f32 v69, v45, v70, v69\n"
"v_fma_f32 v70, v44, v68, v171\n"
"v_fma_f32 v71, v44, v69, v187\n"
"v_fma_f32 v70, -v45, v69, v70\n"
"v_fma_f32 v71, v45, v68, v71\n"
"v_fma_f32 v68, v44, v70, v172\n"
"v_fma_f32 v69, v44, v71, v188\n"
"v_fma_f32 v68, -v45, v71, v68\n"
"v_mfma_f32_32x32x16_bf16 v[240:255], a[136:139], a[180:183], v[240:255]\n"
"ds_read_b128 a[180:183], v154 offset:18432\n"
"v_fma_f32 v69, v45, v70, v69\n"
"v_fma_f32 v70, v44, v68, v173\n"
"v_fma_f32 v71, v44, v69, v189\n"
"v_fma_f32 v70, -v45, v69, v70\n"
"v_fma_f32 v71, v45, v68, v71\n"
"v_fma_f32 v68, v44, v70, v174\n"
"v_fma_f32 v69, v44, v71, v190\n"
"v_fma_f32 v68, -v45, v71, v68\n"
"v_fma_f32 v69, v45, v70, v69\n"
"v_mfma_f32_32x32x16_bf16 v[224:239], a[140:143], a[184:187], v[224:239]\n"
"ds_read_b128 a[184:187], v13 offset:18432\n"
"v_fma_f32 v70, v44, v68, v175\n"
"v_fma_f32 v71, v44, v69, v191\n"
"v_fma_f32 v70, -v45, v69, v70\n"
"v_fma_f32 v71, v45, v68, v71\n"
"v_mov_b32 v68, v70\n"
"v_mov_b32 v69, v71\n"
"s_nop 0\n"
"v_permlane32_swap_b32 v70, v68\n"
"s_waitcnt lgkmcnt(7)\n"
"v_mfma_f32_32x32x16_bf16 v[240:255], a[140:143], a[188:191], v[240:255]\n"
"s_waitcnt lgkmcnt(3)\n"
"ds_read_b128 a[188:191], v155 offset:18432\n"
"v_permlane32_swap_b32 v71, v69\n"
"v_fma_f32 v66, v46, v70, v68\n"
"v_fma_f32 v67, v46, v71, v69\n"
"v_fma_f32 v66, -v47, v71, v66\n"
"v_fma_f32 v67, v47, v70, v67\n"
"v_cndmask_b32 v64, 0, v70, s[16:17]\n"
"v_cndmask_b32 v65, 0, v71, s[16:17]\n"
"v_mfma_f32_32x32x16_bf16 v[224:239], a[144:147], a[160:163], v[224:239]\n"
"ds_write_b64 v29, v[66:67] offset:0\n"
"v_mfma_f32_32x32x16_bf16 v[240:255], a[144:147], a[164:167], v[240:255]\n"
"v_mfma_f32_32x32x16_bf16 v[224:239], a[148:151], a[168:171], v[224:239]\n"
"v_mfma_f32_32x32x16_bf16 v[240:255], a[148:151], a[172:175], v[240:255]\n"
"s_waitcnt lgkmcnt(1)\n"
"v_mfma_f32_32x32x16_bf16 v[224:239], a[152:155], a[176:179], v[224:239]\n"
"v_mfma_f32_32x32x16_bf16 v[240:255], a[152:155], a[180:183], v[240:255]\n"
"v_mfma_f32_32x32x16_bf16 v[224:239], a[156:159], a[184:187], v[224:239]\n"
"s_waitcnt lgkmcnt(0)\n"
"s_barrier\n"
"ds_read_b64 v[68:69], v30 offset:0\n"
"ds_read_b64 v[70:71], v30 offset:512\n"
"ds_read_b64 v[72:73], v30 offset:1024\n"
"v_mov_b32 v77, s72\n"
"v_mov_b32 v79, s73\n"
"s_mov_b32 s70, 0\n"
"v_mfma_f32_32x32x16_bf16 v[240:255], a[156:159], a[188:191], v[240:255]\n"
"v_mfma_f32_32x32x16_bf16 v[224:239], v[40:43], v[88:91], v[224:239]\n"
"s_waitcnt lgkmcnt(0)\n"
"v_mul_f32 v74, v48, v68\n"
"v_mul_f32 v75, v48, v69\n"
"v_fma_f32 v74, -v49, v69, v74\n"
"v_fma_f32 v75, v49, v68, v75\n"
"v_fma_f32 v68, v50, v70, v74\n"
"v_fma_f32 v69, v50, v71, v75\n"
"v_fma_f32 v68, -v51, v71, v68\n"
"v_fma_f32 v69, v51, v70, v69\n"
"v_fma_f32 v74, v52, v72, v68\n"
"v_fma_f32 v75, v52, v73, v69\n"
"v_mfma_f32_32x32x16_bf16 v[240:255], a[196:199], v[88:91], v[240:255]\n"
"v_fma_f32 v74, -v53, v73, v74\n"
"v_fma_f32 v75, v53, v72, v75\n"
"v_fma_f32 v76, v54, v74, v66\n"
"v_fma_f32 v78, v54, v75, v67\n"
"v_fma_f32 v76, -v55, v75, v76\n"
"v_fma_f32 v78, v55, v74, v78\n"
"K1S_c3:\n"
"buffer_store_dwordx4 v[76:79], v35, s[64:67], s70 offen sc1\n"
"v_fma_f32 v66, v46, v74, v64\n"
"v_fma_f32 v67, v46, v75, v65\n"
"v_fma_f32 v66, -v47, v75, v66\n"
"v_fma_f32 v67, v47, v74, v67\n"
"ds_read_b128 v[68:71], v14 offset:0\n"
"ds_read_b128 v[72:75], v14 offset:16\n"
"s_waitcnt lgkmcnt(0)\n"
"v_mul_f32 v192, v192, v68\n"
"v_mul_f32 v208, v208, v68\n"
"v_mul_f32 v193, v193, v69\n"
"v_mul_f32 v209, v209, v69\n"
"v_mul_f32 v194, v194, v70\n"
"v_mul_f32 v210, v210, v70\n"
"v_mul_f32 v195, v195, v71\n"
"v_mul_f32 v211, v211, v71\n"
"v_mul_f32 v196, v196, v72\n"
"v_mul_f32 v212, v212, v72\n"
"v_mul_f32 v197, v197, v73\n"
"v_mul_f32 v213, v213, v73\n"
"v_mul_f32 v198, v198, v74\n"
"v_mul_f32 v214, v214, v74\n"
"v_mul_f32 v199, v199, v75\n"
"v_mul_f32 v215, v215, v75\n"
"ds_read_b128 v[68:71], v14 offset:32\n"
"ds_read_b128 v[72:75], v14 offset:48\n"
"s_waitcnt lgkmcnt(0)\n"
"v_mul_f32 v200, v200, v68\n"
"v_mul_f32 v216, v216, v68\n"
"v_mul_f32 v201, v201, v69\n"
"v_mul_f32 v217, v217, v69\n"
"v_mul_f32 v202, v202, v70\n"
"v_mul_f32 v218, v218, v70\n"
"v_mul_f32 v203, v203, v71\n"
"v_mul_f32 v219, v219, v71\n"
"v_mul_f32 v204, v204, v72\n"
"v_mul_f32 v220, v220, v72\n"
"v_mul_f32 v205, v205, v73\n"
"v_mul_f32 v221, v221, v73\n"
"v_mul_f32 v206, v206, v74\n"
"v_mul_f32 v222, v222, v74\n"
"v_mul_f32 v207, v207, v75\n"
"v_mul_f32 v223, v223, v75\n"
"v_fma_f32 v70, v96, v192, v193\n"
"v_fma_f32 v71, v96, v208, v209\n"
"v_fma_f32 v70, -v97, v208, v70\n"
"v_fma_f32 v71, v97, v192, v71\n"
"v_fma_f32 v68, v96, v70, v194\n"
"v_fma_f32 v69, v96, v71, v210\n"
"v_fma_f32 v68, -v97, v71, v68\n"
"v_fma_f32 v69, v97, v70, v69\n"
"v_fma_f32 v70, v96, v68, v195\n"
"v_fma_f32 v71, v96, v69, v211\n"
"v_fma_f32 v70, -v97, v69, v70\n"
"v_fma_f32 v71, v97, v68, v71\n"
"v_fma_f32 v68, v96, v70, v196\n"
"v_fma_f32 v69, v96, v71, v212\n"
"v_fma_f32 v68, -v97, v71, v68\n"
"v_fma_f32 v69, v97, v70, v69\n"
"v_fma_f32 v70, v96, v68, v197\n"
"v_fma_f32 v71, v96, v69, v213\n"
"v_fma_f32 v70, -v97, v69, v70\n"
"v_fma_f32 v71, v97, v68, v71\n"
"v_fma_f32 v68, v96, v70, v198\n"
"v_fma_f32 v69, v96, v71, v214\n"
"v_fma_f32 v68, -v97, v71, v68\n"
"v_fma_f32 v69, v97, v70, v69\n"
"v_fma_f32 v70, v96, v68, v199\n"
"v_fma_f32 v71, v96, v69, v215\n"
"v_fma_f32 v70, -v97, v69, v70\n"
"v_fma_f32 v71, v97, v68, v71\n"
"v_fma_f32 v68, v96, v70, v200\n"
"v_fma_f32 v69, v96, v71, v216\n"
"v_fma_f32 v68, -v97, v71, v68\n"
"v_fma_f32 v69, v97, v70, v69\n"
"v_fma_f32 v70, v96, v68, v201\n"
"v_fma_f32 v71, v96, v69, v217\n"
"v_fma_f32 v70, -v97, v69, v70\n"
"v_fma_f32 v71, v97, v68, v71\n"
"v_fma_f32 v68, v96, v70, v202\n"
"v_fma_f32 v69, v96, v71, v218\n"
"v_fma_f32 v68, -v97, v71, v68\n"
"v_fma_f32 v69, v97, v70, v69\n"
"v_fma_f32 v70, v96, v68, v203\n"
"v_fma_f32 v71, v96, v69, v219\n"
"v_fma_f32 v70, -v97, v69, v70\n"
"v_fma_f32 v71, v97, v68, v71\n"
"v_fma_f32 v68, v96, v70, v204\n"
"v_fma_f32 v69, v96, v71, v220\n"
"v_fma_f32 v68, -v97, v71, v68\n"
"v_fma_f32 v69, v97, v70, v69\n"
"v_fma_f32 v70, v96, v68, v205\n"
"v_fma_f32 v71, v96, v69, v221\n"
"v_fma_f32 v70, -v97, v69, v70\n"
"v_fma_f32 v71, v97, v68, v71\n"
"v_fma_f32 v68, v96, v70, v206\n"
"v_fma_f32 v69, v96, v71, v222\n"
"v_fma_f32 v68, -v97, v71, v68\n"
"v_fma_f32 v69, v97, v70, v69\n"
"v_fma_f32 v70, v96, v68, v207\n"
"v_fma_f32 v71, v96, v69, v223\n"
"v_fma_f32 v70, -v97, v69, v70\n"
"v_fma_f32 v71, v97, v68, v71\n"
"v_mov_b32 v68, v70\n"
"v_mov_b32 v69, v71\n"
"s_nop 0\n"
"v_permlane32_swap_b32 v70, v68\n"
"v_permlane32_swap_b32 v71, v69\n"
"v_fma_f32 v4, v98, v70, v68\n"
"v_fma_f32 v5, v98, v71, v69\n"
"v_fma_f32 v4, -v99, v71, v4\n"
"v_fma_f32 v5, v99, v70, v5\n"
"v_cndmask_b32 v2, 0, v70, s[16:17]\n"
"v_cndmask_b32 v3, 0, v71, s[16:17]\n"
"ds_write_b64 v29, v[4:5] offset:40960\n"
"s_waitcnt lgkmcnt(0)\n"
"s_barrier\n"
"ds_read_b64 v[68:69], v30 offset:40960\n"
"ds_read_b64 v[70:71], v30 offset:41472\n"
"ds_read_b64 v[72:73], v30 offset:41984\n"
"v_mov_b32 v77, s72\n"
"v_mov_b32 v79, s73\n"
"s_mov_b32 s70, 8192\n"
"s_waitcnt lgkmcnt(0)\n"
"v_mul_f32 v74, v100, v68\n"
"v_mul_f32 v75, v100, v69\n"
"v_fma_f32 v74, -v101, v69, v74\n"
"v_fma_f32 v75, v101, v68, v75\n"
"v_fma_f32 v68, v102, v70, v74\n"
"v_fma_f32 v69, v102, v71, v75\n"
"v_fma_f32 v68, -v103, v71, v68\n"
"v_fma_f32 v69, v103, v70, v69\n"
"v_fma_f32 v74, v104, v72, v68\n"
"v_fma_f32 v75, v104, v73, v69\n"
"v_fma_f32 v74, -v105, v73, v74\n"
"v_fma_f32 v75, v105, v72, v75\n"
"v_fma_f32 v76, v106, v74, v4\n"
"v_fma_f32 v78, v106, v75, v5\n"
"v_fma_f32 v76, -v107, v75, v76\n"
"v_fma_f32 v78, v107, v74, v78\n"
"buffer_store_dwordx4 v[76:79], v35, s[64:67], s70 offen sc1\n"
"v_fma_f32 v4, v98, v74, v2\n"
"v_fma_f32 v5, v98, v75, v3\n"
"v_fma_f32 v4, -v99, v75, v4\n"
"v_fma_f32 v5, v99, v74, v5\n"
"buffer_load_dwordx4 v[136:139], v38, s[64:67], 0 offen sc1\n"
"buffer_load_dwordx4 v[140:143], v39, s[64:67], 0 offen sc1\n"
"ds_read_b128 v[68:71], v14 offset:0\n"
"ds_read_b128 v[72:75], v14 offset:16\n"
"s_waitcnt lgkmcnt(0)\n"
"v_mul_f32 v224, v224, v68\n"
"v_mul_f32 v240, v240, v68\n"
"v_mul_f32 v225, v225, v69\n"
"v_mul_f32 v241, v241, v69\n"
"v_mul_f32 v226, v226, v70\n"
"v_mul_f32 v242, v242, v70\n"
"v_mul_f32 v227, v227, v71\n"
"v_mul_f32 v243, v243, v71\n"
"v_mul_f32 v228, v228, v72\n"
"v_mul_f32 v244, v244, v72\n"
"v_mul_f32 v229, v229, v73\n"
"v_mul_f32 v245, v245, v73\n"
"v_mul_f32 v230, v230, v74\n"
"v_mul_f32 v246, v246, v74\n"
"v_mul_f32 v231, v231, v75\n"
"v_mul_f32 v247, v247, v75\n"
"ds_read_b128 v[68:71], v14 offset:32\n"
"ds_read_b128 v[72:75], v14 offset:48\n"
"s_waitcnt lgkmcnt(0)\n"
"v_mul_f32 v232, v232, v68\n"
"v_mul_f32 v248, v248, v68\n"
"v_mul_f32 v233, v233, v69\n"
"v_mul_f32 v249, v249, v69\n"
"v_mul_f32 v234, v234, v70\n"
"v_mul_f32 v250, v250, v70\n"
"v_mul_f32 v235, v235, v71\n"
"v_mul_f32 v251, v251, v71\n"
"v_mul_f32 v236, v236, v72\n"
"v_mul_f32 v252, v252, v72\n"
"v_mul_f32 v237, v237, v73\n"
"v_mul_f32 v253, v253, v73\n"
"v_mul_f32 v238, v238, v74\n"
"v_mul_f32 v254, v254, v74\n"
"v_mul_f32 v239, v239, v75\n"
"v_mul_f32 v255, v255, v75\n"
"v_fma_f32 v70, v116, v224, v225\n"
"v_fma_f32 v71, v116, v240, v241\n"
"v_fma_f32 v70, -v117, v240, v70\n"
"v_fma_f32 v71, v117, v224, v71\n"
"v_fma_f32 v68, v116, v70, v226\n"
"v_fma_f32 v69, v116, v71, v242\n"
"v_fma_f32 v68, -v117, v71, v68\n"
"v_fma_f32 v69, v117, v70, v69\n"
"v_fma_f32 v70, v116, v68, v227\n"
"v_fma_f32 v71, v116, v69, v243\n"
"v_fma_f32 v70, -v117, v69, v70\n"
"v_fma_f32 v71, v117, v68, v71\n"
"v_fma_f32 v68, v116, v70, v228\n"
"v_fma_f32 v69, v116, v71, v244\n"
"v_fma_f32 v68, -v117, v71, v68\n"
"v_fma_f32 v69, v117, v70, v69\n"
"v_fma_f32 v70, v116, v68, v229\n"
"v_fma_f32 v71, v116, v69, v245\n"
"v_fma_f32 v70, -v117, v69, v70\n"
"v_fma_f32 v71, v117, v68, v71\n"
"v_fma_f32 v68, v116, v70, v230\n"
"v_fma_f32 v69, v116, v71, v246\n"
"v_fma_f32 v68, -v117, v71, v68\n"
"v_fma_f32 v69, v117, v70, v69\n"
"v_fma_f32 v70, v116, v68, v231\n"
"v_fma_f32 v71, v116, v69, v247\n"
"v_fma_f32 v70, -v117, v69, v70\n"
"v_fma_f32 v71, v117, v68, v71\n"
"v_fma_f32 v68, v116, v70, v232\n"
"v_fma_f32 v69, v116, v71, v248\n"
"v_fma_f32 v68, -v117, v71, v68\n"
"v_fma_f32 v69, v117, v70, v69\n"
"v_fma_f32 v70, v116, v68, v233\n"
"v_fma_f32 v71, v116, v69, v249\n"
"v_fma_f32 v70, -v117, v69, v70\n"
"v_fma_f32 v71, v117, v68, v71\n"
"v_fma_f32 v68, v116, v70, v234\n"
"v_fma_f32 v69, v116, v71, v250\n"
"v_fma_f32 v68, -v117, v71, v68\n"
"v_fma_f32 v69, v117, v70, v69\n"
"v_fma_f32 v70, v116, v68, v235\n"
"v_fma_f32 v71, v116, v69, v251\n"
"v_fma_f32 v70, -v117, v69, v70\n"
"v_fma_f32 v71, v117, v68, v71\n"
"v_fma_f32 v68, v116, v70, v236\n"
"v_fma_f32 v69, v116, v71, v252\n"
"v_fma_f32 v68, -v117, v71, v68\n"
"v_fma_f32 v69, v117, v70, v69\n"
"v_fma_f32 v70, v116, v68, v237\n"
"v_fma_f32 v71, v116, v69, v253\n"
"v_fma_f32 v70, -v117, v69, v70\n"
"v_fma_f32 v71, v117, v68, v71\n"
"v_fma_f32 v68, v116, v70, v238\n"
"v_fma_f32 v69, v116, v71, v254\n"
"v_fma_f32 v68, -v117, v71, v68\n"
"v_fma_f32 v69, v117, v70, v69\n"
"v_fma_f32 v70, v116, v68, v239\n"
"v_fma_f32 v71, v116, v69, v255\n"
"v_fma_f32 v70, -v117, v69, v70\n"
"v_fma_f32 v71, v117, v68, v71\n"
"v_mov_b32 v68, v70\n"
"v_mov_b32 v69, v71\n"
"s_nop 0\n"
"v_permlane32_swap_b32 v70, v68\n"
"v_permlane32_swap_b32 v71, v69\n"
"v_fma_f32 v8, v118, v70, v68\n"
"v_fma_f32 v9, v118, v71, v69\n"
"v_fma_f32 v8, -v119, v71, v8\n"
"v_fma_f32 v9, v119, v70, v9\n"
"v_cndmask_b32 v6, 0, v70, s[16:17]\n"
"v_cndmask_b32 v7, 0, v71, s[16:17]\n"
"ds_write_b64 v29, v[8:9] offset:38912\n"
"s_waitcnt vmcnt(0)\n"
"v_xor_b32 v68, s72, v137\n"
"v_xor_b32 v69, s73, v139\n"
"v_or_b32 v68, v68, v69\n"
"v_cmp_ne_u32 s[22:23], 0, v68\n"
"v_xor_b32 v68, s72, v141\n"
"v_xor_b32 v69, s73, v143\n"
"v_or_b32 v68, v68, v69\n"
"v_cmp_ne_u32 s[24:25], 0, v68\n"
"s_and_b64 s[22:23], s[22:23], s[18:19]\n"
"s_and_b64 s[24:25], s[24:25], s[20:21]\n"
"s_or_b64 s[22:23], s[22:23], s[24:25]\n"
"s_cbranch_scc1 K1S_g0_slow\n"
"K1S_g0_back:\n"
"v_mul_f32 v68, v58, v136\n"
"v_mul_f32 v69, v58, v138\n"
"v_fma_f32 v68, -v59, v138, v68\n"
"v_fmac_f32 v69, v59, v136\n"
"v_fmac_f32 v68, v60, v140\n"
"v_fmac_f32 v69, v60, v142\n"
"v_fma_f32 v68, -v61, v142, v68\n"
"v_fmac_f32 v69, v61, v140\n"
"ds_write_b64 v31, v[68:69] offset:0\n"
"s_waitcnt lgkmcnt(0)\n"
"s_barrier\n"
"ds_read_b64 v[68:69], v30 offset:38912\n"
"ds_read_b64 v[70:71], v30 offset:39424\n"
"ds_read_b64 v[72:73], v30 offset:39936\n"
"v_mov_b32 v77, s72\n"
"v_mov_b32 v79, s73\n"
"s_mov_b32 s70, 16384\n"
"s_waitcnt lgkmcnt(0)\n"
"v_mul_f32 v74, v120, v68\n"
"v_mul_f32 v75, v120, v69\n"
"v_fma_f32 v74, -v121, v69, v74\n"
"v_fma_f32 v75, v121, v68, v75\n"
"v_fma_f32 v68, v122, v70, v74\n"
"v_fma_f32 v69, v122, v71, v75\n"
"v_fma_f32 v68, -v123, v71, v68\n"
"v_fma_f32 v69, v123, v70, v69\n"
"v_fma_f32 v74, v124, v72, v68\n"
"v_fma_f32 v75, v124, v73, v69\n"
"v_fma_f32 v74, -v125, v73, v74\n"
"v_fma_f32 v75, v125, v72, v75\n"
"v_fma_f32 v76, v126, v74, v8\n"
"v_fma_f32 v78, v126, v75, v9\n"
"v_fma_f32 v76, -v127, v75, v76\n"
"v_fma_f32 v78, v127, v74, v78\n"
"buffer_store_dwordx4 v[76:79], v35, s[64:67], s70 offen sc1\n"
"v_fma_f32 v8, v118, v74, v6\n"
"v_fma_f32 v9, v118, v75, v7\n"
"v_fma_f32 v8, -v119, v75, v8\n"
"v_fma_f32 v9, v119, v74, v9\n"
"buffer_load_dwordx4 v[144:147], v18, s[64:67], 0 offen sc1\n"
"buffer_load_dwordx4 v[148:151], v19, s[64:67], 0 offen sc1\n"
"s_lshl_b32 s68, s7, 13\n"
"s_add_u32 s68, s68, 23072768\n"
"v_lshl_add_u32 v24, v27, 7, s68\n"
"buffer_load_dwordx2 v[22:23], v24, s[64:67], 0 offen sc1\n"
"v_add_u32 v25, 0x400, v28\n"
"ds_read_b128 v[68:71], v32 offset:0\n"
"ds_read_b128 v[72:75], v32 offset:16\n"
"s_waitcnt lgkmcnt(0)\n"
"v_add_f32 v68, v68, v70\n"
"v_add_f32 v72, v72, v74\n"
"v_add_f32 v69, v69, v71\n"
"v_add_f32 v73, v73, v75\n"
"v_add_f32 v76, v68, v72\n"
"v_add_f32 v77, v69, v73\n"
"ds_read_b128 v[68:71], v32 offset:32\n"
"ds_read_b128 v[72:75], v32 offset:48\n"
"s_waitcnt lgkmcnt(0)\n"
"v_add_f32 v68, v68, v70\n"
"v_add_f32 v72, v72, v74\n"
"v_add_f32 v69, v69, v71\n"
"v_add_f32 v73, v73, v75\n"
"v_add_f32 v68, v68, v72\n"
"v_add_f32 v76, v76, v68\n"
"v_add_f32 v69, v69, v73\n"
"v_add_f32 v77, v77, v69\n"
"v_fma_f32 v68, v56, v76, v66\n"
"v_fma_f32 v69, v56, v77, v67\n"
"v_fma_f32 v68, -v57, v77, v68\n"
"v_fma_f32 v69, v57, v76, v69\n"
"v_fma_f32 v70, v44, v68, v160\n"
"v_fma_f32 v71, v44, v69, v176\n"
"v_fma_f32 v70, -v45, v69, v70\n"
"v_fma_f32 v71, v45, v68, v71\n"
"v_mul_f32 v160, v62, v70\n"
"v_mul_f32 v176, v62, v71\n"
"v_fma_f32 v160, -v63, v71, v160\n"
"v_fma_f32 v176, -v63, v70, -v176\n"
"v_fma_f32 v68, v44, v70, v161\n"
"v_fma_f32 v69, v44, v71, v177\n"
"v_fma_f32 v68, -v45, v71, v68\n"
"v_fma_f32 v69, v45, v70, v69\n"
"v_mul_f32 v161, v62, v68\n"
"v_mul_f32 v177, v62, v69\n"
"v_fma_f32 v161, -v63, v69, v161\n"
"v_fma_f32 v177, -v63, v68, -v177\n"
"v_fma_f32 v70, v44, v68, v162\n"
"v_fma_f32 v71, v44, v69, v178\n"
"v_fma_f32 v70, -v45, v69, v70\n"
"v_fma_f32 v71, v45, v68, v71\n"
"v_mul_f32 v162, v62, v70\n"
"v_mul_f32 v178, v62, v71\n"
"v_fma_f32 v162, -v63, v71, v162\n"
"v_fma_f32 v178, -v63, v70, -v178\n"
"v_fma_f32 v68, v44, v70, v163\n"
"v_fma_f32 v69, v44, v71, v179\n"
"v_fma_f32 v68, -v45, v71, v68\n"
"v_fma_f32 v69, v45, v70, v69\n"
"v_mul_f32 v163, v62, v68\n"
"v_mul_f32 v179, v62, v69\n"
"v_fma_f32 v163, -v63, v69, v163\n"
"v_fma_f32 v179, -v63, v68, -v179\n"
"v_cvt_pk_bf16_f32 v160, v160, v161\n"
"v_cvt_pk_bf16_f32 v161, v162, v163\n"
"ds_write_b64 v33, v[160:161] offset:0\n"
"v_cvt_pk_bf16_f32 v176, v176, v177\n"
"v_cvt_pk_bf16_f32 v177, v178, v179\n"
"ds_write_b64 v33, v[176:177] offset:2304\n"
"v_fma_f32 v70, v44, v68, v164\n"
"v_fma_f32 v71, v44, v69, v180\n"
"v_fma_f32 v70, -v45, v69, v70\n"
"v_fma_f32 v71, v45, v68, v71\n"
"v_mul_f32 v164, v62, v70\n"
"v_mul_f32 v180, v62, v71\n"
"v_fma_f32 v164, -v63, v71, v164\n"
"v_fma_f32 v180, -v63, v70, -v180\n"
"v_fma_f32 v68, v44, v70, v165\n"
"v_fma_f32 v69, v44, v71, v181\n"
"v_fma_f32 v68, -v45, v71, v68\n"
"v_fma_f32 v69, v45, v70, v69\n"
"v_mul_f32 v165, v62, v68\n"
"v_mul_f32 v181, v62, v69\n"
"v_fma_f32 v165, -v63, v69, v165\n"
"v_fma_f32 v181, -v63, v68, -v181\n"
"v_fma_f32 v70, v44, v68, v166\n"
"v_fma_f32 v71, v44, v69, v182\n"
"v_fma_f32 v70, -v45, v69, v70\n"
"v_fma_f32 v71, v45, v68, v71\n"
"v_mul_f32 v166, v62, v70\n"
"v_mul_f32 v182, v62, v71\n"
"v_fma_f32 v166, -v63, v71, v166\n"
"v_fma_f32 v182, -v63, v70, -v182\n"
"v_fma_f32 v68, v44, v70, v167\n"
"v_fma_f32 v69, v44, v71, v183\n"
"v_fma_f32 v68, -v45, v71, v68\n"
"v_fma_f32 v69, v45, v70, v69\n"
"v_mul_f32 v167, v62, v68\n"
"v_mul_f32 v183, v62, v69\n"
"v_fma_f32 v167, -v63, v69, v167\n"
"v_fma_f32 v183, -v63, v68, -v183\n"
"v_cvt_pk_bf16_f32 v164, v164, v165\n"
"v_cvt_pk_bf16_f32 v165, v166, v167\n"
"ds_write_b64 v33, v[164:165] offset:16\n"
"v_cvt_pk_bf16_f32 v180, v180, v181\n"
"v_cvt_pk_bf16_f32 v181, v182, v183\n"
"ds_write_b64 v33, v[180:181] offset:2320\n"
"v_fma_f32 v70, v44, v68, v168\n"
"v_fma_f32 v71, v44, v69, v184\n"
"v_fma_f32 v70, -v45, v69, v70\n"
"v_fma_f32 v71, v45, v68, v71\n"
"v_mul_f32 v168, v62, v70\n"
"v_mul_f32 v184, v62, v71\n"
"v_fma_f32 v168, -v63, v71, v168\n"
"v_fma_f32 v184, -v63, v70, -v184\n"
"v_fma_f32 v68, v44, v70, v169\n"
"v_fma_f32 v69, v44, v71, v185\n"
"v_fma_f32 v68, -v45, v71, v68\n"
"v_fma_f32 v69, v45, v70, v69\n"
"v_mul_f32 v169, v62, v68\n"
"v_mul_f32 v185, v62, v69\n"
"v_fma_f32 v169, -v63, v69, v169\n"
"v_fma_f32 v185, -v63, v68, -v185\n"
"v_fma_f32 v70, v44, v68, v170\n"
"v_fma_f32 v71, v44, v69, v186\n"
"v_fma_f32 v70, -v45, v69, v70\n"
"v_fma_f32 v71, v45, v68, v71\n"
"v_mul_f32 v170, v62, v70\n"
"v_mul_f32 v186, v62, v71\n"
"v_fma_f32 v170, -v63, v71, v170\n"
"v_fma_f32 v186, -v63, v70, -v186\n"
"v_fma_f32 v68, v44, v70, v171\n"
"v_fma_f32 v69, v44, v71, v187\n"
"v_fma_f32 v68, -v45, v71, v68\n"
"v_fma_f32 v69, v45, v70, v69\n"
"v_mul_f32 v171, v62, v68\n"
"v_mul_f32 v187, v62, v69\n"
"v_fma_f32 v171, -v63, v69, v171\n"
"v_fma_f32 v187, -v63, v68, -v187\n"
"v_cvt_pk_bf16_f32 v168, v168, v169\n"
"v_cvt_pk_bf16_f32 v169, v170, v171\n"
"ds_write_b64 v33, v[168:169] offset:32\n"
"v_cvt_pk_bf16_f32 v184, v184, v185\n"
"v_cvt_pk_bf16_f32 v185, v186, v187\n"
"ds_write_b64 v33, v[184:185] offset:2336\n"
"v_fma_f32 v70, v44, v68, v172\n"
"v_fma_f32 v71, v44, v69, v188\n"
"v_fma_f32 v70, -v45, v69, v70\n"
"v_fma_f32 v71, v45, v68, v71\n"
"v_mul_f32 v172, v62, v70\n"
"v_mul_f32 v188, v62, v71\n"
"v_fma_f32 v172, -v63, v71, v172\n"
"v_fma_f32 v188, -v63, v70, -v188\n"
"v_fma_f32 v68, v44, v70, v173\n"
"v_fma_f32 v69, v44, v71, v189\n"
"v_fma_f32 v68, -v45, v71, v68\n"
"v_fma_f32 v69, v45, v70, v69\n"
"v_mul_f32 v173, v62, v68\n"
"v_mul_f32 v189, v62, v69\n"
"v_fma_f32 v173, -v63, v69, v173\n"
"v_fma_f32 v189, -v63, v68, -v189\n"
"v_fma_f32 v70, v44, v68, v174\n"
"v_fma_f32 v71, v44, v69, v190\n"
"v_fma_f32 v70, -v45, v69, v70\n"
"v_fma_f32 v71, v45, v68, v71\n"
"v_mul_f32 v174, v62, v70\n"
"v_mul_f32 v190, v62, v71\n"
"v_fma_f32 v174, -v63, v71, v174\n"
"v_fma_f32 v190, -v63, v70, -v190\n"
"v_fma_f32 v68, v44, v70, v175\n"
"v_fma_f32 v69, v44, v71, v191\n"
"v_fma_f32 v68, -v45, v71, v68\n"
"v_fma_f32 v69, v45, v70, v69\n"
"v_mul_f32 v175, v62, v68\n"
"v_mul_f32 v191, v62, v69\n"
"v_fma_f32 v175, -v63, v69, v175\n"
"v_fma_f32 v191, -v63, v68, -v191\n"
"v_cvt_pk_bf16_f32 v172, v172, v173\n"
"v_cvt_pk_bf16_f32 v173, v174, v175\n"
"ds_write_b64 v33, v[172:173] offset:48\n"
"v_cvt_pk_bf16_f32 v188, v188, v189\n"
"v_cvt_pk_bf16_f32 v189, v190, v191\n"
"ds_write_b64 v33, v[188:189] offset:2352\n"
"ds_read_b64_tr_b16 v[160:161], v34 offset:0\n"
"ds_read_b64_tr_b16 v[162:163], v34 offset:288\n"
"ds_read_b64_tr_b16 v[164:165], v34 offset:32\n"
"ds_read_b64_tr_b16 v[166:167], v34 offset:320\n"
"ds_read_b64_tr_b16 v[168:169], v34 offset:2304\n"
"ds_read_b64_tr_b16 v[170:171], v34 offset:2592\n"
"ds_read_b64_tr_b16 v[172:173], v34 offset:2336\n"
"ds_read_b64_tr_b16 v[174:175], v34 offset:2624\n"
"s_bfe_u32 s26, s100, 0x80000\n"
"s_lshl_b32 s26, s26, 12\n"
"s_add_u32 s26, s26, s43\n"
"s_bfe_u32 s27, s100, 0x80008\n"
"s_lshl_b32 s27, s27, 12\n"
"s_add_u32 s27, s27, s43\n"
"s_waitcnt lgkmcnt(0)\n"
"buffer_store_dwordx4 v[160:163], v28, s[64:67], s26 offen offset:0 sc1\n"
"buffer_store_dwordx4 v[164:167], v28, s[64:67], s26 offen offset:1024 sc1\n"
"buffer_store_dwordx4 v[168:171], v28, s[64:67], s27 offen offset:0 sc1\n"
"buffer_store_dwordx4 v[172:175], v28, s[64:67], s27 offen offset:1024 sc1\n"
"s_waitcnt vmcnt(5)\n"
"v_xor_b32 v68, s72, v145\n"
"v_xor_b32 v69, s73, v147\n"
"v_or_b32 v68, v68, v69\n"
"v_cmp_ne_u32 s[22:23], 0, v68\n"
"v_xor_b32 v68, s72, v149\n"
"v_xor_b32 v69, s73, v151\n"
"v_or_b32 v68, v68, v69\n"
"v_cmp_ne_u32 s[24:25], 0, v68\n"
"s_and_b64 s[22:23], s[22:23], s[18:19]\n"
"s_and_b64 s[24:25], s[24:25], s[20:21]\n"
"s_or_b64 s[22:23], s[22:23], s[24:25]\n"
"s_cbranch_scc1 K1S_g1_slow\n"
"K1S_g1_back:\n"
"v_mul_f32 v68, v110, v144\n"
"v_mul_f32 v69, v110, v146\n"
"v_fma_f32 v68, -v111, v146, v68\n"
"v_fmac_f32 v69, v111, v144\n"
"v_fmac_f32 v68, v112, v148\n"
"v_fmac_f32 v69, v112, v150\n"
"v_fma_f32 v68, -v113, v150, v68\n"
"v_fmac_f32 v69, v113, v148\n"
"ds_write_b64 v31, v[68:69] offset:36864\n"
"buffer_load_dwordx4 v[152:155], v20, s[64:67], 0 offen sc1\n"
"buffer_load_dwordx4 v[156:159], v21, s[64:67], 0 offen sc1\n"
"s_waitcnt vmcnt(6)\n"
"v_xor_b32 v22, s72, v22\n"
"v_xor_b32 v23, s73, v23\n"
"v_or_b32 v22, v22, v23\n"
"v_cmp_ne_u32 vcc, 0, v22\n"
"s_nop 0\n"
"s_cmp_lg_u64 vcc, 0\n"
"s_cbranch_scc1 K1S_s_slow\n"
"K1S_s_back:\n"
"s_waitcnt lgkmcnt(0)\n"
"s_barrier\n"
"ds_read_b128 v[68:71], v32 offset:36864\n"
"ds_read_b128 v[72:75], v32 offset:36880\n"
"s_waitcnt lgkmcnt(0)\n"
"v_add_f32 v68, v68, v70\n"
"v_add_f32 v72, v72, v74\n"
"v_add_f32 v69, v69, v71\n"
"v_add_f32 v73, v73, v75\n"
"v_add_f32 v76, v68, v72\n"
"v_add_f32 v77, v69, v73\n"
"ds_read_b128 v[68:71], v32 offset:36896\n"
"ds_read_b128 v[72:75], v32 offset:36912\n"
"s_waitcnt lgkmcnt(0)\n"
"v_add_f32 v68, v68, v70\n"
"v_add_f32 v72, v72, v74\n"
"v_add_f32 v69, v69, v71\n"
"v_add_f32 v73, v73, v75\n"
"v_add_f32 v68, v68, v72\n"
"v_add_f32 v76, v76, v68\n"
"v_add_f32 v69, v69, v73\n"
"v_add_f32 v77, v77, v69\n"
"v_fma_f32 v68, v108, v76, v4\n"
"v_fma_f32 v69, v108, v77, v5\n"
"s_mov_b32 s33, 0\n"
"buffer_load_dwordx4 a[200:203], v28, s[76:79], s33 offen offset:0 sc1\n"
"v_fma_f32 v68, -v109, v77, v68\n"
"v_fma_f32 v69, v109, v76, v69\n"
"v_fma_f32 v70, v96, v68, v192\n"
"v_fma_f32 v71, v96, v69, v208\n"
"v_fma_f32 v70, -v97, v69, v70\n"
"v_fma_f32 v71, v97, v68, v71\n"
"v_mul_f32 v192, v114, v70\n"
"v_mul_f32 v208, v114, v71\n"
"v_fma_f32 v192, -v115, v71, v192\n"
"v_fma_f32 v208, -v115, v70, -v208\n"
"v_fma_f32 v68, v96, v70, v193\n"
"v_fma_f32 v69, v96, v71, v209\n"
"v_fma_f32 v68, -v97, v71, v68\n"
"v_fma_f32 v69, v97, v70, v69\n"
"v_mul_f32 v193, v114, v68\n"
"v_mul_f32 v209, v114, v69\n"
"v_fma_f32 v193, -v115, v69, v193\n"
"v_fma_f32 v209, -v115, v68, -v209\n"
"v_fma_f32 v70, v96, v68, v194\n"
"v_fma_f32 v71, v96, v69, v210\n"
"buffer_load_dwordx4 a[204:207], v28, s[76:79], s33 offen offset:1024 sc1\n"
"v_fma_f32 v70, -v97, v69, v70\n"
"v_fma_f32 v71, v97, v68, v71\n"
"v_mul_f32 v194, v114, v70\n"
"v_mul_f32 v210, v114, v71\n"
"v_fma_f32 v194, -v115, v71, v194\n"
"v_fma_f32 v210, -v115, v70, -v210\n"
"v_fma_f32 v68, v96, v70, v195\n"
"v_fma_f32 v69, v96, v71, v211\n"
"v_fma_f32 v68, -v97, v71, v68\n"
"v_fma_f32 v69, v97, v70, v69\n"
"v_mul_f32 v195, v114, v68\n"
"v_mul_f32 v211, v114, v69\n"
"v_fma_f32 v195, -v115, v69, v195\n"
"v_fma_f32 v211, -v115, v68, -v211\n"
"v_cvt_pk_bf16_f32 v192, v192, v193\n"
"v_cvt_pk_bf16_f32 v193, v194, v195\n"
"ds_write_b64 v33, v[192:193] offset:0\n"
"v_cvt_pk_bf16_f32 v208, v208, v209\n"
"v_cvt_pk_bf16_f32 v209, v210, v211\n"
"ds_write_b64 v33, v[208:209] offset:2304\n"
"buffer_load_dwordx4 a[208:211], v28, s[76:79], s33 offen offset:2048 sc1\n"
"v_fma_f32 v70, v96, v68, v196\n"
"v_fma_f32 v71, v96, v69, v212\n"
"v_fma_f32 v70, -v97, v69, v70\n"
"v_fma_f32 v71, v97, v68, v71\n"
"v_mul_f32 v196, v114, v70\n"
"v_mul_f32 v212, v114, v71\n"
"v_fma_f32 v196, -v115, v71, v196\n"
"v_fma_f32 v212, -v115, v70, -v212\n"
"v_fma_f32 v68, v96, v70, v197\n"
"v_fma_f32 v69, v96, v71, v213\n"
"v_fma_f32 v68, -v97, v71, v68\n"
"v_fma_f32 v69, v97, v70, v69\n"
"v_mul_f32 v197, v114, v68\n"
"v_mul_f32 v213, v114, v69\n"
"v_fma_f32 v197, -v115, v69, v197\n"
"v_fma_f32 v213, -v115, v68, -v213\n"
"v_fma_f32 v70, v96, v68, v198\n"
"v_fma_f32 v71, v96, v69, v214\n"
"v_fma_f32 v70, -v97, v69, v70\n"
"v_fma_f32 v71, v97, v68, v71\n"
"buffer_load_dwordx4 a[212:215], v28, s[76:79], s33 offen offset:3072 sc1\n"
"v_mul_f32 v198, v114, v70\n"
"v_mul_f32 v214, v114, v71\n"
"v_fma_f32 v198, -v115, v71, v198\n"
"v_fma_f32 v214, -v115, v70, -v214\n"
"v_fma_f32 v68, v96, v70, v199\n"
"v_fma_f32 v69, v96, v71, v215\n"
"v_fma_f32 v68, -v97, v71, v68\n"
"v_fma_f32 v69, v97, v70, v69\n"
"v_mul_f32 v199, v114, v68\n"
"v_mul_f32 v215, v114, v69\n"
"v_fma_f32 v199, -v115, v69, v199\n"
"v_fma_f32 v215, -v115, v68, -v215\n"
"v_cvt_pk_bf16_f32 v196, v196, v197\n"
"v_cvt_pk_bf16_f32 v197, v198, v199\n"
"ds_write_b64 v33, v[196:197] offset:16\n"
"v_cvt_pk_bf16_f32 v212, v212, v213\n"
"v_cvt_pk_bf16_f32 v213, v214, v215\n"
"ds_write_b64 v33, v[212:213] offset:2320\n"
"v_fma_f32 v70, v96, v68, v200\n"
"v_fma_f32 v71, v96, v69, v216\n"
"buffer_load_dwordx4 a[216:219], v25, s[76:79], s33 offen offset:3072 sc1\n"
"v_fma_f32 v70, -v97, v69, v70\n"
"v_fma_f32 v71, v97, v68, v71\n"
"v_mul_f32 v200, v114, v70\n"
"v_mul_f32 v216, v114, v71\n"
"v_fma_f32 v200, -v115, v71, v200\n"
"v_fma_f32 v216, -v115, v70, -v216\n"
"v_fma_f32 v68, v96, v70, v201\n"
"v_fma_f32 v69, v96, v71, v217\n"
"v_fma_f32 v68, -v97, v71, v68\n"
"v_fma_f32 v69, v97, v70, v69\n"
"v_mul_f32 v201, v114, v68\n"
"v_mul_f32 v217, v114, v69\n"
"v_fma_f32 v201, -v115, v69, v201\n"
"v_fma_f32 v217, -v115, v68, -v217\n"
"v_fma_f32 v70, v96, v68, v202\n"
"v_fma_f32 v71, v96, v69, v218\n"
"v_fma_f32 v70, -v97, v69, v70\n"
"v_fma_f32 v71, v97, v68, v71\n"
"v_mul_f32 v202, v114, v70\n"
"v_mul_f32 v218, v114, v71\n"
"s_mov_b32 s33, 40960\n"
"buffer_load_dwordx4 a[220:223], v28, s[76:79], s33 offen offset:0 sc1\n"
"v_fma_f32 v202, -v115, v71, v202\n"
"v_fma_f32 v218, -v115, v70, -v218\n"
"v_fma_f32 v68, v96, v70, v203\n"
"v_fma_f32 v69, v96, v71, v219\n"
"v_fma_f32 v68, -v97, v71, v68\n"
"v_fma_f32 v69, v97, v70, v69\n"
"v_mul_f32 v203, v114, v68\n"
"v_mul_f32 v219, v114, v69\n"
"v_fma_f32 v203, -v115, v69, v203\n"
"v_fma_f32 v219, -v115, v68, -v219\n"
"v_cvt_pk_bf16_f32 v200, v200, v201\n"
"v_cvt_pk_bf16_f32 v201, v202, v203\n"
"ds_write_b64 v33, v[200:201] offset:32\n"
"v_cvt_pk_bf16_f32 v216, v216, v217\n"
"v_cvt_pk_bf16_f32 v217, v218, v219\n"
"ds_write_b64 v33, v[216:217] offset:2336\n"
"v_fma_f32 v70, v96, v68, v204\n"
"v_fma_f32 v71, v96, v69, v220\n"
"v_fma_f32 v70, -v97, v69, v70\n"
"v_fma_f32 v71, v97, v68, v71\n"
"buffer_load_dwordx4 a[224:227], v28, s[76:79], s33 offen offset:1024 sc1\n"
"v_mul_f32 v204, v114, v70\n"
"v_mul_f32 v220, v114, v71\n"
"v_fma_f32 v204, -v115, v71, v204\n"
"v_fma_f32 v220, -v115, v70, -v220\n"
"v_fma_f32 v68, v96, v70, v205\n"
"v_fma_f32 v69, v96, v71, v221\n"
"v_fma_f32 v68, -v97, v71, v68\n"
"v_fma_f32 v69, v97, v70, v69\n"
"v_mul_f32 v205, v114, v68\n"
"v_mul_f32 v221, v114, v69\n"
"v_fma_f32 v205, -v115, v69, v205\n"
"v_fma_f32 v221, -v115, v68, -v221\n"
"v_fma_f32 v70, v96, v68, v206\n"
"v_fma_f32 v71, v96, v69, v222\n"
"v_fma_f32 v70, -v97, v69, v70\n"
"v_fma_f32 v71, v97, v68, v71\n"
"v_mul_f32 v206, v114, v70\n"
"v_mul_f32 v222, v114, v71\n"
"v_fma_f32 v206, -v115, v71, v206\n"
"v_fma_f32 v222, -v115, v70, -v222\n"
"buffer_load_dwordx4 a[228:231], v28, s[76:79], s33 offen offset:2048 sc1\n"
"v_fma_f32 v68, v96, v70, v207\n"
"v_fma_f32 v69, v96, v71, v223\n"
"v_fma_f32 v68, -v97, v71, v68\n"
"v_fma_f32 v69, v97, v70, v69\n"
"v_mul_f32 v207, v114, v68\n"
"v_mul_f32 v223, v114, v69\n"
"v_fma_f32 v207, -v115, v69, v207\n"
"v_fma_f32 v223, -v115, v68, -v223\n"
"v_cvt_pk_bf16_f32 v204, v204, v205\n"
"v_cvt_pk_bf16_f32 v205, v206, v207\n"
"ds_write_b64 v33, v[204:205] offset:48\n"
"v_cvt_pk_bf16_f32 v220, v220, v221\n"
"v_cvt_pk_bf16_f32 v221, v222, v223\n"
"ds_write_b64 v33, v[220:221] offset:2352\n"
"ds_read_b64_tr_b16 v[192:193], v34 offset:0\n"
"ds_read_b64_tr_b16 v[194:195], v34 offset:288\n"
"ds_read_b64_tr_b16 v[196:197], v34 offset:32\n"
"ds_read_b64_tr_b16 v[198:199], v34 offset:320\n"
"ds_read_b64_tr_b16 v[200:201], v34 offset:2304\n"
"ds_read_b64_tr_b16 v[202:203], v34 offset:2592\n"
"buffer_load_dwordx4 a[232:235], v28, s[76:79], s33 offen offset:3072 sc1\n"
"ds_read_b64_tr_b16 v[204:205], v34 offset:2336\n"
"ds_read_b64_tr_b16 v[206:207], v34 offset:2624\n"
"s_bfe_u32 s26, s100, 0x80010\n"
"s_lshl_b32 s26, s26, 12\n"
"s_add_u32 s26, s26, s43\n"
"s_bfe_u32 s27, s100, 0x80018\n"
"s_lshl_b32 s27, s27, 12\n"
"s_add_u32 s27, s27, s43\n"
"s_waitcnt lgkmcnt(0)\n"
"buffer_store_dwordx4 v[192:195], v28, s[64:67], s26 offen offset:0 sc1\n"
"buffer_store_dwordx4 v[196:199], v28, s[64:67], s26 offen offset:1024 sc1\n"
"buffer_store_dwordx4 v[200:203], v28, s[64:67], s27 offen offset:0 sc1\n"
"buffer_store_dwordx4 v[204:207], v28, s[64:67], s27 offen offset:1024 sc1\n"
"s_waitcnt vmcnt(13)\n"
"v_xor_b32 v68, s72, v153\n"
"v_xor_b32 v69, s73, v155\n"
"v_or_b32 v68, v68, v69\n"
"v_cmp_ne_u32 s[22:23], 0, v68\n"
"v_xor_b32 v68, s72, v157\n"
"v_xor_b32 v69, s73, v159\n"
"v_or_b32 v68, v68, v69\n"
"v_cmp_ne_u32 s[24:25], 0, v68\n"
"buffer_load_dwordx4 a[236:239], v25, s[76:79], s33 offen offset:3072 sc1\n"
"s_and_b64 s[22:23], s[22:23], s[18:19]\n"
"s_and_b64 s[24:25], s[24:25], s[20:21]\n"
"s_or_b64 s[22:23], s[22:23], s[24:25]\n"
"s_cbranch_scc1 K1S_g2_slow\n"
"K1S_g2_back:\n"
"v_mul_f32 v68, v130, v152\n"
"v_mul_f32 v69, v130, v154\n"
"v_fma_f32 v68, -v131, v154, v68\n"
"v_fmac_f32 v69, v131, v152\n"
"v_fmac_f32 v68, v132, v156\n"
"v_fmac_f32 v69, v132, v158\n"
"v_fma_f32 v68, -v133, v158, v68\n"
"v_fmac_f32 v69, v133, v156\n"
"ds_write_b64 v31, v[68:69] offset:38912\n"
"s_waitcnt lgkmcnt(0)\n"
"s_barrier\n"
"ds_read_b128 v[68:71], v32 offset:38912\n"
"ds_read_b128 v[72:75], v32 offset:38928\n"
"s_waitcnt lgkmcnt(0)\n"
"v_add_f32 v68, v68, v70\n"
"v_add_f32 v72, v72, v74\n"
"v_add_f32 v69, v69, v71\n"
"v_add_f32 v73, v73, v75\n"
"s_mov_b32 s33, 81920\n"
"buffer_load_dwordx4 a[240:243], v28, s[76:79], s33 offen offset:0 sc1\n"
"v_add_f32 v76, v68, v72\n"
"v_add_f32 v77, v69, v73\n"
"ds_read_b128 v[68:71], v32 offset:38944\n"
"ds_read_b128 v[72:75], v32 offset:38960\n"
"s_waitcnt lgkmcnt(0)\n"
"v_add_f32 v68, v68, v70\n"
"v_add_f32 v72, v72, v74\n"
"v_add_f32 v69, v69, v71\n"
"v_add_f32 v73, v73, v75\n"
"v_add_f32 v68, v68, v72\n"
"v_add_f32 v76, v76, v68\n"
"v_add_f32 v69, v69, v73\n"
"v_add_f32 v77, v77, v69\n"
"v_fma_f32 v68, v128, v76, v8\n"
"v_fma_f32 v69, v128, v77, v9\n"
"v_fma_f32 v68, -v129, v77, v68\n"
"v_fma_f32 v69, v129, v76, v69\n"
"v_fma_f32 v70, v116, v68, v224\n"
"v_fma_f32 v71, v116, v69, v240\n"
"v_fma_f32 v70, -v117, v69, v70\n"
"v_fma_f32 v71, v117, v68, v71\n"
"buffer_load_dwordx4 a[244:247], v28, s[76:79], s33 offen offset:1024 sc1\n"
"v_mul_f32 v224, v134, v70\n"
"v_mul_f32 v240, v134, v71\n"
"v_fma_f32 v224, -v135, v71, v224\n"
"v_fma_f32 v240, -v135, v70, -v240\n"
"v_fma_f32 v68, v116, v70, v225\n"
"v_fma_f32 v69, v116, v71, v241\n"
"v_fma_f32 v68, -v117, v71, v68\n"
"v_fma_f32 v69, v117, v70, v69\n"
"v_mul_f32 v225, v134, v68\n"
"v_mul_f32 v241, v134, v69\n"
"v_fma_f32 v225, -v135, v69, v225\n"
"v_fma_f32 v241, -v135, v68, -v241\n"
"v_fma_f32 v70, v116, v68, v226\n"
"v_fma_f32 v71, v116, v69, v242\n"
"v_fma_f32 v70, -v117, v69, v70\n"
"v_fma_f32 v71, v117, v68, v71\n"
"v_mul_f32 v226, v134, v70\n"
"v_mul_f32 v242, v134, v71\n"
"v_fma_f32 v226, -v135, v71, v226\n"
"v_fma_f32 v242, -v135, v70, -v242\n"
"buffer_load_dwordx4 a[248:251], v28, s[76:79], s33 offen offset:2048 sc1\n"
"v_fma_f32 v68, v116, v70, v227\n"
"v_fma_f32 v69, v116, v71, v243\n"
"v_fma_f32 v68, -v117, v71, v68\n"
"v_fma_f32 v69, v117, v70, v69\n"
"v_mul_f32 v227, v134, v68\n"
"v_mul_f32 v243, v134, v69\n"
"v_fma_f32 v227, -v135, v69, v227\n"
"v_fma_f32 v243, -v135, v68, -v243\n"
"v_cvt_pk_bf16_f32 v224, v224, v225\n"
"v_cvt_pk_bf16_f32 v225, v226, v227\n"
"ds_write_b64 v33, v[224:225] offset:0\n"
"v_cvt_pk_bf16_f32 v240, v240, v241\n"
"v_cvt_pk_bf16_f32 v241, v242, v243\n"
"ds_write_b64 v33, v[240:241] offset:2304\n"
"v_fma_f32 v70, v116, v68, v228\n"
"v_fma_f32 v71, v116, v69, v244\n"
"v_fma_f32 v70, -v117, v69, v70\n"
"v_fma_f32 v71, v117, v68, v71\n"
"v_mul_f32 v228, v134, v70\n"
"v_mul_f32 v244, v134, v71\n"
"buffer_load_dwordx4 a[252:255], v28, s[76:79], s33 offen offset:3072 sc1\n"
"v_fma_f32 v228, -v135, v71, v228\n"
"v_fma_f32 v244, -v135, v70, -v244\n"
"v_fma_f32 v68, v116, v70, v229\n"
"v_fma_f32 v69, v116, v71, v245\n"
"v_fma_f32 v68, -v117, v71, v68\n"
"v_fma_f32 v69, v117, v70, v69\n"
"v_mul_f32 v229, v134, v68\n"
"v_mul_f32 v245, v134, v69\n"
"v_fma_f32 v229, -v135, v69, v229\n"
"v_fma_f32 v245, -v135, v68, -v245\n"
"v_fma_f32 v70, v116, v68, v230\n"
"v_fma_f32 v71, v116, v69, v246\n"
"v_fma_f32 v70, -v117, v69, v70\n"
"v_fma_f32 v71, v117, v68, v71\n"
"v_mul_f32 v230, v134, v70\n"
"v_mul_f32 v246, v134, v71\n"
"v_fma_f32 v230, -v135, v71, v230\n"
"v_fma_f32 v246, -v135, v70, -v246\n"
"v_fma_f32 v68, v116, v70, v231\n"
"v_fma_f32 v69, v116, v71, v247\n"
"buffer_load_dwordx4 a[192:195], v25, s[76:79], s33 offen offset:3072 sc1\n"
"v_fma_f32 v68, -v117, v71, v68\n"
"v_fma_f32 v69, v117, v70, v69\n"
"v_mul_f32 v231, v134, v68\n"
"v_mul_f32 v247, v134, v69\n"
"v_fma_f32 v231, -v135, v69, v231\n"
"v_fma_f32 v247, -v135, v68, -v247\n"
"v_cvt_pk_bf16_f32 v228, v228, v229\n"
"v_cvt_pk_bf16_f32 v229, v230, v231\n"
"ds_write_b64 v33, v[228:229] offset:16\n"
"v_cvt_pk_bf16_f32 v244, v244, v245\n"
"v_cvt_pk_bf16_f32 v245, v246, v247\n"
"ds_write_b64 v33, v[244:245] offset:2320\n"
"v_fma_f32 v70, v116, v68, v232\n"
"v_fma_f32 v71, v116, v69, v248\n"
"v_fma_f32 v70, -v117, v69, v70\n"
"v_fma_f32 v71, v117, v68, v71\n"
"v_mul_f32 v232, v134, v70\n"
"v_mul_f32 v248, v134, v71\n"
"v_fma_f32 v232, -v135, v71, v232\n"
"v_fma_f32 v248, -v135, v70, -v248\n"
"s_mov_b32 s33, 122880\n"
"buffer_load_dwordx4 a[196:199], v28, s[76:79], s33 offen offset:0 sc1\n"
"v_fma_f32 v68, v116, v70, v233\n"
"v_fma_f32 v69, v116, v71, v249\n"
"v_fma_f32 v68, -v117, v71, v68\n"
"v_fma_f32 v69, v117, v70, v69\n"
"v_mul_f32 v233, v134, v68\n"
"v_mul_f32 v249, v134, v69\n"
"v_fma_f32 v233, -v135, v69, v233\n"
"v_fma_f32 v249, -v135, v68, -v249\n"
"v_fma_f32 v70, v116, v68, v234\n"
"v_fma_f32 v71, v116, v69, v250\n"
"v_fma_f32 v70, -v117, v69, v70\n"
"v_fma_f32 v71, v117, v68, v71\n"
"v_mul_f32 v234, v134, v70\n"
"v_mul_f32 v250, v134, v71\n"
"v_fma_f32 v234, -v135, v71, v234\n"
"v_fma_f32 v250, -v135, v70, -v250\n"
"v_fma_f32 v68, v116, v70, v235\n"
"v_fma_f32 v69, v116, v71, v251\n"
"v_fma_f32 v68, -v117, v71, v68\n"
"v_fma_f32 v69, v117, v70, v69\n"
"buffer_load_dwordx4 v[80:83], v28, s[76:79], s33 offen offset:1024 sc1\n"
"v_mul_f32 v235, v134, v68\n"
"v_mul_f32 v251, v134, v69\n"
"v_fma_f32 v235, -v135, v69, v235\n"
"v_fma_f32 v251, -v135, v68, -v251\n"
"v_cvt_pk_bf16_f32 v232, v232, v233\n"
"v_cvt_pk_bf16_f32 v233, v234, v235\n"
"ds_write_b64 v33, v[232:233] offset:32\n"
"v_cvt_pk_bf16_f32 v248, v248, v249\n"
"v_cvt_pk_bf16_f32 v249, v250, v251\n"
"ds_write_b64 v33, v[248:249] offset:2336\n"
"v_fma_f32 v70, v116, v68, v236\n"
"v_fma_f32 v71, v116, v69, v252\n"
"v_fma_f32 v70, -v117, v69, v70\n"
"v_fma_f32 v71, v117, v68, v71\n"
"v_mul_f32 v236, v134, v70\n"
"v_mul_f32 v252, v134, v71\n"
"v_fma_f32 v236, -v135, v71, v236\n"
"v_fma_f32 v252, -v135, v70, -v252\n"
"v_fma_f32 v68, v116, v70, v237\n"
"v_fma_f32 v69, v116, v71, v253\n"
"buffer_load_dwordx4 v[84:87], v28, s[76:79], s33 offen offset:2048 sc1\n"
"v_fma_f32 v68, -v117, v71, v68\n"
"v_fma_f32 v69, v117, v70, v69\n"
"v_mul_f32 v237, v134, v68\n"
"v_mul_f32 v253, v134, v69\n"
"v_fma_f32 v237, -v135, v69, v237\n"
"v_fma_f32 v253, -v135, v68, -v253\n"
"v_fma_f32 v70, v116, v68, v238\n"
"v_fma_f32 v71, v116, v69, v254\n"
"v_fma_f32 v70, -v117, v69, v70\n"
"v_fma_f32 v71, v117, v68, v71\n"
"v_mul_f32 v238, v134, v70\n"
"v_mul_f32 v254, v134, v71\n"
"v_fma_f32 v238, -v135, v71, v238\n"
"v_fma_f32 v254, -v135, v70, -v254\n"
"v_fma_f32 v68, v116, v70, v239\n"
"v_fma_f32 v69, v116, v71, v255\n"
"v_fma_f32 v68, -v117, v71, v68\n"
"v_fma_f32 v69, v117, v70, v69\n"
"v_mul_f32 v239, v134, v68\n"
"v_mul_f32 v255, v134, v69\n"
"buffer_load_dwordx4 v[88:91], v28, s[76:79], s33 offen offset:3072 sc1\n"
"v_fma_f32 v239, -v135, v69, v239\n"
"v_fma_f32 v255, -v135, v68, -v255\n"
"v_cvt_pk_bf16_f32 v236, v236, v237\n"
"v_cvt_pk_bf16_f32 v237, v238, v239\n"
"ds_write_b64 v33, v[236:237] offset:48\n"
"v_cvt_pk_bf16_f32 v252, v252, v253\n"
"v_cvt_pk_bf16_f32 v253, v254, v255\n"
"ds_write_b64 v33, v[252:253] offset:2352\n"
"ds_read_b64_tr_b16 v[224:225], v34 offset:0\n"
"ds_read_b64_tr_b16 v[226:227], v34 offset:288\n"
"ds_read_b64_tr_b16 v[228:229], v34 offset:32\n"
"ds_read_b64_tr_b16 v[230:231], v34 offset:320\n"
"ds_read_b64_tr_b16 v[232:233], v34 offset:2304\n"
"ds_read_b64_tr_b16 v[234:235], v34 offset:2592\n"
"ds_read_b64_tr_b16 v[236:237], v34 offset:2336\n"
"ds_read_b64_tr_b16 v[238:239], v34 offset:2624\n"
"s_bfe_u32 s26, s101, 0x80000\n"
"s_lshl_b32 s26, s26, 12\n"
"s_add_u32 s26, s26, s43\n"
"s_bfe_u32 s27, s101, 0x80008\n"
"s_lshl_b32 s27, s27, 12\n"
"s_add_u32 s27, s27, s43\n"
"s_waitcnt lgkmcnt(0)\n"
"buffer_store_dwordx4 v[224:227], v28, s[64:67], s26 offen offset:0 sc1\n"
"buffer_store_dwordx4 v[228:231], v28, s[64:67], s26 offen offset:1024 sc1\n"
"buffer_store_dwordx4 v[232:235], v28, s[64:67], s27 offen offset:0 sc1\n"
"buffer_store_dwordx4 v[236:239], v28, s[64:67], s27 offen offset:1024 sc1\n"
"buffer_load_dwordx4 v[92:95], v25, s[76:79], s33 offen offset:3072 sc1\n"
"s_waitcnt vmcnt(1)\n"
"v_mov_b32 v40, s72\n"
"v_mov_b32 v41, s73\n"
"v_mov_b32 v42, 65536\n"
"buffer_store_dwordx2 v[40:41], v42, s[64:67], s36 offen sc1\n"
"s_lshl_b32 s68, s30, 11\n"
"s_add_u32 s68, s68, 23134208\n"
"v_and_b32 v3, 3, v27\n"
"v_lshl_add_u32 v3, v3, 3, s68\n"
"v_lshrrev_b32 v42, 2, v27\n"
"v_lshl_add_u32 v3, v42, 7, v3\n"
"s_mov_b32 s70, 0\n"
"buffer_load_dwordx2 v[42:43], v3, s[64:67], s70 offen sc1\n"
"s_mov_b32 s33, 163840\n"
"buffer_load_dwordx4 v[224:227], v28, s[76:79], s33 offen offset:0 sc1\n"
"buffer_load_dwordx4 v[228:231], v28, s[76:79], s33 offen offset:1024 sc1\n"
"buffer_load_dwordx4 v[232:235], v28, s[76:79], s33 offen offset:2048 sc1\n"
"buffer_load_dwordx4 v[236:239], v28, s[76:79], s33 offen offset:3072 sc1\n"
"buffer_load_dwordx4 v[240:243], v25, s[76:79], s33 offen offset:3072 sc1\n"
"s_mov_b32 s33, 204800\n"
"buffer_load_dwordx4 v[244:247], v28, s[76:79], s33 offen offset:0 sc1\n"
"buffer_load_dwordx4 v[248:251], v28, s[76:79], s33 offen offset:1024 sc1\n"
"buffer_load_dwordx4 v[252:255], v28, s[76:79], s33 offen offset:2048 sc1\n"
"buffer_load_dwordx4 v[128:131], v28, s[76:79], s33 offen offset:3072 sc1\n"
"buffer_load_dwordx4 v[132:135], v25, s[76:79], s33 offen offset:3072 sc1\n"
"s_mov_b32 s33, 245760\n"
"buffer_load_dwordx4 v[136:139], v28, s[76:79], s33 offen offset:0 sc1\n"
"buffer_load_dwordx4 v[140:143], v28, s[76:79], s33 offen offset:1024 sc1\n"
"s_waitcnt vmcnt(12)\n"
"v_xor_b32 v42, s72, v42\n"
"v_xor_b32 v43, s73, v43\n"
"v_or_b32 v42, v42, v43\n"
"v_cmp_ne_u32 vcc, 0, v42\n"
"s_nop 0\n"
"s_cmp_lg_u64 vcc, 0\n"
"s_cbranch_scc1 K1S_r0_slow\n"
"K1S_r0_back:\n"
"s_mov_b32 s32, 0\n"
"buffer_load_dwordx4 a[160:163], v28, s[84:87], s32 offen offset:0 sc1\n"
"buffer_load_dwordx4 a[164:167], v28, s[84:87], s32 offen offset:1024 sc1\n"
"buffer_load_dwordx4 a[168:171], v28, s[84:87], s32 offen offset:2048 sc1\n"
"buffer_load_dwordx4 a[172:175], v28, s[84:87], s32 offen offset:3072 sc1\n"
"s_mov_b32 s32, 4096\n"
"buffer_load_dwordx4 a[176:179], v28, s[84:87], s32 offen offset:0 sc1\n"
"buffer_load_dwordx4 a[180:183], v28, s[84:87], s32 offen offset:1024 sc1\n"
"buffer_load_dwordx4 a[184:187], v28, s[84:87], s32 offen offset:2048 sc1\n"
"buffer_load_dwordx4 a[188:191], v28, s[84:87], s32 offen offset:3072 sc1\n"
"s_mov_b32 s32, 8192\n"
"buffer_load_dwordx4 v[192:195], v28, s[84:87], s32 offen offset:0 sc1\n"
"buffer_load_dwordx4 v[196:199], v28, s[84:87], s32 offen offset:1024 sc1\n"
"buffer_load_dwordx4 v[200:203], v28, s[84:87], s32 offen offset:2048 sc1\n"
"buffer_load_dwordx4 v[204:207], v28, s[84:87], s32 offen offset:3072 sc1\n"
"s_mov_b32 s32, 0\n"
"buffer_load_dwordx4 v[208:211], v28, s[80:83], s32 offen offset:0 sc1\n"
"buffer_load_dwordx4 v[212:215], v28, s[80:83], s32 offen offset:1024 sc1\n"
"buffer_load_dwordx4 v[216:219], v28, s[80:83], s32 offen offset:2048 sc1\n"
"buffer_load_dwordx4 v[220:223], v28, s[80:83], s32 offen offset:3072 sc1\n"
"K1S_c4:\n"
"s_waitcnt vmcnt(15)\n"
"v_mfma_f32_16x16x32_bf16 a[80:83], a[200:203], a[160:163], 0\n"
"s_waitcnt vmcnt(14)\n"
"v_mfma_f32_16x16x32_bf16 a[84:87], a[200:203], a[164:167], 0\n"
"s_mov_b32 s32, 4096\n"
"buffer_load_dwordx4 v[144:147], v28, s[80:83], s32 offen offset:0 sc1\n"
"s_waitcnt vmcnt(14)\n"
"v_mfma_f32_16x16x32_bf16 a[88:91], a[200:203], a[168:171], 0\n"
"s_waitcnt vmcnt(13)\n"
"v_mfma_f32_16x16x32_bf16 a[92:95], a[200:203], a[172:175], 0\n"
"buffer_load_dwordx4 v[148:151], v28, s[80:83], s32 offen offset:1024 sc1\n"
"v_mfma_f32_16x16x32_bf16 a[96:99], a[204:207], a[160:163], 0\n"
"v_mfma_f32_16x16x32_bf16 a[100:103], a[204:207], a[164:167], 0\n"
"buffer_load_dwordx4 v[152:155], v28, s[80:83], s32 offen offset:2048 sc1\n"
"v_mfma_f32_16x16x32_bf16 a[104:107], a[204:207], a[168:171], 0\n"
"v_mfma_f32_16x16x32_bf16 a[108:111], a[204:207], a[172:175], 0\n"
"v_mfma_f32_16x16x32_bf16 a[112:115], a[208:211], a[160:163], 0\n"
"v_mfma_f32_16x16x32_bf16 a[116:119], a[208:211], a[164:167], 0\n"
"buffer_load_dwordx4 v[156:159], v28, s[80:83], s32 offen offset:3072 sc1\n"
"v_mfma_f32_16x16x32_bf16 a[120:123], a[208:211], a[168:171], 0\n"
"v_mfma_f32_16x16x32_bf16 a[124:127], a[208:211], a[172:175], 0\n"
"s_mov_b32 s32, 8192\n"
"buffer_load_dwordx4 v[160:163], v28, s[80:83], s32 offen offset:0 sc1\n"
"v_mfma_f32_16x16x32_bf16 a[128:131], a[212:215], a[160:163], 0\n"
"v_mfma_f32_16x16x32_bf16 a[132:135], a[212:215], a[164:167], 0\n"
"buffer_load_dwordx4 v[164:167], v28, s[80:83], s32 offen offset:1024 sc1\n"
"v_mfma_f32_16x16x32_bf16 a[136:139], a[212:215], a[168:171], 0\n"
"v_mfma_f32_16x16x32_bf16 a[140:143], a[212:215], a[172:175], 0\n"
"v_mfma_f32_16x16x32_bf16 a[144:147], a[216:219], a[160:163], 0\n"
"s_waitcnt vmcnt(14)\n"
"v_mfma_f32_16x16x32_bf16 a[148:151], a[216:219], a[164:167], 0\n"
"buffer_load_dwordx4 v[168:171], v28, s[80:83], s32 offen offset:2048 sc1\n"
"v_mfma_f32_16x16x32_bf16 a[152:155], a[216:219], a[168:171], 0\n"
"v_mfma_f32_16x16x32_bf16 a[156:159], a[216:219], a[172:175], 0\n"
"v_mfma_f32_16x16x32_bf16 a[80:83], a[220:223], a[176:179], a[80:83]\n"
"v_mfma_f32_16x16x32_bf16 a[84:87], a[220:223], a[180:183], a[84:87]\n"
"buffer_load_dwordx4 v[172:175], v28, s[80:83], s32 offen offset:3072 sc1\n"
"v_mfma_f32_16x16x32_bf16 a[88:91], a[220:223], a[184:187], a[88:91]\n"
"v_mfma_f32_16x16x32_bf16 a[92:95], a[220:223], a[188:191], a[92:95]\n"
"buffer_load_dwordx4 v[176:179], v28, s[76:79], s33 offen offset:2048 sc1\n"
"v_mfma_f32_16x16x32_bf16 a[96:99], a[224:227], a[176:179], a[96:99]\n"
"v_mfma_f32_16x16x32_bf16 a[100:103], a[224:227], a[180:183], a[100:103]\n"
"buffer_load_dwordx4 v[180:183], v28, s[76:79], s33 offen offset:3072 sc1\n"
"v_mfma_f32_16x16x32_bf16 a[104:107], a[224:227], a[184:187], a[104:107]\n"
"v_mfma_f32_16x16x32_bf16 a[108:111], a[224:227], a[188:191], a[108:111]\n"
"v_mfma_f32_16x16x32_bf16 a[112:115], a[228:231], a[176:179], a[112:115]\n"
"v_mfma_f32_16x16x32_bf16 a[116:119], a[228:231], a[180:183], a[116:119]\n"
"buffer_load_dwordx4 v[184:187], v25, s[76:79], s33 offen offset:3072 sc1\n"
"v_mfma_f32_16x16x32_bf16 a[120:123], a[228:231], a[184:187], a[120:123]\n"
"v_mfma_f32_16x16x32_bf16 a[124:127], a[228:231], a[188:191], a[124:127]\n"
"s_mov_b32 s33, 286720\n"
"buffer_load_dwordx4 v[188:191], v28, s[76:79], s33 offen offset:0 sc1\n"
"v_mfma_f32_16x16x32_bf16 a[128:131], a[232:235], a[176:179], a[128:131]\n"
"v_mfma_f32_16x16x32_bf16 a[132:135], a[232:235], a[180:183], a[132:135]\n"
"buffer_load_dwordx4 v[116:119], v28, s[76:79], s33 offen offset:1024 sc1\n"
"v_mfma_f32_16x16x32_bf16 a[136:139], a[232:235], a[184:187], a[136:139]\n"
"v_mfma_f32_16x16x32_bf16 a[140:143], a[232:235], a[188:191], a[140:143]\n"
"v_mfma_f32_16x16x32_bf16 a[144:147], a[236:239], a[176:179], a[144:147]\n"
"s_waitcnt vmcnt(17)\n"
"v_mfma_f32_16x16x32_bf16 a[148:151], a[236:239], a[180:183], a[148:151]\n"
"buffer_load_dwordx4 v[120:123], v28, s[76:79], s33 offen offset:2048 sc1\n"
"v_mfma_f32_16x16x32_bf16 a[152:155], a[236:239], a[184:187], a[152:155]\n"
"v_mfma_f32_16x16x32_bf16 a[156:159], a[236:239], a[188:191], a[156:159]\n"
"v_mfma_f32_16x16x32_bf16 a[80:83], a[240:243], v[192:195], a[80:83]\n"
"v_mfma_f32_16x16x32_bf16 a[84:87], a[240:243], v[196:199], a[84:87]\n"
"buffer_load_dwordx4 v[124:127], v28, s[76:79], s33 offen offset:3072 sc1\n"
"v_mfma_f32_16x16x32_bf16 a[88:91], a[240:243], v[200:203], a[88:91]\n"
"v_mfma_f32_16x16x32_bf16 a[92:95], a[240:243], v[204:207], a[92:95]\n"
"buffer_load_dwordx4 v[44:47], v25, s[76:79], s33 offen offset:3072 sc1\n"
"v_mfma_f32_16x16x32_bf16 a[96:99], a[244:247], v[192:195], a[96:99]\n"
"v_mfma_f32_16x16x32_bf16 a[100:103], a[244:247], v[196:199], a[100:103]\n"
"s_mov_b32 s33, 327680\n"
"buffer_load_dwordx4 v[48:51], v28, s[76:79], s33 offen offset:0 sc1\n"
"v_mfma_f32_16x16x32_bf16 a[104:107], a[244:247], v[200:203], a[104:107]\n"
"s_load_dwordx2 s[20:21], s[4:5], 0x0\n"
"s_load_dwordx4 s[88:91], s[4:5], 0x60\n"
"v_mfma_f32_16x16x32_bf16 a[108:111], a[244:247], v[204:207], a[108:111]\n"
"s_mov_b32 s70, 32768\n"
"buffer_load_dwordx2 v[42:43], v3, s[64:67], s70 offen sc1\n"
"v_mfma_f32_16x16x32_bf16 a[112:115], a[248:251], v[192:195], a[112:115]\n"
"s_and_b32 s68, s6, 1\n"
"s_lshl_b32 s69, s30, 1\n"
"v_mfma_f32_16x16x32_bf16 a[116:119], a[248:251], v[196:199], a[116:119]\n"
"buffer_load_dwordx4 v[52:55], v28, s[76:79], s33 offen offset:1024 sc1\n"
"v_mfma_f32_16x16x32_bf16 a[120:123], a[248:251], v[200:203], a[120:123]\n"
"s_add_u32 s68, s68, s69\n"
"s_lshl_b32 s68, s68, 2\n"
"v_mfma_f32_16x16x32_bf16 a[124:127], a[248:251], v[204:207], a[124:127]\n"
"buffer_load_dwordx4 v[56:59], v28, s[76:79], s33 offen offset:2048 sc1\n"
"v_mfma_f32_16x16x32_bf16 a[128:131], a[252:255], v[192:195], a[128:131]\n"
"s_add_u32 s68, s68, s7\n"
"s_lshl_b32 s100, s68, 8\n"
"v_mfma_f32_16x16x32_bf16 a[132:135], a[252:255], v[196:199], a[132:135]\n"
"buffer_load_dwordx4 v[60:63], v28, s[76:79], s33 offen offset:3072 sc1\n"
"v_mfma_f32_16x16x32_bf16 a[136:139], a[252:255], v[200:203], a[136:139]\n"
"s_mul_i32 s69, s68, 20480\n"
"s_mul_i32 s68, s31, 160\n"
"v_mfma_f32_16x16x32_bf16 a[140:143], a[252:255], v[204:207], a[140:143]\n"
"s_add_u32 s69, s69, s68\n"
"v_mfma_f32_16x16x32_bf16 a[144:147], a[192:195], v[192:195], a[144:147]\n"
"s_waitcnt vmcnt(21)\n"
"v_mfma_f32_16x16x32_bf16 a[148:151], a[192:195], v[196:199], a[148:151]\n"
"buffer_load_dwordx4 v[64:67], v25, s[76:79], s33 offen offset:3072 sc1\n"
"v_mfma_f32_16x16x32_bf16 a[152:155], a[192:195], v[200:203], a[152:155]\n"
"v_mfma_f32_16x16x32_bf16 a[156:159], a[192:195], v[204:207], a[156:159]\n"
"v_mfma_f32_16x16x32_bf16 a[0:3], a[200:203], v[208:211], 0\n"
"v_mfma_f32_16x16x32_bf16 a[4:7], a[200:203], v[212:215], 0\n"
"s_mov_b32 s33, 368640\n"
"buffer_load_dwordx4 v[68:71], v28, s[76:79], s33 offen offset:0 sc1\n"
"v_mfma_f32_16x16x32_bf16 a[8:11], a[200:203], v[216:219], 0\n"
"v_mfma_f32_16x16x32_bf16 a[12:15], a[200:203], v[220:223], 0\n"
"buffer_load_dwordx4 v[72:75], v28, s[76:79], s33 offen offset:1024 sc1\n"
"s_waitcnt lgkmcnt(0)\n"
"v_mfma_f32_16x16x32_bf16 a[16:19], a[204:207], v[208:211], 0\n"
"s_add_u32 s48, s20, s69\n"
"s_addc_u32 s49, s21, 0\n"
"v_mfma_f32_16x16x32_bf16 a[20:23], a[204:207], v[212:215], 0\n"
"buffer_load_dwordx4 a[160:163], v28, s[76:79], s33 offen offset:2048 sc1\n"
"v_mfma_f32_16x16x32_bf16 a[24:27], a[204:207], v[216:219], 0\n"
"s_and_b32 s49, s49, 0xffff\n"
"s_mov_b32 s50, 19360\n"
"v_mfma_f32_16x16x32_bf16 a[28:31], a[204:207], v[220:223], 0\n"
"s_mov_b32 s51, 0x20000\n"
"s_lshl_b32 s69, s69, 1\n"
"v_mfma_f32_16x16x32_bf16 a[32:35], a[208:211], v[208:211], 0\n"
"s_add_u32 s52, s90, s69\n"
"s_addc_u32 s53, s91, 0\n"
"v_mfma_f32_16x16x32_bf16 a[36:39], a[208:211], v[212:215], 0\n"
"buffer_load_dwordx4 a[164:167], v28, s[76:79], s33 offen offset:3072 sc1\n"
"v_mfma_f32_16x16x32_bf16 a[40:43], a[208:211], v[216:219], 0\n"
"s_and_b32 s53, s53, 0xffff\n"
"s_mov_b32 s54, 38720\n"
"v_mfma_f32_16x16x32_bf16 a[44:47], a[208:211], v[220:223], 0\n"
"buffer_load_dwordx4 a[168:171], v25, s[76:79], s33 offen offset:3072 sc1\n"
"v_mfma_f32_16x16x32_bf16 a[48:51], a[212:215], v[208:211], 0\n"
"s_mov_b32 s55, 0x20000\n"
"s_add_u32 s24, s88, s68\n"
"s_addc_u32 s25, s89, 0\n"
"v_mfma_f32_16x16x32_bf16 a[52:55], a[212:215], v[212:215], 0\n"
"s_mov_b32 s33, 409600\n"
"buffer_load_dwordx4 a[172:175], v28, s[76:79], s33 offen offset:0 sc1\n"
"v_mfma_f32_16x16x32_bf16 a[56:59], a[212:215], v[216:219], 0\n"
"s_and_b32 s25, s25, 0xffff\n"
"s_mov_b32 s26, 160\n"
"v_mfma_f32_16x16x32_bf16 a[60:63], a[212:215], v[220:223], 0\n"
"s_mov_b32 s27, 0x20000\n"
"s_add_u32 s69, s100, 20971520\n"
"v_mfma_f32_16x16x32_bf16 a[64:67], a[216:219], v[208:211], 0\n"
"s_waitcnt vmcnt(24)\n"
"s_add_u32 s16, s38, s69\n"
"s_addc_u32 s17, s39, 0\n"
"v_mfma_f32_16x16x32_bf16 a[68:71], a[216:219], v[212:215], 0\n"
"buffer_load_dwordx4 a[176:179], v28, s[76:79], s33 offen offset:1024 sc1\n"
"v_mfma_f32_16x16x32_bf16 a[72:75], a[216:219], v[216:219], 0\n"
"s_and_b32 s17, s17, 0xffff\n"
"s_mov_b32 s18, 256\n"
"v_mfma_f32_16x16x32_bf16 a[76:79], a[216:219], v[220:223], 0\n"
"s_barrier\n"
"s_mov_b32 s19, 0x20000\n"
"v_mfma_f32_16x16x32_bf16 a[0:3], a[220:223], v[144:147], a[0:3]\n"
"s_waitcnt vmcnt(11)\n"
"v_xor_b32 v42, s72, v42\n"
"v_xor_b32 v43, s73, v43\n"
"v_or_b32 v42, v42, v43\n"
"v_cmp_ne_u32 vcc, 0, v42\n"
"s_nop 0\n"
"s_cmp_lg_u64 vcc, 0\n"
"s_cbranch_scc1 K1S_r1_slow\n"
"K1S_r1_back:\n"
"v_mfma_f32_16x16x32_bf16 a[4:7], a[220:223], v[148:151], a[4:7]\n"
"s_mov_b32 s32, 12288\n"
"buffer_load_dwordx4 a[180:183], v28, s[84:87], s32 offen offset:0 sc1\n"
"v_mfma_f32_16x16x32_bf16 a[8:11], a[220:223], v[152:155], a[8:11]\n"
"v_and_b32 v76, 3, v27\n"
"v_bfe_u32 v77, v27, 3, 1\n"
"v_lshl_add_u32 v76, v77, 2, v76\n"
"v_mfma_f32_16x16x32_bf16 a[12:15], a[220:223], v[156:159], a[12:15]\n"
"buffer_load_dwordx4 a[184:187], v28, s[84:87], s32 offen offset:1024 sc1\n"
"v_mfma_f32_16x16x32_bf16 a[16:19], a[224:227], v[144:147], a[16:19]\n"
"v_bfe_u32 v77, v27, 2, 1\n"
"v_lshl_add_u32 v76, v77, 4, v76\n"
"v_mul_u32_u24 v76, 320, v76\n"
"v_mfma_f32_16x16x32_bf16 a[20:23], a[224:227], v[148:151], a[20:23]\n"
"buffer_load_dwordx4 a[188:191], v28, s[84:87], s32 offen offset:2048 sc1\n"
"v_mfma_f32_16x16x32_bf16 a[24:27], a[224:227], v[152:155], a[24:27]\n"
"v_bfe_u32 v78, v27, 1, 2\n"
"v_lshrrev_b32 v77, 4, v27\n"
"v_xor_b32 v78, v78, v77\n"
"v_mfma_f32_16x16x32_bf16 a[28:31], a[224:227], v[156:159], a[28:31]\n"
"v_lshl_add_u32 v76, v78, 4, v76\n"
"s_mul_i32 s68, s7, 20480\n"
"v_mfma_f32_16x16x32_bf16 a[32:35], a[228:231], v[144:147], a[32:35]\n"
"v_add_u32 v2, s68, v76\n"
"v_add_u32 v1, 81920, v2\n"
"v_mfma_f32_16x16x32_bf16 a[36:39], a[228:231], v[148:151], a[36:39]\n"
"buffer_load_dwordx4 v[192:195], v28, s[84:87], s32 offen offset:3072 sc1\n"
"v_mfma_f32_16x16x32_bf16 a[40:43], a[228:231], v[152:155], a[40:43]\n"
"s_mul_i32 s68, s7, 5120\n"
"s_and_b32 s69, s7, 1\n"
"v_mfma_f32_16x16x32_bf16 a[44:47], a[228:231], v[156:159], a[44:47]\n"
"s_mov_b32 s32, 16384\n"
"buffer_load_dwordx4 v[196:199], v28, s[84:87], s32 offen offset:0 sc1\n"
"v_mfma_f32_16x16x32_bf16 a[48:51], a[232:235], v[144:147], a[48:51]\n"
"s_lshl_b32 s69, s69, 1\n"
"v_add_u32 v76, 0, v27\n"
"v_mfma_f32_16x16x32_bf16 a[52:55], a[232:235], v[148:151], a[52:55]\n"
"buffer_load_dwordx4 v[200:203], v28, s[84:87], s32 offen offset:1024 sc1\n"
"v_mfma_f32_16x16x32_bf16 a[56:59], a[232:235], v[152:155], a[56:59]\n"
"v_mul_u32_u24 v77, 3277, v76\n"
"v_lshrrev_b32 v77, 16, v77\n"
"v_mul_u32_u24 v78, 20, v77\n"
"v_mfma_f32_16x16x32_bf16 a[60:63], a[232:235], v[156:159], a[60:63]\n"
"v_sub_u32 v78, v76, v78\n"
"v_mul_u32_u24 v76, 1280, v77\n"
"v_lshl_add_u32 v10, v78, 3, v76\n"
"v_mfma_f32_16x16x32_bf16 a[64:67], a[236:239], v[144:147], a[64:67]\n"
"v_bfe_u32 v76, v77, 1, 1\n"
"v_or_b32 v76, s69, v76\n"
"v_mfma_f32_16x16x32_bf16 a[68:71], a[236:239], v[148:151], a[68:71]\n"
"buffer_load_dwordx4 v[204:207], v28, s[84:87], s32 offen offset:2048 sc1\n"
"v_mfma_f32_16x16x32_bf16 a[72:75], a[236:239], v[152:155], a[72:75]\n"
"v_xor_b32 v78, v78, v76\n"
"v_mul_u32_u24 v76, 320, v77\n"
"v_lshl_add_u32 v76, v78, 4, v76\n"
"v_mfma_f32_16x16x32_bf16 a[76:79], a[236:239], v[156:159], a[76:79]\n"
"v_add_u32 v4, s68, v76\n"
"v_add_u32 v76, 64, v27\n"
"v_mul_u32_u24 v77, 3277, v76\n"
"v_mfma_f32_16x16x32_bf16 a[0:3], a[240:243], v[160:163], a[0:3]\n"
"v_lshrrev_b32 v77, 16, v77\n"
"v_mul_u32_u24 v78, 20, v77\n"
"v_sub_u32 v78, v76, v78\n"
"v_mfma_f32_16x16x32_bf16 a[4:7], a[240:243], v[164:167], a[4:7]\n"
"buffer_load_dwordx4 a[200:203], v28, s[84:87], s32 offen offset:3072 sc1\n"
"v_mfma_f32_16x16x32_bf16 a[8:11], a[240:243], v[168:171], a[8:11]\n"
"v_mul_u32_u24 v76, 1280, v77\n"
"v_lshl_add_u32 v11, v78, 3, v76\n"
"v_bfe_u32 v76, v77, 1, 1\n"
"v_mfma_f32_16x16x32_bf16 a[12:15], a[240:243], v[172:175], a[12:15]\n"
"s_mov_b32 s32, 20480\n"
"buffer_load_dwordx4 a[204:207], v28, s[84:87], s32 offen offset:0 sc1\n"
"v_mfma_f32_16x16x32_bf16 a[16:19], a[244:247], v[160:163], a[16:19]\n"
"v_or_b32 v76, s69, v76\n"
"v_xor_b32 v78, v78, v76\n"
"v_mul_u32_u24 v76, 320, v77\n"
"v_mfma_f32_16x16x32_bf16 a[20:23], a[244:247], v[164:167], a[20:23]\n"
"buffer_load_dwordx4 a[208:211], v28, s[84:87], s32 offen offset:1024 sc1\n"
"v_mfma_f32_16x16x32_bf16 a[24:27], a[244:247], v[168:171], a[24:27]\n"
"v_lshl_add_u32 v76, v78, 4, v76\n"
"v_add_u32 v5, s68, v76\n"
"v_add_u32 v76, 128, v27\n"
"v_mfma_f32_16x16x32_bf16 a[28:31], a[244:247], v[172:175], a[28:31]\n"
"v_mul_u32_u24 v77, 3277, v76\n"
"v_lshrrev_b32 v77, 16, v77\n"
"v_mul_u32_u24 v78, 20, v77\n"
"v_mfma_f32_16x16x32_bf16 a[32:35], a[248:251], v[160:163], a[32:35]\n"
"v_sub_u32 v78, v76, v78\n"
"v_mul_u32_u24 v76, 1280, v77\n"
"v_lshl_add_u32 v12, v78, 3, v76\n"
"v_mfma_f32_16x16x32_bf16 a[36:39], a[248:251], v[164:167], a[36:39]\n"
"buffer_load_dwordx4 a[212:215], v28, s[84:87], s32 offen offset:2048 sc1\n"
"v_mfma_f32_16x16x32_bf16 a[40:43], a[248:251], v[168:171], a[40:43]\n"
"v_bfe_u32 v76, v77, 1, 1\n"
"v_or_b32 v76, s69, v76\n"
"v_xor_b32 v78, v78, v76\n"
"v_mfma_f32_16x16x32_bf16 a[44:47], a[248:251], v[172:175], a[44:47]\n"
"buffer_load_dwordx4 a[216:219], v28, s[84:87], s32 offen offset:3072 sc1\n"
"v_mfma_f32_16x16x32_bf16 a[48:51], a[252:255], v[160:163], a[48:51]\n"
"v_mul_u32_u24 v76, 320, v77\n"
"v_lshl_add_u32 v76, v78, 4, v76\n"
"v_add_u32 v6, s68, v76\n"
"v_mfma_f32_16x16x32_bf16 a[52:55], a[252:255], v[164:167], a[52:55]\n"
"s_mov_b32 s32, 24576\n"
"buffer_load_dwordx4 v[208:211], v28, s[84:87], s32 offen offset:0 sc1\n"
"v_mfma_f32_16x16x32_bf16 a[56:59], a[252:255], v[168:171], a[56:59]\n"
"v_add_u32 v76, 192, v27\n"
"v_mul_u32_u24 v77, 3277, v76\n"
"v_lshrrev_b32 v77, 16, v77\n"
"v_mfma_f32_16x16x32_bf16 a[60:63], a[252:255], v[172:175], a[60:63]\n"
"v_mul_u32_u24 v78, 20, v77\n"
"v_sub_u32 v78, v76, v78\n"
"v_mul_u32_u24 v76, 1280, v77\n"
"v_mfma_f32_16x16x32_bf16 a[64:67], a[192:195], v[160:163], a[64:67]\n"
"s_waitcnt vmcnt(9)\n"
"v_lshl_add_u32 v13, v78, 3, v76\n"
"v_bfe_u32 v76, v77, 1, 1\n"
"v_mfma_f32_16x16x32_bf16 a[68:71], a[192:195], v[164:167], a[68:71]\n"
"buffer_load_dwordx4 v[212:215], v28, s[84:87], s32 offen offset:1024 sc1\n"
"v_mfma_f32_16x16x32_bf16 a[72:75], a[192:195], v[168:171], a[72:75]\n"
"v_or_b32 v76, s69, v76\n"
"v_xor_b32 v78, v78, v76\n"
"v_mul_u32_u24 v76, 320, v77\n"
"v_mfma_f32_16x16x32_bf16 a[76:79], a[192:195], v[172:175], a[76:79]\n"
"v_lshl_add_u32 v76, v78, 4, v76\n"
"v_add_u32 v7, s68, v76\n"
"v_add_u32 v76, 256, v27\n"
"v_mfma_f32_16x16x32_bf16 a[80:83], a[196:199], a[180:183], a[80:83]\n"
"v_mul_u32_u24 v77, 3277, v76\n"
"v_lshrrev_b32 v77, 16, v77\n"
"v_mul_u32_u24 v78, 20, v77\n"
"v_mfma_f32_16x16x32_bf16 a[84:87], a[196:199], a[184:187], a[84:87]\n"
"buffer_load_dwordx4 v[216:219], v28, s[84:87], s32 offen offset:2048 sc1\n"
"v_mfma_f32_16x16x32_bf16 a[88:91], a[196:199], a[188:191], a[88:91]\n"
"v_sub_u32 v78, v76, v78\n"
"v_mul_u32_u24 v76, 1280, v77\n"
"v_lshl_add_u32 v14, v78, 3, v76\n"
"v_mfma_f32_16x16x32_bf16 a[92:95], a[196:199], v[192:195], a[92:95]\n"
"buffer_load_dwordx4 v[220:223], v28, s[84:87], s32 offen offset:3072 sc1\n"
"v_mfma_f32_16x16x32_bf16 a[96:99], v[80:83], a[180:183], a[96:99]\n"
"v_bfe_u32 v76, v77, 1, 1\n"
"v_or_b32 v76, s69, v76\n"
"v_xor_b32 v78, v78, v76\n"
"v_mfma_f32_16x16x32_bf16 a[100:103], v[80:83], a[184:187], a[100:103]\n"
"s_mov_b32 s32, 28672\n"
"buffer_load_dwordx4 a[220:223], v28, s[84:87], s32 offen offset:0 sc1\n"
"v_mfma_f32_16x16x32_bf16 a[104:107], v[80:83], a[188:191], a[104:107]\n"
"v_mul_u32_u24 v76, 320, v77\n"
"v_lshl_add_u32 v76, v78, 4, v76\n"
"v_add_u32 v8, s68, v76\n"
"v_mfma_f32_16x16x32_bf16 a[108:111], v[80:83], v[192:195], a[108:111]\n"
"s_and_b32 s68, s72, 0xfff\n"
"v_mov_b32 v20, s68\n"
"v_mfma_f32_16x16x32_bf16 a[112:115], v[84:87], a[180:183], a[112:115]\n"
"v_mov_b32 v21, 0xfffff000\n"
"v_add_u32 v4, 81920, v4\n"
"v_add_u32 v5, 81920, v5\n"
"v_mfma_f32_16x16x32_bf16 a[116:119], v[84:87], a[184:187], a[116:119]\n"
"buffer_load_dwordx4 a[224:227], v28, s[84:87], s32 offen offset:1024 sc1\n"
"v_mfma_f32_16x16x32_bf16 a[120:123], v[84:87], a[188:191], a[120:123]\n"
"v_add_u32 v6, 81920, v6\n"
"v_add_u32 v7, 81920, v7\n"
"v_add_u32 v8, 81920, v8\n"
"v_mfma_f32_16x16x32_bf16 a[124:127], v[84:87], v[192:195], a[124:127]\n"
"buffer_load_dwordx4 a[228:231], v28, s[84:87], s32 offen offset:2048 sc1\n"
"v_mfma_f32_16x16x32_bf16 a[128:131], v[88:91], a[180:183], a[128:131]\n"
"v_mfma_f32_16x16x32_bf16 a[132:135], v[88:91], a[184:187], a[132:135]\n"
"buffer_load_dwordx4 a[232:235], v28, s[84:87], s32 offen offset:3072 sc1\n"
"v_mfma_f32_16x16x32_bf16 a[136:139], v[88:91], a[188:191], a[136:139]\n"
"v_mfma_f32_16x16x32_bf16 a[140:143], v[88:91], v[192:195], a[140:143]\n"
"v_mfma_f32_16x16x32_bf16 a[144:147], v[92:95], a[180:183], a[144:147]\n"
"s_waitcnt vmcnt(12)\n"
"v_mfma_f32_16x16x32_bf16 a[148:151], v[92:95], a[184:187], a[148:151]\n"
"s_mov_b32 s32, 32768\n"
"buffer_load_dwordx4 a[236:239], v28, s[84:87], s32 offen offset:0 sc1\n"
"v_mfma_f32_16x16x32_bf16 a[152:155], v[92:95], a[188:191], a[152:155]\n"
"v_mfma_f32_16x16x32_bf16 a[156:159], v[92:95], v[192:195], a[156:159]\n"
"v_mfma_f32_16x16x32_bf16 a[80:83], v[224:227], v[196:199], a[80:83]\n"
"v_mfma_f32_16x16x32_bf16 a[84:87], v[224:227], v[200:203], a[84:87]\n"
"buffer_load_dwordx4 v[144:147], v28, s[84:87], s32 offen offset:1024 sc1\n"
"v_mfma_f32_16x16x32_bf16 a[88:91], v[224:227], v[204:207], a[88:91]\n"
"v_mfma_f32_16x16x32_bf16 a[92:95], v[224:227], a[200:203], a[92:95]\n"
"buffer_load_dwordx4 v[148:151], v28, s[84:87], s32 offen offset:2048 sc1\n"
"v_mfma_f32_16x16x32_bf16 a[96:99], v[228:231], v[196:199], a[96:99]\n"
"v_mfma_f32_16x16x32_bf16 a[100:103], v[228:231], v[200:203], a[100:103]\n"
"buffer_load_dwordx4 v[152:155], v28, s[84:87], s32 offen offset:3072 sc1\n"
"v_mfma_f32_16x16x32_bf16 a[104:107], v[228:231], v[204:207], a[104:107]\n"
"v_mfma_f32_16x16x32_bf16 a[108:111], v[228:231], a[200:203], a[108:111]\n"
"s_mov_b32 s70, 65536\n"
"buffer_load_dwordx2 v[42:43], v3, s[64:67], s70 offen sc1\n"
"v_mfma_f32_16x16x32_bf16 a[112:115], v[232:235], v[196:199], a[112:115]\n"
"v_mfma_f32_16x16x32_bf16 a[116:119], v[232:235], v[200:203], a[116:119]\n"
"buffer_load_dwordx4 v[156:159], v28, s[76:79], s33 offen offset:2048 sc1\n"
"v_mfma_f32_16x16x32_bf16 a[120:123], v[232:235], v[204:207], a[120:123]\n"
"v_mfma_f32_16x16x32_bf16 a[124:127], v[232:235], a[200:203], a[124:127]\n"
"buffer_load_dwordx4 a[240:243], v28, s[76:79], s33 offen offset:3072 sc1\n"
"v_mfma_f32_16x16x32_bf16 a[128:131], v[236:239], v[196:199], a[128:131]\n"
"v_mfma_f32_16x16x32_bf16 a[132:135], v[236:239], v[200:203], a[132:135]\n"
"buffer_load_dwordx4 a[244:247], v25, s[76:79], s33 offen offset:3072 sc1\n"
"v_mfma_f32_16x16x32_bf16 a[136:139], v[236:239], v[204:207], a[136:139]\n"
"v_mfma_f32_16x16x32_bf16 a[140:143], v[236:239], a[200:203], a[140:143]\n"
"v_mfma_f32_16x16x32_bf16 a[144:147], v[240:243], v[196:199], a[144:147]\n"
"s_waitcnt vmcnt(16)\n"
"v_mfma_f32_16x16x32_bf16 a[148:151], v[240:243], v[200:203], a[148:151]\n"
"s_mov_b32 s33, 450560\n"
"buffer_load_dwordx4 a[248:251], v28, s[76:79], s33 offen offset:0 sc1\n"
"v_mfma_f32_16x16x32_bf16 a[152:155], v[240:243], v[204:207], a[152:155]\n"
"v_mfma_f32_16x16x32_bf16 a[156:159], v[240:243], a[200:203], a[156:159]\n"
"v_mfma_f32_16x16x32_bf16 a[80:83], v[244:247], a[204:207], a[80:83]\n"
"v_mfma_f32_16x16x32_bf16 a[84:87], v[244:247], a[208:211], a[84:87]\n"
"buffer_load_dwordx4 a[252:255], v28, s[76:79], s33 offen offset:1024 sc1\n"
"v_mfma_f32_16x16x32_bf16 a[88:91], v[244:247], a[212:215], a[88:91]\n"
"v_mfma_f32_16x16x32_bf16 a[92:95], v[244:247], a[216:219], a[92:95]\n"
"buffer_load_dwordx4 a[192:195], v28, s[76:79], s33 offen offset:2048 sc1\n"
"v_mfma_f32_16x16x32_bf16 a[96:99], v[248:251], a[204:207], a[96:99]\n"
"v_mfma_f32_16x16x32_bf16 a[100:103], v[248:251], a[208:211], a[100:103]\n"
"buffer_load_dwordx4 v[160:163], v28, s[76:79], s33 offen offset:3072 sc1\n"
"v_mfma_f32_16x16x32_bf16 a[104:107], v[248:251], a[212:215], a[104:107]\n"
"v_mfma_f32_16x16x32_bf16 a[108:111], v[248:251], a[216:219], a[108:111]\n"
"v_mfma_f32_16x16x32_bf16 a[112:115], v[252:255], a[204:207], a[112:115]\n"
"v_mfma_f32_16x16x32_bf16 a[116:119], v[252:255], a[208:211], a[116:119]\n"
"buffer_load_dwordx4 v[164:167], v25, s[76:79], s33 offen offset:3072 sc1\n"
"v_mfma_f32_16x16x32_bf16 a[120:123], v[252:255], a[212:215], a[120:123]\n"
"v_mfma_f32_16x16x32_bf16 a[124:127], v[252:255], a[216:219], a[124:127]\n"
"v_mfma_f32_16x16x32_bf16 a[128:131], v[128:131], a[204:207], a[128:131]\n"
"v_mfma_f32_16x16x32_bf16 a[132:135], v[128:131], a[208:211], a[132:135]\n"
"v_mfma_f32_16x16x32_bf16 a[136:139], v[128:131], a[212:215], a[136:139]\n"
"v_mfma_f32_16x16x32_bf16 a[140:143], v[128:131], a[216:219], a[140:143]\n"
"v_mfma_f32_16x16x32_bf16 a[144:147], v[132:135], a[204:207], a[144:147]\n"
"s_waitcnt vmcnt(17)\n"
"v_mfma_f32_16x16x32_bf16 a[148:151], v[132:135], a[208:211], a[148:151]\n"
"v_mfma_f32_16x16x32_bf16 a[152:155], v[132:135], a[212:215], a[152:155]\n"
"v_mfma_f32_16x16x32_bf16 a[156:159], v[132:135], a[216:219], a[156:159]\n"
"v_mfma_f32_16x16x32_bf16 a[80:83], v[136:139], v[208:211], a[80:83]\n"
"s_waitcnt vmcnt(8)\n"
"v_xor_b32 v42, s72, v42\n"
"v_xor_b32 v43, s73, v43\n"
"v_or_b32 v42, v42, v43\n"
"v_cmp_ne_u32 vcc, 0, v42\n"
"s_nop 0\n"
"s_cmp_lg_u64 vcc, 0\n"
"s_cbranch_scc1 K1S_r2_slow\n"
"K1S_r2_back:\n"
"v_mfma_f32_16x16x32_bf16 a[84:87], v[136:139], v[212:215], a[84:87]\n"
"s_mov_b32 s32, 36864\n"
"buffer_load_dwordx4 v[168:171], v28, s[84:87], s32 offen offset:0 sc1\n"
"v_mfma_f32_16x16x32_bf16 a[88:91], v[136:139], v[216:219], a[88:91]\n"
"v_mfma_f32_16x16x32_bf16 a[92:95], v[136:139], v[220:223], a[92:95]\n"
"v_mfma_f32_16x16x32_bf16 a[96:99], v[140:143], v[208:211], a[96:99]\n"
"v_mfma_f32_16x16x32_bf16 a[100:103], v[140:143], v[212:215], a[100:103]\n"
"buffer_load_dwordx4 v[172:175], v28, s[84:87], s32 offen offset:1024 sc1\n"
"v_mfma_f32_16x16x32_bf16 a[104:107], v[140:143], v[216:219], a[104:107]\n"
"v_mfma_f32_16x16x32_bf16 a[108:111], v[140:143], v[220:223], a[108:111]\n"
"v_mfma_f32_16x16x32_bf16 a[112:115], v[176:179], v[208:211], a[112:115]\n"
"v_mfma_f32_16x16x32_bf16 a[116:119], v[176:179], v[212:215], a[116:119]\n"
"buffer_load_dwordx4 a[180:183], v28, s[84:87], s32 offen offset:2048 sc1\n"
"v_mfma_f32_16x16x32_bf16 a[120:123], v[176:179], v[216:219], a[120:123]\n"
"v_mfma_f32_16x16x32_bf16 a[124:127], v[176:179], v[220:223], a[124:127]\n"
"v_mfma_f32_16x16x32_bf16 a[128:131], v[180:183], v[208:211], a[128:131]\n"
"v_mfma_f32_16x16x32_bf16 a[132:135], v[180:183], v[212:215], a[132:135]\n"
"buffer_load_dwordx4 a[184:187], v28, s[84:87], s32 offen offset:3072 sc1\n"
"v_mfma_f32_16x16x32_bf16 a[136:139], v[180:183], v[216:219], a[136:139]\n"
"v_mfma_f32_16x16x32_bf16 a[140:143], v[180:183], v[220:223], a[140:143]\n"
"v_mfma_f32_16x16x32_bf16 a[144:147], v[184:187], v[208:211], a[144:147]\n"
"v_mfma_f32_16x16x32_bf16 a[148:151], v[184:187], v[212:215], a[148:151]\n"
"s_mov_b32 s32, 40960\n"
"buffer_load_dwordx4 a[188:191], v28, s[84:87], s32 offen offset:0 sc1\n"
"v_mfma_f32_16x16x32_bf16 a[152:155], v[184:187], v[216:219], a[152:155]\n"
"v_mfma_f32_16x16x32_bf16 a[156:159], v[184:187], v[220:223], a[156:159]\n"
"v_mfma_f32_16x16x32_bf16 a[80:83], v[188:191], a[220:223], a[80:83]\n"
"v_mfma_f32_16x16x32_bf16 a[84:87], v[188:191], a[224:227], a[84:87]\n"
"buffer_load_dwordx4 v[192:195], v28, s[84:87], s32 offen offset:1024 sc1\n"
"v_mfma_f32_16x16x32_bf16 a[88:91], v[188:191], a[228:231], a[88:91]\n"
"v_mfma_f32_16x16x32_bf16 a[92:95], v[188:191], a[232:235], a[92:95]\n"
"v_mfma_f32_16x16x32_bf16 a[96:99], v[116:119], a[220:223], a[96:99]\n"
"v_mfma_f32_16x16x32_bf16 a[100:103], v[116:119], a[224:227], a[100:103]\n"
"buffer_load_dwordx4 v[196:199], v28, s[84:87], s32 offen offset:2048 sc1\n"
"v_mfma_f32_16x16x32_bf16 a[104:107], v[116:119], a[228:231], a[104:107]\n"
"v_mfma_f32_16x16x32_bf16 a[108:111], v[116:119], a[232:235], a[108:111]\n"
"v_mfma_f32_16x16x32_bf16 a[112:115], v[120:123], a[220:223], a[112:115]\n"
"v_mfma_f32_16x16x32_bf16 a[116:119], v[120:123], a[224:227], a[116:119]\n"
"buffer_load_dwordx4 v[200:203], v28, s[84:87], s32 offen offset:3072 sc1\n"
"v_mfma_f32_16x16x32_bf16 a[120:123], v[120:123], a[228:231], a[120:123]\n"
"v_mfma_f32_16x16x32_bf16 a[124:127], v[120:123], a[232:235], a[124:127]\n"
"v_mfma_f32_16x16x32_bf16 a[128:131], v[124:127], a[220:223], a[128:131]\n"
"v_mfma_f32_16x16x32_bf16 a[132:135], v[124:127], a[224:227], a[132:135]\n"
"s_mov_b32 s32, 45056\n"
"buffer_load_dwordx4 v[204:207], v28, s[84:87], s32 offen offset:0 sc1\n"
"v_mfma_f32_16x16x32_bf16 a[136:139], v[124:127], a[228:231], a[136:139]\n"
"v_mfma_f32_16x16x32_bf16 a[140:143], v[124:127], a[232:235], a[140:143]\n"
"v_mfma_f32_16x16x32_bf16 a[144:147], v[44:47], a[220:223], a[144:147]\n"
"v_mfma_f32_16x16x32_bf16 a[148:151], v[44:47], a[224:227], a[148:151]\n"
"buffer_load_dwordx4 a[200:203], v28, s[84:87], s32 offen offset:1024 sc1\n"
"v_mfma_f32_16x16x32_bf16 a[152:155], v[44:47], a[228:231], a[152:155]\n"
"v_mfma_f32_16x16x32_bf16 a[156:159], v[44:47], a[232:235], a[156:159]\n"
"v_mfma_f32_16x16x32_bf16 a[80:83], v[48:51], a[236:239], a[80:83]\n"
"v_mfma_f32_16x16x32_bf16 a[84:87], v[48:51], v[144:147], a[84:87]\n"
"buffer_load_dwordx4 a[204:207], v28, s[84:87], s32 offen offset:2048 sc1\n"
"v_mfma_f32_16x16x32_bf16 a[88:91], v[48:51], v[148:151], a[88:91]\n"
"v_mfma_f32_16x16x32_bf16 a[92:95], v[48:51], v[152:155], a[92:95]\n"
"v_mfma_f32_16x16x32_bf16 a[96:99], v[52:55], a[236:239], a[96:99]\n"
"v_mfma_f32_16x16x32_bf16 a[100:103], v[52:55], v[144:147], a[100:103]\n"
"buffer_load_dwordx4 a[208:211], v28, s[84:87], s32 offen offset:3072 sc1\n"
"v_mfma_f32_16x16x32_bf16 a[104:107], v[52:55], v[148:151], a[104:107]\n"
"v_mfma_f32_16x16x32_bf16 a[108:111], v[52:55], v[152:155], a[108:111]\n"
"v_mfma_f32_16x16x32_bf16 a[112:115], v[56:59], a[236:239], a[112:115]\n"
"v_mfma_f32_16x16x32_bf16 a[116:119], v[56:59], v[144:147], a[116:119]\n"
"s_mov_b32 s32, 12288\n"
"buffer_load_dwordx4 a[212:215], v28, s[80:83], s32 offen offset:0 sc1\n"
"v_mfma_f32_16x16x32_bf16 a[120:123], v[56:59], v[148:151], a[120:123]\n"
"v_mfma_f32_16x16x32_bf16 a[124:127], v[56:59], v[152:155], a[124:127]\n"
"v_mfma_f32_16x16x32_bf16 a[128:131], v[60:63], a[236:239], a[128:131]\n"
"v_mfma_f32_16x16x32_bf16 a[132:135], v[60:63], v[144:147], a[132:135]\n"
"buffer_load_dwordx4 a[216:219], v28, s[80:83], s32 offen offset:1024 sc1\n"
"v_mfma_f32_16x16x32_bf16 a[136:139], v[60:63], v[148:151], a[136:139]\n"
"v_mfma_f32_16x16x32_bf16 a[140:143], v[60:63], v[152:155], a[140:143]\n"
"v_mfma_f32_16x16x32_bf16 a[144:147], v[64:67], a[236:239], a[144:147]\n"
"s_waitcnt vmcnt(10)\n"
"v_mfma_f32_16x16x32_bf16 a[148:151], v[64:67], v[144:147], a[148:151]\n"
"buffer_load_dwordx4 v[208:211], v28, s[80:83], s32 offen offset:2048 sc1\n"
"v_mfma_f32_16x16x32_bf16 a[152:155], v[64:67], v[148:151], a[152:155]\n"
"s_waitcnt vmcnt(3)\n"
"v_mfma_f32_16x16x32_bf16 a[156:159], v[64:67], v[152:155], a[156:159]\n"
"v_mfma_f32_16x16x32_bf16 a[80:83], v[68:71], v[168:171], a[80:83]\n"
"v_mfma_f32_16x16x32_bf16 a[84:87], v[68:71], v[172:175], a[84:87]\n"
"buffer_load_dwordx4 v[212:215], v28, s[80:83], s32 offen offset:3072 sc1\n"
"v_mfma_f32_16x16x32_bf16 a[88:91], v[68:71], a[180:183], a[88:91]\n"
"v_mfma_f32_16x16x32_bf16 a[92:95], v[68:71], a[184:187], a[92:95]\n"
"v_mfma_f32_16x16x32_bf16 a[80:83], a[172:175], a[188:191], a[80:83]\n"
"v_mfma_f32_16x16x32_bf16 a[84:87], a[172:175], v[192:195], a[84:87]\n"
"s_mov_b32 s32, 16384\n"
"buffer_load_dwordx4 v[216:219], v28, s[80:83], s32 offen offset:0 sc1\n"
"v_mfma_f32_16x16x32_bf16 a[88:91], a[172:175], v[196:199], a[88:91]\n"
"v_mfma_f32_16x16x32_bf16 a[92:95], a[172:175], v[200:203], a[92:95]\n"
"v_mfma_f32_16x16x32_bf16 a[80:83], a[248:251], v[204:207], a[80:83]\n"
"v_mfma_f32_16x16x32_bf16 a[84:87], a[248:251], a[200:203], a[84:87]\n"
"buffer_load_dwordx4 v[220:223], v28, s[80:83], s32 offen offset:1024 sc1\n"
"v_mfma_f32_16x16x32_bf16 a[88:91], a[248:251], a[204:207], a[88:91]\n"
"v_mfma_f32_16x16x32_bf16 a[92:95], a[248:251], a[208:211], a[92:95]\n"
"v_mfma_f32_16x16x32_bf16 a[96:99], v[72:75], v[168:171], a[96:99]\n"
"v_mfma_f32_16x16x32_bf16 a[100:103], v[72:75], v[172:175], a[100:103]\n"
"buffer_load_dwordx4 a[220:223], v28, s[80:83], s32 offen offset:2048 sc1\n"
"v_mfma_f32_16x16x32_bf16 a[104:107], v[72:75], a[180:183], a[104:107]\n"
"ds_write_b128 v1, a[80:83] offset:0\n"
"v_mfma_f32_16x16x32_bf16 a[108:111], v[72:75], a[184:187], a[108:111]\n"
"v_mfma_f32_16x16x32_bf16 a[96:99], a[176:179], a[188:191], a[96:99]\n"
"v_mfma_f32_16x16x32_bf16 a[100:103], a[176:179], v[192:195], a[100:103]\n"
"buffer_load_dwordx4 a[224:227], v28, s[80:83], s32 offen offset:3072 sc1\n"
"ds_write_b128 v1, a[84:87] offset:2560\n"
"v_mfma_f32_16x16x32_bf16 a[104:107], a[176:179], v[196:199], a[104:107]\n"
"v_mfma_f32_16x16x32_bf16 a[108:111], a[176:179], v[200:203], a[108:111]\n"
"v_mfma_f32_16x16x32_bf16 a[96:99], a[252:255], v[204:207], a[96:99]\n"
"ds_write_b128 v1, a[88:91] offset:10240\n"
"v_mfma_f32_16x16x32_bf16 a[100:103], a[252:255], a[200:203], a[100:103]\n"
"s_mov_b32 s32, 20480\n"
"buffer_load_dwordx4 a[228:231], v28, s[80:83], s32 offen offset:0 sc1\n"
"v_mfma_f32_16x16x32_bf16 a[104:107], a[252:255], a[204:207], a[104:107]\n"
"v_mfma_f32_16x16x32_bf16 a[108:111], a[252:255], a[208:211], a[108:111]\n"
"ds_write_b128 v1, a[92:95] offset:12800\n"
"v_mfma_f32_16x16x32_bf16 a[112:115], a[160:163], v[168:171], a[112:115]\n"
"v_mfma_f32_16x16x32_bf16 a[116:119], a[160:163], v[172:175], a[116:119]\n"
"buffer_load_dwordx4 a[232:235], v28, s[80:83], s32 offen offset:1024 sc1\n"
"v_mfma_f32_16x16x32_bf16 a[120:123], a[160:163], a[180:183], a[120:123]\n"
"ds_write_b128 v1, a[96:99] offset:64\n"
"v_mfma_f32_16x16x32_bf16 a[124:127], a[160:163], a[184:187], a[124:127]\n"
"v_mfma_f32_16x16x32_bf16 a[112:115], v[156:159], a[188:191], a[112:115]\n"
"v_mfma_f32_16x16x32_bf16 a[116:119], v[156:159], v[192:195], a[116:119]\n"
"buffer_load_dwordx4 a[236:239], v28, s[80:83], s32 offen offset:2048 sc1\n"
"ds_write_b128 v1, a[100:103] offset:2624\n"
"v_mfma_f32_16x16x32_bf16 a[120:123], v[156:159], v[196:199], a[120:123]\n"
"v_mfma_f32_16x16x32_bf16 a[124:127], v[156:159], v[200:203], a[124:127]\n"
"v_mfma_f32_16x16x32_bf16 a[112:115], a[192:195], v[204:207], a[112:115]\n"
"ds_write_b128 v1, a[104:107] offset:10304\n"
"v_mfma_f32_16x16x32_bf16 a[116:119], a[192:195], a[200:203], a[116:119]\n"
"buffer_load_dwordx4 v[144:147], v28, s[80:83], s32 offen offset:3072 sc1\n"
"v_mfma_f32_16x16x32_bf16 a[120:123], a[192:195], a[204:207], a[120:123]\n"
"v_mfma_f32_16x16x32_bf16 a[124:127], a[192:195], a[208:211], a[124:127]\n"
"ds_write_b128 v1, a[108:111] offset:12864\n"
"v_mfma_f32_16x16x32_bf16 a[128:131], a[164:167], v[168:171], a[128:131]\n"
"v_mfma_f32_16x16x32_bf16 a[132:135], a[164:167], v[172:175], a[132:135]\n"
"s_mov_b32 s32, 24576\n"
"buffer_load_dwordx4 v[148:151], v28, s[80:83], s32 offen offset:0 sc1\n"
"v_mfma_f32_16x16x32_bf16 a[136:139], a[164:167], a[180:183], a[136:139]\n"
"ds_write_b128 v1, a[112:115] offset:128\n"
"v_mfma_f32_16x16x32_bf16 a[140:143], a[164:167], a[184:187], a[140:143]\n"
"v_mfma_f32_16x16x32_bf16 a[128:131], a[240:243], a[188:191], a[128:131]\n"
"v_mfma_f32_16x16x32_bf16 a[132:135], a[240:243], v[192:195], a[132:135]\n"
"buffer_load_dwordx4 v[152:155], v28, s[80:83], s32 offen offset:1024 sc1\n"
"ds_write_b128 v1, a[116:119] offset:2688\n"
"v_mfma_f32_16x16x32_bf16 a[136:139], a[240:243], v[196:199], a[136:139]\n"
"v_mfma_f32_16x16x32_bf16 a[140:143], a[240:243], v[200:203], a[140:143]\n"
"v_mfma_f32_16x16x32_bf16 a[128:131], v[160:163], v[204:207], a[128:131]\n"
"ds_write_b128 v1, a[120:123] offset:10368\n"
"v_mfma_f32_16x16x32_bf16 a[132:135], v[160:163], a[200:203], a[132:135]\n"
"v_mfma_f32_16x16x32_bf16 a[136:139], v[160:163], a[204:207], a[136:139]\n"
"v_mfma_f32_16x16x32_bf16 a[140:143], v[160:163], a[208:211], a[140:143]\n"
"ds_write_b128 v1, a[124:127] offset:12928\n"
"v_mfma_f32_16x16x32_bf16 a[144:147], a[168:171], v[168:171], a[144:147]\n"
"v_mfma_f32_16x16x32_bf16 a[148:151], a[168:171], v[172:175], a[148:151]\n"
"v_mfma_f32_16x16x32_bf16 a[152:155], a[168:171], a[180:183], a[152:155]\n"
"ds_write_b128 v1, a[128:131] offset:192\n"
"v_mfma_f32_16x16x32_bf16 a[156:159], a[168:171], a[184:187], a[156:159]\n"
"v_mfma_f32_16x16x32_bf16 a[144:147], a[244:247], a[188:191], a[144:147]\n"
"v_mfma_f32_16x16x32_bf16 a[148:151], a[244:247], v[192:195], a[148:151]\n"
"ds_write_b128 v1, a[132:135] offset:2752\n"
"v_mfma_f32_16x16x32_bf16 a[152:155], a[244:247], v[196:199], a[152:155]\n"
"v_mfma_f32_16x16x32_bf16 a[156:159], a[244:247], v[200:203], a[156:159]\n"
"v_mfma_f32_16x16x32_bf16 a[144:147], v[164:167], v[204:207], a[144:147]\n"
"s_waitcnt vmcnt(10)\n"
"ds_write_b128 v1, a[136:139] offset:10432\n"
"v_mfma_f32_16x16x32_bf16 a[148:151], v[164:167], a[200:203], a[148:151]\n"
"v_mfma_f32_16x16x32_bf16 a[152:155], v[164:167], a[204:207], a[152:155]\n"
"v_mfma_f32_16x16x32_bf16 a[156:159], v[164:167], a[208:211], a[156:159]\n"
"ds_write_b128 v1, a[140:143] offset:12992\n"
"v_mfma_f32_16x16x32_bf16 a[0:3], a[196:199], a[212:215], a[0:3]\n"
"v_mfma_f32_16x16x32_bf16 a[4:7], a[196:199], a[216:219], a[4:7]\n"
"buffer_load_dwordx4 v[168:171], v28, s[80:83], s32 offen offset:2048 sc1\n"
"v_mfma_f32_16x16x32_bf16 a[8:11], a[196:199], v[208:211], a[8:11]\n"
"ds_write_b128 v1, a[144:147] offset:256\n"
"v_mfma_f32_16x16x32_bf16 a[12:15], a[196:199], v[212:215], a[12:15]\n"
"v_mfma_f32_16x16x32_bf16 a[16:19], v[80:83], a[212:215], a[16:19]\n"
"v_mfma_f32_16x16x32_bf16 a[20:23], v[80:83], a[216:219], a[20:23]\n"
"buffer_load_dwordx4 v[172:175], v28, s[80:83], s32 offen offset:3072 sc1\n"
"ds_write_b128 v1, a[148:151] offset:2816\n"
"v_mfma_f32_16x16x32_bf16 a[24:27], v[80:83], v[208:211], a[24:27]\n"
"v_mfma_f32_16x16x32_bf16 a[28:31], v[80:83], v[212:215], a[28:31]\n"
"v_mfma_f32_16x16x32_bf16 a[32:35], v[84:87], a[212:215], a[32:35]\n"
"ds_write_b128 v1, a[152:155] offset:10496\n"
"v_mfma_f32_16x16x32_bf16 a[36:39], v[84:87], a[216:219], a[36:39]\n"
"s_mov_b32 s32, 28672\n"
"buffer_load_dwordx4 a[180:183], v28, s[80:83], s32 offen offset:0 sc1\n"
"v_mfma_f32_16x16x32_bf16 a[40:43], v[84:87], v[208:211], a[40:43]\n"
"v_mfma_f32_16x16x32_bf16 a[44:47], v[84:87], v[212:215], a[44:47]\n"
"ds_write_b128 v1, a[156:159] offset:13056\n"
"v_mfma_f32_16x16x32_bf16 a[48:51], v[88:91], a[212:215], a[48:51]\n"
"v_mfma_f32_16x16x32_bf16 a[52:55], v[88:91], a[216:219], a[52:55]\n"
"s_waitcnt lgkmcnt(0)\n"
"s_barrier\n"
"buffer_load_dwordx4 a[184:187], v28, s[80:83], s32 offen offset:1024 sc1\n"
"v_mfma_f32_16x16x32_bf16 a[56:59], v[88:91], v[208:211], a[56:59]\n"
"ds_read_b128 v[30:33], v4 offset:0\n"
"ds_read_b128 v[34:37], v4 offset:20480\n"
"ds_read_b128 v[38:41], v4 offset:40960\n"
"v_mfma_f32_16x16x32_bf16 a[60:63], v[88:91], v[212:215], a[60:63]\n"
"ds_read_b128 v[76:79], v4 offset:61440\n"
"ds_read_b128 v[96:99], v5 offset:0\n"
"ds_read_b128 v[100:103], v5 offset:20480\n"
"v_mfma_f32_16x16x32_bf16 a[64:67], v[92:95], a[212:215], a[64:67]\n"
"s_waitcnt vmcnt(10)\n"
"ds_read_b128 v[104:107], v5 offset:40960\n"
"ds_read_b128 v[108:111], v5 offset:61440\n"
"v_mfma_f32_16x16x32_bf16 a[68:71], v[92:95], a[216:219], a[68:71]\n"
"buffer_load_dwordx4 a[188:191], v28, s[80:83], s32 offen offset:2048 sc1\n"
"v_mfma_f32_16x16x32_bf16 a[72:75], v[92:95], v[208:211], a[72:75]\n"
"v_mfma_f32_16x16x32_bf16 a[76:79], v[92:95], v[212:215], a[76:79]\n"
"s_waitcnt lgkmcnt(4)\n"
"v_add_f32 v30, v30, v34\n"
"v_add_f32 v38, v38, v76\n"
"v_add_f32 v31, v31, v35\n"
"v_mfma_f32_16x16x32_bf16 a[0:3], v[224:227], v[216:219], a[0:3]\n"
"v_add_f32 v39, v39, v77\n"
"v_add_f32 v32, v32, v36\n"
"v_add_f32 v40, v40, v78\n"
"v_mfma_f32_16x16x32_bf16 a[4:7], v[224:227], v[220:223], a[4:7]\n"
"buffer_load_dwordx4 v[192:195], v28, s[80:83], s32 offen offset:3072 sc1\n"
"v_mfma_f32_16x16x32_bf16 a[8:11], v[224:227], a[220:223], a[8:11]\n"
"v_add_f32 v33, v33, v37\n"
"v_add_f32 v41, v41, v79\n"
"v_add_f32 v30, v30, v38\n"
"v_mfma_f32_16x16x32_bf16 a[12:15], v[224:227], a[224:227], a[12:15]\n"
"v_add_f32 v31, v31, v39\n"
"v_add_f32 v32, v32, v40\n"
"v_add_f32 v33, v33, v41\n"
"v_mfma_f32_16x16x32_bf16 a[16:19], v[228:231], v[216:219], a[16:19]\n"
"s_add_u32 s69, s34, 0\n"
"v_and_or_b32 v16, v30, v21, v20\n"
"v_mfma_f32_16x16x32_bf16 a[20:23], v[228:231], v[220:223], a[20:23]\n"
"s_mov_b32 s32, 32768\n"
"buffer_load_dwordx4 v[196:199], v28, s[80:83], s32 offen offset:0 sc1\n"
"v_mfma_f32_16x16x32_bf16 a[24:27], v[228:231], a[220:223], a[24:27]\n"
"v_and_or_b32 v17, v31, v21, v20\n"
"v_and_or_b32 v18, v32, v21, v20\n"
"v_and_or_b32 v19, v33, v21, v20\n"
"v_mfma_f32_16x16x32_bf16 a[28:31], v[228:231], a[224:227], a[28:31]\n"
"buffer_store_dwordx4 v[16:19], v28, s[64:67], s69 offen offset:0 sc1\n"
"v_mfma_f32_16x16x32_bf16 a[32:35], v[232:235], v[216:219], a[32:35]\n"
"ds_read_b128 v[30:33], v6 offset:0\n"
"ds_read_b128 v[34:37], v6 offset:20480\n"
"ds_read_b128 v[38:41], v6 offset:40960\n"
"v_mfma_f32_16x16x32_bf16 a[36:39], v[232:235], v[220:223], a[36:39]\n"
"buffer_load_dwordx4 v[200:203], v28, s[80:83], s32 offen offset:1024 sc1\n"
"v_mfma_f32_16x16x32_bf16 a[40:43], v[232:235], a[220:223], a[40:43]\n"
"ds_read_b128 v[76:79], v6 offset:61440\n"
"v_mfma_f32_16x16x32_bf16 a[44:47], v[232:235], a[224:227], a[44:47]\n"
"v_mfma_f32_16x16x32_bf16 a[48:51], v[236:239], v[216:219], a[48:51]\n"
"v_mfma_f32_16x16x32_bf16 a[52:55], v[236:239], v[220:223], a[52:55]\n"
"buffer_load_dwordx4 v[204:207], v28, s[80:83], s32 offen offset:2048 sc1\n"
"v_mfma_f32_16x16x32_bf16 a[56:59], v[236:239], a[220:223], a[56:59]\n"
"v_mfma_f32_16x16x32_bf16 a[60:63], v[236:239], a[224:227], a[60:63]\n"
"v_mfma_f32_16x16x32_bf16 a[64:67], v[240:243], v[216:219], a[64:67]\n"
"s_waitcnt vmcnt(12)\n"
"v_mfma_f32_16x16x32_bf16 a[68:71], v[240:243], v[220:223], a[68:71]\n"
"buffer_load_dwordx4 a[200:203], v28, s[80:83], s32 offen offset:3072 sc1\n"
"v_mfma_f32_16x16x32_bf16 a[72:75], v[240:243], a[220:223], a[72:75]\n"
"v_mfma_f32_16x16x32_bf16 a[76:79], v[240:243], a[224:227], a[76:79]\n"
"v_mfma_f32_16x16x32_bf16 a[0:3], v[244:247], a[228:231], a[0:3]\n"
"v_mfma_f32_16x16x32_bf16 a[4:7], v[244:247], a[232:235], a[4:7]\n"
"s_mov_b32 s32, 36864\n"
"buffer_load_dwordx4 a[204:207], v28, s[80:83], s32 offen offset:0 sc1\n"
"v_mfma_f32_16x16x32_bf16 a[8:11], v[244:247], a[236:239], a[8:11]\n"
"s_waitcnt lgkmcnt(4)\n"
"v_add_f32 v96, v96, v100\n"
"v_add_f32 v104, v104, v108\n"
"v_add_f32 v97, v97, v101\n"
"v_mfma_f32_16x16x32_bf16 a[12:15], v[244:247], v[144:147], a[12:15]\n"
"v_add_f32 v105, v105, v109\n"
"v_add_f32 v98, v98, v102\n"
"v_add_f32 v106, v106, v110\n"
"v_mfma_f32_16x16x32_bf16 a[16:19], v[248:251], a[228:231], a[16:19]\n"
"v_add_f32 v99, v99, v103\n"
"v_add_f32 v107, v107, v111\n"
"v_add_f32 v96, v96, v104\n"
"v_mfma_f32_16x16x32_bf16 a[20:23], v[248:251], a[232:235], a[20:23]\n"
"buffer_load_dwordx4 a[208:211], v28, s[80:83], s32 offen offset:1024 sc1\n"
"v_mfma_f32_16x16x32_bf16 a[24:27], v[248:251], a[236:239], a[24:27]\n"
"v_add_f32 v97, v97, v105\n"
"v_add_f32 v98, v98, v106\n"
"v_add_f32 v99, v99, v107\n"
"v_mfma_f32_16x16x32_bf16 a[28:31], v[248:251], v[144:147], a[28:31]\n"
"v_and_or_b32 v16, v96, v21, v20\n"
"v_and_or_b32 v17, v97, v21, v20\n"
"v_and_or_b32 v18, v98, v21, v20\n"
"v_mfma_f32_16x16x32_bf16 a[32:35], v[252:255], a[228:231], a[32:35]\n"
"v_and_or_b32 v19, v99, v21, v20\n"
"v_mfma_f32_16x16x32_bf16 a[36:39], v[252:255], a[232:235], a[36:39]\n"
"buffer_load_dwordx4 a[196:199], v28, s[80:83], s32 offen offset:2048 sc1\n"
"v_mfma_f32_16x16x32_bf16 a[40:43], v[252:255], a[236:239], a[40:43]\n"
"buffer_store_dwordx4 v[16:19], v28, s[64:67], s69 offen offset:1024 sc1\n"
"v_mfma_f32_16x16x32_bf16 a[44:47], v[252:255], v[144:147], a[44:47]\n"
"ds_read_b128 v[96:99], v7 offset:0\n"
"ds_read_b128 v[100:103], v7 offset:20480\n"
"ds_read_b128 v[104:107], v7 offset:40960\n"
"v_mfma_f32_16x16x32_bf16 a[48:51], v[128:131], a[228:231], a[48:51]\n"
"ds_read_b128 v[108:111], v7 offset:61440\n"
"v_mfma_f32_16x16x32_bf16 a[52:55], v[128:131], a[232:235], a[52:55]\n"
"buffer_load_dwordx4 v[80:83], v28, s[80:83], s32 offen offset:3072 sc1\n"
"v_mfma_f32_16x16x32_bf16 a[56:59], v[128:131], a[236:239], a[56:59]\n"
"v_mfma_f32_16x16x32_bf16 a[60:63], v[128:131], v[144:147], a[60:63]\n"
"v_mfma_f32_16x16x32_bf16 a[64:67], v[132:135], a[228:231], a[64:67]\n"
"s_waitcnt vmcnt(14)\n"
"v_mfma_f32_16x16x32_bf16 a[68:71], v[132:135], a[232:235], a[68:71]\n"
"s_mov_b32 s32, 40960\n"
"buffer_load_dwordx4 v[84:87], v28, s[80:83], s32 offen offset:0 sc1\n"
"v_mfma_f32_16x16x32_bf16 a[72:75], v[132:135], a[236:239], a[72:75]\n"
"v_mfma_f32_16x16x32_bf16 a[76:79], v[132:135], v[144:147], a[76:79]\n"
"v_mfma_f32_16x16x32_bf16 a[0:3], v[136:139], v[148:151], a[0:3]\n"
"v_mfma_f32_16x16x32_bf16 a[4:7], v[136:139], v[152:155], a[4:7]\n"
"buffer_load_dwordx4 v[88:91], v28, s[80:83], s32 offen offset:1024 sc1\n"
"v_mfma_f32_16x16x32_bf16 a[8:11], v[136:139], v[168:171], a[8:11]\n"
"v_mfma_f32_16x16x32_bf16 a[12:15], v[136:139], v[172:175], a[12:15]\n"
"s_waitcnt lgkmcnt(4)\n"
"v_add_f32 v30, v30, v34\n"
"v_add_f32 v38, v38, v76\n"
"v_add_f32 v31, v31, v35\n"
"v_mfma_f32_16x16x32_bf16 a[16:19], v[140:143], v[148:151], a[16:19]\n"
"v_add_f32 v39, v39, v77\n"
"v_add_f32 v32, v32, v36\n"
"v_add_f32 v40, v40, v78\n"
"v_mfma_f32_16x16x32_bf16 a[20:23], v[140:143], v[152:155], a[20:23]\n"
"buffer_load_dwordx4 v[92:95], v28, s[80:83], s32 offen offset:2048 sc1\n"
"v_mfma_f32_16x16x32_bf16 a[24:27], v[140:143], v[168:171], a[24:27]\n"
"v_add_f32 v33, v33, v37\n"
"v_add_f32 v41, v41, v79\n"
"v_add_f32 v30, v30, v38\n"
"v_mfma_f32_16x16x32_bf16 a[28:31], v[140:143], v[172:175], a[28:31]\n"
"v_add_f32 v31, v31, v39\n"
"v_add_f32 v32, v32, v40\n"
"v_add_f32 v33, v33, v41\n"
"v_mfma_f32_16x16x32_bf16 a[32:35], v[176:179], v[148:151], a[32:35]\n"
"v_and_or_b32 v16, v30, v21, v20\n"
"v_and_or_b32 v17, v31, v21, v20\n"
"v_and_or_b32 v18, v32, v21, v20\n"
"v_mfma_f32_16x16x32_bf16 a[36:39], v[176:179], v[152:155], a[36:39]\n"
"buffer_load_dwordx4 a[212:215], v28, s[80:83], s32 offen offset:3072 sc1\n"
"v_mfma_f32_16x16x32_bf16 a[40:43], v[176:179], v[168:171], a[40:43]\n"
"v_and_or_b32 v19, v33, v21, v20\n"
"v_mfma_f32_16x16x32_bf16 a[44:47], v[176:179], v[172:175], a[44:47]\n"
"buffer_store_dwordx4 v[16:19], v28, s[64:67], s69 offen offset:2048 sc1\n"
"v_mfma_f32_16x16x32_bf16 a[48:51], v[180:183], v[148:151], a[48:51]\n"
"ds_read_b128 v[30:33], v8 offset:0\n"
"ds_read_b128 v[34:37], v8 offset:20480\n"
"ds_read_b128 v[38:41], v8 offset:40960\n"
"v_mfma_f32_16x16x32_bf16 a[52:55], v[180:183], v[152:155], a[52:55]\n"
"s_mov_b32 s32, 45056\n"
"buffer_load_dwordx4 a[216:219], v28, s[80:83], s32 offen offset:0 sc1\n"
"v_mfma_f32_16x16x32_bf16 a[56:59], v[180:183], v[168:171], a[56:59]\n"
"ds_read_b128 v[76:79], v8 offset:61440\n"
"v_mfma_f32_16x16x32_bf16 a[60:63], v[180:183], v[172:175], a[60:63]\n"
"v_mfma_f32_16x16x32_bf16 a[64:67], v[184:187], v[148:151], a[64:67]\n"
"s_waitcnt vmcnt(16)\n"
"v_mfma_f32_16x16x32_bf16 a[68:71], v[184:187], v[152:155], a[68:71]\n"
"buffer_load_dwordx4 v[208:211], v28, s[80:83], s32 offen offset:1024 sc1\n"
"v_mfma_f32_16x16x32_bf16 a[72:75], v[184:187], v[168:171], a[72:75]\n"
"v_mfma_f32_16x16x32_bf16 a[76:79], v[184:187], v[172:175], a[76:79]\n"
"v_mfma_f32_16x16x32_bf16 a[0:3], v[188:191], a[180:183], a[0:3]\n"
"v_mfma_f32_16x16x32_bf16 a[4:7], v[188:191], a[184:187], a[4:7]\n"
"buffer_load_dwordx4 v[212:215], v28, s[80:83], s32 offen offset:2048 sc1\n"
"v_mfma_f32_16x16x32_bf16 a[8:11], v[188:191], a[188:191], a[8:11]\n"
"v_mfma_f32_16x16x32_bf16 a[12:15], v[188:191], v[192:195], a[12:15]\n"
"v_mfma_f32_16x16x32_bf16 a[16:19], v[116:119], a[180:183], a[16:19]\n"
"v_mfma_f32_16x16x32_bf16 a[20:23], v[116:119], a[184:187], a[20:23]\n"
"buffer_load_dwordx4 v[216:219], v28, s[80:83], s32 offen offset:3072 sc1\n"
"v_mfma_f32_16x16x32_bf16 a[24:27], v[116:119], a[188:191], a[24:27]\n"
"s_waitcnt lgkmcnt(4)\n"
"v_add_f32 v96, v96, v100\n"
"v_add_f32 v104, v104, v108\n"
"v_add_f32 v97, v97, v101\n"
"v_mfma_f32_16x16x32_bf16 a[28:31], v[116:119], v[192:195], a[28:31]\n"
"v_add_f32 v105, v105, v109\n"
"v_add_f32 v98, v98, v102\n"
"v_add_f32 v106, v106, v110\n"
"v_mfma_f32_16x16x32_bf16 a[32:35], v[120:123], a[180:183], a[32:35]\n"
"v_add_f32 v99, v99, v103\n"
"v_add_f32 v107, v107, v111\n"
"v_add_f32 v96, v96, v104\n"
"v_mfma_f32_16x16x32_bf16 a[36:39], v[120:123], a[184:187], a[36:39]\n"
"v_add_f32 v97, v97, v105\n"
"v_add_f32 v98, v98, v106\n"
"v_add_f32 v99, v99, v107\n"
"v_mfma_f32_16x16x32_bf16 a[40:43], v[120:123], a[188:191], a[40:43]\n"
"v_and_or_b32 v16, v96, v21, v20\n"
"v_and_or_b32 v17, v97, v21, v20\n"
"v_and_or_b32 v18, v98, v21, v20\n"
"v_mfma_f32_16x16x32_bf16 a[44:47], v[120:123], v[192:195], a[44:47]\n"
"v_and_or_b32 v19, v99, v21, v20\n"
"v_mfma_f32_16x16x32_bf16 a[48:51], v[124:127], a[180:183], a[48:51]\n"
"buffer_store_dwordx4 v[16:19], v28, s[64:67], s69 offen offset:3072 sc1\n"
"v_mfma_f32_16x16x32_bf16 a[52:55], v[124:127], a[184:187], a[52:55]\n"
"v_mfma_f32_16x16x32_bf16 a[56:59], v[124:127], a[188:191], a[56:59]\n"
"v_mfma_f32_16x16x32_bf16 a[60:63], v[124:127], v[192:195], a[60:63]\n"
"v_mfma_f32_16x16x32_bf16 a[64:67], v[44:47], a[180:183], a[64:67]\n"
"s_waitcnt vmcnt(15)\n"
"v_mfma_f32_16x16x32_bf16 a[68:71], v[44:47], a[184:187], a[68:71]\n"
"v_mfma_f32_16x16x32_bf16 a[72:75], v[44:47], a[188:191], a[72:75]\n"
"v_mfma_f32_16x16x32_bf16 a[76:79], v[44:47], v[192:195], a[76:79]\n"
"v_mfma_f32_16x16x32_bf16 a[0:3], v[48:51], v[196:199], a[0:3]\n"
"v_mfma_f32_16x16x32_bf16 a[4:7], v[48:51], v[200:203], a[4:7]\n"
"v_mfma_f32_16x16x32_bf16 a[8:11], v[48:51], v[204:207], a[8:11]\n"
"v_mfma_f32_16x16x32_bf16 a[12:15], v[48:51], a[200:203], a[12:15]\n"
"v_mfma_f32_16x16x32_bf16 a[16:19], v[52:55], v[196:199], a[16:19]\n"
"v_mfma_f32_16x16x32_bf16 a[20:23], v[52:55], v[200:203], a[20:23]\n"
"v_mfma_f32_16x16x32_bf16 a[24:27], v[52:55], v[204:207], a[24:27]\n"
"v_mfma_f32_16x16x32_bf16 a[28:31], v[52:55], a[200:203], a[28:31]\n"
"s_waitcnt lgkmcnt(0)\n"
"v_add_f32 v30, v30, v34\n"
"v_add_f32 v38, v38, v76\n"
"v_add_f32 v31, v31, v35\n"
"v_mfma_f32_16x16x32_bf16 a[32:35], v[56:59], v[196:199], a[32:35]\n"
"v_add_f32 v39, v39, v77\n"
"v_add_f32 v32, v32, v36\n"
"v_add_f32 v40, v40, v78\n"
"v_mfma_f32_16x16x32_bf16 a[36:39], v[56:59], v[200:203], a[36:39]\n"
"v_add_f32 v33, v33, v37\n"
"v_add_f32 v41, v41, v79\n"
"v_add_f32 v30, v30, v38\n"
"v_mfma_f32_16x16x32_bf16 a[40:43], v[56:59], v[204:207], a[40:43]\n"
"v_add_f32 v31, v31, v39\n"
"v_add_f32 v32, v32, v40\n"
"v_add_f32 v33, v33, v41\n"
"v_mfma_f32_16x16x32_bf16 a[44:47], v[56:59], a[200:203], a[44:47]\n"
"s_add_u32 s69, s34, 4096\n"
"v_and_or_b32 v16, v30, v21, v20\n"
"v_mfma_f32_16x16x32_bf16 a[48:51], v[60:63], v[196:199], a[48:51]\n"
"v_and_or_b32 v17, v31, v21, v20\n"
"v_and_or_b32 v18, v32, v21, v20\n"
"v_and_or_b32 v19, v33, v21, v20\n"
"v_mfma_f32_16x16x32_bf16 a[52:55], v[60:63], v[200:203], a[52:55]\n"
"buffer_store_dwordx4 v[16:19], v28, s[64:67], s69 offen offset:0 sc1\n"
"v_mfma_f32_16x16x32_bf16 a[56:59], v[60:63], v[204:207], a[56:59]\n"
"v_subrev_u32 v4, 81920, v4\n"
"v_subrev_u32 v5, 81920, v5\n"
"v_subrev_u32 v6, 81920, v6\n"
"v_mfma_f32_16x16x32_bf16 a[60:63], v[60:63], a[200:203], a[60:63]\n"
"v_subrev_u32 v7, 81920, v7\n"
"v_subrev_u32 v8, 81920, v8\n"
"v_mfma_f32_16x16x32_bf16 a[64:67], v[64:67], v[196:199], a[64:67]\n"
"s_waitcnt vmcnt(11)\n"
"buffer_load_dword v224, v10, s[48:51], 0 offen offset:0\n"
"v_mfma_f32_16x16x32_bf16 a[68:71], v[64:67], v[200:203], a[68:71]\n"
"buffer_load_dword v225, v10, s[48:51], 0 offen offset:4\n"
"v_mfma_f32_16x16x32_bf16 a[72:75], v[64:67], v[204:207], a[72:75]\n"
"s_waitcnt vmcnt(4)\n"
"v_mul_u32_u24 v9, 52, v10\n"
"v_mfma_f32_16x16x32_bf16 a[76:79], v[64:67], a[200:203], a[76:79]\n"
"v_lshrrev_b32 v9, 16, v9\n"
"v_mul_u32_u24 v15, 1280, v9\n"
"v_sub_u32 v15, v10, v15\n"
"v_mfma_f32_16x16x32_bf16 a[0:3], v[68:71], a[204:207], a[0:3]\n"
"v_lshlrev_b32 v9, 4, v9\n"
"v_mfma_f32_16x16x32_bf16 a[4:7], v[68:71], a[208:211], a[4:7]\n"
"buffer_load_dword v234, v15, s[24:27], 0 offen offset:0\n"
"v_mfma_f32_16x16x32_bf16 a[8:11], v[68:71], a[196:199], a[8:11]\n"
"buffer_load_dword v235, v15, s[24:27], 0 offen offset:4\n"
"v_mfma_f32_16x16x32_bf16 a[12:15], v[68:71], v[80:83], a[12:15]\n"
"buffer_load_dword v244, v9, s[16:19], 0 offen offset:0 sc1\n"
"v_mfma_f32_16x16x32_bf16 a[0:3], a[172:175], v[84:87], a[0:3]\n"
"buffer_load_dword v245, v9, s[16:19], 0 offen offset:8 sc1\n"
"v_mfma_f32_16x16x32_bf16 a[4:7], a[172:175], v[88:91], a[4:7]\n"
"buffer_load_dword v226, v11, s[48:51], 0 offen offset:0\n"
"v_mfma_f32_16x16x32_bf16 a[8:11], a[172:175], v[92:95], a[8:11]\n"
"buffer_load_dword v227, v11, s[48:51], 0 offen offset:4\n"
"v_mfma_f32_16x16x32_bf16 a[12:15], a[172:175], a[212:215], a[12:15]\n"
"v_mul_u32_u24 v9, 52, v11\n"
"v_lshrrev_b32 v9, 16, v9\n"
"v_mul_u32_u24 v15, 1280, v9\n"
"v_mfma_f32_16x16x32_bf16 a[0:3], a[248:251], a[216:219], a[0:3]\n"
"v_sub_u32 v15, v11, v15\n"
"v_lshlrev_b32 v9, 4, v9\n"
"v_mfma_f32_16x16x32_bf16 a[4:7], a[248:251], v[208:211], a[4:7]\n"
"buffer_load_dword v236, v15, s[24:27], 0 offen offset:0\n"
"v_mfma_f32_16x16x32_bf16 a[8:11], a[248:251], v[212:215], a[8:11]\n"
"buffer_load_dword v237, v15, s[24:27], 0 offen offset:4\n"
"v_mfma_f32_16x16x32_bf16 a[12:15], a[248:251], v[216:219], a[12:15]\n"
"buffer_load_dword v246, v9, s[16:19], 0 offen offset:0 sc1\n"
"v_mfma_f32_16x16x32_bf16 a[16:19], v[72:75], a[204:207], a[16:19]\n"
"buffer_load_dword v247, v9, s[16:19], 0 offen offset:8 sc1\n"
"v_mfma_f32_16x16x32_bf16 a[20:23], v[72:75], a[208:211], a[20:23]\n"
"buffer_load_dword v228, v12, s[48:51], 0 offen offset:0\n"
"v_mfma_f32_16x16x32_bf16 a[24:27], v[72:75], a[196:199], a[24:27]\n"
"ds_write_b128 v2, a[0:3] offset:0\n"
"buffer_load_dword v229, v12, s[48:51], 0 offen offset:4\n"
"v_mfma_f32_16x16x32_bf16 a[28:31], v[72:75], v[80:83], a[28:31]\n"
"v_mul_u32_u24 v9, 52, v12\n"
"v_lshrrev_b32 v9, 16, v9\n"
"v_mul_u32_u24 v15, 1280, v9\n"
"v_mfma_f32_16x16x32_bf16 a[16:19], a[176:179], v[84:87], a[16:19]\n"
"v_sub_u32 v15, v12, v15\n"
"v_lshlrev_b32 v9, 4, v9\n"
"v_mfma_f32_16x16x32_bf16 a[20:23], a[176:179], v[88:91], a[20:23]\n"
"ds_write_b128 v2, a[4:7] offset:2560\n"
"buffer_load_dword v238, v15, s[24:27], 0 offen offset:0\n"
"v_mfma_f32_16x16x32_bf16 a[24:27], a[176:179], v[92:95], a[24:27]\n"
"buffer_load_dword v239, v15, s[24:27], 0 offen offset:4\n"
"v_mfma_f32_16x16x32_bf16 a[28:31], a[176:179], a[212:215], a[28:31]\n"
"buffer_load_dword v248, v9, s[16:19], 0 offen offset:0 sc1\n"
"v_mfma_f32_16x16x32_bf16 a[16:19], a[252:255], a[216:219], a[16:19]\n"
"ds_write_b128 v2, a[8:11] offset:10240\n"
"buffer_load_dword v249, v9, s[16:19], 0 offen offset:8 sc1\n"
"v_mfma_f32_16x16x32_bf16 a[20:23], a[252:255], v[208:211], a[20:23]\n"
"buffer_load_dword v230, v13, s[48:51], 0 offen offset:0\n"
"v_mfma_f32_16x16x32_bf16 a[24:27], a[252:255], v[212:215], a[24:27]\n"
"buffer_load_dword v231, v13, s[48:51], 0 offen offset:4\n"
"v_mfma_f32_16x16x32_bf16 a[28:31], a[252:255], v[216:219], a[28:31]\n"
"ds_write_b128 v2, a[12:15] offset:12800\n"
"v_mul_u32_u24 v9, 52, v13\n"
"v_lshrrev_b32 v9, 16, v9\n"
"v_mfma_f32_16x16x32_bf16 a[32:35], a[160:163], a[204:207], a[32:35]\n"
"v_mul_u32_u24 v15, 1280, v9\n"
"v_sub_u32 v15, v13, v15\n"
"v_lshlrev_b32 v9, 4, v9\n"
"v_mfma_f32_16x16x32_bf16 a[36:39], a[160:163], a[208:211], a[36:39]\n"
"buffer_load_dword v240, v15, s[24:27], 0 offen offset:0\n"
"v_mfma_f32_16x16x32_bf16 a[40:43], a[160:163], a[196:199], a[40:43]\n"
"ds_write_b128 v2, a[16:19] offset:64\n"
"buffer_load_dword v241, v15, s[24:27], 0 offen offset:4\n"
"v_mfma_f32_16x16x32_bf16 a[44:47], a[160:163], v[80:83], a[44:47]\n"
"buffer_load_dword v250, v9, s[16:19], 0 offen offset:0 sc1\n"
"v_mfma_f32_16x16x32_bf16 a[32:35], v[156:159], v[84:87], a[32:35]\n"
"buffer_load_dword v251, v9, s[16:19], 0 offen offset:8 sc1\n"
"v_mfma_f32_16x16x32_bf16 a[36:39], v[156:159], v[88:91], a[36:39]\n"
"ds_write_b128 v2, a[20:23] offset:2624\n"
"buffer_load_dword v232, v14, s[48:51], 0 offen offset:0\n"
"v_mfma_f32_16x16x32_bf16 a[40:43], v[156:159], v[92:95], a[40:43]\n"
"buffer_load_dword v233, v14, s[48:51], 0 offen offset:4\n"
"v_mfma_f32_16x16x32_bf16 a[44:47], v[156:159], a[212:215], a[44:47]\n"
"v_mul_u32_u24 v9, 52, v14\n"
"v_lshrrev_b32 v9, 16, v9\n"
"v_mul_u32_u24 v15, 1280, v9\n"
"v_mfma_f32_16x16x32_bf16 a[32:35], a[192:195], a[216:219], a[32:35]\n"
"ds_write_b128 v2, a[24:27] offset:10304\n"
"v_sub_u32 v15, v14, v15\n"
"v_lshlrev_b32 v9, 4, v9\n"
"v_mfma_f32_16x16x32_bf16 a[36:39], a[192:195], v[208:211], a[36:39]\n"
"buffer_load_dword v242, v15, s[24:27], 0 offen offset:0\n"
"v_mfma_f32_16x16x32_bf16 a[40:43], a[192:195], v[212:215], a[40:43]\n"
"buffer_load_dword v243, v15, s[24:27], 0 offen offset:4\n"
"v_mfma_f32_16x16x32_bf16 a[44:47], a[192:195], v[216:219], a[44:47]\n"
"ds_write_b128 v2, a[28:31] offset:12864\n"
"buffer_load_dword v252, v9, s[16:19], 0 offen offset:0 sc1\n"
"v_mfma_f32_16x16x32_bf16 a[48:51], a[164:167], a[204:207], a[48:51]\n"
"buffer_load_dword v253, v9, s[16:19], 0 offen offset:8 sc1\n"
"v_mfma_f32_16x16x32_bf16 a[52:55], a[164:167], a[208:211], a[52:55]\n"
"v_mfma_f32_16x16x32_bf16 a[56:59], a[164:167], a[196:199], a[56:59]\n"
"ds_write_b128 v2, a[32:35] offset:128\n"
"v_mfma_f32_16x16x32_bf16 a[60:63], a[164:167], v[80:83], a[60:63]\n"
"v_mfma_f32_16x16x32_bf16 a[48:51], a[240:243], v[84:87], a[48:51]\n"
"v_mfma_f32_16x16x32_bf16 a[52:55], a[240:243], v[88:91], a[52:55]\n"
"ds_write_b128 v2, a[36:39] offset:2688\n"
"v_mfma_f32_16x16x32_bf16 a[56:59], a[240:243], v[92:95], a[56:59]\n"
"v_mfma_f32_16x16x32_bf16 a[60:63], a[240:243], a[212:215], a[60:63]\n"
"v_mfma_f32_16x16x32_bf16 a[48:51], v[160:163], a[216:219], a[48:51]\n"
"ds_write_b128 v2, a[40:43] offset:10368\n"
"v_mfma_f32_16x16x32_bf16 a[52:55], v[160:163], v[208:211], a[52:55]\n"
"v_mfma_f32_16x16x32_bf16 a[56:59], v[160:163], v[212:215], a[56:59]\n"
"v_mfma_f32_16x16x32_bf16 a[60:63], v[160:163], v[216:219], a[60:63]\n"
"ds_write_b128 v2, a[44:47] offset:12928\n"
"v_mfma_f32_16x16x32_bf16 a[64:67], a[168:171], a[204:207], a[64:67]\n"
"v_mfma_f32_16x16x32_bf16 a[68:71], a[168:171], a[208:211], a[68:71]\n"
"v_mfma_f32_16x16x32_bf16 a[72:75], a[168:171], a[196:199], a[72:75]\n"
"ds_write_b128 v2, a[48:51] offset:192\n"
"v_mfma_f32_16x16x32_bf16 a[76:79], a[168:171], v[80:83], a[76:79]\n"
"v_mfma_f32_16x16x32_bf16 a[64:67], a[244:247], v[84:87], a[64:67]\n"
"s_add_u32 s68, s35, 0\n"
"v_mfma_f32_16x16x32_bf16 a[68:71], a[244:247], v[88:91], a[68:71]\n"
"ds_write_b128 v2, a[52:55] offset:2752\n"
"buffer_load_dwordx4 v[96:99], v28, s[64:67], s68 offen offset:0 sc1\n"
"v_mfma_f32_16x16x32_bf16 a[72:75], a[244:247], v[92:95], a[72:75]\n"
"buffer_load_dwordx4 v[100:103], v28, s[64:67], s68 offen offset:1024 sc1\n"
"v_mfma_f32_16x16x32_bf16 a[76:79], a[244:247], a[212:215], a[76:79]\n"
"buffer_load_dwordx4 v[104:107], v28, s[64:67], s68 offen offset:2048 sc1\n"
"v_mfma_f32_16x16x32_bf16 a[64:67], v[164:167], a[216:219], a[64:67]\n"
"ds_write_b128 v2, a[56:59] offset:10432\n"
"buffer_load_dwordx4 v[108:111], v28, s[64:67], s68 offen offset:3072 sc1\n"
"v_mfma_f32_16x16x32_bf16 a[68:71], v[164:167], v[208:211], a[68:71]\n"
"s_add_u32 s68, s35, 4096\n"
"v_mfma_f32_16x16x32_bf16 a[72:75], v[164:167], v[212:215], a[72:75]\n"
"buffer_load_dwordx4 v[112:115], v28, s[64:67], s68 offen offset:0 sc1\n"
"v_mfma_f32_16x16x32_bf16 a[76:79], v[164:167], v[216:219], a[76:79]\n"
"ds_write_b128 v2, a[60:63] offset:12992\n"
"ds_write_b128 v2, a[64:67] offset:256\n"
"s_nop 1\n"
"ds_write_b128 v2, a[68:71] offset:2816\n"
"s_nop 0\n"
"ds_write_b128 v2, a[72:75] offset:10496\n"
"s_nop 0\n"
"ds_write_b128 v2, a[76:79] offset:13056\n"
"s_waitcnt vmcnt(5)\n"
"v_mul_f32 v245, -v245, v244\n"
"v_mul_f32 v247, -v247, v246\n"
"v_mul_f32 v249, -v249, v248\n"
"v_mul_f32 v251, -v251, v250\n"
"v_mul_f32 v253, -v253, v252\n"
"s_waitcnt lgkmcnt(0)\n"
"s_barrier\n"
"ds_read_b128 v[128:131], v4 offset:0\n"
"ds_read_b128 v[132:135], v4 offset:20480\n"
"ds_read_b128 v[136:139], v4 offset:40960\n"
"ds_read_b128 v[140:143], v4 offset:61440\n"
"ds_read_b128 v[144:147], v5 offset:0\n"
"ds_read_b128 v[148:151], v5 offset:20480\n"
"ds_read_b128 v[152:155], v5 offset:40960\n"
"ds_read_b128 v[156:159], v5 offset:61440\n"
"ds_read_b128 v[160:163], v6 offset:0\n"
"ds_read_b128 v[164:167], v6 offset:20480\n"
"ds_read_b128 v[168:171], v6 offset:40960\n"
"ds_read_b128 v[172:175], v6 offset:61440\n"
"ds_read_b128 v[176:179], v7 offset:0\n"
"ds_read_b128 v[180:183], v7 offset:20480\n"
"ds_read_b128 v[184:187], v7 offset:40960\n"
"ds_read_b128 v[188:191], v7 offset:61440\n"
"ds_read_b128 v[192:195], v8 offset:0\n"
"ds_read_b128 v[196:199], v8 offset:20480\n"
"ds_read_b128 v[200:203], v8 offset:40960\n"
"ds_read_b128 v[204:207], v8 offset:61440\n"
"s_waitcnt vmcnt(0)\n"
"v_or3_b32 v76, v96, v97, v96\n"
"v_bitop3_b32 v77, v96, v97, v96 bitop3:0x80\n"
"v_or3_b32 v76, v98, v99, v76\n"
"v_bitop3_b32 v77, v98, v99, v77 bitop3:0x80\n"
"v_or3_b32 v76, v100, v101, v76\n"
"v_bitop3_b32 v77, v100, v101, v77 bitop3:0x80\n"
"v_or3_b32 v76, v102, v103, v76\n"
"v_bitop3_b32 v77, v102, v103, v77 bitop3:0x80\n"
"v_or3_b32 v76, v104, v105, v76\n"
"v_bitop3_b32 v77, v104, v105, v77 bitop3:0x80\n"
"v_or3_b32 v76, v106, v107, v76\n"
"v_bitop3_b32 v77, v106, v107, v77 bitop3:0x80\n"
"v_or3_b32 v76, v108, v109, v76\n"
"v_bitop3_b32 v77, v108, v109, v77 bitop3:0x80\n"
"v_or3_b32 v76, v110, v111, v76\n"
"v_bitop3_b32 v77, v110, v111, v77 bitop3:0x80\n"
"v_or3_b32 v76, v112, v113, v76\n"
"v_bitop3_b32 v77, v112, v113, v77 bitop3:0x80\n"
"v_or3_b32 v76, v114, v115, v76\n"
"v_bitop3_b32 v77, v114, v115, v77 bitop3:0x80\n"
"v_xor_b32 v76, v76, v20\n"
"v_xor_b32 v77, v77, v20\n"
"v_or_b32 v76, v76, v77\n"
"v_and_b32 v76, 0xfff, v76\n"
"v_cmp_ne_u32 vcc, 0, v76\n"
"s_nop 0\n"
"s_cmp_lg_u64 vcc, 0\n"
"s_cbranch_scc1 K1S_x_slow\n"
"K1S_x_back:\n"
"s_waitcnt lgkmcnt(15)\n"
"v_pk_add_f32 v[128:129], v[128:129], v[132:133]\n"
"v_pk_add_f32 v[136:137], v[136:137], v[140:141]\n"
"v_pk_add_f32 v[130:131], v[130:131], v[134:135]\n"
"v_pk_add_f32 v[138:139], v[138:139], v[142:143]\n"
"v_lshlrev_b32 v132, 16, v224\n"
"v_and_b32 v133, s74, v224\n"
"v_lshlrev_b32 v140, 16, v234\n"
"v_and_b32 v141, s74, v234\n"
"v_lshlrev_b32 v134, 16, v225\n"
"v_and_b32 v135, s74, v225\n"
"v_lshlrev_b32 v142, 16, v235\n"
"v_and_b32 v143, s74, v235\n"
"v_pk_add_f32 v[128:129], v[128:129], v[136:137]\n"
"v_pk_add_f32 v[130:131], v[130:131], v[138:139]\n"
"v_and_b32 v96, v21, v96\n"
"v_and_b32 v97, v21, v97\n"
"v_and_b32 v98, v21, v98\n"
"v_and_b32 v99, v21, v99\n"
"v_pk_add_f32 v[136:137], v[132:133], v[96:97]\n"
"v_pk_add_f32 v[138:139], v[134:135], v[98:99]\n"
"v_pk_fma_f32 v[132:133], v[132:133], v[244:245], v[244:245] op_sel:[0,0,1] op_sel_hi:[1,0,1]\n"
"v_pk_add_f32 v[128:129], v[128:129], v[136:137]\n"
"v_pk_fma_f32 v[134:135], v[134:135], v[244:245], v[244:245] op_sel:[0,0,1] op_sel_hi:[1,0,1]\n"
"v_pk_add_f32 v[130:131], v[130:131], v[138:139]\n"
"v_lshlrev_b32 v76, 1, v10\n"
"v_pk_fma_f32 v[128:129], v[132:133], v[140:141], v[128:129]\n"
"v_pk_fma_f32 v[130:131], v[134:135], v[142:143], v[130:131]\n"
"buffer_store_dwordx4 v[128:131], v76, s[52:55], 0 offen sc1\n"
"s_waitcnt lgkmcnt(12)\n"
"v_pk_add_f32 v[144:145], v[144:145], v[148:149]\n"
"v_pk_add_f32 v[152:153], v[152:153], v[156:157]\n"
"v_pk_add_f32 v[146:147], v[146:147], v[150:151]\n"
"v_pk_add_f32 v[154:155], v[154:155], v[158:159]\n"
"v_lshlrev_b32 v148, 16, v226\n"
"v_and_b32 v149, s74, v226\n"
"v_lshlrev_b32 v156, 16, v236\n"
"v_and_b32 v157, s74, v236\n"
"v_lshlrev_b32 v150, 16, v227\n"
"v_and_b32 v151, s74, v227\n"
"v_lshlrev_b32 v158, 16, v237\n"
"v_and_b32 v159, s74, v237\n"
"v_pk_add_f32 v[144:145], v[144:145], v[152:153]\n"
"v_pk_add_f32 v[146:147], v[146:147], v[154:155]\n"
"v_and_b32 v100, v21, v100\n"
"v_and_b32 v101, v21, v101\n"
"v_and_b32 v102, v21, v102\n"
"v_and_b32 v103, v21, v103\n"
"v_pk_add_f32 v[152:153], v[148:149], v[100:101]\n"
"v_pk_add_f32 v[154:155], v[150:151], v[102:103]\n"
"v_pk_fma_f32 v[148:149], v[148:149], v[246:247], v[246:247] op_sel:[0,0,1] op_sel_hi:[1,0,1]\n"
"v_pk_add_f32 v[144:145], v[144:145], v[152:153]\n"
"v_pk_fma_f32 v[150:151], v[150:151], v[246:247], v[246:247] op_sel:[0,0,1] op_sel_hi:[1,0,1]\n"
"v_pk_add_f32 v[146:147], v[146:147], v[154:155]\n"
"v_lshlrev_b32 v76, 1, v11\n"
"v_pk_fma_f32 v[144:145], v[148:149], v[156:157], v[144:145]\n"
"v_pk_fma_f32 v[146:147], v[150:151], v[158:159], v[146:147]\n"
"buffer_store_dwordx4 v[144:147], v76, s[52:55], 0 offen sc1\n"
"s_waitcnt lgkmcnt(8)\n"
"v_pk_add_f32 v[160:161], v[160:161], v[164:165]\n"
"v_pk_add_f32 v[168:169], v[168:169], v[172:173]\n"
"v_pk_add_f32 v[162:163], v[162:163], v[166:167]\n"
"v_pk_add_f32 v[170:171], v[170:171], v[174:175]\n"
"v_lshlrev_b32 v164, 16, v228\n"
"v_and_b32 v165, s74, v228\n"
"v_lshlrev_b32 v172, 16, v238\n"
"v_and_b32 v173, s74, v238\n"
"v_lshlrev_b32 v166, 16, v229\n"
"v_and_b32 v167, s74, v229\n"
"v_lshlrev_b32 v174, 16, v239\n"
"v_and_b32 v175, s74, v239\n"
"v_pk_add_f32 v[160:161], v[160:161], v[168:169]\n"
"v_pk_add_f32 v[162:163], v[162:163], v[170:171]\n"
"v_and_b32 v104, v21, v104\n"
"v_and_b32 v105, v21, v105\n"
"v_and_b32 v106, v21, v106\n"
"v_and_b32 v107, v21, v107\n"
"v_pk_add_f32 v[168:169], v[164:165], v[104:105]\n"
"v_pk_add_f32 v[170:171], v[166:167], v[106:107]\n"
"v_pk_fma_f32 v[164:165], v[164:165], v[248:249], v[248:249] op_sel:[0,0,1] op_sel_hi:[1,0,1]\n"
"v_pk_add_f32 v[160:161], v[160:161], v[168:169]\n"
"v_pk_fma_f32 v[166:167], v[166:167], v[248:249], v[248:249] op_sel:[0,0,1] op_sel_hi:[1,0,1]\n"
"v_pk_add_f32 v[162:163], v[162:163], v[170:171]\n"
"v_lshlrev_b32 v76, 1, v12\n"
"v_pk_fma_f32 v[160:161], v[164:165], v[172:173], v[160:161]\n"
"v_pk_fma_f32 v[162:163], v[166:167], v[174:175], v[162:163]\n"
"buffer_store_dwordx4 v[160:163], v76, s[52:55], 0 offen sc1\n"
"s_waitcnt lgkmcnt(4)\n"
"v_pk_add_f32 v[176:177], v[176:177], v[180:181]\n"
"v_pk_add_f32 v[184:185], v[184:185], v[188:189]\n"
"v_pk_add_f32 v[178:179], v[178:179], v[182:183]\n"
"v_pk_add_f32 v[186:187], v[186:187], v[190:191]\n"
"v_lshlrev_b32 v180, 16, v230\n"
"v_and_b32 v181, s74, v230\n"
"v_lshlrev_b32 v188, 16, v240\n"
"v_and_b32 v189, s74, v240\n"
"v_lshlrev_b32 v182, 16, v231\n"
"v_and_b32 v183, s74, v231\n"
"v_lshlrev_b32 v190, 16, v241\n"
"v_and_b32 v191, s74, v241\n"
"v_pk_add_f32 v[176:177], v[176:177], v[184:185]\n"
"v_pk_add_f32 v[178:179], v[178:179], v[186:187]\n"
"v_and_b32 v108, v21, v108\n"
"v_and_b32 v109, v21, v109\n"
"v_and_b32 v110, v21, v110\n"
"v_and_b32 v111, v21, v111\n"
"v_pk_add_f32 v[184:185], v[180:181], v[108:109]\n"
"v_pk_add_f32 v[186:187], v[182:183], v[110:111]\n"
"v_pk_fma_f32 v[180:181], v[180:181], v[250:251], v[250:251] op_sel:[0,0,1] op_sel_hi:[1,0,1]\n"
"v_pk_add_f32 v[176:177], v[176:177], v[184:185]\n"
"v_pk_fma_f32 v[182:183], v[182:183], v[250:251], v[250:251] op_sel:[0,0,1] op_sel_hi:[1,0,1]\n"
"v_pk_add_f32 v[178:179], v[178:179], v[186:187]\n"
"v_lshlrev_b32 v76, 1, v13\n"
"v_pk_fma_f32 v[176:177], v[180:181], v[188:189], v[176:177]\n"
"v_pk_fma_f32 v[178:179], v[182:183], v[190:191], v[178:179]\n"
"buffer_store_dwordx4 v[176:179], v76, s[52:55], 0 offen sc1\n"
"s_waitcnt lgkmcnt(0)\n"
"v_pk_add_f32 v[192:193], v[192:193], v[196:197]\n"
"v_pk_add_f32 v[200:201], v[200:201], v[204:205]\n"
"v_pk_add_f32 v[194:195], v[194:195], v[198:199]\n"
"v_pk_add_f32 v[202:203], v[202:203], v[206:207]\n"
"v_lshlrev_b32 v196, 16, v232\n"
"v_and_b32 v197, s74, v232\n"
"v_lshlrev_b32 v204, 16, v242\n"
"v_and_b32 v205, s74, v242\n"
"v_lshlrev_b32 v198, 16, v233\n"
"v_and_b32 v199, s74, v233\n"
"v_lshlrev_b32 v206, 16, v243\n"
"v_and_b32 v207, s74, v243\n"
"v_pk_add_f32 v[192:193], v[192:193], v[200:201]\n"
"v_pk_add_f32 v[194:195], v[194:195], v[202:203]\n"
"v_and_b32 v112, v21, v112\n"
"v_and_b32 v113, v21, v113\n"
"v_and_b32 v114, v21, v114\n"
"v_and_b32 v115, v21, v115\n"
"v_pk_add_f32 v[200:201], v[196:197], v[112:113]\n"
"v_pk_add_f32 v[202:203], v[198:199], v[114:115]\n"
"v_pk_fma_f32 v[196:197], v[196:197], v[252:253], v[252:253] op_sel:[0,0,1] op_sel_hi:[1,0,1]\n"
"v_pk_add_f32 v[192:193], v[192:193], v[200:201]\n"
"v_pk_fma_f32 v[198:199], v[198:199], v[252:253], v[252:253] op_sel:[0,0,1] op_sel_hi:[1,0,1]\n"
"v_pk_add_f32 v[194:195], v[194:195], v[202:203]\n"
"v_lshlrev_b32 v76, 1, v14\n"
"v_pk_fma_f32 v[192:193], v[196:197], v[204:205], v[192:193]\n"
"v_pk_fma_f32 v[194:195], v[198:199], v[206:207], v[194:195]\n"
"buffer_store_dwordx4 v[192:195], v76, s[52:55], 0 offen sc1\n"
"s_endpgm\n"
"K1F_s_slow:\n"
"s_nop 4\n"
"s_mov_b32 s71, 0\n"
"K1F_s_loop:\n"
"s_sleep 2\n"
"buffer_load_dwordx2 v[22:23], v24, s[64:67], 0 offen sc1\n"
"s_waitcnt vmcnt(0)\n"
"v_xor_b32 v22, s72, v22\n"
"v_xor_b32 v23, s73, v23\n"
"v_or_b32 v22, v22, v23\n"
"v_cmp_ne_u32 vcc, 0, v22\n"
"s_nop 0\n"
"s_cmp_lg_u64 vcc, 0\n"
"s_cbranch_scc0 K1F_s_back\n"
"s_add_u32 s71, s71, 1\n"
"s_cmp_lt_u32 s71, 262144\n"
"s_cbranch_scc1 K1F_s_loop\n"
"s_branch K1F_s_back\n"
"K1F_l_slow:\n"
"s_nop 4\n"
"s_mov_b32 s71, 0\n"
"K1F_l_loop:\n"
"s_sleep 2\n"
"buffer_load_dwordx4 v[64:67], v78, s[64:67], 0 offen offset:0 sc1\n"
"buffer_load_dwordx4 a[192:195], v79, s[64:67], s93 offen offset:0 sc1\n"
"s_waitcnt vmcnt(0)\n"
"v_xor_b32 v69, s72, v65\n"
"v_xor_b32 v70, s73, v67\n"
"v_or3_b32 v68, v69, v70, 0\n"
"v_accvgpr_read_b32 v69, a193\n"
"v_accvgpr_read_b32 v70, a195\n"
"v_xor_b32 v69, s72, v69\n"
"v_xor_b32 v70, s73, v70\n"
"v_or3_b32 v68, v69, v70, v68\n"
"v_cmp_ne_u32 vcc, 0, v68\n"
"s_nop 0\n"
"s_cmp_lg_u64 vcc, 0\n"
"s_cbranch_scc0 K1F_l_back\n"
"s_add_u32 s71, s71, 1\n"
"s_cmp_lt_u32 s71, 262144\n"
"s_cbranch_scc1 K1F_l_loop\n"
"s_branch K1F_l_back\n"
"K1F_m_slow:\n"
"s_nop 4\n"
"s_mov_b32 s71, 0\n"
"K1F_m_loop:\n"
"s_sleep 2\n"
"buffer_load_dwordx4 v[80:83], v15, s[64:67], s92 offen offset:0 sc1\n"
"buffer_load_dwordx4 v[84:87], v15, s[64:67], s92 offen offset:1024 sc1\n"
"buffer_load_dwordx4 v[88:91], v15, s[64:67], s92 offen offset:2048 sc1\n"
"buffer_load_dwordx4 v[92:95], v15, s[64:67], s92 offen offset:3072 sc1\n"
"s_waitcnt vmcnt(0)\n"
"v_xor_b32 v69, s72, v81\n"
"v_xor_b32 v70, s73, v83\n"
"v_or3_b32 v68, v69, v70, 0\n"
"v_xor_b32 v69, s72, v85\n"
"v_xor_b32 v70, s73, v87\n"
"v_or3_b32 v68, v69, v70, v68\n"
"v_xor_b32 v69, s72, v89\n"
"v_xor_b32 v70, s73, v91\n"
"v_or3_b32 v68, v69, v70, v68\n"
"v_xor_b32 v69, s72, v93\n"
"v_xor_b32 v70, s73, v95\n"
"v_or3_b32 v68, v69, v70, v68\n"
"v_cmp_ne_u32 vcc, 0, v68\n"
"s_nop 0\n"
"s_cmp_lg_u64 vcc, 0\n"
"s_cbranch_scc0 K1F_m_back\n"
"s_add_u32 s71, s71, 1\n"
"s_cmp_lt_u32 s71, 262144\n"
"s_cbranch_scc1 K1F_m_loop\n"
"s_branch K1F_m_back\n"
"K1F_g0_slow:\n"
"s_nop 4\n"
"s_mov_b32 s71, 0\n"
"K1F_g0_loop:\n"
"s_sleep 2\n"
"buffer_load_dwordx4 v[68:71], v38, s[64:67], 0 offen sc1\n"
"buffer_load_dwordx4 v[72:75], v39, s[64:67], 0 offen sc1\n"
"s_waitcnt vmcnt(0)\n"
"v_xor_b32 v76, s72, v69\n"
"v_xor_b32 v77, s73, v71\n"
"v_or_b32 v76, v76, v77\n"
"v_cmp_ne_u32 s[22:23], 0, v76\n"
"v_xor_b32 v76, s72, v73\n"
"v_xor_b32 v77, s73, v75\n"
"v_or_b32 v76, v76, v77\n"
"v_cmp_ne_u32 s[24:25], 0, v76\n"
"s_and_b64 s[22:23], s[22:23], s[18:19]\n"
"s_and_b64 s[24:25], s[24:25], s[20:21]\n"
"s_or_b64 s[22:23], s[22:23], s[24:25]\n"
"s_cbranch_scc0 K1F_g0_back\n"
"s_add_u32 s71, s71, 1\n"
"s_cmp_lt_u32 s71, 262144\n"
"s_cbranch_scc1 K1F_g0_loop\n"
"s_branch K1F_g0_back\n"
"K1F_r0_slow:\n"
"s_nop 4\n"
"s_mov_b32 s71, 0\n"
"K1F_r0_loop:\n"
"s_sleep 2\n"
"s_mov_b32 s70, 0\n"
"buffer_load_dwordx2 v[42:43], v3, s[64:67], s70 offen sc1\n"
"s_waitcnt vmcnt(0)\n"
"v_xor_b32 v42, s72, v42\n"
"v_xor_b32 v43, s73, v43\n"
"v_or_b32 v42, v42, v43\n"
"v_cmp_ne_u32 vcc, 0, v42\n"
"s_nop 0\n"
"s_cmp_lg_u64 vcc, 0\n"
"s_cbranch_scc0 K1F_r0_back\n"
"s_add_u32 s71, s71, 1\n"
"s_cmp_lt_u32 s71, 262144\n"
"s_cbranch_scc1 K1F_r0_loop\n"
"s_branch K1F_r0_back\n"
"K1F_r1_slow:\n"
"s_nop 4\n"
"s_mov_b32 s71, 0\n"
"K1F_r1_loop:\n"
"s_sleep 2\n"
"s_mov_b32 s70, 32768\n"
"buffer_load_dwordx2 v[42:43], v3, s[64:67], s70 offen sc1\n"
"s_waitcnt vmcnt(0)\n"
"v_xor_b32 v42, s72, v42\n"
"v_xor_b32 v43, s73, v43\n"
"v_or_b32 v42, v42, v43\n"
"v_cmp_ne_u32 vcc, 0, v42\n"
"s_nop 0\n"
"s_cmp_lg_u64 vcc, 0\n"
"s_cbranch_scc0 K1F_r1_back\n"
"s_add_u32 s71, s71, 1\n"
"s_cmp_lt_u32 s71, 262144\n"
"s_cbranch_scc1 K1F_r1_loop\n"
"s_branch K1F_r1_back\n"
"K1F_r2_slow:\n"
"s_nop 4\n"
"s_mov_b32 s71, 0\n"
"K1F_r2_loop:\n"
"s_sleep 2\n"
"s_mov_b32 s70, 65536\n"
"buffer_load_dwordx2 v[42:43], v3, s[64:67], s70 offen sc1\n"
"s_waitcnt vmcnt(0)\n"
"v_xor_b32 v42, s72, v42\n"
"v_xor_b32 v43, s73, v43\n"
"v_or_b32 v42, v42, v43\n"
"v_cmp_ne_u32 vcc, 0, v42\n"
"s_nop 0\n"
"s_cmp_lg_u64 vcc, 0\n"
"s_cbranch_scc0 K1F_r2_back\n"
"s_add_u32 s71, s71, 1\n"
"s_cmp_lt_u32 s71, 262144\n"
"s_cbranch_scc1 K1F_r2_loop\n"
"s_branch K1F_r2_back\n"
"K1F_x_slow:\n"
"s_nop 4\n"
"s_mov_b32 s71, 0\n"
"K1F_x_loop:\n"
"s_sleep 2\n"
"s_add_u32 s68, s35, 0\n"
"buffer_load_dwordx4 v[96:99], v28, s[64:67], s68 offen offset:0 sc1\n"
"buffer_load_dwordx4 v[100:103], v28, s[64:67], s68 offen offset:1024 sc1\n"
"buffer_load_dwordx4 v[104:107], v28, s[64:67], s68 offen offset:2048 sc1\n"
"buffer_load_dwordx4 v[108:111], v28, s[64:67], s68 offen offset:3072 sc1\n"
"s_add_u32 s68, s35, 4096\n"
"buffer_load_dwordx4 v[112:115], v28, s[64:67], s68 offen offset:0 sc1\n"
"s_waitcnt vmcnt(0)\n"
"v_or3_b32 v76, v96, v97, v96\n"
"v_bitop3_b32 v77, v96, v97, v96 bitop3:0x80\n"
"v_or3_b32 v76, v98, v99, v76\n"
"v_bitop3_b32 v77, v98, v99, v77 bitop3:0x80\n"
"v_or3_b32 v76, v100, v101, v76\n"
"v_bitop3_b32 v77, v100, v101, v77 bitop3:0x80\n"
"v_or3_b32 v76, v102, v103, v76\n"
"v_bitop3_b32 v77, v102, v103, v77 bitop3:0x80\n"
"v_or3_b32 v76, v104, v105, v76\n"
"v_bitop3_b32 v77, v104, v105, v77 bitop3:0x80\n"
"v_or3_b32 v76, v106, v107, v76\n"
"v_bitop3_b32 v77, v106, v107, v77 bitop3:0x80\n"
"v_or3_b32 v76, v108, v109, v76\n"
"v_bitop3_b32 v77, v108, v109, v77 bitop3:0x80\n"
"v_or3_b32 v76, v110, v111, v76\n"
"v_bitop3_b32 v77, v110, v111, v77 bitop3:0x80\n"
"v_or3_b32 v76, v112, v113, v76\n"
"v_bitop3_b32 v77, v112, v113, v77 bitop3:0x80\n"
"v_or3_b32 v76, v114, v115, v76\n"
"v_bitop3_b32 v77, v114, v115, v77 bitop3:0x80\n"
"v_xor_b32 v76, v76, v20\n"
"v_xor_b32 v77, v77, v20\n"
"v_or_b32 v76, v76, v77\n"
"v_and_b32 v76, 0xfff, v76\n"
"v_cmp_ne_u32 vcc, 0, v76\n"
"s_nop 0\n"
"s_cmp_lg_u64 vcc, 0\n"
"s_cbranch_scc0 K1F_x_back\n"
"s_add_u32 s71, s71, 1\n"
"s_cmp_lt_u32 s71, 262144\n"
"s_cbranch_scc1 K1F_x_loop\n"
"s_branch K1F_x_back\n"
"K1X_s_slow:\n"
"s_nop 4\n"
"s_mov_b32 s71, 0\n"
"K1X_s_loop:\n"
"s_sleep 2\n"
"buffer_load_dwordx2 v[22:23], v24, s[64:67], 0 offen sc1\n"
"s_waitcnt vmcnt(0)\n"
"v_xor_b32 v22, s72, v22\n"
"v_xor_b32 v23, s73, v23\n"
"v_or_b32 v22, v22, v23\n"
"v_cmp_ne_u32 vcc, 0, v22\n"
"s_nop 0\n"
"s_cmp_lg_u64 vcc, 0\n"
"s_cbranch_scc0 K1X_s_back\n"
"s_add_u32 s71, s71, 1\n"
"s_cmp_lt_u32 s71, 262144\n"
"s_cbranch_scc1 K1X_s_loop\n"
"s_branch K1X_s_back\n"
"K1X_l_slow:\n"
"s_nop 4\n"
"s_mov_b32 s71, 0\n"
"K1X_l_loop:\n"
"s_sleep 2\n"
"buffer_load_dwordx4 v[64:67], v78, s[64:67], 0 offen offset:0 sc1\n"
"buffer_load_dwordx4 v[88:91], v79, s[64:67], s93 offen offset:0 sc1\n"
"s_waitcnt vmcnt(0)\n"
"v_xor_b32 v69, s72, v65\n"
"v_xor_b32 v70, s73, v67\n"
"v_or3_b32 v68, v69, v70, 0\n"
"v_xor_b32 v69, s72, v89\n"
"v_xor_b32 v70, s73, v91\n"
"v_or3_b32 v68, v69, v70, v68\n"
"v_cmp_ne_u32 vcc, 0, v68\n"
"s_nop 0\n"
"s_cmp_lg_u64 vcc, 0\n"
"s_cbranch_scc0 K1X_l_back\n"
"s_add_u32 s71, s71, 1\n"
"s_cmp_lt_u32 s71, 262144\n"
"s_cbranch_scc1 K1X_l_loop\n"
"s_branch K1X_l_back\n"
"K1X_m_slow:\n"
"s_nop 4\n"
"s_mov_b32 s71, 0\n"
"K1X_m_loop:\n"
"s_sleep 2\n"
"buffer_load_dwordx4 v[80:83], v79, s[64:67], s92 offen offset:0 sc1\n"
"buffer_load_dwordx4 v[84:87], v79, s[64:67], s92 offen offset:1024 sc1\n"
"s_waitcnt vmcnt(0)\n"
"v_xor_b32 v69, s72, v81\n"
"v_xor_b32 v70, s73, v83\n"
"v_or3_b32 v68, v69, v70, 0\n"
"v_xor_b32 v69, s72, v85\n"
"v_xor_b32 v70, s73, v87\n"
"v_or3_b32 v68, v69, v70, v68\n"
"v_cmp_ne_u32 vcc, 0, v68\n"
"s_nop 0\n"
"s_cmp_lg_u64 vcc, 0\n"
"s_cbranch_scc0 K1X_m_back\n"
"s_add_u32 s71, s71, 1\n"
"s_cmp_lt_u32 s71, 262144\n"
"s_cbranch_scc1 K1X_m_loop\n"
"s_branch K1X_m_back\n"
"K1X_g0_slow:\n"
"s_nop 4\n"
"s_mov_b32 s71, 0\n"
"K1X_g0_loop:\n"
"s_sleep 2\n"
"buffer_load_dwordx4 v[68:71], v38, s[64:67], 0 offen sc1\n"
"buffer_load_dwordx4 v[72:75], v39, s[64:67], 0 offen sc1\n"
"s_waitcnt vmcnt(0)\n"
"v_xor_b32 v76, s72, v69\n"
"v_xor_b32 v77, s73, v71\n"
"v_or_b32 v76, v76, v77\n"
"v_cmp_ne_u32 s[22:23], 0, v76\n"
"v_xor_b32 v76, s72, v73\n"
"v_xor_b32 v77, s73, v75\n"
"v_or_b32 v76, v76, v77\n"
"v_cmp_ne_u32 s[24:25], 0, v76\n"
"s_and_b64 s[22:23], s[22:23], s[18:19]\n"
"s_and_b64 s[24:25], s[24:25], s[20:21]\n"
"s_or_b64 s[22:23], s[22:23], s[24:25]\n"
"s_cbranch_scc0 K1X_g0_back\n"
"s_add_u32 s71, s71, 1\n"
"s_cmp_lt_u32 s71, 262144\n"
"s_cbranch_scc1 K1X_g0_loop\n"
"s_branch K1X_g0_back\n"
"K1X_r0_slow:\n"
"s_nop 4\n"
"s_mov_b32 s71, 0\n"
"K1X_r0_loop:\n"
"s_sleep 2\n"
"s_mov_b32 s70, 0\n"
"buffer_load_dwordx2 v[42:43], v3, s[64:67], s70 offen sc1\n"
"s_waitcnt vmcnt(0)\n"
"v_xor_b32 v42, s72, v42\n"
"v_xor_b32 v43, s73, v43\n"
"v_or_b32 v42, v42, v43\n"
"v_cmp_ne_u32 vcc, 0, v42\n"
"s_nop 0\n"
"s_cmp_lg_u64 vcc, 0\n"
"s_cbranch_scc0 K1X_r0_back\n"
"s_add_u32 s71, s71, 1\n"
"s_cmp_lt_u32 s71, 262144\n"
"s_cbranch_scc1 K1X_r0_loop\n"
"s_branch K1X_r0_back\n"
"K1X_r1_slow:\n"
"s_nop 4\n"
"s_mov_b32 s71, 0\n"
"K1X_r1_loop:\n"
"s_sleep 2\n"
"s_mov_b32 s70, 32768\n"
"buffer_load_dwordx2 v[42:43], v3, s[64:67], s70 offen sc1\n"
"s_waitcnt vmcnt(0)\n"
"v_xor_b32 v42, s72, v42\n"
"v_xor_b32 v43, s73, v43\n"
"v_or_b32 v42, v42, v43\n"
"v_cmp_ne_u32 vcc, 0, v42\n"
"s_nop 0\n"
"s_cmp_lg_u64 vcc, 0\n"
"s_cbranch_scc0 K1X_r1_back\n"
"s_add_u32 s71, s71, 1\n"
"s_cmp_lt_u32 s71, 262144\n"
"s_cbranch_scc1 K1X_r1_loop\n"
"s_branch K1X_r1_back\n"
"K1X_r2_slow:\n"
"s_nop 4\n"
"s_mov_b32 s71, 0\n"
"K1X_r2_loop:\n"
"s_sleep 2\n"
"s_mov_b32 s70, 65536\n"
"buffer_load_dwordx2 v[42:43], v3, s[64:67], s70 offen sc1\n"
"s_waitcnt vmcnt(0)\n"
"v_xor_b32 v42, s72, v42\n"
"v_xor_b32 v43, s73, v43\n"
"v_or_b32 v42, v42, v43\n"
"v_cmp_ne_u32 vcc, 0, v42\n"
"s_nop 0\n"
"s_cmp_lg_u64 vcc, 0\n"
"s_cbranch_scc0 K1X_r2_back\n"
"s_add_u32 s71, s71, 1\n"
"s_cmp_lt_u32 s71, 262144\n"
"s_cbranch_scc1 K1X_r2_loop\n"
"s_branch K1X_r2_back\n"
"K1X_x_slow:\n"
"s_nop 4\n"
"s_mov_b32 s71, 0\n"
"K1X_x_loop:\n"
"s_sleep 2\n"
"s_add_u32 s68, s35, 0\n"
"buffer_load_dwordx4 v[96:99], v28, s[64:67], s68 offen offset:0 sc1\n"
"buffer_load_dwordx4 v[100:103], v28, s[64:67], s68 offen offset:1024 sc1\n"
"buffer_load_dwordx4 v[104:107], v28, s[64:67], s68 offen offset:2048 sc1\n"
"buffer_load_dwordx4 v[108:111], v28, s[64:67], s68 offen offset:3072 sc1\n"
"s_add_u32 s68, s35, 4096\n"
"buffer_load_dwordx4 v[112:115], v28, s[64:67], s68 offen offset:0 sc1\n"
"s_waitcnt vmcnt(0)\n"
"v_or3_b32 v76, v96, v97, v96\n"
"v_bitop3_b32 v77, v96, v97, v96 bitop3:0x80\n"
"v_or3_b32 v76, v98, v99, v76\n"
"v_bitop3_b32 v77, v98, v99, v77 bitop3:0x80\n"
"v_or3_b32 v76, v100, v101, v76\n"
"v_bitop3_b32 v77, v100, v101, v77 bitop3:0x80\n"
"v_or3_b32 v76, v102, v103, v76\n"
"v_bitop3_b32 v77, v102, v103, v77 bitop3:0x80\n"
"v_or3_b32 v76, v104, v105, v76\n"
"v_bitop3_b32 v77, v104, v105, v77 bitop3:0x80\n"
"v_or3_b32 v76, v106, v107, v76\n"
"v_bitop3_b32 v77, v106, v107, v77 bitop3:0x80\n"
"v_or3_b32 v76, v108, v109, v76\n"
"v_bitop3_b32 v77, v108, v109, v77 bitop3:0x80\n"
"v_or3_b32 v76, v110, v111, v76\n"
"v_bitop3_b32 v77, v110, v111, v77 bitop3:0x80\n"
"v_or3_b32 v76, v112, v113, v76\n"
"v_bitop3_b32 v77, v112, v113, v77 bitop3:0x80\n"
"v_or3_b32 v76, v114, v115, v76\n"
"v_bitop3_b32 v77, v114, v115, v77 bitop3:0x80\n"
"v_xor_b32 v76, v76, v20\n"
"v_xor_b32 v77, v77, v20\n"
"v_or_b32 v76, v76, v77\n"
"v_and_b32 v76, 0xfff, v76\n"
"v_cmp_ne_u32 vcc, 0, v76\n"
"s_nop 0\n"
"s_cmp_lg_u64 vcc, 0\n"
"s_cbranch_scc0 K1X_x_back\n"
"s_add_u32 s71, s71, 1\n"
"s_cmp_lt_u32 s71, 262144\n"
"s_cbranch_scc1 K1X_x_loop\n"
"s_branch K1X_x_back\n"
"K1S_s_slow:\n"
"s_nop 4\n"
"s_mov_b32 s71, 0\n"
"K1S_s_loop:\n"
"s_sleep 2\n"
"buffer_load_dwordx2 v[22:23], v24, s[64:67], 0 offen sc1\n"
"s_waitcnt vmcnt(0)\n"
"v_xor_b32 v22, s72, v22\n"
"v_xor_b32 v23, s73, v23\n"
"v_or_b32 v22, v22, v23\n"
"v_cmp_ne_u32 vcc, 0, v22\n"
"s_nop 0\n"
"s_cmp_lg_u64 vcc, 0\n"
"s_cbranch_scc0 K1S_s_back\n"
"s_add_u32 s71, s71, 1\n"
"s_cmp_lt_u32 s71, 262144\n"
"s_cbranch_scc1 K1S_s_loop\n"
"s_branch K1S_s_back\n"
"K1S_l_slow:\n"
"s_nop 4\n"
"s_mov_b32 s71, 0\n"
"K1S_l_loop:\n"
"s_sleep 2\n"
"buffer_load_dwordx4 v[64:67], v78, s[64:67], 0 offen offset:0 sc1\n"
"buffer_load_dwordx4 v[80:83], v79, s[64:67], s93 offen offset:0 sc1\n"
"buffer_load_dwordx4 v[84:87], v79, s[64:67], s93 offen offset:1024 sc1\n"
"buffer_load_dwordx4 v[88:91], v79, s[64:67], s93 offen offset:2048 sc1\n"
"s_waitcnt vmcnt(0)\n"
"v_xor_b32 v69, s72, v65\n"
"v_xor_b32 v70, s73, v67\n"
"v_or3_b32 v68, v69, v70, 0\n"
"v_xor_b32 v69, s72, v81\n"
"v_xor_b32 v70, s73, v83\n"
"v_or3_b32 v68, v69, v70, v68\n"
"v_xor_b32 v69, s72, v85\n"
"v_xor_b32 v70, s73, v87\n"
"v_or3_b32 v68, v69, v70, v68\n"
"v_xor_b32 v69, s72, v89\n"
"v_xor_b32 v70, s73, v91\n"
"v_or3_b32 v68, v69, v70, v68\n"
"v_cmp_ne_u32 vcc, 0, v68\n"
"s_nop 0\n"
"s_cmp_lg_u64 vcc, 0\n"
"s_cbranch_scc0 K1S_l_back\n"
"s_add_u32 s71, s71, 1\n"
"s_cmp_lt_u32 s71, 262144\n"
"s_cbranch_scc1 K1S_l_loop\n"
"s_branch K1S_l_back\n"
"K1S_g0_slow:\n"
"s_nop 4\n"
"s_mov_b32 s71, 0\n"
"K1S_g0_loop:\n"
"s_sleep 2\n"
"buffer_load_dwordx4 v[136:139], v38, s[64:67], 0 offen sc1\n"
"buffer_load_dwordx4 v[140:143], v39, s[64:67], 0 offen sc1\n"
"s_waitcnt vmcnt(0)\n"
"v_xor_b32 v68, s72, v137\n"
"v_xor_b32 v69, s73, v139\n"
"v_or_b32 v68, v68, v69\n"
"v_cmp_ne_u32 s[22:23], 0, v68\n"
"v_xor_b32 v68, s72, v141\n"
"v_xor_b32 v69, s73, v143\n"
"v_or_b32 v68, v68, v69\n"
"v_cmp_ne_u32 s[24:25], 0, v68\n"
"s_and_b64 s[22:23], s[22:23], s[18:19]\n"
"s_and_b64 s[24:25], s[24:25], s[20:21]\n"
"s_or_b64 s[22:23], s[22:23], s[24:25]\n"
"s_cbranch_scc0 K1S_g0_back\n"
"s_add_u32 s71, s71, 1\n"
"s_cmp_lt_u32 s71, 262144\n"
"s_cbranch_scc1 K1S_g0_loop\n"
"s_branch K1S_g0_back\n"
"K1S_g1_slow:\n"
"s_nop 4\n"
"s_mov_b32 s71, 0\n"
"K1S_g1_loop:\n"
"s_sleep 2\n"
"buffer_load_dwordx4 v[144:147], v18, s[64:67], 0 offen sc1\n"
"buffer_load_dwordx4 v[148:151], v19, s[64:67], 0 offen sc1\n"
"s_waitcnt vmcnt(0)\n"
"v_xor_b32 v68, s72, v145\n"
"v_xor_b32 v69, s73, v147\n"
"v_or_b32 v68, v68, v69\n"
"v_cmp_ne_u32 s[22:23], 0, v68\n"
"v_xor_b32 v68, s72, v149\n"
"v_xor_b32 v69, s73, v151\n"
"v_or_b32 v68, v68, v69\n"
"v_cmp_ne_u32 s[24:25], 0, v68\n"
"s_and_b64 s[22:23], s[22:23], s[18:19]\n"
"s_and_b64 s[24:25], s[24:25], s[20:21]\n"
"s_or_b64 s[22:23], s[22:23], s[24:25]\n"
"s_cbranch_scc0 K1S_g1_back\n"
"s_add_u32 s71, s71, 1\n"
"s_cmp_lt_u32 s71, 262144\n"
"s_cbranch_scc1 K1S_g1_loop\n"
"s_branch K1S_g1_back\n"
"K1S_g2_slow:\n"
"s_nop 4\n"
"s_mov_b32 s71, 0\n"
"K1S_g2_loop:\n"
"s_sleep 2\n"
"buffer_load_dwordx4 v[152:155], v20, s[64:67], 0 offen sc1\n"
"buffer_load_dwordx4 v[156:159], v21, s[64:67], 0 offen sc1\n"
"s_waitcnt vmcnt(0)\n"
"v_xor_b32 v68, s72, v153\n"
"v_xor_b32 v69, s73, v155\n"
"v_or_b32 v68, v68, v69\n"
"v_cmp_ne_u32 s[22:23], 0, v68\n"
"v_xor_b32 v68, s72, v157\n"
"v_xor_b32 v69, s73, v159\n"
"v_or_b32 v68, v68, v69\n"
"v_cmp_ne_u32 s[24:25], 0, v68\n"
"s_and_b64 s[22:23], s[22:23], s[18:19]\n"
"s_and_b64 s[24:25], s[24:25], s[20:21]\n"
"s_or_b64 s[22:23], s[22:23], s[24:25]\n"
"s_cbranch_scc0 K1S_g2_back\n"
"s_add_u32 s71, s71, 1\n"
"s_cmp_lt_u32 s71, 262144\n"
"s_cbranch_scc1 K1S_g2_loop\n"
"s_branch K1S_g2_back\n"
"K1S_r0_slow:\n"
"s_nop 4\n"
"s_mov_b32 s71, 0\n"
"K1S_r0_loop:\n"
"s_sleep 2\n"
"s_mov_b32 s70, 0\n"
"buffer_load_dwordx2 v[42:43], v3, s[64:67], s70 offen sc1\n"
"s_waitcnt vmcnt(0)\n"
"v_xor_b32 v42, s72, v42\n"
"v_xor_b32 v43, s73, v43\n"
"v_or_b32 v42, v42, v43\n"
"v_cmp_ne_u32 vcc, 0, v42\n"
"s_nop 0\n"
"s_cmp_lg_u64 vcc, 0\n"
"s_cbranch_scc0 K1S_r0_back\n"
"s_add_u32 s71, s71, 1\n"
"s_cmp_lt_u32 s71, 262144\n"
"s_cbranch_scc1 K1S_r0_loop\n"
"s_branch K1S_r0_back\n"
"K1S_r1_slow:\n"
"s_nop 4\n"
"s_mov_b32 s71, 0\n"
"K1S_r1_loop:\n"
"s_sleep 2\n"
"s_mov_b32 s70, 32768\n"
"buffer_load_dwordx2 v[42:43], v3, s[64:67], s70 offen sc1\n"
"s_waitcnt vmcnt(0)\n"
"v_xor_b32 v42, s72, v42\n"
"v_xor_b32 v43, s73, v43\n"
"v_or_b32 v42, v42, v43\n"
"v_cmp_ne_u32 vcc, 0, v42\n"
"s_nop 0\n"
"s_cmp_lg_u64 vcc, 0\n"
"s_cbranch_scc0 K1S_r1_back\n"
"s_add_u32 s71, s71, 1\n"
"s_cmp_lt_u32 s71, 262144\n"
"s_cbranch_scc1 K1S_r1_loop\n"
"s_branch K1S_r1_back\n"
"K1S_r2_slow:\n"
"s_nop 4\n"
"s_mov_b32 s71, 0\n"
"K1S_r2_loop:\n"
"s_sleep 2\n"
"s_mov_b32 s70, 65536\n"
"buffer_load_dwordx2 v[42:43], v3, s[64:67], s70 offen sc1\n"
"s_waitcnt vmcnt(0)\n"
"v_xor_b32 v42, s72, v42\n"
"v_xor_b32 v43, s73, v43\n"
"v_or_b32 v42, v42, v43\n"
"v_cmp_ne_u32 vcc, 0, v42\n"
"s_nop 0\n"
"s_cmp_lg_u64 vcc, 0\n"
"s_cbranch_scc0 K1S_r2_back\n"
"s_add_u32 s71, s71, 1\n"
"s_cmp_lt_u32 s71, 262144\n"
"s_cbranch_scc1 K1S_r2_loop\n"
"s_branch K1S_r2_back\n"
"K1S_x_slow:\n"
"s_nop 4\n"
"s_mov_b32 s71, 0\n"
"K1S_x_loop:\n"
"s_sleep 2\n"
"s_add_u32 s68, s35, 0\n"
"buffer_load_dwordx4 v[96:99], v28, s[64:67], s68 offen offset:0 sc1\n"
"buffer_load_dwordx4 v[100:103], v28, s[64:67], s68 offen offset:1024 sc1\n"
"buffer_load_dwordx4 v[104:107], v28, s[64:67], s68 offen offset:2048 sc1\n"
"buffer_load_dwordx4 v[108:111], v28, s[64:67], s68 offen offset:3072 sc1\n"
"s_add_u32 s68, s35, 4096\n"
"buffer_load_dwordx4 v[112:115], v28, s[64:67], s68 offen offset:0 sc1\n"
"s_waitcnt vmcnt(0)\n"
"v_or3_b32 v76, v96, v97, v96\n"
"v_bitop3_b32 v77, v96, v97, v96 bitop3:0x80\n"
"v_or3_b32 v76, v98, v99, v76\n"
"v_bitop3_b32 v77, v98, v99, v77 bitop3:0x80\n"
"v_or3_b32 v76, v100, v101, v76\n"
"v_bitop3_b32 v77, v100, v101, v77 bitop3:0x80\n"
"v_or3_b32 v76, v102, v103, v76\n"
"v_bitop3_b32 v77, v102, v103, v77 bitop3:0x80\n"
"v_or3_b32 v76, v104, v105, v76\n"
"v_bitop3_b32 v77, v104, v105, v77 bitop3:0x80\n"
"v_or3_b32 v76, v106, v107, v76\n"
"v_bitop3_b32 v77, v106, v107, v77 bitop3:0x80\n"
"v_or3_b32 v76, v108, v109, v76\n"
"v_bitop3_b32 v77, v108, v109, v77 bitop3:0x80\n"
"v_or3_b32 v76, v110, v111, v76\n"
"v_bitop3_b32 v77, v110, v111, v77 bitop3:0x80\n"
"v_or3_b32 v76, v112, v113, v76\n"
"v_bitop3_b32 v77, v112, v113, v77 bitop3:0x80\n"
"v_or3_b32 v76, v114, v115, v76\n"
"v_bitop3_b32 v77, v114, v115, v77 bitop3:0x80\n"
"v_xor_b32 v76, v76, v20\n"
"v_xor_b32 v77, v77, v20\n"
"v_or_b32 v76, v76, v77\n"
"v_and_b32 v76, 0xfff, v76\n"
"v_cmp_ne_u32 vcc, 0, v76\n"
"s_nop 0\n"
"s_cmp_lg_u64 vcc, 0\n"
"s_cbranch_scc0 K1S_x_back\n"
"s_add_u32 s71, s71, 1\n"
"s_cmp_lt_u32 s71, 262144\n"
"s_cbranch_scc1 K1S_x_loop\n"
"s_branch K1S_x_back\n"
"K1END:\n"
  :: "s"(__builtin_amdgcn_kernarg_segment_ptr()), "s"(blockIdx.x), "v"(threadIdx.x), "s"((unsigned)(size_t)lds) : CLOB);
}

extern "C" void kernel_launch(void* const* d_in, const int* in_sizes, int n_in, void* d_out, int out_size, void* d_ws, size_t ws_size,
                              hipStream_t stream) {
  const Tab tab = {{0x34330100u, 0x504u, 0x36350302u, 0x706u, 0x38370d0cu, 0x908u, 0x3f390f0eu, 0x1110u, 0x41401918u, 0x1312u, 0x4342241au, 0x1514u, 0x45442625u, 0x1c1bu, 0x4c4b3130u, 0x1e1du, 0x4e4d3c32u, 0x201fu, 0x504f3e3du, 0x2721u, 0x58574948u, 0x2928u, 0x5a59544au, 0x2b2au, 0x5c5b5655u, 0x2d2cu, 0xb0au, 0x1716u, 0x2f2e2322u, 0x3b3au, 0x52514746u, 0x5e5du, 0x7060504u, 0x11100908u, 0x15141312u, 0x1e1d1c1bu, 0x2721201fu, 0x2b2a2928u, 0x17162d2cu, 0x2f2e2322u, 0x47463b3au, 0x5e5d5251u, 0x34330100u, 0x36350302u, 0x38370d0cu, 0x3f390f0eu, 0x41401918u, 0x4342241au, 0x45442625u, 0x4c4b3130u, 0x4e4d3c32u, 0x504f3e3du, 0x58574948u, 0x5a59544au, 0x5c5b5655u, 0xb0au}};
  s5_block<<<256, 256, 0, stream>>>(d_in[0], d_in[1], d_in[2], d_in[3], d_in[4], d_in[5], d_in[6], d_in[7], d_in[10], d_in[11], d_in[12], d_ws,
                                    d_in[8], d_out, tab);
}
```
